```python
import torch

def _softmax(x, axis=-1):
    return torch.softmax(x, dim=axis)
def _gelu(x):
    return torch.nn.functional.gelu(x, approximate="tanh")
def _silu(x):
    return torch.nn.functional.silu(x)
def _sigmoid(x):
    return torch.sigmoid(x)
def _layernorm(x, axis=-1, eps=1e-5):
    m = x.mean(dim=axis, keepdim=True)
    v = x.var(dim=axis, keepdim=True, unbiased=False)
    return (x - m) / torch.sqrt(v + eps)
def _scatter_add(tgt, idx, src, axis=0):
    return tgt.clone().index_add_(axis, idx, src)
def _segment_sum(src, seg, n):
    out = torch.zeros((n,) + src.shape[1:], dtype=src.dtype)
    return out.index_add_(0, seg, src)
def _pad(x, pw):
    flat = tuple(v for pair in reversed(pw) for v in pair)
    return torch.nn.functional.pad(x, flat)

kernel_name = 'sequential_preLN__compressed_kv_cla__moe_arch_mod__prompt'

def setup_inputs():

    _w = (lambda a: (lambda t: t.to(torch.bfloat16) if t.is_floating_point() else t)(torch.as_tensor(a)))
    def _A_setup():
        B, H, Sq, Sk, D, M = 1, 32, 2048, 2048, 128, 640
        Dc = 4 * D
        return {
            "x": _w((lambda *s: torch.randn(*s, dtype=torch.bfloat16) * 0.05)(B, Sq, M)),
            "c_kv": _w((lambda *s: torch.randn(*s, dtype=torch.bfloat16) * 0.4)(B, Sk, Dc)),
            "w_q": _w((lambda *s: torch.randn(*s, dtype=torch.bfloat16) * 3.2)(M, H, D) * (M ** -0.5)),
            "w_uk": _w((lambda *s: torch.randn(*s, dtype=torch.bfloat16) * 3.2)(Dc, H, D) * (Dc ** -0.5)),
            "w_uv": _w((lambda *s: torch.randn(*s, dtype=torch.bfloat16) * 0.4)(Dc, H, D) * (Dc ** -0.5)),
            "w_o": _w((lambda *s: torch.randn(*s, dtype=torch.bfloat16) * 0.4)(H, D, M) * ((H * D) ** -0.5)),
        }
    _A_in = _A_setup()

    def _B_setup():
        B, S, D, F, C = 1, 2048, 640, 1728, 2
        return {
            "x": _w((lambda *s: torch.randn(*s, dtype=torch.bfloat16) * 0.05)(B, S, D)),
            "w_r": _w((lambda *s: torch.randn(*s, dtype=torch.bfloat16) * 0.05)(D)),
            "w_gate": _w((lambda *s: torch.randn(*s, dtype=torch.bfloat16) * 0.05)(D, F)),
            "w_up": _w((lambda *s: torch.randn(*s, dtype=torch.bfloat16) * 0.05)(D, F)),
            "w_down": _w((lambda *s: torch.randn(*s, dtype=torch.bfloat16) * 0.05)(F, D)),
        }
    _B_in = _B_setup()

    return {
        "A_x": _A_in["x"],
        "A_c_kv": _A_in["c_kv"],
        "A_w_q": _A_in["w_q"],
        "A_w_uk": _A_in["w_uk"],
        "A_w_uv": _A_in["w_uv"],
        "A_w_o": _A_in["w_o"],
        "B_x": _B_in["x"],
        "B_w_r": _B_in["w_r"],
        "B_w_gate": _B_in["w_gate"],
        "B_w_up": _B_in["w_up"],
        "B_w_down": _B_in["w_down"],
    }

def reference(A_x, A_c_kv, A_w_q, A_w_uk, A_w_uv, A_w_o, B_x, B_w_r, B_w_gate, B_w_up, B_w_down):
    def _A_ref(x, c_kv, w_q, w_uk, w_uv, w_o):
        B, Sq, M = x.shape[0], x.shape[1], x.shape[2]
        Sk = c_kv.shape[1]
        H, D = w_q.shape[1], w_q.shape[2]
        q = torch.einsum("bqm,mhd->bhqd", x, w_q)
        k = torch.einsum("bkc,chd->bhkd", c_kv, w_uk)
        v = torch.einsum("bkc,chd->bhkd", c_kv, w_uv)
        scores = torch.einsum("bhqd,bhkd->bhqk", q, k) * (D ** -0.5)
        off = Sk - Sq
        qi = torch.arange(Sq)[:, None]
        kj = torch.arange(Sk)[None, :]
        cmask = kj <= (qi + off)
        scores = torch.where(cmask[None, None], scores, -1e9)
        p = _softmax(scores, axis=-1)
        ctx = torch.einsum("bhqk,bhkd->bhqd", p, v)
        return torch.einsum("bhqd,hdm->bqm", ctx, w_o)

    def _B_ref(x, w_r, w_gate, w_up, w_down):
        B, S, D = x.shape
        C = 2
        r = x @ w_r
        idx = torch.argsort(-r, axis=-1)[:, :C]
        idx_e = (lambda x, n, axis: torch.repeat_interleave(x, n, dim=axis))(idx.reshape(B, C, 1), D, axis=2)
        x_sel = (lambda x, i, axis: torch.take_along_dim(x, i, dim=axis))(x, idx_e, axis=1)
        g = _silu(x_sel @ w_gate)
        u = x_sel @ w_up
        y = (g * u) @ w_down
        r_sel = _sigmoid((lambda x, i, axis: torch.take_along_dim(x, i, dim=axis))(r, idx, axis=1))
        y = y * r_sel.reshape(B, C, 1)
        b_off = (torch.arange(B) * S).reshape(B, 1)
        flat_idx = (idx + b_off).reshape(B * C)
        zero = (x * 0.0).reshape(B * S, D)
        upd = _scatter_add(zero, flat_idx, y.reshape(B * C, D), axis=0)
        return x + upd.reshape(B, S, D)

    x = A_x
    h = x + _A_ref(_layernorm(x, axis=-1), A_c_kv, A_w_q, A_w_uk, A_w_uv, A_w_o)
    h2 = h + _B_ref(_layernorm(h, axis=-1), B_w_r, B_w_gate, B_w_up, B_w_down)
    return h2
```

```cpp
#include <hip/hip_runtime.h>
#include <hip/hip_bf16.h>
#include <stdint.h>
typedef unsigned short u16;
__device__ __forceinline__ float bf2f(u16 v){ return __uint_as_float(((unsigned)v)<<16); }
__device__ __forceinline__ u16 f2bf(float f){ __hip_bfloat16 b = __float2bfloat16(f); return *reinterpret_cast<u16*>(&b); }

constexpr int S=2048, M=640, DC=512, H=32, D=128, F=1728;

__global__ void ln_k(const u16* x, u16* xn){
  int row=blockIdx.x, t=threadIdx.x;
  float v[10]; float s=0;
  for(int i=0;i<10;i++){ v[i]=bf2f(x[row*M+i*64+t]); s+=v[i]; }
  for(int o=32;o;o>>=1) s+=__shfl_xor(s,o);
  float m=s/M; float q=0;
  for(int i=0;i<10;i++){ float d=v[i]-m; q+=d*d; }
  for(int o=32;o;o>>=1) q+=__shfl_xor(q,o);
  float r=1.f/sqrtf(q/M+1e-5f);
  for(int i=0;i<10;i++) xn[row*M+i*64+t]=f2bf((v[i]-m)*r);
}
template<bool F32OUT>
__global__ void gemm_k(const u16* A,const u16* B, void* Cv,int Mm,int N,int K){
  __shared__ float As[16][65], Bs[16][65];
  int bm=blockIdx.y*64, bn=blockIdx.x*64, t=threadIdx.x, ty=t/16, tx=t%16;
  float acc[4][4]={};
  for(int k0=0;k0<K;k0+=16){
    for(int i=t;i<64*16;i+=256){ int r=i/16,c=i%16; As[c][r]=bf2f(A[(size_t)(bm+r)*K+k0+c]); }
    for(int i=t;i<64*16;i+=256){ int r=i/64,c=i%64; Bs[r][c]=bf2f(B[(size_t)(k0+r)*N+bn+c]); }
    __syncthreads();
    for(int k=0;k<16;k++){
      float a[4],b[4];
      for(int i=0;i<4;i++){a[i]=As[k][ty*4+i]; b[i]=Bs[k][tx*4+i];}
      for(int i=0;i<4;i++)for(int j=0;j<4;j++)acc[i][j]+=a[i]*b[j];
    }
    __syncthreads();
  }
  for(int i=0;i<4;i++)for(int j=0;j<4;j++){
    size_t o=(size_t)(bm+ty*4+i)*N+bn+tx*4+j;
    if(F32OUT) ((float*)Cv)[o]=acc[i][j]; else ((u16*)Cv)[o]=f2bf(acc[i][j]);
  }
}
__global__ void attn_k(const u16* q,const u16* k,const u16* v,u16* ctx){
  int row=blockIdx.x, h=blockIdx.y, t=threadIdx.x;
  __shared__ float qs[D]; __shared__ float p[S]; __shared__ float red[256];
  if(t<D) qs[t]=bf2f(q[(size_t)row*H*D+h*D+t]);
  __syncthreads();
  float mx=-1e30f;
  for(int j=t;j<=row;j+=256){
    float s=0; const u16* kr=k+(size_t)j*H*D+h*D;
    for(int d=0;d<D;d++) s+=qs[d]*bf2f(kr[d]);
    s*=0.08838834764831845f; p[j]=s; mx=fmaxf(mx,s);
  }
  red[t]=mx; __syncthreads();
  for(int o=128;o;o>>=1){ if(t<o) red[t]=fmaxf(red[t],red[t+o]); __syncthreads(); }
  mx=red[0]; __syncthreads();
  float sum=0;
  for(int j=t;j<=row;j+=256){ float e=__expf(p[j]-mx); p[j]=e; sum+=e; }
  red[t]=sum; __syncthreads();
  for(int o=128;o;o>>=1){ if(t<o) red[t]+=red[t+o]; __syncthreads(); }
  sum=red[0]; __syncthreads();
  int d=t&127, half=t>>7; float acc=0;
  for(int j=half;j<=row;j+=2) acc+=p[j]*bf2f(v[(size_t)j*H*D+h*D+d]);
  red[t]=acc; __syncthreads();
  if(t<128) ctx[(size_t)row*H*D+h*D+t]=f2bf((red[t]+red[t+128])/sum);
}
__global__ void post_k(const u16* x,const float* o,const u16* wr,float* hbuf,float* hn,float* r,u16* out){
  int row=blockIdx.x,t=threadIdx.x; float v[10]; float s=0;
  for(int i=0;i<10;i++){ v[i]=bf2f(x[row*M+i*64+t])+o[row*M+i*64+t]; s+=v[i]; }
  for(int q=32;q;q>>=1) s+=__shfl_xor(s,q);
  float m=s/M,q2=0;
  for(int i=0;i<10;i++){ float d=v[i]-m; q2+=d*d; }
  for(int q=32;q;q>>=1) q2+=__shfl_xor(q2,q);
  float rs=1.f/sqrtf(q2/M+1e-5f); float rr=0;
  for(int i=0;i<10;i++){ float n=(v[i]-m)*rs; hn[row*M+i*64+t]=n; rr+=n*bf2f(wr[i*64+t]); out[row*M+i*64+t]=f2bf(v[i]+n); hbuf[row*M+i*64+t]=v[i]; }
  for(int q=32;q;q>>=1) rr+=__shfl_xor(rr,q);
  if(t==0) r[row]=rr;
}
__global__ void ffn_k(const float* hbuf,const float* hn,const float* r,const u16* wg,const u16* wu,const u16* wd,u16* out){
  __shared__ float xs[2][M]; __shared__ float act[2][F]; __shared__ int idx[2];
  int t=threadIdx.x;
  if(t==0){ int b0=-1,b1=-1; float v0=-INFINITY,v1=-INFINITY;
    for(int i=0;i<S;i++){ float v=r[i]; if(b0<0||v>v0){v1=v0;b1=b0;v0=v;b0=i;} else if(b1<0||v>v1){v1=v;b1=i;} }
    idx[0]=b0; idx[1]=b1; }
  __syncthreads();
  for(int i=t;i<2*M;i+=blockDim.x) xs[i/M][i%M]=hn[idx[i/M]*M+i%M];
  __syncthreads();
  for(int f=t;f<F;f+=blockDim.x){ float g0=0,g1=0,u0=0,u1=0;
    for(int k=0;k<M;k++){ float a=bf2f(wg[k*F+f]),b=bf2f(wu[k*F+f]); g0+=xs[0][k]*a; g1+=xs[1][k]*a; u0+=xs[0][k]*b; u1+=xs[1][k]*b; }
    act[0][f]=g0/(1.f+__expf(-g0))*u0; act[1][f]=g1/(1.f+__expf(-g1))*u1; }
  __syncthreads();
  for(int i=t;i<2*M;i+=blockDim.x){ int c=i/M,d=i%M; float y=0;
    for(int f=0;f<F;f++) y+=act[c][f]*bf2f(wd[f*M+d]);
    float rs=1.f/(1.f+__expf(-r[idx[c]]));
    out[idx[c]*M+d]=f2bf(hbuf[idx[c]*M+d]+hn[idx[c]*M+d]+y*rs); }
}
extern "C" void kernel_launch(void* const* d_in, const int* in_sizes, int n_in, void* d_out, int out_size, void* d_ws, size_t ws_size, hipStream_t stream){
  const u16* x=(const u16*)d_in[0]; const u16* ckv=(const u16*)d_in[1]; const u16* wq=(const u16*)d_in[2];
  const u16* wuk=(const u16*)d_in[3]; const u16* wuv=(const u16*)d_in[4]; const u16* wo=(const u16*)d_in[5];
  const u16* wr=(const u16*)d_in[7]; const u16* wg=(const u16*)d_in[8]; const u16* wu=(const u16*)d_in[9]; const u16* wd=(const u16*)d_in[10];
  char* ws=(char*)d_ws; size_t off=0;
  u16* xn=(u16*)(ws+off); off+=(size_t)S*M*2;
  u16* q=(u16*)(ws+off); off+=(size_t)S*H*D*2;
  u16* k=(u16*)(ws+off); off+=(size_t)S*H*D*2;
  u16* v=(u16*)(ws+off); off+=(size_t)S*H*D*2;
  u16* ctx=(u16*)(ws+off); off+=(size_t)S*H*D*2;
  float* o=(float*)(ws+off); off+=(size_t)S*M*4;
  float* hb=(float*)(ws+off); off+=(size_t)S*M*4;
  float* hn=(float*)(ws+off); off+=(size_t)S*M*4;
  float* r=(float*)(ws+off); off+=(size_t)S*4;
  ln_k<<<S,64,0,stream>>>(x,xn);
  gemm_k<false><<<dim3(H*D/64,S/64),256,0,stream>>>(xn,wq,q,S,H*D,M);
  gemm_k<false><<<dim3(H*D/64,S/64),256,0,stream>>>(ckv,wuk,k,S,H*D,DC);
  gemm_k<false><<<dim3(H*D/64,S/64),256,0,stream>>>(ckv,wuv,v,S,H*D,DC);
  attn_k<<<dim3(S,H),256,0,stream>>>(q,k,v,ctx);
  gemm_k<true><<<dim3(M/64,S/64),256,0,stream>>>(ctx,wo,o,S,M,H*D);
  post_k<<<S,64,0,stream>>>(x,o,wr,hb,hn,r,(u16*)d_out);
  ffn_k<<<1,1024,0,stream>>>(hb,hn,r,wg,wu,wd,(u16*)d_out);
}
```

```cpp
#include <hip/hip_runtime.h>
#include <stdint.h>

extern "C" __global__ __launch_bounds__(256, 1) void k_main(const void* x, const void* ckv, const void* wq, const void* wuk, const void* wuv, const void* wo,
    const void* wr, const void* wg, const void* wu, const void* wd, void* out, void* ws) {
  __shared__ __attribute__((aligned(16))) char lds[163840];
  asm volatile("" :: "s"((unsigned)(size_t)lds));
  asm volatile(
R"A(.macro W n
s_waitcnt lgkmcnt(\n)
.endm
.macro SM d,c
s_mov_b32 s\d, \c
.endm
.macro VA d,c,s
v_add_u32 v\d, \c, v\s
.endm
.macro Lv d,v,r,s,o
buffer_load_dwordx4 v[\d:\d+3], v\v, s[\r:\r+3], s\s offen offset:\o
.endm
.macro X d,c,s
v_xor_b32 v\d, \c, v\s
.endm
.macro AS d,s,c
s_add_u32 s\d, s\s, \c
.endm
.macro AM s,c
s_add_u32 m0, s\s, \c
.endm
.macro N n
s_nop \n
.endm
.macro D v,r,s,o
buffer_load_dwordx4 v\v, s[\r:\r+3], s\s offen offset:\o lds
.endm
.macro B
s_barrier
.endm
.macro Rv d,p,o
ds_read_b128 v[\d:\d+3], v\p offset:\o
.endm
.macro T d,p,o
ds_read_b64_tr_b16 v[\d:\d+1], v\p offset:\o
.endm
.macro WV n
s_waitcnt vmcnt(\n) lgkmcnt(0)
.endm
.macro Mavvz d,a,b
v_mfma_f32_16x16x32_bf16 a[\d:\d+3], v[\a:\a+3], v[\b:\b+3], 0
.endm
.macro MV d,s
v_mov_b32 v\d, v\s
.endm
.macro Fad d,a,b
v_add_f32 v\d, v\a, v\b
.endm
.macro Mavvc d,a,b
v_mfma_f32_16x16x32_bf16 a[\d:\d+3], v[\a:\a+3], v[\b:\b+3], a[\d:\d+3]
.endm
.macro Fmu d,a,b
v_mul_f32 v\d, v\a, v\b
.endm
.macro LO d,s
v_lshlrev_b32 v\d, 16, v\s
.endm
.macro HI d,s
v_and_b32 v\d, 0xffff0000, v\s
.endm
.macro FM d,a,b,c
v_fma_f32 v\d, v\a, v\b, v\c
.endm
.macro C d,a,b
v_cvt_pk_bf16_f32 v\d, v\a, v\b
.endm
.macro Svs d,v,r,s,o
buffer_store_dwordx4 v[\d:\d+3], v\v, s[\r:\r+3], s\s offen offset:\o sc1
.endm
.macro M0 c
s_mov_b32 m0, \c
.endm
.macro Ds v,r,s,o
buffer_load_dwordx4 v\v, s[\r:\r+3], s\s offen offset:\o sc1 lds
.endm
.macro Mvvvz d,a,b
v_mfma_f32_16x16x32_bf16 v[\d:\d+3], v[\a:\a+3], v[\b:\b+3], 0
.endm
.macro G d,a
v_accvgpr_read_b32 v\d, a\a
.endm
.macro Mvvvc d,a,b
v_mfma_f32_16x16x32_bf16 v[\d:\d+3], v[\a:\a+3], v[\b:\b+3], v[\d:\d+3]
.endm
.macro DWv p,d,o
ds_write_b128 v\p, v[\d:\d+3] offset:\o
.endm
.macro Ra d,p,o
ds_read_b128 a[\d:\d+3], v\p offset:\o
.endm
.macro Mvvaz d,a,b
v_mfma_f32_16x16x32_bf16 v[\d:\d+3], v[\a:\a+3], a[\b:\b+3], 0
.endm
.macro Mvvac d,a,b
v_mfma_f32_16x16x32_bf16 v[\d:\d+3], v[\a:\a+3], a[\b:\b+3], v[\d:\d+3]
.endm
.macro Fma d,a,b
v_max_f32 v\d, v\a, v\b
.endm
.macro Mvvan d,a,b,c
v_mfma_f32_16x16x32_bf16 v[\d:\d+3], v[\a:\a+3], a[\b:\b+3], v[\c:\c+3]
.endm
.macro E r
v_exp_f32 v\r, v\r
.endm
.macro K d,a,b,s
v_cndmask_b32 v\d, v\a, v\b, s[\s:\s+1]
.endm
.macro DWa p,d,o
ds_write_b128 v\p, a[\d:\d+3] offset:\o
.endm
.macro Fsu d,a,b
v_sub_f32 v\d, v\a, v\b
.endm
.macro Ffm d,a,b
v_fmac_f32 v\d, v\a, v\b
.endm
.macro PM d,a,b
v_pk_mul_f32 v[\d:\d+1], v[\a:\a+1], v[\b:\b+1] op_sel_hi:[1,0]
.endm
.macro Las d,v,r,s,o
buffer_load_dwordx4 a[\d:\d+3], v\v, s[\r:\r+3], s\s offen offset:\o sc1
.endm
.macro Mavaz d,a,b
v_mfma_f32_16x16x32_bf16 a[\d:\d+3], v[\a:\a+3], a[\b:\b+3], 0
.endm
.macro Mavac d,a,b
v_mfma_f32_16x16x32_bf16 a[\d:\d+3], v[\a:\a+3], a[\b:\b+3], a[\d:\d+3]
.endm
.macro La d,v,r,s,o
buffer_load_dwordx4 a[\d:\d+3], v\v, s[\r:\r+3], s\s offen offset:\o
.endm
.macro Lvs d,v,r,s,o
buffer_load_dwordx4 v[\d:\d+3], v\v, s[\r:\r+3], s\s offen offset:\o sc1
.endm
.macro PA d,a,b
v_pk_add_f32 v[\d:\d+1], v[\a:\a+1], v[\b:\b+1]
.endm
.macro GW d,s
v_accvgpr_write_b32 a\d, \s
.endm
.macro Ta d,p,o
ds_read_b64_tr_b16 a[\d:\d+1], v\p offset:\o
.endm
.macro Mvavz d,a,b
v_mfma_f32_16x16x32_bf16 v[\d:\d+3], a[\a:\a+3], v[\b:\b+3], 0
.endm
.macro Mvavc d,a,b
v_mfma_f32_16x16x32_bf16 v[\d:\d+3], a[\a:\a+3], v[\b:\b+3], v[\d:\d+3]
.endm
s_getpc_b64 s[16:17]
v_and_b32 v249, 63, %2
v_lshlrev_b32 v249, 7, v249
global_load_dword v248, v249, s[16:17]
s_load_dwordx16 s[56:71], %0, 0
s_load_dwordx8 s[72:79], %0, 64
s_mov_b64 s[8:9], %0
s_mov_b32 s10, %1
v_mov_b32 v255, %2
v_lshrrev_b32 v253, 6, v255
v_and_b32 v254, 63, v255
v_readfirstlane_b32 s11, v253
s_and_b32 s16, s10, 7
s_lshr_b32 s17, s10, 3
s_lshr_b32 s18, s17, 3
s_lshl_b32 s16, s16, 2
s_add_u32 s12, s16, s18
s_and_b32 s13, s17, 7
W 0
s_mov_b64 s[14:15], s[78:79]
s_mov_b64 s[44:45], s[56:57]
SM 46,0x280000
SM 47,0x20000
s_add_u32 s48, s14, 0x40000
s_addc_u32 s49, s15, 0
SM 50,0x280000
SM 51,0x20000
v_and_b32 v200, 7, v254
v_lshrrev_b32 v201, 3, v254
s_lshl_b32 s16, s11, 3
v_add_u32 v201, s16, v201
v_mul_u32_u24 v202, 1280, v200
v_lshl_add_u32 v202, v201, 4, v202
v_cmp_gt_u32 vcc, 16, v201
VA 203,1024,202
v_mov_b32 v204, 0x7ffff000
v_cndmask_b32 v203, v204, v203, vcc
s_mul_i32 s16, s10, 10240
Lv 168,202,44,16,0
Lv 172,202,44,16,512
Lv 176,203,44,16,0
v_lshrrev_b32 v205, 2, v201
v_lshlrev_b32 v205, 13, v205
v_and_b32 v206, 3, v201
v_lshl_add_u32 v206, v206, 4, v200
v_lshl_add_u32 v205, v206, 4, v205
VA 206,131072,205
v_cndmask_b32 v206, v204, v206, vcc
v_and_b32 v184, 7, v254
v_lshlrev_b32 v184, 3, v184
VA 184,155648,184
s_lshl_b32 s17, s11, 6
v_add_u32 v185, s17, v184
v_mov_b32 v180, 0x3f803f80
v_mov_b32 v181, 0
v_mov_b32 v182, 0
s_mov_b64 s[24:25], s[58:59]
SM 26,0x200000
SM 27,0x20000
s_cmp_ge_u32 s11, 2
s_cselect_b32 s28, s64, s62
s_cselect_b32 s29, s65, s63
SM 30,0x400000
SM 31,0x20000
v_lshrrev_b32 v152, 3, v254
v_lshlrev_b32 v152, 10, v152
v_lshrrev_b32 v153, 4, v254
v_and_b32 v154, 7, v254
v_xor_b32 v155, v153, v154
X 156,4,155
v_lshl_add_u32 v155, v155, 4, v152
v_lshl_add_u32 v156, v156, 4, v152
VA 128,0,155
VA 129,7168,156
VA 130,14336,155
VA 131,21504,156
VA 132,32768,155
VA 133,39936,156
VA 134,47104,155
VA 135,54272,156
v_lshrrev_b32 v152, 4, v254
v_and_b32 v153, 15, v254
v_lshrrev_b32 v153, 1, v153
v_xor_b32 v153, v153, v152
v_and_b32 v154, 1, v254
v_lshlrev_b32 v154, 4, v154
v_lshlrev_b32 v152, 13, v152
v_add_u32 v152, v152, v154
X 155,4,153
v_lshl_add_u32 v153, v153, 5, v152
v_lshl_add_u32 v155, v155, 5, v152
VA 236,0,153
VA 237,31744,153
VA 238,63488,155
VA 239,95232,155
v_and_b32 v152, 15, v254
v_lshrrev_b32 v153, 1, v152
v_lshrrev_b32 v154, 4, v254
v_xor_b32 v153, v153, v154
v_lshlrev_b32 v153, 4, v153
v_lshl_add_u32 v153, v152, 7, v153
s_and_b32 s16, s11, 1
s_lshl_b32 s16, s16, 14
v_add_u32 v140, s16, v153
X 141,64,140
VA 142,65536,140
VA 143,65536,141
v_lshrrev_b32 v152, 4, v254
v_and_b32 v153, 15, v254
v_lshrrev_b32 v153, 2, v153
v_and_b32 v154, 1, v152
v_lshl_or_b32 v154, v154, 2, v153
v_lshl_add_u32 v152, v152, 3, v153
v_lshlrev_b32 v152, 8, v152
v_and_b32 v153, 3, v254
v_lshl_add_u32 v152, v153, 3, v152
v_lshl_add_u32 v152, v154, 5, v152
s_lshr_b32 s16, s11, 1
s_lshl_b32 s16, s16, 13
AS 16,16,98304
v_add_u32 v152, s16, v152
X 144,0,152
X 145,32,152
X 146,64,152
X 147,96,152
X 148,128,152
X 149,160,152
X 150,192,152
X 151,224,152
s_lshl_b32 s16, s13, 18
s_lshl_b32 s17, s11, 16
s_add_u32 s32, s16, s17
s_and_b32 s16, s11, 1
s_lshl_b32 s16, s16, 17
s_lshl_b32 s17, s12, 8
s_add_u32 s33, s16, s17
s_lshl_b32 s34, s11, 13
s_lshr_b32 s16, s11, 1
s_lshl_b32 s16, s16, 13
s_and_b32 s17, s11, 1
s_lshl_b32 s17, s17, 12
s_add_u32 s16, s16, s17
AS 35,16,98304
s_mov_b64 s[44:45], s[60:61]
SM 46,0x500000
SM 47,0x20000
s_add_u32 s48, s14, 0x40000
s_addc_u32 s49, s15, 0
SM 50,0x280000
SM 51,0x20000
v_lshlrev_b32 v243, 4, v254
v_lshrrev_b32 v224, 4, v254
v_and_b32 v225, 15, v254
v_lshrrev_b32 v225, 2, v225
v_and_b32 v226, 1, v224
v_lshl_or_b32 v226, v226, 2, v225
v_lshl_add_u32 v224, v224, 3, v225
v_lshlrev_b32 v224, 8, v224
v_and_b32 v225, 3, v254
v_lshl_add_u32 v224, v225, 3, v224
v_lshl_add_u32 v224, v226, 5, v224
VA 240,98304,224
s_lshl_b32 s16, s11, 13
AS 16,16,0
v_add_u32 v244, s16, v243
VA 245,65536,244
s_lshl_b32 s16, s11, 17
s_lshl_b32 s17, s12, 8
s_add_u32 s52, s16, s17
s_sub_u32 s16, 15, s13
s_mul_i32 s16, s16, 163840
s_mul_i32 s18, s13, 163840
s_and_b32 s17, s11, 1
s_lshl_b32 s17, s17, 12
s_add_u32 s16, s16, s17
s_add_u32 s18, s18, s17
s_cmp_ge_u32 s11, 2
s_cselect_b32 s53, s18, s16
s_cselect_b32 s54, s16, s18
s_load_dword s79, s[14:15], 61440
s_cmp_eq_u32 s11, 1
s_cbranch_scc1 FPA_W1%=
s_cmp_eq_u32 s11, 2
s_cbranch_scc1 FPA_W2%=
s_cmp_eq_u32 s11, 3
s_cbranch_scc1 FPA_W3%=
FPA_W0%=:
AM 35,0
N 0
D 236,28,33,0
D 237,28,33,1024
D 238,28,33,2048
D 239,28,33,3072
AS 33,33,262144
AM 34,0
N 0
D 128,24,32,0
D 129,24,32,1024
D 130,24,32,2048
D 131,24,32,3072
AM 34,4096
N 0
D 132,24,32,0
D 133,24,32,1024
D 134,24,32,2048
D 135,24,32,3072
s_branch FPA_END%=
FPA_W1%=:
AM 35,0
N 0
D 236,28,33,0
D 237,28,33,1024
D 238,28,33,2048
D 239,28,33,3072
AS 33,33,262144
AM 34,0
N 0
D 128,24,32,0
D 129,24,32,1024
D 130,24,32,2048
D 131,24,32,3072
AM 34,4096
N 0
D 132,24,32,0
D 133,24,32,1024
D 134,24,32,2048
D 135,24,32,3072
s_branch FPA_END%=
FPA_W2%=:
AM 35,0
N 0
D 236,28,33,0
D 237,28,33,1024
D 238,28,33,2048
D 239,28,33,3072
AS 33,33,262144
AM 34,0
N 0
D 128,24,32,0
D 129,24,32,1024
D 130,24,32,2048
D 131,24,32,3072
AM 34,4096
N 0
D 132,24,32,0
D 133,24,32,1024
D 134,24,32,2048
D 135,24,32,3072
s_branch FPA_END%=
FPA_W3%=:
AM 35,0
N 0
D 236,28,33,0
D 237,28,33,1024
D 238,28,33,2048
D 239,28,33,3072
AS 33,33,262144
AM 34,0
N 0
D 128,24,32,0
D 129,24,32,1024
D 130,24,32,2048
D 131,24,32,3072
AM 34,4096
N 0
D 132,24,32,0
D 133,24,32,1024
D 134,24,32,2048
D 135,24,32,3072
FPA_END%=:
s_cmp_eq_u32 s11, 1
s_cbranch_scc1 FPB_W1%=
s_cmp_eq_u32 s11, 2
s_cbranch_scc1 FPB_W2%=
s_cmp_eq_u32 s11, 3
s_cbranch_scc1 FPB_W3%=
FPB_W0%=:
AS 32,32,128
AM 35,16384
N 0
D 236,28,33,0
D 237,28,33,1024
D 238,28,33,2048
D 239,28,33,3072
AS 33,33,262144
AM 34,32768
N 0
D 128,24,32,0
D 129,24,32,1024
D 130,24,32,2048
D 131,24,32,3072
AM 34,36864
N 0
D 132,24,32,0
D 133,24,32,1024
D 134,24,32,2048
D 135,24,32,3072
AS 32,32,128
AM 35,32768
N 0
D 236,28,33,0
D 237,28,33,1024
D 238,28,33,2048
D 239,28,33,3072
AS 33,33,262144
AM 34,65536
N 0
D 128,24,32,0
D 129,24,32,1024
D 130,24,32,2048
D 131,24,32,3072
s_getpc_b64 s[16:17]
1:
s_add_u32 s16, s16, FM_W0%=-1b
s_addc_u32 s17, s17, 0
s_lshr_b32 s21, s10, 3
v_lshlrev_b32 v249, 5, v254
v_add_u32 v249, s21, v249
v_min_u32 v249, ((QS0%=-FM_W0%=)>>7), v249
v_lshlrev_b32 v249, 7, v249
global_load_dword v248, v249, s[16:17]
s_branch FPB_END%=
FPB_W1%=:
AS 32,32,128
AM 35,16384
N 0
D 236,28,33,0
D 237,28,33,1024
D 238,28,33,2048
D 239,28,33,3072
AS 33,33,262144
AM 34,32768
N 0
D 128,24,32,0
D 129,24,32,1024
D 130,24,32,2048
D 131,24,32,3072
AM 34,36864
N 0
D 132,24,32,0
D 133,24,32,1024
D 134,24,32,2048
D 135,24,32,3072
AS 32,32,128
AM 35,32768
N 0
D 236,28,33,0
D 237,28,33,1024
D 238,28,33,2048
D 239,28,33,3072
AS 33,33,262144
AM 34,65536
N 0
D 128,24,32,0
D 129,24,32,1024
D 130,24,32,2048
D 131,24,32,3072
s_getpc_b64 s[16:17]
1:
s_add_u32 s16, s16, FM_W1%=-1b
s_addc_u32 s17, s17, 0
s_lshr_b32 s21, s10, 3
v_lshlrev_b32 v249, 5, v254
v_add_u32 v249, s21, v249
v_min_u32 v249, ((QS1%=-FM_W1%=)>>7), v249
v_lshlrev_b32 v249, 7, v249
global_load_dword v248, v249, s[16:17]
s_branch FPB_END%=
FPB_W2%=:
AS 32,32,128
AM 35,16384
N 0
D 236,28,33,0
D 237,28,33,1024
D 238,28,33,2048
D 239,28,33,3072
AS 33,33,262144
AM 34,32768
N 0
D 128,24,32,0
D 129,24,32,1024
D 130,24,32,2048
D 131,24,32,3072
AM 34,36864
N 0
D 132,24,32,0
D 133,24,32,1024
D 134,24,32,2048
D 135,24,32,3072
AS 32,32,128
AM 35,32768
N 0
D 236,28,33,0
D 237,28,33,1024
D 238,28,33,2048
D 239,28,33,3072
AS 33,33,262144
AM 34,65536
N 0
D 128,24,32,0
D 129,24,32,1024
D 130,24,32,2048
D 131,24,32,3072
s_getpc_b64 s[16:17]
1:
s_add_u32 s16, s16, FM_W2%=-1b
s_addc_u32 s17, s17, 0
s_lshr_b32 s21, s10, 3
v_lshlrev_b32 v249, 5, v254
v_add_u32 v249, s21, v249
v_min_u32 v249, ((QS2%=-FM_W2%=)>>7), v249
v_lshlrev_b32 v249, 7, v249
global_load_dword v248, v249, s[16:17]
s_branch FPB_END%=
FPB_W3%=:
AS 32,32,128
AM 35,16384
N 0
D 236,28,33,0
D 237,28,33,1024
D 238,28,33,2048
D 239,28,33,3072
AS 33,33,262144
AM 34,32768
N 0
D 128,24,32,0
D 129,24,32,1024
D 130,24,32,2048
D 131,24,32,3072
AM 34,36864
N 0
D 132,24,32,0
D 133,24,32,1024
D 134,24,32,2048
D 135,24,32,3072
AS 32,32,128
AM 35,32768
N 0
D 236,28,33,0
D 237,28,33,1024
D 238,28,33,2048
D 239,28,33,3072
AS 33,33,262144
AM 34,65536
N 0
D 128,24,32,0
D 129,24,32,1024
D 130,24,32,2048
D 131,24,32,3072
s_getpc_b64 s[16:17]
1:
s_add_u32 s16, s16, FM_W3%=-1b
s_addc_u32 s17, s17, 0
s_lshr_b32 s21, s10, 3
v_lshlrev_b32 v249, 5, v254
v_add_u32 v249, s21, v249
v_min_u32 v249, ((QS3%=-FM_W3%=)>>7), v249
v_lshlrev_b32 v249, 7, v249
global_load_dword v248, v249, s[16:17]
FPB_END%=:
s_cmp_eq_u32 s11, 1
s_cbranch_scc0 FM_N1%=
s_getpc_b64 s[16:17]
FJ1%=:
)A"
R"A(s_add_u32 s16, s16, FM_W1%=-FJ1%=
s_addc_u32 s17, s17, 0
s_setpc_b64 s[16:17]
FM_N1%=:
s_cmp_eq_u32 s11, 2
s_cbranch_scc0 FM_N2%=
s_getpc_b64 s[16:17]
FJ2%=:
s_add_u32 s16, s16, FM_W2%=-FJ2%=
s_addc_u32 s17, s17, 0
s_setpc_b64 s[16:17]
FM_N2%=:
s_cmp_eq_u32 s11, 3
s_cbranch_scc0 FM_N3%=
s_getpc_b64 s[16:17]
FJ3%=:
s_add_u32 s16, s16, FM_W3%=-FJ3%=
s_addc_u32 s17, s17, 0
s_setpc_b64 s[16:17]
FM_N3%=:
FM_W0%=:
s_waitcnt vmcnt(20)
B
Rv 0,140,0
T 32,144,0
T 34,144,1024
Rv 4,140,2048
T 36,145,0
T 38,145,1024
Rv 8,140,4096
T 40,146,0
T 42,146,1024
Rv 12,140,6144
T 44,147,0
T 46,147,1024
Rv 16,140,8192
T 48,148,0
T 50,148,1024
Rv 20,140,10240
T 52,149,0
T 54,149,1024
Rv 24,140,12288
T 56,150,0
T 58,150,1024
Rv 28,140,14336
T 60,151,0
T 62,151,1024
WV 16
B
AS 79,79,1
Mavvz 0,32,0
Mavvz 4,32,4
AM 35,0
Mavvz 8,32,8
v_dot2c_f32_bf16 v181, v168, v180
D 236,28,33,0
Mavvz 12,32,12
Rv 64,141,0
v_dot2c_f32_bf16 v182, v168, v168
Mavvz 16,32,16
T 96,144,16384
v_dot2c_f32_bf16 v181, v169, v180
Mavvz 20,32,20
T 98,144,17408
v_dot2c_f32_bf16 v182, v169, v169
Mavvz 24,32,24
Rv 68,141,2048
Mavvz 28,32,28
v_dot2c_f32_bf16 v181, v170, v180
Mavvz 60,36,28
T 100,145,16384
v_dot2c_f32_bf16 v182, v170, v170
Mavvz 56,36,24
v_dot2c_f32_bf16 v181, v171, v180
D 237,28,33,1024
Mavvz 52,36,20
T 102,145,17408
Mavvz 48,36,16
v_dot2c_f32_bf16 v182, v171, v171
v_dot2c_f32_bf16 v181, v172, v180
Mavvz 44,36,12
Rv 72,141,4096
Mavvz 40,36,8
v_dot2c_f32_bf16 v182, v172, v172
Mavvz 36,36,4
T 104,146,16384
Mavvz 32,36,0
v_dot2c_f32_bf16 v181, v173, v180
v_dot2c_f32_bf16 v182, v173, v173
Mavvz 64,40,0
v_dot2c_f32_bf16 v181, v174, v180
D 238,28,33,2048
Mavvz 68,40,4
T 106,146,17408
v_dot2c_f32_bf16 v182, v174, v174
Mavvz 72,40,8
Rv 76,141,6144
Mavvz 76,40,12
v_dot2c_f32_bf16 v181, v175, v180
Mavvz 80,40,16
T 108,147,16384
Mavvz 84,40,20
v_dot2c_f32_bf16 v182, v175, v175
v_dot2c_f32_bf16 v181, v176, v180
Mavvz 88,40,24
T 110,147,17408
v_dot2c_f32_bf16 v182, v176, v176
Mavvz 92,40,28
v_dot2c_f32_bf16 v181, v177, v180
D 239,28,33,3072
Mavvz 124,44,28
AS 33,33,262144
Mavvz 120,44,24
Rv 80,141,8192
v_dot2c_f32_bf16 v182, v177, v177
Mavvz 116,44,20
T 112,148,16384
v_dot2c_f32_bf16 v181, v178, v180
Mavvz 112,44,16
T 114,148,17408
Mavvz 108,44,12
v_dot2c_f32_bf16 v182, v178, v178
v_dot2c_f32_bf16 v181, v179, v180
Mavvz 104,44,8
AM 34,69632
Mavvz 100,44,4
v_dot2c_f32_bf16 v182, v179, v179
D 132,24,32,0
Mavvz 96,44,0
Rv 84,141,10240
N 3
Mavvz 128,48,0
T 116,149,16384
v_add_f32_dpp v181, v181, v181 row_ror:8 row_mask:0xf bank_mask:0xf
Mavvz 132,48,4
T 118,149,17408
Mavvz 136,48,8
v_add_f32_dpp v182, v182, v182 row_ror:8 row_mask:0xf bank_mask:0xf
MV 186,181
Mavvz 140,48,12
Rv 88,141,12288
Mavvz 144,48,16
MV 187,182
Mavvz 148,48,20
N 1
D 133,24,32,1024
Mavvz 152,48,24
T 120,150,16384
v_permlane16_swap_b32 v181, v186
Mavvz 156,48,28
T 122,150,17408
Mavvz 188,52,28
v_permlane16_swap_b32 v182, v187
N 1
Mavvz 184,52,24
Rv 92,141,14336
Mavvz 180,52,20
Fad 181,181,186
Mavvz 176,52,16
T 124,151,16384
Fad 182,182,187
Mavvz 172,52,12
MV 186,181
D 134,24,32,2048
Mavvz 168,52,8
T 126,151,17408
Mavvz 164,52,4
MV 187,182
Mavvz 160,52,0
N 1
Mavvz 192,56,0
v_permlane32_swap_b32 v181, v186
Mavvz 196,56,4
v_permlane32_swap_b32 v182, v187
Mavvz 200,56,8
N 1
Mavvz 204,56,12
Fad 181,181,186
D 135,24,32,3072
Mavvz 208,56,16
AS 32,32,128
Mavvz 212,56,20
Fad 182,182,187
Mavvz 216,56,24
MV 186,181
Mavvz 220,56,28
MV 187,182
Mavvz 252,60,28
ds_write_b64 v185, v[186:187]
Mavvz 248,60,24
Mavvz 244,60,20
Mavvz 240,60,16
Mavvz 236,60,12
Mavvz 232,60,8
Mavvz 228,60,4
Mavvz 224,60,0
WV 12
B
Mavvc 0,96,64
Mavvc 4,96,68
AM 35,16384
Mavvc 8,96,72
D 236,28,33,0
Mavvc 12,96,76
Rv 0,140,32768
Mavvc 16,96,80
T 32,144,32768
Mavvc 20,96,84
T 34,144,33792
Mavvc 24,96,88
Rv 4,140,34816
Mavvc 28,96,92
Mavvc 60,100,92
T 36,145,32768
s_lshr_b32 s21, s10, 3
Mavvc 56,100,88
D 237,28,33,1024
Mavvc 52,100,84
T 38,145,33792
Mavvc 48,100,80
s_mul_i32 s21, s21, 160
Mavvc 44,100,76
Rv 8,140,36864
Mavvc 40,100,72
Mavvc 36,100,68
T 40,146,32768
AS 21,21,0
Mavvc 32,100,64
Mavvc 64,104,64
D 238,28,33,2048
Mavvc 68,104,68
T 42,146,33792
v_min_u32 v249, 63, v254
Mavvc 72,104,72
Rv 12,140,38912
Mavvc 76,104,76
Mavvc 80,104,80
T 44,147,32768
v_add_u32 v249, s21, v249
Mavvc 84,104,84
Mavvc 88,104,88
T 46,147,33792
Mavvc 92,104,92
v_and_b32 v248, 7, v249
D 239,28,33,3072
Mavvc 124,108,92
AS 33,33,262144
Mavvc 120,108,88
Rv 16,140,40960
Mavvc 116,108,84
T 48,148,32768
v_lshrrev_b32 v249, 3, v249
Mavvc 112,108,80
T 50,148,33792
Mavvc 108,108,76
Mavvc 104,108,72
v_lshlrev_b32 v249, 13, v249
AM 34,0
Mavvc 100,108,68
D 128,24,32,0
Mavvc 96,108,64
Rv 20,140,43008
Mavvc 128,112,64
T 52,149,32768
v_lshl_add_u32 v249, v248, 7, v249
Mavvc 132,112,68
T 54,149,33792
Mavvc 136,112,72
Mavvc 140,112,76
Rv 24,140,45056
s_and_b32 s21, s10, 7
Mavvc 144,112,80
Mavvc 148,112,84
D 129,24,32,1024
Mavvc 152,112,88
T 56,150,32768
s_lshl_b32 s21, s21, 10
Mavvc 156,112,92
T 58,150,33792
Mavvc 188,116,92
Mavvc 184,116,88
Rv 28,140,47104
buffer_load_dword v248, v249, s[44:47], s21 offen
Mavvc 180,116,84
Mavvc 176,116,80
T 60,151,32768
Mavvc 172,116,76
D 130,24,32,2048
Mavvc 168,116,72
T 62,151,33792
Mavvc 164,116,68
Mavvc 160,116,64
Mavvc 192,120,64
Mavvc 196,120,68
Mavvc 200,120,72
Mavvc 204,120,76
D 131,24,32,3072
Mavvc 208,120,80
Mavvc 212,120,84
Mavvc 216,120,88
Mavvc 220,120,92
Mavvc 252,124,92
Mavvc 248,124,88
Mavvc 244,124,84
ds_read_b64 v[188:189], v184 offset:0
Mavvc 240,124,80
ds_read_b64 v[190:191], v184 offset:64
Mavvc 236,124,76
ds_read_b64 v[192:193], v184 offset:128
Mavvc 232,124,72
ds_read_b64 v[194:195], v184 offset:192
Mavvc 228,124,68
Mavvc 224,124,64
WV 13
B
Mavvc 0,32,0
Mavvc 4,32,4
Fad 181,188,190
AM 35,32768
Mavvc 8,32,8
Fad 182,189,191
D 236,28,33,0
Mavvc 12,32,12
Rv 64,141,32768
Fad 186,192,194
Mavvc 16,32,16
T 96,144,0
Fad 187,193,195
Mavvc 20,32,20
T 98,144,1024
Fad 181,181,186
Mavvc 24,32,24
Rv 68,141,34816
Fad 182,182,187
Mavvc 28,32,28
v_mul_f32 v181, 0x3acccccd, v181
v_mul_f32 v182, 0x3acccccd, v182
Mavvc 60,36,28
T 100,145,0
v_fma_f32 v182, -v181, v181, v182
Mavvc 56,36,24
v_max_f32 v182, 0, v182
D 237,28,33,1024
Mavvc 52,36,20
T 102,145,1024
v_add_f32 v182, 0x3727c5ac, v182
Mavvc 48,36,16
v_rsq_f32 v182, v182
Mavvc 44,36,12
Rv 72,141,36864
N 0
Mavvc 40,36,8
v_mul_f32 v182, 0x3e0293ee, v182
Fmu 181,181,182
Mavvc 36,36,4
T 104,146,0
v_xor_b32 v181, 0x80000000, v181
Mavvc 32,36,0
LO 196,168
HI 197,168
Mavvc 64,40,0
FM 196,196,182,181
D 238,28,33,2048
Mavvc 68,40,4
T 106,146,1024
FM 197,197,182,181
Mavvc 72,40,8
Rv 76,141,38912
C 168,196,197
Mavvc 76,40,12
LO 196,169
HI 197,169
Mavvc 80,40,16
T 108,147,0
FM 196,196,182,181
Mavvc 84,40,20
FM 197,197,182,181
C 169,196,197
Mavvc 88,40,24
T 110,147,1024
LO 196,170
Mavvc 92,40,28
HI 197,170
D 239,28,33,3072
Mavvc 124,44,28
FM 196,196,182,181
AS 33,33,262144
Mavvc 120,44,24
Rv 80,141,40960
FM 197,197,182,181
Mavvc 116,44,20
T 112,148,0
C 170,196,197
Mavvc 112,44,16
T 114,148,1024
LO 196,171
Mavvc 108,44,12
HI 197,171
FM 196,196,182,181
Mavvc 104,44,8
FM 197,197,182,181
AM 34,4096
Mavvc 100,44,4
C 171,196,197
D 132,24,32,0
Mavvc 96,44,0
Rv 84,141,43008
LO 196,172
Mavvc 128,48,0
T 116,149,0
HI 197,172
Mavvc 132,48,4
T 118,149,1024
FM 196,196,182,181
Mavvc 136,48,8
FM 197,197,182,181
C 172,196,197
Mavvc 140,48,12
Rv 88,141,45056
LO 196,173
Mavvc 144,48,16
HI 197,173
FM 196,196,182,181
Mavvc 148,48,20
FM 197,197,182,181
D 133,24,32,1024
Mavvc 152,48,24
T 120,150,0
C 173,196,197
Mavvc 156,48,28
T 122,150,1024
LO 196,174
Mavvc 188,52,28
HI 197,174
FM 196,196,182,181
Mavvc 184,52,24
Rv 92,141,47104
FM 197,197,182,181
Mavvc 180,52,20
C 174,196,197
LO 196,175
Mavvc 176,52,16
T 124,151,0
HI 197,175
Mavvc 172,52,12
FM 196,196,182,181
D 134,24,32,2048
Mavvc 168,52,8
T 126,151,1024
FM 197,197,182,181
Mavvc 164,52,4
C 175,196,197
Mavvc 160,52,0
LO 196,176
Mavvc 192,56,0
HI 197,176
Mavvc 196,56,4
FM 196,196,182,181
Mavvc 200,56,8
FM 197,197,182,181
C 176,196,197
Mavvc 204,56,12
LO 196,177
D 135,24,32,3072
Mavvc 208,56,16
HI 197,177
AS 32,32,128
Mavvc 212,56,20
FM 196,196,182,181
Mavvc 216,56,24
FM 197,197,182,181
Mavvc 220,56,28
C 177,196,197
Mavvc 252,60,28
LO 196,178
Mavvc 248,60,24
HI 197,178
Mavvc 244,60,20
FM 196,196,182,181
Mavvc 240,60,16
FM 197,197,182,181
Mavvc 236,60,12
C 178,196,197
Mavvc 232,60,8
LO 196,179
Mavvc 228,60,4
HI 197,179
Mavvc 224,60,0
FM 196,196,182,181
FM 197,197,182,181
C 179,196,197
s_lshr_b32 s18, s10, 4
s_mul_i32 s18, s18, 163840
s_lshr_b32 s19, s10, 1
s_and_b32 s19, s19, 7
s_lshl_b32 s19, s19, 10
s_add_u32 s18, s18, s19
s_and_b32 s19, s10, 1
s_lshl_b32 s19, s19, 7
s_add_u32 s18, s18, s19
AS 19,18,65536
Svs 168,205,48,18,0
Svs 172,205,48,19,0
Svs 176,206,48,18,0
WV 16
B
Mavvc 0,96,64
Mavvc 4,96,68
AM 35,0
Mavvc 8,96,72
D 236,28,33,0
Mavvc 12,96,76
Rv 0,142,0
Mavvc 16,96,80
T 32,144,16384
Mavvc 20,96,84
T 34,144,17408
Mavvc 24,96,88
Rv 4,142,2048
Mavvc 28,96,92
Mavvc 60,100,92
T 36,145,16384
Mavvc 56,100,88
D 237,28,33,1024
Mavvc 52,100,84
T 38,145,17408
Mavvc 48,100,80
Mavvc 44,100,76
Rv 8,142,4096
Mavvc 40,100,72
Mavvc 36,100,68
T 40,146,16384
Mavvc 32,100,64
Mavvc 64,104,64
D 238,28,33,2048
Mavvc 68,104,68
T 42,146,17408
Mavvc 72,104,72
Rv 12,142,6144
Mavvc 76,104,76
Mavvc 80,104,80
T 44,147,16384
Mavvc 84,104,84
Mavvc 88,104,88
T 46,147,17408
Mavvc 92,104,92
D 239,28,33,3072
Mavvc 124,108,92
AS 33,33,262144
Mavvc 120,108,88
Rv 16,142,8192
Mavvc 116,108,84
T 48,148,16384
Mavvc 112,108,80
T 50,148,17408
Mavvc 108,108,76
Mavvc 104,108,72
AM 34,32768
Mavvc 100,108,68
D 128,24,32,0
Mavvc 96,108,64
Rv 20,142,10240
Mavvc 128,112,64
T 52,149,16384
Mavvc 132,112,68
T 54,149,17408
Mavvc 136,112,72
Mavvc 140,112,76
Rv 24,142,12288
Mavvc 144,112,80
Mavvc 148,112,84
D 129,24,32,1024
Mavvc 152,112,88
T 56,150,16384
Mavvc 156,112,92
T 58,150,17408
Mavvc 188,116,92
Mavvc 184,116,88
Rv 28,142,14336
Mavvc 180,116,84
Mavvc 176,116,80
T 60,151,16384
Mavvc 172,116,76
D 130,24,32,2048
Mavvc 168,116,72
T 62,151,17408
Mavvc 164,116,68
Mavvc 160,116,64
Mavvc 192,120,64
Mavvc 196,120,68
Mavvc 200,120,72
Mavvc 204,120,76
D 131,24,32,3072
Mavvc 208,120,80
Mavvc 212,120,84
Mavvc 216,120,88
Mavvc 220,120,92
Mavvc 252,124,92
Mavvc 248,124,88
Mavvc 244,124,84
Mavvc 240,124,80
Mavvc 236,124,76
Mavvc 232,124,72
Mavvc 228,124,68
Mavvc 224,124,64
WV 15
B
Mavvc 0,32,0
Mavvc 4,32,4
AM 35,16384
Mavvc 8,32,8
D 236,28,33,0
Mavvc 12,32,12
Rv 64,143,0
Mavvc 16,32,16
T 96,144,32768
Mavvc 20,32,20
T 98,144,33792
Mavvc 24,32,24
Rv 68,143,2048
Mavvc 28,32,28
Mavvc 60,36,28
T 100,145,32768
Mavvc 56,36,24
D 237,28,33,1024
Mavvc 52,36,20
T 102,145,33792
Mavvc 48,36,16
Mavvc 44,36,12
Rv 72,143,4096
Mavvc 40,36,8
Mavvc 36,36,4
T 104,146,32768
Mavvc 32,36,0
Mavvc 64,40,0
D 238,28,33,2048
Mavvc 68,40,4
T 106,146,33792
Mavvc 72,40,8
Rv 76,143,6144
Mavvc 76,40,12
Mavvc 80,40,16
T 108,147,32768
Mavvc 84,40,20
Mavvc 88,40,24
T 110,147,33792
Mavvc 92,40,28
D 239,28,33,3072
Mavvc 124,44,28
AS 33,33,262144
Mavvc 120,44,24
Rv 80,143,8192
Mavvc 116,44,20
T 112,148,32768
Mavvc 112,44,16
T 114,148,33792
Mavvc 108,44,12
Mavvc 104,44,8
AM 34,36864
Mavvc 100,44,4
D 132,24,32,0
Mavvc 96,44,0
Rv 84,143,10240
Mavvc 128,48,0
T 116,149,32768
Mavvc 132,48,4
T 118,149,33792
Mavvc 136,48,8
Mavvc 140,48,12
Rv 88,143,12288
Mavvc 144,48,16
Mavvc 148,48,20
D 133,24,32,1024
Mavvc 152,48,24
T 120,150,32768
Mavvc 156,48,28
T 122,150,33792
Mavvc 188,52,28
Mavvc 184,52,24
Rv 92,143,14336
Mavvc 180,52,20
Mavvc 176,52,16
T 124,151,32768
Mavvc 172,52,12
D 134,24,32,2048
Mavvc 168,52,8
T 126,151,33792
Mavvc 164,52,4
Mavvc 160,52,0
Mavvc 192,56,0
Mavvc 196,56,4
Mavvc 200,56,8
Mavvc 204,56,12
D 135,24,32,3072
Mavvc 208,56,16
AS 32,32,128
Mavvc 212,56,20
Mavvc 216,56,24
Mavvc 220,56,28
Mavvc 252,60,28
Mavvc 248,60,24
Mavvc 244,60,20
Mavvc 240,60,16
Mavvc 236,60,12
Mavvc 232,60,8
Mavvc 228,60,4
Mavvc 224,60,0
WV 12
B
s_lshr_b32 s23, s10, 4
s_and_b32 s19, s10, 15
s_lshl_b32 s21, s23, 8
AS 21,21,0
s_lshl_b32 s22, s19, 2
s_add_u32 s21, s21, s22
v_mov_b32 v247, s21
v_mov_b32 v246, s79
s_mov_b64 exec, 1
global_store_dword v247, v246, s[14:15] sc1
)A"
R"A(s_mov_b64 exec, -1
N 3
Mavvc 0,96,64
Mavvc 4,96,68
AM 35,32768
Mavvc 8,96,72
D 236,28,33,0
Mavvc 12,96,76
Rv 0,140,0
Mavvc 16,96,80
T 32,144,0
Mavvc 20,96,84
T 34,144,1024
Mavvc 24,96,88
Rv 4,140,2048
Mavvc 28,96,92
Mavvc 60,100,92
T 36,145,0
Mavvc 56,100,88
D 237,28,33,1024
Mavvc 52,100,84
T 38,145,1024
Mavvc 48,100,80
Mavvc 44,100,76
Rv 8,140,4096
Mavvc 40,100,72
Mavvc 36,100,68
T 40,146,0
Mavvc 32,100,64
Mavvc 64,104,64
D 238,28,33,2048
Mavvc 68,104,68
T 42,146,1024
Mavvc 72,104,72
Rv 12,140,6144
Mavvc 76,104,76
Mavvc 80,104,80
T 44,147,0
Mavvc 84,104,84
Mavvc 88,104,88
T 46,147,1024
Mavvc 92,104,92
D 239,28,33,3072
Mavvc 124,108,92
AS 33,33,262144
Mavvc 120,108,88
Rv 16,140,8192
Mavvc 116,108,84
T 48,148,0
Mavvc 112,108,80
T 50,148,1024
Mavvc 108,108,76
Mavvc 104,108,72
AM 34,65536
Mavvc 100,108,68
D 128,24,32,0
Mavvc 96,108,64
Rv 20,140,10240
Mavvc 128,112,64
T 52,149,0
Mavvc 132,112,68
T 54,149,1024
Mavvc 136,112,72
Mavvc 140,112,76
Rv 24,140,12288
Mavvc 144,112,80
Mavvc 148,112,84
D 129,24,32,1024
Mavvc 152,112,88
T 56,150,0
Mavvc 156,112,92
T 58,150,1024
Mavvc 188,116,92
Mavvc 184,116,88
Rv 28,140,14336
Mavvc 180,116,84
Mavvc 176,116,80
T 60,151,0
Mavvc 172,116,76
D 130,24,32,2048
Mavvc 168,116,72
T 62,151,1024
Mavvc 164,116,68
Mavvc 160,116,64
Mavvc 192,120,64
Mavvc 196,120,68
Mavvc 200,120,72
Mavvc 204,120,76
D 131,24,32,3072
Mavvc 208,120,80
Mavvc 212,120,84
Mavvc 216,120,88
Mavvc 220,120,92
Mavvc 252,124,92
Mavvc 248,124,88
Mavvc 244,124,84
Mavvc 240,124,80
Mavvc 236,124,76
Mavvc 232,124,72
Mavvc 228,124,68
Mavvc 224,124,64
WV 13
B
Mavvc 0,32,0
Mavvc 4,32,4
AM 35,0
Mavvc 8,32,8
D 236,28,33,0
Mavvc 12,32,12
Rv 64,141,0
Mavvc 16,32,16
T 96,144,16384
Mavvc 20,32,20
T 98,144,17408
Mavvc 24,32,24
Rv 68,141,2048
Mavvc 28,32,28
Mavvc 60,36,28
T 100,145,16384
Mavvc 56,36,24
D 237,28,33,1024
Mavvc 52,36,20
T 102,145,17408
Mavvc 48,36,16
Mavvc 44,36,12
Rv 72,141,4096
Mavvc 40,36,8
Mavvc 36,36,4
T 104,146,16384
Mavvc 32,36,0
Mavvc 64,40,0
D 238,28,33,2048
Mavvc 68,40,4
T 106,146,17408
Mavvc 72,40,8
Rv 76,141,6144
Mavvc 76,40,12
Mavvc 80,40,16
T 108,147,16384
Mavvc 84,40,20
Mavvc 88,40,24
T 110,147,17408
Mavvc 92,40,28
D 239,28,33,3072
Mavvc 124,44,28
AS 33,33,262144
Mavvc 120,44,24
Rv 80,141,8192
Mavvc 116,44,20
T 112,148,16384
Mavvc 112,44,16
T 114,148,17408
Mavvc 108,44,12
Mavvc 104,44,8
AM 34,69632
Mavvc 100,44,4
D 132,24,32,0
Mavvc 96,44,0
Rv 84,141,10240
Mavvc 128,48,0
T 116,149,16384
Mavvc 132,48,4
T 118,149,17408
Mavvc 136,48,8
Mavvc 140,48,12
Rv 88,141,12288
Mavvc 144,48,16
Mavvc 148,48,20
D 133,24,32,1024
Mavvc 152,48,24
T 120,150,16384
Mavvc 156,48,28
T 122,150,17408
Mavvc 188,52,28
Mavvc 184,52,24
Rv 92,141,14336
Mavvc 180,52,20
Mavvc 176,52,16
T 124,151,16384
Mavvc 172,52,12
D 134,24,32,2048
Mavvc 168,52,8
T 126,151,17408
Mavvc 164,52,4
Mavvc 160,52,0
Mavvc 192,56,0
Mavvc 196,56,4
Mavvc 200,56,8
Mavvc 204,56,12
D 135,24,32,3072
Mavvc 208,56,16
AS 32,32,128
Mavvc 212,56,20
Mavvc 216,56,24
Mavvc 220,56,28
Mavvc 252,60,28
Mavvc 248,60,24
Mavvc 244,60,20
Mavvc 240,60,16
Mavvc 236,60,12
Mavvc 232,60,8
Mavvc 228,60,4
Mavvc 224,60,0
WV 12
B
s_getpc_b64 s[16:17]
1:
s_add_u32 s16, s16, QS0%=-1b
s_addc_u32 s17, s17, 0
s_lshr_b32 s21, s10, 3
v_lshlrev_b32 v249, 5, v254
v_add_u32 v249, s21, v249
v_min_u32 v249, ((QE0%=-QS0%=)>>7), v249
v_lshlrev_b32 v249, 7, v249
global_load_dword v248, v249, s[16:17]
Mavvc 0,96,64
Mavvc 4,96,68
AM 35,16384
Mavvc 8,96,72
D 236,28,33,0
Mavvc 12,96,76
Rv 0,140,32768
Mavvc 16,96,80
T 32,144,32768
Mavvc 20,96,84
T 34,144,33792
Mavvc 24,96,88
Rv 4,140,34816
Mavvc 28,96,92
Mavvc 60,100,92
T 36,145,32768
Mavvc 56,100,88
D 237,28,33,1024
Mavvc 52,100,84
T 38,145,33792
Mavvc 48,100,80
Mavvc 44,100,76
Rv 8,140,36864
Mavvc 40,100,72
Mavvc 36,100,68
T 40,146,32768
Mavvc 32,100,64
Mavvc 64,104,64
D 238,28,33,2048
Mavvc 68,104,68
T 42,146,33792
Mavvc 72,104,72
Rv 12,140,38912
Mavvc 76,104,76
Mavvc 80,104,80
T 44,147,32768
Mavvc 84,104,84
Mavvc 88,104,88
T 46,147,33792
Mavvc 92,104,92
D 239,28,33,3072
Mavvc 124,108,92
AS 33,33,262144
Mavvc 120,108,88
Rv 16,140,40960
Mavvc 116,108,84
T 48,148,32768
Mavvc 112,108,80
T 50,148,33792
Mavvc 108,108,76
Mavvc 104,108,72
AM 34,0
Mavvc 100,108,68
D 128,24,32,0
Mavvc 96,108,64
Rv 20,140,43008
Mavvc 128,112,64
T 52,149,32768
Mavvc 132,112,68
T 54,149,33792
Mavvc 136,112,72
Mavvc 140,112,76
Rv 24,140,45056
Mavvc 144,112,80
Mavvc 148,112,84
D 129,24,32,1024
Mavvc 152,112,88
T 56,150,32768
Mavvc 156,112,92
T 58,150,33792
Mavvc 188,116,92
Mavvc 184,116,88
Rv 28,140,47104
Mavvc 180,116,84
Mavvc 176,116,80
T 60,151,32768
Mavvc 172,116,76
D 130,24,32,2048
Mavvc 168,116,72
T 62,151,33792
Mavvc 164,116,68
Mavvc 160,116,64
Mavvc 192,120,64
Mavvc 196,120,68
Mavvc 200,120,72
Mavvc 204,120,76
D 131,24,32,3072
Mavvc 208,120,80
Mavvc 212,120,84
Mavvc 216,120,88
Mavvc 220,120,92
Mavvc 252,124,92
Mavvc 248,124,88
Mavvc 244,124,84
Mavvc 240,124,80
Mavvc 236,124,76
Mavvc 232,124,72
Mavvc 228,124,68
Mavvc 224,124,64
WV 13
B
Mavvc 0,32,0
Mavvc 4,32,4
AM 35,32768
Mavvc 8,32,8
D 236,28,33,0
Mavvc 12,32,12
Rv 64,141,32768
Mavvc 16,32,16
T 96,144,0
Mavvc 20,32,20
T 98,144,1024
Mavvc 24,32,24
Rv 68,141,34816
Mavvc 28,32,28
Mavvc 60,36,28
T 100,145,0
Mavvc 56,36,24
D 237,28,33,1024
Mavvc 52,36,20
T 102,145,1024
Mavvc 48,36,16
Mavvc 44,36,12
Rv 72,141,36864
Mavvc 40,36,8
Mavvc 36,36,4
T 104,146,0
Mavvc 32,36,0
Mavvc 64,40,0
D 238,28,33,2048
Mavvc 68,40,4
T 106,146,1024
Mavvc 72,40,8
Rv 76,141,38912
Mavvc 76,40,12
Mavvc 80,40,16
T 108,147,0
Mavvc 84,40,20
Mavvc 88,40,24
T 110,147,1024
Mavvc 92,40,28
D 239,28,33,3072
Mavvc 124,44,28
AS 33,33,262144
Mavvc 120,44,24
Rv 80,141,40960
Mavvc 116,44,20
T 112,148,0
Mavvc 112,44,16
T 114,148,1024
Mavvc 108,44,12
Mavvc 104,44,8
AM 34,4096
Mavvc 100,44,4
D 132,24,32,0
Mavvc 96,44,0
Rv 84,141,43008
Mavvc 128,48,0
T 116,149,0
Mavvc 132,48,4
T 118,149,1024
Mavvc 136,48,8
Mavvc 140,48,12
Rv 88,141,45056
Mavvc 144,48,16
Mavvc 148,48,20
D 133,24,32,1024
Mavvc 152,48,24
T 120,150,0
Mavvc 156,48,28
T 122,150,1024
Mavvc 188,52,28
Mavvc 184,52,24
Rv 92,141,47104
Mavvc 180,52,20
Mavvc 176,52,16
T 124,151,0
Mavvc 172,52,12
D 134,24,32,2048
Mavvc 168,52,8
T 126,151,1024
Mavvc 164,52,4
Mavvc 160,52,0
Mavvc 192,56,0
Mavvc 196,56,4
Mavvc 200,56,8
Mavvc 204,56,12
D 135,24,32,3072
Mavvc 208,56,16
AS 32,32,128
Mavvc 212,56,20
Mavvc 216,56,24
Mavvc 220,56,28
Mavvc 252,60,28
Mavvc 248,60,24
Mavvc 244,60,20
Mavvc 240,60,16
Mavvc 236,60,12
Mavvc 232,60,8
Mavvc 228,60,4
Mavvc 224,60,0
WV 12
B
s_sub_u32 s23, 15, s13
s_cmp_ge_u32 s11, 2
s_cselect_b32 s23, s13, s23
s_lshl_b32 s21, s23, 8
AS 21,21,0
v_mov_b32 v247, s21
v_min_u32 v246, 15, v254
v_lshl_add_u32 v247, v246, 2, v247
global_load_dword v246, v247, s[14:15] sc1
Mavvc 0,96,64
Mavvc 4,96,68
AM 35,0
Mavvc 8,96,72
D 236,28,33,0
Mavvc 12,96,76
Rv 0,142,0
Mavvc 16,96,80
T 32,144,16384
Mavvc 20,96,84
T 34,144,17408
Mavvc 24,96,88
Rv 4,142,2048
Mavvc 28,96,92
Mavvc 60,100,92
T 36,145,16384
Mavvc 56,100,88
D 237,28,33,1024
Mavvc 52,100,84
T 38,145,17408
Mavvc 48,100,80
Mavvc 44,100,76
Rv 8,142,4096
Mavvc 40,100,72
Mavvc 36,100,68
T 40,146,16384
Mavvc 32,100,64
Mavvc 64,104,64
D 238,28,33,2048
Mavvc 68,104,68
T 42,146,17408
Mavvc 72,104,72
Rv 12,142,6144
Mavvc 76,104,76
Mavvc 80,104,80
T 44,147,16384
Mavvc 84,104,84
Mavvc 88,104,88
T 46,147,17408
Mavvc 92,104,92
D 239,28,33,3072
Mavvc 124,108,92
AS 33,33,262144
Mavvc 120,108,88
Rv 16,142,8192
Mavvc 116,108,84
T 48,148,16384
Mavvc 112,108,80
T 50,148,17408
Mavvc 108,108,76
Mavvc 104,108,72
AM 34,32768
Mavvc 100,108,68
D 128,24,32,0
Mavvc 96,108,64
Rv 20,142,10240
Mavvc 128,112,64
T 52,149,16384
Mavvc 132,112,68
T 54,149,17408
Mavvc 136,112,72
Mavvc 140,112,76
Rv 24,142,12288
Mavvc 144,112,80
Mavvc 148,112,84
D 129,24,32,1024
Mavvc 152,112,88
T 56,150,16384
Mavvc 156,112,92
T 58,150,17408
Mavvc 188,116,92
Mavvc 184,116,88
Rv 28,142,14336
Mavvc 180,116,84
Mavvc 176,116,80
T 60,151,16384
Mavvc 172,116,76
D 130,24,32,2048
Mavvc 168,116,72
T 62,151,17408
Mavvc 164,116,68
Mavvc 160,116,64
Mavvc 192,120,64
Mavvc 196,120,68
Mavvc 200,120,72
Mavvc 204,120,76
D 131,24,32,3072
Mavvc 208,120,80
Mavvc 212,120,84
Mavvc 216,120,88
Mavvc 220,120,92
Mavvc 252,124,92
Mavvc 248,124,88
Mavvc 244,124,84
Mavvc 240,124,80
Mavvc 236,124,76
Mavvc 232,124,72
Mavvc 228,124,68
Mavvc 224,124,64
WV 13
B
Mavvc 0,32,0
Mavvc 4,32,4
AM 35,16384
Mavvc 8,32,8
D 236,28,33,0
Mavvc 12,32,12
Rv 64,143,0
Mavvc 16,32,16
T 96,144,32768
Mavvc 20,32,20
T 98,144,33792
Mavvc 24,32,24
Rv 68,143,2048
Mavvc 28,32,28
Mavvc 60,36,28
T 100,145,32768
Mavvc 56,36,24
D 237,28,33,1024
Mavvc 52,36,20
T 102,145,33792
Mavvc 48,36,16
Mavvc 44,36,12
Rv 72,143,4096
Mavvc 40,36,8
Mavvc 36,36,4
T 104,146,32768
Mavvc 32,36,0
Mavvc 64,40,0
D 238,28,33,2048
Mavvc 68,40,4
T 106,146,33792
Mavvc 72,40,8
Rv 76,143,6144
Mavvc 76,40,12
Mavvc 80,40,16
T 108,147,32768
Mavvc 84,40,20
Mavvc 88,40,24
T 110,147,33792
Mavvc 92,40,28
D 239,28,33,3072
Mavvc 124,44,28
AS 33,33,262144
Mavvc 120,44,24
Rv 80,143,8192
Mavvc 116,44,20
T 112,148,32768
Mavvc 112,44,16
T 114,148,33792
Mavvc 108,44,12
Mavvc 104,44,8
AM 34,36864
Mavvc 100,44,4
D 132,24,32,0
Mavvc 96,44,0
Rv 84,143,10240
Mavvc 128,48,0
T 116,149,32768
Mavvc 132,48,4
T 118,149,33792
Mavvc 136,48,8
Mavvc 140,48,12
Rv 88,143,12288
Mavvc 144,48,16
Mavvc 148,48,20
D 133,24,32,1024
Mavvc 152,48,24
T 120,150,32768
Mavvc 156,48,28
T 122,150,33792
Mavvc 188,52,28
Mavvc 184,52,24
Rv 92,143,14336
Mavvc 180,52,20
Mavvc 176,52,16
T 124,151,32768
Mavvc 172,52,12
D 134,24,32,2048
Mavvc 168,52,8
T 126,151,33792
Mavvc 164,52,4
Mavvc 160,52,0
Mavvc 192,56,0
Mavvc 196,56,4
Mavvc 200,56,8
Mavvc 204,56,12
D 135,24,32,3072
Mavvc 208,56,16
AS 32,32,128
Mavvc 212,56,20
Mavvc 216,56,24
Mavvc 220,56,28
Mavvc 252,60,28
Mavvc 248,60,24
Mavvc 244,60,20
Mavvc 240,60,16
Mavvc 236,60,12
Mavvc 232,60,8
Mavvc 228,60,4
Mavvc 224,60,0
WV 12
B
v_cmp_ne_u32 vcc, s79, v246
N 4
s_cbranch_vccnz XN0_SPIN%=
XN0_OK%=:
Mavvc 0,96,64
Mavvc 4,96,68
AM 35,32768
Mavvc 8,96,72
D 236,28,33,0
Mavvc 12,96,76
Rv 0,140,0
Mavvc 16,96,80
T 32,144,0
Mavvc 20,96,84
T 34,144,1024
Mavvc 24,96,88
Rv 4,140,2048
s_and_b32 s21, s12, 3
Mavvc 28,96,92
Mavvc 60,100,92
T 36,145,0
s_mul_i32 s21, s21, 40960
Mavvc 56,100,88
D 237,28,33,1024
Mavvc 52,100,84
T 38,145,1024
s_add_u32 s21, s21, s53
Mavvc 48,100,80
Mavvc 44,100,76
Rv 8,140,4096
AS 21,21,0
Mavvc 40,100,72
Mavvc 36,100,68
T 40,146,0
v_min_u32 v249, 63, v254
Mavvc 32,100,64
Mavvc 64,104,64
v_lshlrev_b32 v249, 7, v249
D 238,28,33,2048
Mavvc 68,104,68
T 42,146,1024
Mavvc 72,104,72
Rv 12,140,6144
buffer_load_dword v248, v249, s[48:51], s21 offen sc1
Mavvc 76,104,76
Mavvc 80,104,80
T 44,147,0
Mavvc 84,104,84
Mavvc 88,104,88
T 46,147,1024
Mavvc 92,104,92
D 239,28,33,3072
Mavvc 124,108,92
AS 33,33,262144
Mavvc 120,108,88
Rv 16,140,8192
Mavvc 116,108,84
T 48,148,0
Mavvc 112,108,80
T 50,148,1024
Mavvc 108,108,76
Mavvc 104,108,72
Rv 20,140,10240
Mavvc 100,108,68
Mavvc 96,108,64
T 52,149,0
Mavvc 128,112,64
Mavvc 132,112,68
T 54,149,1024
Mavvc 136,112,72
Mavvc 140,112,76
Rv 24,140,12288
AS 55,53,0
M0 65536
Mavvc 144,112,80
Ds 243,48,55,0
Mavvc 148,112,84
T 56,150,0
Mavvc 152,112,88
Mavvc 156,112,92
T 58,150,1024
Mavvc 188,116,92
Mavvc 184,116,88
Rv 28,140,14336
Mavvc 180,116,84
Ds 243,48,55,1024
Mavvc 176,116,80
T 60,151,0
Mavvc 172,116,76
Mavvc 168,116,72
T 62,151,1024
Mavvc 164,116,68
Mavvc 160,116,64
Mavvc 192,120,64
Ds 243,48,55,2048
Mavvc 196,120,68
Mavvc 200,120,72
Mavvc 204,120,76
Mavvc 208,120,80
Mavvc 212,120,84
Mavvc 216,120,88
Ds 243,48,55,3072
Mavvc 220,120,92
Mavvc 252,124,92
Mavvc 248,124,88
Mavvc 244,124,84
Mavvc 240,124,80
Mavvc 236,124,76
Mavvc 232,124,72
Mavvc 228,124,68
Mavvc 224,124,64
WV 13
B
Mavvc 0,32,0
Mavvc 4,32,4
AM 35,0
Mavvc 8,32,8
D 236,28,33,0
Mavvc 12,32,12
Rv 64,141,0
Mavvc 16,32,16
T 96,144,16384
Mavvc 20,32,20
T 98,144,17408
Mavvc 24,32,24
Rv 68,141,2048
s_and_b32 s21, s12, 3
Mavvc 28,32,28
Mavvc 60,36,28
T 100,145,16384
s_mul_i32 s21, s21, 40960
Mavvc 56,36,24
D 237,28,33,1024
Mavvc 52,36,20
T 102,145,17408
s_add_u32 s21, s21, s53
Mavvc 48,36,16
Mavvc 44,36,12
)A"
R"A(Rv 72,141,4096
AS 21,21,8192
Mavvc 40,36,8
Mavvc 36,36,4
T 104,146,16384
v_min_u32 v249, 63, v254
Mavvc 32,36,0
Mavvc 64,40,0
v_lshlrev_b32 v249, 7, v249
D 238,28,33,2048
Mavvc 68,40,4
T 106,146,17408
Mavvc 72,40,8
Rv 76,141,6144
buffer_load_dword v248, v249, s[48:51], s21 offen sc1
Mavvc 76,40,12
Mavvc 80,40,16
T 108,147,16384
Mavvc 84,40,20
Mavvc 88,40,24
T 110,147,17408
Mavvc 92,40,28
D 239,28,33,3072
Mavvc 124,44,28
AS 33,33,262144
Mavvc 120,44,24
Rv 80,141,8192
Mavvc 116,44,20
T 112,148,16384
Mavvc 112,44,16
T 114,148,17408
Mavvc 108,44,12
Mavvc 104,44,8
Rv 84,141,10240
Mavvc 100,44,4
Mavvc 96,44,0
T 116,149,16384
Mavvc 128,48,0
Mavvc 132,48,4
T 118,149,17408
Mavvc 136,48,8
Mavvc 140,48,12
Rv 88,141,12288
AS 55,53,8192
M0 69632
Mavvc 144,48,16
Ds 243,48,55,0
Mavvc 148,48,20
T 120,150,16384
Mavvc 152,48,24
Mavvc 156,48,28
T 122,150,17408
Mavvc 188,52,28
Mavvc 184,52,24
Rv 92,141,14336
Mavvc 180,52,20
Ds 243,48,55,1024
Mavvc 176,52,16
T 124,151,16384
Mavvc 172,52,12
Mavvc 168,52,8
T 126,151,17408
Mavvc 164,52,4
Mavvc 160,52,0
Mavvc 192,56,0
Ds 243,48,55,2048
Mavvc 196,56,4
Mavvc 200,56,8
Mavvc 204,56,12
Mavvc 208,56,16
Mavvc 212,56,20
Mavvc 216,56,24
Ds 243,48,55,3072
Mavvc 220,56,28
Mavvc 252,60,28
Mavvc 248,60,24
Mavvc 244,60,20
Mavvc 240,60,16
Mavvc 236,60,12
Mavvc 232,60,8
Mavvc 228,60,4
Mavvc 224,60,0
WV 13
B
Mavvc 0,96,64
Mavvc 4,96,68
Rv 0,140,32768
Mavvc 8,96,72
T 32,144,32768
s_add_u32 s55, s52, 0x0
M0 114688
Mavvc 12,96,76
D 236,44,55,0
Mavvc 16,96,80
T 34,144,33792
Mavvc 20,96,84
Mavvc 24,96,88
Rv 4,140,34816
s_and_b32 s21, s12, 3
Mavvc 28,96,92
Mavvc 60,100,92
T 36,145,32768
s_mul_i32 s21, s21, 40960
Mavvc 56,100,88
Mavvc 52,100,84
T 38,145,33792
s_add_u32 s21, s21, s53
D 237,44,55,1024
Mavvc 48,100,80
Mavvc 44,100,76
Rv 8,140,36864
AS 21,21,16384
Mavvc 40,100,72
Mavvc 36,100,68
T 40,146,32768
v_min_u32 v249, 31, v254
Mavvc 32,100,64
Mavvc 64,104,64
T 42,146,33792
v_lshlrev_b32 v249, 7, v249
Mavvc 68,104,68
D 238,44,55,2048
Mavvc 72,104,72
Rv 12,140,38912
buffer_load_dword v248, v249, s[48:51], s21 offen sc1
Mavvc 76,104,76
Mavvc 80,104,80
T 44,147,32768
Mavvc 84,104,84
Mavvc 88,104,88
T 46,147,33792
Mavvc 92,104,92
Mavvc 124,108,92
Rv 16,140,40960
D 239,44,55,3072
Mavvc 120,108,88
T 48,148,32768
Mavvc 116,108,84
Mavvc 112,108,80
T 50,148,33792
Mavvc 108,108,76
Mavvc 104,108,72
Rv 20,140,43008
Mavvc 100,108,68
Mavvc 96,108,64
T 52,149,32768
AS 55,53,16384
M0 0
Mavvc 128,112,64
Ds 243,48,55,0
Mavvc 132,112,68
T 54,149,33792
Mavvc 136,112,72
Mavvc 140,112,76
Rv 24,140,45056
Mavvc 144,112,80
Mavvc 148,112,84
T 56,150,32768
Mavvc 152,112,88
Mavvc 156,112,92
T 58,150,33792
Ds 243,48,55,1024
Mavvc 188,116,92
Mavvc 184,116,88
Rv 28,140,47104
Mavvc 180,116,84
Mavvc 176,116,80
T 60,151,32768
Mavvc 172,116,76
Mavvc 168,116,72
T 62,151,33792
Mavvc 164,116,68
Ds 243,48,55,2048
Mavvc 160,116,64
Mavvc 192,120,64
Mavvc 196,120,68
Mavvc 200,120,72
Mavvc 204,120,76
Mavvc 208,120,80
Mavvc 212,120,84
Ds 243,48,55,3072
Mavvc 216,120,88
Mavvc 220,120,92
Mavvc 252,124,92
Mavvc 248,124,88
Mavvc 244,124,84
Mavvc 240,124,80
Mavvc 236,124,76
Mavvc 232,124,72
Mavvc 228,124,68
Mavvc 224,124,64
WV 13
B
Mavvc 0,32,0
Mavvc 4,32,4
Rv 64,141,32768
Mavvc 8,32,8
T 96,144,0
s_add_u32 s55, s52, 0x80000
M0 131072
Mavvc 12,32,12
D 236,44,55,0
Mavvc 16,32,16
T 98,144,1024
Mavvc 20,32,20
Mavvc 24,32,24
Rv 68,141,34816
Mavvc 28,32,28
Mavvc 60,36,28
T 100,145,0
Mavvc 56,36,24
Mavvc 52,36,20
T 102,145,1024
D 237,44,55,1024
Mavvc 48,36,16
Mavvc 44,36,12
Rv 72,141,36864
Mavvc 40,36,8
Mavvc 36,36,4
T 104,146,0
Mavvc 32,36,0
Mavvc 64,40,0
T 106,146,1024
Mavvc 68,40,4
D 238,44,55,2048
Mavvc 72,40,8
Rv 76,141,38912
Mavvc 76,40,12
Mavvc 80,40,16
T 108,147,0
Mavvc 84,40,20
Mavvc 88,40,24
T 110,147,1024
Mavvc 92,40,28
Mavvc 124,44,28
Rv 80,141,40960
D 239,44,55,3072
Mavvc 120,44,24
T 112,148,0
Mavvc 116,44,20
Mavvc 112,44,16
T 114,148,1024
Mavvc 108,44,12
Mavvc 104,44,8
Rv 84,141,43008
Mavvc 100,44,4
Mavvc 96,44,0
T 116,149,0
AS 55,53,24576
M0 4096
Mavvc 128,48,0
Ds 243,48,55,0
Mavvc 132,48,4
T 118,149,1024
Mavvc 136,48,8
Mavvc 140,48,12
Rv 88,141,45056
Mavvc 144,48,16
Mavvc 148,48,20
T 120,150,0
Mavvc 152,48,24
Mavvc 156,48,28
T 122,150,1024
Ds 243,48,55,1024
Mavvc 188,52,28
Mavvc 184,52,24
Rv 92,141,47104
Mavvc 180,52,20
Mavvc 176,52,16
T 124,151,0
Mavvc 172,52,12
Mavvc 168,52,8
T 126,151,1024
Mavvc 164,52,4
Ds 243,48,55,2048
Mavvc 160,52,0
Mavvc 192,56,0
Mavvc 196,56,4
Mavvc 200,56,8
Mavvc 204,56,12
Mavvc 208,56,16
Mavvc 212,56,20
Ds 243,48,55,3072
Mavvc 216,56,24
Mavvc 220,56,28
Mavvc 252,60,28
Mavvc 248,60,24
Mavvc 244,60,20
Mavvc 240,60,16
Mavvc 236,60,12
Mavvc 232,60,8
Mavvc 228,60,4
Mavvc 224,60,0
WV 12
B
Mavvc 0,96,64
Mavvc 4,96,68
Mavvc 8,96,72
s_add_u32 s55, s52, 0x100000
M0 98304
Mavvc 12,96,76
D 236,44,55,0
Mavvc 16,96,80
Rv 192,245,0
Mavvc 20,96,84
Mavvc 24,96,88
Mavvc 28,96,92
D 237,44,55,1024
Mavvc 60,100,92
Rv 196,245,1024
Mavvc 56,100,88
Mavvc 52,100,84
Rv 200,245,2048
Mavvc 48,100,80
Mavvc 44,100,76
D 238,44,55,2048
Mavvc 40,100,72
Mavvc 36,100,68
Rv 204,245,3072
Mavvc 32,100,64
Mavvc 64,104,64
Mavvc 68,104,68
D 239,44,55,3072
Mavvc 72,104,72
T 128,240,16384
Mavvc 76,104,76
T 130,240,17408
Mavvc 80,104,80
X 242,32,240
T 132,242,16384
T 134,242,17408
Mavvc 84,104,84
AS 55,53,32768
M0 32768
Mavvc 88,104,88
Ds 243,48,55,0
Mavvc 92,104,92
Mavvc 124,108,92
X 241,64,240
Mavvc 120,108,88
T 136,241,16384
Mavvc 116,108,84
T 138,241,17408
Mavvc 112,108,80
Ds 243,48,55,1024
Mavvc 108,108,76
X 242,96,240
Mavvc 104,108,72
T 140,242,16384
Mavvc 100,108,68
T 142,242,17408
Mavvc 96,108,64
X 241,128,240
T 144,241,16384
Mavvc 128,112,64
T 146,241,17408
Ds 243,48,55,2048
Mavvc 132,112,68
Mavvc 136,112,72
X 242,160,240
Mavvc 140,112,76
T 148,242,16384
T 150,242,17408
Mavvc 144,112,80
Ds 243,48,55,3072
Mavvc 148,112,84
X 241,192,240
Mavvc 152,112,88
T 152,241,16384
Mavvc 156,112,92
T 154,241,17408
Mavvc 188,116,92
AS 55,53,40960
M0 36864
Mavvc 184,116,88
X 242,224,240
Ds 243,48,55,0
Mavvc 180,116,84
T 156,242,16384
Mavvc 176,116,80
T 158,242,17408
Mavvc 172,116,76
Mavvc 168,116,72
Mavvc 164,116,68
Ds 243,48,55,1024
Mavvc 160,116,64
Mavvc 192,120,64
Mavvc 196,120,68
Mavvc 200,120,72
Mavvc 204,120,76
Ds 243,48,55,2048
Mavvc 208,120,80
Mavvc 212,120,84
Mavvc 216,120,88
Mavvc 220,120,92
Mavvc 252,124,92
Ds 243,48,55,3072
Mavvc 248,124,88
Mavvc 244,124,84
Mavvc 240,124,80
Mavvc 236,124,76
Mavvc 232,124,72
Mavvc 228,124,68
Mavvc 224,124,64
s_add_u32 s36, s14, 0x400000
s_addc_u32 s37, s15, 0
SM 38,0x1000000
SM 39,0x20000
s_lshl_b32 s16, s12, 19
s_lshl_b32 s17, s13, 16
s_add_u32 s40, s16, s17
s_add_u32 s24, s14, 0x400000
s_addc_u32 s25, s15, 0
SM 26,0x2000000
SM 27,0x20000
s_add_u32 s28, s14, 0x1400000
s_addc_u32 s29, s15, 0
SM 30,0x1000000
SM 31,0x20000
s_lshl_b32 s16, s12, 19
s_lshl_b32 s17, s11, 12
s_add_u32 s56, s16, s17
s_mov_b32 s57, s56
AS 58,17,0
AS 59,17,65536
AS 56,56,32768
QS0%=:
W 0
Mvvvz 0,128,192
Rv 208,245,4096
G 224,0
Mvvvz 4,128,196
Rv 212,245,5120
G 225,1
Mvvvz 8,128,200
G 226,2
G 227,3
Mvvvz 12,128,204
Rv 216,245,6144
G 228,32
Mvvvz 28,132,204
G 229,33
G 230,34
Mvvvz 24,132,200
Rv 220,245,7168
G 231,35
Mvvvz 20,132,196
C 232,224,225
C 233,226,227
Mvvvz 16,132,192
T 160,240,24576
C 234,228,229
Mvvvz 32,136,192
T 162,240,25600
C 235,230,231
Mvvvz 36,136,196
X 242,32,240
Svs 232,243,36,40,0
Mvvvz 40,136,200
T 164,242,24576
G 224,64
Mvvvz 44,136,204
T 166,242,25600
G 225,65
Mvvvz 60,140,204
X 241,64,240
G 226,66
Mvvvz 56,140,200
T 168,241,24576
G 227,67
Mvvvz 52,140,196
T 170,241,25600
G 228,96
Mvvvz 48,140,192
X 242,96,240
G 229,97
Mvvvz 64,144,192
T 172,242,24576
G 230,98
Mvvvz 68,144,196
T 174,242,25600
G 231,99
Mvvvz 72,144,200
X 241,128,240
C 232,224,225
Mvvvz 76,144,204
T 176,241,24576
C 233,226,227
Mvvvz 92,148,204
T 178,241,25600
C 234,228,229
Mvvvz 88,148,200
X 242,160,240
C 235,230,231
Mvvvz 84,148,196
T 180,242,24576
Svs 232,243,36,40,1024
Mvvvz 80,148,192
T 182,242,25600
G 224,128
Mvvvz 96,152,192
X 241,192,240
G 225,129
Mvvvz 100,152,196
T 184,241,24576
G 226,130
Mvvvz 104,152,200
T 186,241,25600
G 227,131
Mvvvz 108,152,204
X 242,224,240
G 228,160
Mvvvz 124,156,204
T 188,242,24576
G 229,161
Mvvvz 120,156,200
T 190,242,25600
G 230,162
Mvvvz 116,156,196
G 231,163
Mvvvz 112,156,192
C 232,224,225
C 233,226,227
C 234,228,229
C 235,230,231
Svs 232,243,36,40,2048
G 224,192
G 225,193
G 226,194
G 227,195
G 228,224
G 229,225
G 230,226
G 231,227
C 232,224,225
C 233,226,227
C 234,228,229
C 235,230,231
Svs 232,243,36,40,3072
WV 16
B
Mvvvc 0,160,208
Rv 192,244,0
AS 40,40,4096
Mvvvc 4,160,212
Rv 196,244,1024
G 224,4
Mvvvc 8,160,216
G 225,5
G 226,6
Mvvvc 12,160,220
Rv 200,244,2048
G 227,7
Mvvvc 28,164,220
G 228,36
G 229,37
Mvvvc 24,164,216
Rv 204,244,3072
G 230,38
Mvvvc 20,164,212
G 231,39
C 232,224,225
Mvvvc 16,164,208
T 128,240,32768
C 233,226,227
Mvvvc 32,168,208
T 130,240,33792
C 234,228,229
Mvvvc 36,168,212
X 242,32,240
C 235,230,231
Mvvvc 40,168,216
T 132,242,32768
Svs 232,243,36,40,0
Mvvvc 44,168,220
T 134,242,33792
G 224,68
Mvvvc 60,172,220
X 241,64,240
G 225,69
Mvvvc 56,172,216
T 136,241,32768
G 226,70
Mvvvc 52,172,212
T 138,241,33792
G 227,71
Mvvvc 48,172,208
X 242,96,240
G 228,100
Mvvvc 64,176,208
T 140,242,32768
G 229,101
Mvvvc 68,176,212
T 142,242,33792
G 230,102
Mvvvc 72,176,216
X 241,128,240
G 231,103
Mvvvc 76,176,220
T 144,241,32768
C 232,224,225
Mvvvc 92,180,220
T 146,241,33792
C 233,226,227
Mvvvc 88,180,216
X 242,160,240
C 234,228,229
Mvvvc 84,180,212
T 148,242,32768
C 235,230,231
Mvvvc 80,180,208
T 150,242,33792
Svs 232,243,36,40,1024
Mvvvc 96,184,208
X 241,192,240
G 224,132
Mvvvc 100,184,212
T 152,241,32768
G 225,133
Mvvvc 104,184,216
T 154,241,33792
G 226,134
Mvvvc 108,184,220
X 242,224,240
G 227,135
Mvvvc 124,188,220
T 156,242,32768
G 228,164
Mvvvc 120,188,216
T 158,242,33792
G 229,165
Mvvvc 116,188,212
G 230,166
Mvvvc 112,188,208
G 231,167
C 232,224,225
C 233,226,227
C 234,228,229
C 235,230,231
Svs 232,243,36,40,2048
G 224,196
G 225,197
G 226,198
G 227,199
G 228,228
G 229,229
G 230,230
G 231,231
C 232,224,225
C 233,226,227
C 234,228,229
C 235,230,231
Svs 232,243,36,40,3072
W 0
Mvvvc 0,128,192
Rv 208,244,4096
AS 40,40,4096
Mvvvc 4,128,196
Rv 212,244,5120
G 224,8
Mvvvc 8,128,200
G 225,9
G 226,10
Mvvvc 12,128,204
Rv 216,244,6144
Mvvvc 28,132,204
G 227,11
G 228,40
Mvvvc 24,132,200
Rv 220,244,7168
G 229,41
Mvvvc 20,132,196
G 230,42
G 231,43
Mvvvc 16,132,192
T 160,240,40960
C 232,224,225
Mvvvc 32,136,192
T 162,240,41984
C 233,226,227
Mvvvc 36,136,196
X 242,32,240
C 234,228,229
Mvvvc 40,136,200
T 164,242,40960
C 235,230,231
Mvvvc 44,136,204
T 166,242,41984
Svs 232,243,36,40,0
Mvvvc 60,140,204
X 241,64,240
G 224,72
Mvvvc 56,140,200
T 168,241,40960
G 225,73
Mvvvc 52,140,196
T 170,241,41984
G 226,74
Mvvvc 48,140,192
X 242,96,240
G 227,75
Mvvvc 64,144,192
T 172,242,40960
G 228,104
Mvvvc 68,144,196
T 174,242,41984
G 229,105
Mvvvc 72,144,200
X 241,128,240
G 230,106
Mvvvc 76,144,204
T 176,241,40960
G 231,107
Mvvvc 92,148,204
T 178,241,41984
C 232,224,225
Mvvvc 88,148,200
X 242,160,240
C 233,226,227
Mvvvc 84,148,196
T 180,242,40960
C 234,228,229
Mvvvc 80,148,192
T 182,242,41984
C 235,230,231
Mvvvc 96,152,192
X 241,192,240
Svs 232,243,36,40,1024
Mvvvc 100,152,196
T 184,241,40960
G 224,136
Mvvvc 104,152,200
T 186,241,41984
G 225,137
Mvvvc 108,152,204
X 242,224,240
G 226,138
Mvvvc 124,156,204
T 188,242,40960
G 227,139
Mvvvc 120,156,200
T 190,242,41984
G 228,168
Mvvvc 116,156,196
G 229,169
Mvvvc 112,156,192
G 230,170
G 231,171
C 232,224,225
C 233,226,227
C 234,228,229
C 235,230,231
Svs 232,243,36,40,2048
WV 11
B
Mvvvc 0,160,208
Rv 192,244,32768
G 224,200
Mvvvc 4,160,212
Rv 196,244,33792
G 225,201
Mvvvc 8,160,216
G 226,202
G 227,203
Mvvvc 12,160,220
Rv 200,244,34816
G 228,232
Mvvvc 28,164,220
G 229,233
G 230,234
Mvvvc 24,164,216
Rv 204,244,35840
G 231,235
Mvvvc 20,164,212
C 232,224,225
C 233,226,227
Mvvvc 16,164,208
T 128,240,0
C 234,228,229
Mvvvc 32,168,208
T 130,240,1024
C 235,230,231
Mvvvc 36,168,212
X 242,32,240
Svs 232,243,36,40,3072
Mvvvc 40,168,216
T 132,242,0
AS 40,40,4096
Mvvvc 44,168,220
T 134,242,1024
)A"
R"A(G 224,12
Mvvvc 60,172,220
X 241,64,240
G 225,13
Mvvvc 56,172,216
T 136,241,0
G 226,14
Mvvvc 52,172,212
T 138,241,1024
G 227,15
Mvvvc 48,172,208
X 242,96,240
G 228,44
Mvvvc 64,176,208
T 140,242,0
G 229,45
Mvvvc 68,176,212
T 142,242,1024
G 230,46
Mvvvc 72,176,216
X 241,128,240
G 231,47
Mvvvc 76,176,220
T 144,241,0
C 232,224,225
Mvvvc 92,180,220
T 146,241,1024
C 233,226,227
Mvvvc 88,180,216
X 242,160,240
C 234,228,229
Mvvvc 84,180,212
T 148,242,0
C 235,230,231
Mvvvc 80,180,208
T 150,242,1024
Svs 232,243,36,40,0
Mvvvc 96,184,208
X 241,192,240
G 224,76
Mvvvc 100,184,212
T 152,241,0
G 225,77
Mvvvc 104,184,216
T 154,241,1024
G 226,78
Mvvvc 108,184,220
X 242,224,240
G 227,79
Mvvvc 124,188,220
T 156,242,0
G 228,108
Mvvvc 120,188,216
T 158,242,1024
G 229,109
Mvvvc 116,188,212
G 230,110
Mvvvc 112,188,208
G 231,111
C 232,224,225
C 233,226,227
C 234,228,229
C 235,230,231
Svs 232,243,36,40,1024
G 224,140
G 225,141
G 226,142
G 227,143
G 228,172
G 229,173
G 230,174
G 231,175
C 232,224,225
C 233,226,227
C 234,228,229
C 235,230,231
Svs 232,243,36,40,2048
W 0
Mvvvc 0,128,192
Rv 208,244,36864
G 224,204
Mvvvc 4,128,196
Rv 212,244,37888
G 225,205
Mvvvc 8,128,200
G 226,206
G 227,207
Mvvvc 12,128,204
Rv 216,244,38912
Mvvvc 28,132,204
G 228,236
G 229,237
Mvvvc 24,132,200
Rv 220,244,39936
G 230,238
Mvvvc 20,132,196
G 231,239
C 232,224,225
Mvvvc 16,132,192
T 160,240,8192
C 233,226,227
Mvvvc 32,136,192
T 162,240,9216
C 234,228,229
Mvvvc 36,136,196
X 242,32,240
C 235,230,231
Mvvvc 40,136,200
T 164,242,8192
Svs 232,243,36,40,3072
Mvvvc 44,136,204
T 166,242,9216
AS 40,40,4096
Mvvvc 60,140,204
X 241,64,240
G 224,16
Mvvvc 56,140,200
T 168,241,8192
G 225,17
Mvvvc 52,140,196
T 170,241,9216
G 226,18
Mvvvc 48,140,192
X 242,96,240
G 227,19
Mvvvc 64,144,192
T 172,242,8192
G 228,48
Mvvvc 68,144,196
T 174,242,9216
G 229,49
Mvvvc 72,144,200
X 241,128,240
G 230,50
Mvvvc 76,144,204
T 176,241,8192
G 231,51
Mvvvc 92,148,204
T 178,241,9216
C 232,224,225
Mvvvc 88,148,200
X 242,160,240
C 233,226,227
Mvvvc 84,148,196
T 180,242,8192
C 234,228,229
Mvvvc 80,148,192
T 182,242,9216
C 235,230,231
Mvvvc 96,152,192
X 241,192,240
Svs 232,243,36,40,0
Mvvvc 100,152,196
T 184,241,8192
G 224,80
Mvvvc 104,152,200
T 186,241,9216
G 225,81
Mvvvc 108,152,204
X 242,224,240
G 226,82
Mvvvc 124,156,204
T 188,242,8192
G 227,83
Mvvvc 120,156,200
T 190,242,9216
G 228,112
Mvvvc 116,156,196
G 229,113
Mvvvc 112,156,192
G 230,114
G 231,115
C 232,224,225
C 233,226,227
C 234,228,229
C 235,230,231
Svs 232,243,36,40,1024
W 0
B
Mvvvc 0,160,208
Rv 192,245,0
G 224,144
Mvvvc 4,160,212
Rv 196,245,1024
G 225,145
Mvvvc 8,160,216
G 226,146
G 227,147
Mvvvc 12,160,220
Rv 200,245,2048
G 228,176
Mvvvc 28,164,220
G 229,177
G 230,178
Mvvvc 24,164,216
Rv 204,245,3072
G 231,179
Mvvvc 20,164,212
C 232,224,225
C 233,226,227
Mvvvc 16,164,208
T 128,240,16384
C 234,228,229
Mvvvc 32,168,208
T 130,240,17408
C 235,230,231
Mvvvc 36,168,212
X 242,32,240
Svs 232,243,36,40,2048
Mvvvc 40,168,216
T 132,242,16384
G 224,208
Mvvvc 44,168,220
T 134,242,17408
G 225,209
Mvvvc 60,172,220
X 241,64,240
G 226,210
Mvvvc 56,172,216
T 136,241,16384
G 227,211
Mvvvc 52,172,212
T 138,241,17408
G 228,240
Mvvvc 48,172,208
X 242,96,240
G 229,241
Mvvvc 64,176,208
T 140,242,16384
G 230,242
Mvvvc 68,176,212
T 142,242,17408
G 231,243
Mvvvc 72,176,216
X 241,128,240
C 232,224,225
Mvvvc 76,176,220
T 144,241,16384
C 233,226,227
Mvvvc 92,180,220
T 146,241,17408
C 234,228,229
Mvvvc 88,180,216
X 242,160,240
C 235,230,231
Mvvvc 84,180,212
T 148,242,16384
Svs 232,243,36,40,3072
Mvvvc 80,180,208
T 150,242,17408
AS 40,40,4096
Mvvvc 96,184,208
X 241,192,240
G 224,20
Mvvvc 100,184,212
T 152,241,16384
G 225,21
Mvvvc 104,184,216
T 154,241,17408
G 226,22
Mvvvc 108,184,220
X 242,224,240
G 227,23
Mvvvc 124,188,220
T 156,242,16384
G 228,52
Mvvvc 120,188,216
T 158,242,17408
G 229,53
Mvvvc 116,188,212
G 230,54
Mvvvc 112,188,208
G 231,55
C 232,224,225
C 233,226,227
C 234,228,229
C 235,230,231
Svs 232,243,36,40,0
G 224,84
G 225,85
G 226,86
G 227,87
G 228,116
G 229,117
G 230,118
G 231,119
C 232,224,225
C 233,226,227
C 234,228,229
C 235,230,231
Svs 232,243,36,40,1024
W 0
Mvvvc 0,128,192
Rv 208,245,4096
G 224,148
Mvvvc 4,128,196
Rv 212,245,5120
G 225,149
Mvvvc 8,128,200
G 226,150
G 227,151
Mvvvc 12,128,204
Rv 216,245,6144
Mvvvc 28,132,204
G 228,180
G 229,181
Mvvvc 24,132,200
Rv 220,245,7168
G 230,182
Mvvvc 20,132,196
G 231,183
C 232,224,225
Mvvvc 16,132,192
T 160,240,24576
C 233,226,227
Mvvvc 32,136,192
T 162,240,25600
C 234,228,229
Mvvvc 36,136,196
X 242,32,240
C 235,230,231
Mvvvc 40,136,200
T 164,242,24576
Svs 232,243,36,40,2048
Mvvvc 44,136,204
T 166,242,25600
G 224,212
Mvvvc 60,140,204
X 241,64,240
G 225,213
Mvvvc 56,140,200
T 168,241,24576
G 226,214
Mvvvc 52,140,196
T 170,241,25600
G 227,215
Mvvvc 48,140,192
X 242,96,240
G 228,244
Mvvvc 64,144,192
T 172,242,24576
G 229,245
Mvvvc 68,144,196
T 174,242,25600
G 230,246
Mvvvc 72,144,200
X 241,128,240
G 231,247
Mvvvc 76,144,204
T 176,241,24576
C 232,224,225
Mvvvc 92,148,204
T 178,241,25600
C 233,226,227
Mvvvc 88,148,200
X 242,160,240
C 234,228,229
Mvvvc 84,148,196
T 180,242,24576
C 235,230,231
Mvvvc 80,148,192
T 182,242,25600
Svs 232,243,36,40,3072
Mvvvc 96,152,192
X 241,192,240
AS 40,40,4096
Mvvvc 100,152,196
T 184,241,24576
G 224,24
Mvvvc 104,152,200
T 186,241,25600
G 225,25
Mvvvc 108,152,204
X 242,224,240
G 226,26
Mvvvc 124,156,204
T 188,242,24576
G 227,27
Mvvvc 120,156,200
T 190,242,25600
G 228,56
Mvvvc 116,156,196
G 229,57
Mvvvc 112,156,192
G 230,58
G 231,59
C 232,224,225
C 233,226,227
C 234,228,229
C 235,230,231
Svs 232,243,36,40,0
W 0
B
Mvvvc 0,160,208
Rv 192,244,0
G 224,88
Mvvvc 4,160,212
Rv 196,244,1024
G 225,89
Mvvvc 8,160,216
G 226,90
G 227,91
Mvvvc 12,160,220
Rv 200,244,2048
G 228,120
Mvvvc 28,164,220
G 229,121
G 230,122
Mvvvc 24,164,216
Rv 204,244,3072
G 231,123
Mvvvc 20,164,212
C 232,224,225
C 233,226,227
Mvvvc 16,164,208
T 128,240,32768
C 234,228,229
Mvvvc 32,168,208
T 130,240,33792
C 235,230,231
Mvvvc 36,168,212
X 242,32,240
Svs 232,243,36,40,1024
Mvvvc 40,168,216
T 132,242,32768
G 224,152
Mvvvc 44,168,220
T 134,242,33792
G 225,153
Mvvvc 60,172,220
X 241,64,240
G 226,154
Mvvvc 56,172,216
T 136,241,32768
G 227,155
Mvvvc 52,172,212
T 138,241,33792
G 228,184
Mvvvc 48,172,208
X 242,96,240
G 229,185
Mvvvc 64,176,208
T 140,242,32768
G 230,186
Mvvvc 68,176,212
T 142,242,33792
G 231,187
Mvvvc 72,176,216
X 241,128,240
C 232,224,225
Mvvvc 76,176,220
T 144,241,32768
C 233,226,227
Mvvvc 92,180,220
T 146,241,33792
C 234,228,229
Mvvvc 88,180,216
X 242,160,240
C 235,230,231
Mvvvc 84,180,212
T 148,242,32768
Svs 232,243,36,40,2048
Mvvvc 80,180,208
T 150,242,33792
G 224,216
Mvvvc 96,184,208
X 241,192,240
G 225,217
Mvvvc 100,184,212
T 152,241,32768
G 226,218
Mvvvc 104,184,216
T 154,241,33792
G 227,219
Mvvvc 108,184,220
X 242,224,240
G 228,248
Mvvvc 124,188,220
T 156,242,32768
G 229,249
Mvvvc 120,188,216
T 158,242,33792
G 230,250
Mvvvc 116,188,212
G 231,251
Mvvvc 112,188,208
C 232,224,225
C 233,226,227
C 234,228,229
C 235,230,231
Svs 232,243,36,40,3072
AS 40,40,4096
G 224,28
G 225,29
G 226,30
G 227,31
G 228,60
G 229,61
G 230,62
G 231,63
C 232,224,225
C 233,226,227
C 234,228,229
C 235,230,231
Svs 232,243,36,40,0
W 0
Mvvvc 0,128,192
Rv 208,244,4096
G 224,92
Mvvvc 4,128,196
Rv 212,244,5120
G 225,93
Mvvvc 8,128,200
G 226,94
G 227,95
Mvvvc 12,128,204
Rv 216,244,6144
Mvvvc 28,132,204
G 228,124
G 229,125
Mvvvc 24,132,200
Rv 220,244,7168
G 230,126
Mvvvc 20,132,196
G 231,127
C 232,224,225
Mvvvc 16,132,192
T 160,240,40960
C 233,226,227
Mvvvc 32,136,192
T 162,240,41984
C 234,228,229
Mvvvc 36,136,196
X 242,32,240
C 235,230,231
Mvvvc 40,136,200
T 164,242,40960
Svs 232,243,36,40,1024
Mvvvc 44,136,204
T 166,242,41984
G 224,156
Mvvvc 60,140,204
X 241,64,240
G 225,157
Mvvvc 56,140,200
T 168,241,40960
G 226,158
Mvvvc 52,140,196
T 170,241,41984
G 227,159
Mvvvc 48,140,192
X 242,96,240
G 228,188
Mvvvc 64,144,192
T 172,242,40960
G 229,189
Mvvvc 68,144,196
T 174,242,41984
G 230,190
Mvvvc 72,144,200
X 241,128,240
G 231,191
Mvvvc 76,144,204
T 176,241,40960
C 232,224,225
Mvvvc 92,148,204
T 178,241,41984
C 233,226,227
Mvvvc 88,148,200
X 242,160,240
C 234,228,229
Mvvvc 84,148,196
T 180,242,40960
C 235,230,231
Mvvvc 80,148,192
T 182,242,41984
Svs 232,243,36,40,2048
Mvvvc 96,152,192
X 241,192,240
G 224,220
Mvvvc 100,152,196
T 184,241,40960
G 225,221
Mvvvc 104,152,200
T 186,241,41984
G 226,222
Mvvvc 108,152,204
X 242,224,240
G 227,223
Mvvvc 124,156,204
T 188,242,40960
G 228,252
Mvvvc 120,156,200
T 190,242,41984
G 229,253
Mvvvc 116,156,196
G 230,254
Mvvvc 112,156,192
G 231,255
C 232,224,225
C 233,226,227
C 234,228,229
C 235,230,231
Svs 232,243,36,40,3072
W 0
B
Mvvvc 0,160,208
Rv 192,244,32768
Mvvvc 4,160,212
s_add_u32 s55, s52, 0x380000
M0 131072
Mvvvc 8,160,216
Rv 196,244,33792
D 236,44,55,0
Mvvvc 12,160,220
Rv 200,244,34816
Mvvvc 28,164,220
D 237,44,55,1024
Mvvvc 24,164,216
Rv 204,244,35840
Mvvvc 20,164,212
D 238,44,55,2048
Mvvvc 16,164,208
T 128,240,0
Mvvvc 32,168,208
T 130,240,1024
Mvvvc 36,168,212
X 242,32,240
T 132,242,0
D 239,44,55,3072
Mvvvc 40,168,216
T 134,242,1024
s_add_u32 s55, s52, 0x3c0000
M0 139264
Mvvvc 44,168,220
D 236,44,55,0
Mvvvc 60,172,220
X 241,64,240
T 136,241,0
Mvvvc 56,172,216
T 138,241,1024
D 237,44,55,1024
Mvvvc 52,172,212
X 242,96,240
Mvvvc 48,172,208
T 140,242,0
T 142,242,1024
Mvvvc 64,176,208
X 241,128,240
D 238,44,55,2048
Mvvvc 68,176,212
T 144,241,0
T 146,241,1024
Mvvvc 72,176,216
X 242,160,240
T 148,242,0
D 239,44,55,3072
Mvvvc 76,176,220
T 150,242,1024
AS 55,53,114688
M0 0
Mvvvc 92,180,220
X 241,192,240
Ds 243,48,55,0
Mvvvc 88,180,216
T 152,241,0
Mvvvc 84,180,212
T 154,241,1024
X 242,224,240
Mvvvc 80,180,208
T 156,242,0
Ds 243,48,55,1024
Mvvvc 96,184,208
T 158,242,1024
Mvvvc 100,184,212
Ds 243,48,55,2048
Mvvvc 104,184,216
Mvvvc 108,184,220
Ds 243,48,55,3072
Mvvvc 124,188,220
Mvvvc 120,188,216
Mvvvc 116,188,212
Mvvvc 112,188,208
W 0
Mvvvc 0,128,192
Rv 208,244,36864
Mvvvc 4,128,196
AS 55,54,114688
M0 16384
Mvvvc 8,128,200
Rv 212,244,37888
Ds 243,48,55,0
Mvvvc 12,128,204
Rv 216,244,38912
Mvvvc 28,132,204
Ds 243,48,55,1024
Mvvvc 24,132,200
Rv 220,244,39936
Mvvvc 20,132,196
Ds 243,48,55,2048
Mvvvc 16,132,192
T 160,240,8192
Mvvvc 32,136,192
T 162,240,9216
Mvvvc 36,136,196
X 242,32,240
T 164,242,8192
Ds 243,48,55,3072
Mvvvc 40,136,200
T 166,242,9216
AS 55,53,122880
M0 4096
Mvvvc 44,136,204
Ds 243,48,55,0
Mvvvc 60,140,204
X 241,64,240
T 168,241,8192
Mvvvc 56,140,200
T 170,241,9216
Ds 243,48,55,1024
Mvvvc 52,140,196
X 242,96,240
Mvvvc 48,140,192
T 172,242,8192
T 174,242,9216
Mvvvc 64,144,192
X 241,128,240
Ds 243,48,55,2048
Mvvvc 68,144,196
T 176,241,8192
T 178,241,9216
Mvvvc 72,144,200
X 242,160,240
T 180,242,8192
Ds 243,48,55,3072
Mvvvc 76,144,204
T 182,242,9216
AS 55,54,122880
M0 20480
Mvvvc 92,148,204
X 241,192,240
Ds 243,48,55,0
Mvvvc 88,148,200
T 184,241,8192
Mvvvc 84,148,196
T 186,241,9216
X 242,224,240
Mvvvc 80,148,192
T 188,242,8192
Ds 243,48,55,1024
Mvvvc 96,152,192
T 190,242,9216
Mvvvc 100,152,196
Ds 243,48,55,2048
Mvvvc 104,152,200
Mvvvc 108,152,204
Ds 243,48,55,3072
Mvvvc 124,156,204
Mvvvc 120,156,200
Mvvvc 116,156,196
Mvvvc 112,156,192
W 0
B
s_getpc_b64 s[16:17]
1:
s_add_u32 s16, s16, QE0%=-1b
s_addc_u32 s17, s17, 0
s_lshr_b32 s21, s10, 3
v_lshlrev_b32 v249, 5, v254
v_add_u32 v249, s21, v249
v_min_u32 v249, ((AE0%=-QE0%=)>>7), v249
v_lshlrev_b32 v249, 7, v249
global_load_dword v248, v249, s[16:17]
Mvvvc 0,160,208
Rv 192,245,0
Mvvvc 4,160,212
s_add_u32 s55, s52, 0x400000
M0 98304
Mvvvc 8,160,216
Rv 196,245,1024
D 236,44,55,0
Mvvvc 12,160,220
Rv 200,245,2048
Mvvvc 28,164,220
D 237,44,55,1024
Mvvvc 24,164,216
Rv 204,245,3072
Mvvvc 20,164,212
D 238,44,55,2048
Mvvvc 16,164,208
T 128,240,16384
Mvvvc 32,168,208
T 130,240,17408
Mvvvc 36,168,212
X 242,32,240
T 132,242,16384
D 239,44,55,3072
Mvvvc 40,168,216
T 134,242,17408
s_add_u32 s55, s52, 0x440000
M0 106496
Mvvvc 44,168,220
D 236,44,55,0
Mvvvc 60,172,220
X 241,64,240
T 136,241,16384
Mvvvc 56,172,216
T 138,241,17408
D 237,44,55,1024
Mvvvc 52,172,212
X 242,96,240
Mvvvc 48,172,208
T 140,242,16384
T 142,242,17408
Mvvvc 64,176,208
X 241,128,240
)A"
R"A(D 238,44,55,2048
Mvvvc 68,176,212
T 144,241,16384
T 146,241,17408
Mvvvc 72,176,216
X 242,160,240
T 148,242,16384
D 239,44,55,3072
Mvvvc 76,176,220
T 150,242,17408
AS 55,53,131072
M0 32768
Mvvvc 92,180,220
X 241,192,240
Ds 243,48,55,0
Mvvvc 88,180,216
T 152,241,16384
Mvvvc 84,180,212
T 154,241,17408
X 242,224,240
Mvvvc 80,180,208
T 156,242,16384
Ds 243,48,55,1024
Mvvvc 96,184,208
T 158,242,17408
Mvvvc 100,184,212
Ds 243,48,55,2048
Mvvvc 104,184,216
Mvvvc 108,184,220
Ds 243,48,55,3072
Mvvvc 124,188,220
Mvvvc 120,188,216
Mvvvc 116,188,212
Mvvvc 112,188,208
W 0
s_load_dwordx2 s[42:43], s[8:9], 40
s_load_dwordx2 s[36:37], s[8:9], 40
s_load_dwordx2 s[38:39], s[8:9], 72
Mvvvc 0,128,192
Rv 208,245,4096
Mvvvc 4,128,196
AS 55,54,131072
M0 49152
Mvvvc 8,128,200
Rv 212,245,5120
Ds 243,48,55,0
Mvvvc 12,128,204
Rv 216,245,6144
Mvvvc 28,132,204
Ds 243,48,55,1024
Mvvvc 24,132,200
Rv 220,245,7168
Mvvvc 20,132,196
Ds 243,48,55,2048
Mvvvc 16,132,192
T 160,240,24576
Mvvvc 32,136,192
T 162,240,25600
Mvvvc 36,136,196
X 242,32,240
T 164,242,24576
Ds 243,48,55,3072
Mvvvc 40,136,200
T 166,242,25600
AS 55,53,139264
M0 36864
Mvvvc 44,136,204
Ds 243,48,55,0
Mvvvc 60,140,204
X 241,64,240
T 168,241,24576
Mvvvc 56,140,200
T 170,241,25600
Ds 243,48,55,1024
Mvvvc 52,140,196
X 242,96,240
Mvvvc 48,140,192
T 172,242,24576
T 174,242,25600
Mvvvc 64,144,192
X 241,128,240
Ds 243,48,55,2048
Mvvvc 68,144,196
T 176,241,24576
T 178,241,25600
Mvvvc 72,144,200
X 242,160,240
T 180,242,24576
Ds 243,48,55,3072
Mvvvc 76,144,204
T 182,242,25600
AS 55,54,139264
M0 53248
Mvvvc 92,148,204
X 241,192,240
Ds 243,48,55,0
Mvvvc 88,148,200
T 184,241,24576
Mvvvc 84,148,196
T 186,241,25600
X 242,224,240
Mvvvc 80,148,192
T 188,242,24576
Ds 243,48,55,1024
Mvvvc 96,152,192
T 190,242,25600
Mvvvc 100,152,196
Ds 243,48,55,2048
Mvvvc 104,152,200
Mvvvc 108,152,204
Ds 243,48,55,3072
Mvvvc 124,156,204
Mvvvc 120,156,200
Mvvvc 116,156,196
Mvvvc 112,156,192
WV 25
B
s_lshl_b32 s21, s12, 8
AS 21,21,4096
s_lshl_b32 s22, s13, 2
s_add_u32 s21, s21, s22
v_mov_b32 v247, s21
v_mov_b32 v246, s79
s_mov_b64 exec, 1
global_store_dword v247, v246, s[14:15] sc1
s_mov_b64 exec, -1
N 3
Mvvvc 0,160,208
Rv 192,244,0
Mvvvc 4,160,212
s_add_u32 s55, s52, 0x480000
M0 114688
Mvvvc 8,160,216
Rv 196,244,1024
D 236,44,55,0
Mvvvc 12,160,220
Rv 200,244,2048
Mvvvc 28,164,220
D 237,44,55,1024
Mvvvc 24,164,216
Rv 204,244,3072
Mvvvc 20,164,212
D 238,44,55,2048
Mvvvc 16,164,208
T 128,240,32768
Mvvvc 32,168,208
T 130,240,33792
Mvvvc 36,168,212
X 242,32,240
T 132,242,32768
D 239,44,55,3072
Mvvvc 40,168,216
T 134,242,33792
s_add_u32 s55, s52, 0x4c0000
M0 122880
Mvvvc 44,168,220
D 236,44,55,0
Mvvvc 60,172,220
X 241,64,240
T 136,241,32768
Mvvvc 56,172,216
T 138,241,33792
D 237,44,55,1024
Mvvvc 52,172,212
X 242,96,240
Mvvvc 48,172,208
T 140,242,32768
T 142,242,33792
Mvvvc 64,176,208
X 241,128,240
D 238,44,55,2048
Mvvvc 68,176,212
T 144,241,32768
T 146,241,33792
Mvvvc 72,176,216
X 242,160,240
T 148,242,32768
D 239,44,55,3072
Mvvvc 76,176,220
T 150,242,33792
AS 55,53,147456
M0 65536
Mvvvc 92,180,220
X 241,192,240
Ds 243,48,55,0
Mvvvc 88,180,216
T 152,241,32768
Mvvvc 84,180,212
T 154,241,33792
X 242,224,240
Mvvvc 80,180,208
T 156,242,32768
Ds 243,48,55,1024
Mvvvc 96,184,208
T 158,242,33792
Mvvvc 100,184,212
Ds 243,48,55,2048
Mvvvc 104,184,216
Mvvvc 108,184,220
Ds 243,48,55,3072
Mvvvc 124,188,220
Mvvvc 120,188,216
Mvvvc 116,188,212
Mvvvc 112,188,208
W 0
s_lshl_b32 s21, s12, 8
AS 21,21,4096
v_mov_b32 v247, s21
v_min_u32 v246, 7, v254
v_lshl_add_u32 v247, v246, 2, v247
global_load_dword v246, v247, s[14:15] sc1
Mvvvc 0,128,192
Rv 208,244,4096
Mvvvc 4,128,196
AS 55,54,147456
M0 81920
Mvvvc 8,128,200
Rv 212,244,5120
Ds 243,48,55,0
Mvvvc 12,128,204
Rv 216,244,6144
Mvvvc 28,132,204
Ds 243,48,55,1024
Mvvvc 24,132,200
Rv 220,244,7168
Mvvvc 20,132,196
Ds 243,48,55,2048
Mvvvc 16,132,192
T 160,240,40960
Mvvvc 32,136,192
T 162,240,41984
Mvvvc 36,136,196
X 242,32,240
T 164,242,40960
Ds 243,48,55,3072
Mvvvc 40,136,200
T 166,242,41984
AS 55,53,155648
M0 69632
Mvvvc 44,136,204
Ds 243,48,55,0
Mvvvc 60,140,204
X 241,64,240
T 168,241,40960
Mvvvc 56,140,200
T 170,241,41984
Ds 243,48,55,1024
Mvvvc 52,140,196
X 242,96,240
Mvvvc 48,140,192
T 172,242,40960
T 174,242,41984
Mvvvc 64,144,192
X 241,128,240
Ds 243,48,55,2048
Mvvvc 68,144,196
T 176,241,40960
T 178,241,41984
Mvvvc 72,144,200
X 242,160,240
T 180,242,40960
Ds 243,48,55,3072
Mvvvc 76,144,204
T 182,242,41984
AS 55,54,155648
M0 86016
Mvvvc 92,148,204
X 241,192,240
Ds 243,48,55,0
Mvvvc 88,148,200
T 184,241,40960
Mvvvc 84,148,196
T 186,241,41984
X 242,224,240
Mvvvc 80,148,192
T 188,242,40960
Ds 243,48,55,1024
Mvvvc 96,152,192
T 190,242,41984
Mvvvc 100,152,196
Ds 243,48,55,2048
Mvvvc 104,152,200
Mvvvc 108,152,204
Ds 243,48,55,3072
Mvvvc 124,156,204
Mvvvc 120,156,200
Mvvvc 116,156,196
Mvvvc 112,156,192
WV 12
B
v_cmp_ne_u32 vcc, s79, v246
N 4
s_cbranch_vccnz KP0_SPIN%=
KP0_OK%=:
Mvvvc 0,160,208
Rv 192,244,32768
Mvvvc 4,160,212
Rv 196,244,33792
Mvvvc 8,160,216
s_lshl_b32 s21, s12, 19
AS 21,21,0
M0 0
Mvvvc 12,160,220
Rv 200,244,34816
Ds 243,24,21,0
Mvvvc 28,164,220
Mvvvc 24,164,216
Rv 204,244,35840
Mvvvc 20,164,212
Ds 243,24,21,1024
Mvvvc 16,164,208
T 128,240,0
Mvvvc 32,168,208
T 130,240,1024
Mvvvc 36,168,212
X 242,32,240
Ds 243,24,21,2048
Mvvvc 40,168,216
T 132,242,0
Mvvvc 44,168,220
T 134,242,1024
Mvvvc 60,172,220
X 241,64,240
Mvvvc 56,172,216
T 136,241,0
Ds 243,24,21,3072
Mvvvc 52,172,212
T 138,241,1024
Mvvvc 48,172,208
X 242,96,240
s_lshl_b32 s21, s12, 19
AS 21,21,4096
M0 4096
Mvvvc 64,176,208
T 140,242,0
Ds 243,24,21,0
Mvvvc 68,176,212
T 142,242,1024
Mvvvc 72,176,216
X 241,128,240
Mvvvc 76,176,220
T 144,241,0
Ds 243,24,21,1024
Mvvvc 92,180,220
T 146,241,1024
Mvvvc 88,180,216
X 242,160,240
Mvvvc 84,180,212
T 148,242,0
Mvvvc 80,180,208
T 150,242,1024
Ds 243,24,21,2048
Mvvvc 96,184,208
X 241,192,240
Mvvvc 100,184,212
T 152,241,0
Mvvvc 104,184,216
T 154,241,1024
Ds 243,24,21,3072
Mvvvc 108,184,220
X 242,224,240
Mvvvc 124,188,220
T 156,242,0
Mvvvc 120,188,216
T 158,242,1024
Mvvvc 116,188,212
Mvvvc 112,188,208
W 0
Mvvvc 0,128,192
Rv 208,244,36864
Mvvvc 4,128,196
Rv 212,244,37888
Mvvvc 8,128,200
s_lshl_b32 s21, s12, 19
AS 21,21,16384
M0 16384
Mvvvc 12,128,204
Rv 216,244,38912
Ds 243,24,21,0
Mvvvc 28,132,204
Mvvvc 24,132,200
Rv 220,244,39936
Mvvvc 20,132,196
Ds 243,24,21,1024
Mvvvc 16,132,192
T 160,240,8192
Mvvvc 32,136,192
T 162,240,9216
Mvvvc 36,136,196
X 242,32,240
Ds 243,24,21,2048
Mvvvc 40,136,200
T 164,242,8192
Mvvvc 44,136,204
T 166,242,9216
Mvvvc 60,140,204
X 241,64,240
Mvvvc 56,140,200
T 168,241,8192
Ds 243,24,21,3072
Mvvvc 52,140,196
T 170,241,9216
Mvvvc 48,140,192
X 242,96,240
s_lshl_b32 s21, s12, 19
AS 21,21,20480
M0 20480
Mvvvc 64,144,192
T 172,242,8192
Ds 243,24,21,0
Mvvvc 68,144,196
T 174,242,9216
Mvvvc 72,144,200
X 241,128,240
Mvvvc 76,144,204
T 176,241,8192
Ds 243,24,21,1024
Mvvvc 92,148,204
T 178,241,9216
Mvvvc 88,148,200
X 242,160,240
Mvvvc 84,148,196
T 180,242,8192
Mvvvc 80,148,192
T 182,242,9216
Ds 243,24,21,2048
Mvvvc 96,152,192
X 241,192,240
Mvvvc 100,152,196
T 184,241,8192
Mvvvc 104,152,200
T 186,241,9216
Ds 243,24,21,3072
Mvvvc 108,152,204
X 242,224,240
Mvvvc 124,156,204
T 188,242,8192
Mvvvc 120,156,200
T 190,242,9216
Mvvvc 116,156,196
Mvvvc 112,156,192
WV 16
B
Mvvvc 0,160,208
Rv 192,245,0
Mvvvc 4,160,212
Rv 196,245,1024
Mvvvc 8,160,216
Mvvvc 12,160,220
Rv 200,245,2048
Mvvvc 28,164,220
Mvvvc 24,164,216
Rv 204,245,3072
Mvvvc 20,164,212
Mvvvc 16,164,208
T 128,240,16384
Mvvvc 32,168,208
T 130,240,17408
Mvvvc 36,168,212
X 242,32,240
Mvvvc 40,168,216
T 132,242,16384
Mvvvc 44,168,220
T 134,242,17408
Mvvvc 60,172,220
X 241,64,240
Mvvvc 56,172,216
T 136,241,16384
Mvvvc 52,172,212
T 138,241,17408
Mvvvc 48,172,208
X 242,96,240
Mvvvc 64,176,208
T 140,242,16384
Mvvvc 68,176,212
T 142,242,17408
Mvvvc 72,176,216
X 241,128,240
Mvvvc 76,176,220
T 144,241,16384
Mvvvc 92,180,220
T 146,241,17408
Mvvvc 88,180,216
X 242,160,240
Mvvvc 84,180,212
T 148,242,16384
Mvvvc 80,180,208
T 150,242,17408
Mvvvc 96,184,208
X 241,192,240
Mvvvc 100,184,212
T 152,241,16384
Mvvvc 104,184,216
T 154,241,17408
Mvvvc 108,184,220
X 242,224,240
Mvvvc 124,188,220
T 156,242,16384
Mvvvc 120,188,216
T 158,242,17408
Mvvvc 116,188,212
Mvvvc 112,188,208
W 0
Mvvvc 0,128,192
Rv 208,245,4096
Mvvvc 4,128,196
Rv 212,245,5120
Mvvvc 8,128,200
Mvvvc 12,128,204
Rv 216,245,6144
Mvvvc 28,132,204
Mvvvc 24,132,200
Rv 220,245,7168
Mvvvc 20,132,196
Mvvvc 16,132,192
T 160,240,24576
Mvvvc 32,136,192
T 162,240,25600
Mvvvc 36,136,196
X 242,32,240
Mvvvc 40,136,200
T 164,242,24576
Mvvvc 44,136,204
T 166,242,25600
Mvvvc 60,140,204
X 241,64,240
Mvvvc 56,140,200
T 168,241,24576
Mvvvc 52,140,196
T 170,241,25600
Mvvvc 48,140,192
X 242,96,240
Mvvvc 64,144,192
T 172,242,24576
Mvvvc 68,144,196
T 174,242,25600
Mvvvc 72,144,200
X 241,128,240
Mvvvc 76,144,204
T 176,241,24576
Mvvvc 92,148,204
T 178,241,25600
Mvvvc 88,148,200
X 242,160,240
Mvvvc 84,148,196
T 180,242,24576
Mvvvc 80,148,192
T 182,242,25600
Mvvvc 96,152,192
X 241,192,240
Mvvvc 100,152,196
T 184,241,24576
Mvvvc 104,152,200
T 186,241,25600
Mvvvc 108,152,204
X 242,224,240
Mvvvc 124,156,204
T 188,242,24576
Mvvvc 120,156,200
T 190,242,25600
Mvvvc 116,156,196
Mvvvc 112,156,192
W 0
B
Mvvvc 0,160,208
Mvvvc 4,160,212
Mvvvc 8,160,216
Mvvvc 12,160,220
Mvvvc 28,164,220
Mvvvc 24,164,216
Mvvvc 20,164,212
Mvvvc 16,164,208
Mvvvc 32,168,208
Mvvvc 36,168,212
Mvvvc 40,168,216
Mvvvc 44,168,220
Mvvvc 60,172,220
Mvvvc 56,172,216
Mvvvc 52,172,212
Mvvvc 48,172,208
Mvvvc 64,176,208
Mvvvc 68,176,212
Mvvvc 72,176,216
Mvvvc 76,176,220
Mvvvc 92,180,220
Mvvvc 88,180,216
Mvvvc 84,180,212
Mvvvc 80,180,208
Mvvvc 96,184,208
Mvvvc 100,184,212
Mvvvc 104,184,216
Mvvvc 108,184,220
Mvvvc 124,188,220
Mvvvc 120,188,216
Mvvvc 116,188,212
Mvvvc 112,188,208
QE0%=:
WV 0
B
VA 224,98304,243
C 128,0,1
C 129,2,3
C 130,16,17
C 131,18,19
DWv 224,128,0
C 132,32,33
C 133,34,35
C 134,48,49
C 135,50,51
DWv 224,132,1024
C 136,64,65
C 137,66,67
C 138,80,81
C 139,82,83
DWv 224,136,2048
C 140,96,97
C 141,98,99
C 142,112,113
C 143,114,115
DWv 224,140,3072
C 144,4,5
C 145,6,7
C 146,20,21
C 147,22,23
DWv 224,144,4096
C 148,36,37
C 149,38,39
C 150,52,53
C 151,54,55
DWv 224,148,5120
C 152,68,69
C 153,70,71
C 154,84,85
C 155,86,87
DWv 224,152,6144
C 156,100,101
C 157,102,103
C 158,116,117
C 159,118,119
DWv 224,156,7168
C 128,8,9
C 129,10,11
C 130,24,25
C 131,26,27
DWv 224,128,8192
C 132,40,41
C 133,42,43
C 134,56,57
C 135,58,59
DWv 224,132,9216
C 136,72,73
C 137,74,75
C 138,88,89
C 139,90,91
DWv 224,136,10240
C 140,104,105
C 141,106,107
C 142,120,121
C 143,122,123
DWv 224,140,11264
C 144,12,13
C 145,14,15
C 146,28,29
C 147,30,31
DWv 224,144,12288
C 148,44,45
C 149,46,47
C 150,60,61
C 151,62,63
DWv 224,148,13312
C 152,76,77
C 153,78,79
C 154,92,93
C 155,94,95
DWv 224,152,14336
C 156,108,109
C 157,110,111
C 158,124,125
C 159,126,127
DWv 224,156,15360
W 0
B
Ra 128,224,0
Ra 132,224,1024
Ra 136,224,2048
Ra 140,224,3072
Ra 144,224,4096
Ra 148,224,5120
Ra 152,224,6144
Ra 156,224,7168
Ra 160,224,8192
Ra 164,224,9216
Ra 168,224,10240
Ra 172,224,11264
Ra 176,224,12288
Ra 180,224,13312
Ra 184,224,14336
Ra 188,224,15360
s_add_u32 s32, s14, 0x2400000
s_addc_u32 s33, s15, 0
SM 34,0x1000000
SM 35,0x20000
v_lshlrev_b32 v196, 4, v254
VA 197,0,196
VA 198,65536,196
v_mov_b32 v199, 0xff800000
v_mov_b32 v192, 0x3f803f80
v_mov_b32 v193, 0x3f803f80
v_mov_b32 v194, 0x3f803f80
v_mov_b32 v195, 0x3f803f80
v_and_b32 v200, 15, v254
v_lshrrev_b32 v201, 4, v254
v_lshlrev_b32 v201, 2, v201
VA 202,0,201
v_cmp_le_u32 s[62:63], v202, v200
VA 202,1,201
v_cmp_le_u32 s[64:65], v202, v200
VA 202,2,201
v_cmp_le_u32 s[66:67], v202, v200
VA 202,3,201
v_cmp_le_u32 s[68:69], v202, v200
s_lshl_b32 s61, s13, 1
AS 61,61,2
SM 60,0
s_mov_b64 s[70:71], 0
s_mov_b64 s[50:51], s[42:43]
VA 237,0,254
v_mul_u32_u24 v238, 3277, v237
v_lshrrev_b32 v238, 16, v238
v_mul_u32_u24 v239, 20, v238
v_sub_u32 v239, v237, v239
v_lshrrev_b32 v240, 1, v239
v_lshrrev_b32 v241, 2, v238
v_and_b32 v241, 1, v241
v_xor_b32 v240, v240, v241
v_and_b32 v239, 1, v239
v_lshlrev_b32 v239, 4, v239
v_lshl_add_u32 v239, v240, 5, v239
)A"
R"A(v_mul_u32_u24 v238, 1280, v238
v_add_u32 v238, v238, v239
MV 232,238
VA 237,64,254
v_mul_u32_u24 v238, 3277, v237
v_lshrrev_b32 v238, 16, v238
v_mul_u32_u24 v239, 20, v238
v_sub_u32 v239, v237, v239
v_lshrrev_b32 v240, 1, v239
v_lshrrev_b32 v241, 2, v238
v_and_b32 v241, 1, v241
v_xor_b32 v240, v240, v241
v_and_b32 v239, 1, v239
v_lshlrev_b32 v239, 4, v239
v_lshl_add_u32 v239, v240, 5, v239
v_mul_u32_u24 v238, 1280, v238
v_add_u32 v238, v238, v239
v_subrev_u32 v233, 1024, v238
VA 237,128,254
v_mul_u32_u24 v238, 3277, v237
v_lshrrev_b32 v238, 16, v238
v_mul_u32_u24 v239, 20, v238
v_sub_u32 v239, v237, v239
v_lshrrev_b32 v240, 1, v239
v_lshrrev_b32 v241, 2, v238
v_and_b32 v241, 1, v241
v_xor_b32 v240, v240, v241
v_and_b32 v239, 1, v239
v_lshlrev_b32 v239, 4, v239
v_lshl_add_u32 v239, v240, 5, v239
v_mul_u32_u24 v238, 1280, v238
v_add_u32 v238, v238, v239
v_subrev_u32 v234, 2048, v238
VA 237,192,254
v_mul_u32_u24 v238, 3277, v237
v_lshrrev_b32 v238, 16, v238
v_mul_u32_u24 v239, 20, v238
v_sub_u32 v239, v237, v239
v_lshrrev_b32 v240, 1, v239
v_lshrrev_b32 v241, 2, v238
v_and_b32 v241, 1, v241
v_xor_b32 v240, v240, v241
v_and_b32 v239, 1, v239
v_lshlrev_b32 v239, 4, v239
v_lshl_add_u32 v239, v240, 5, v239
v_mul_u32_u24 v238, 1280, v238
v_add_u32 v238, v238, v239
v_subrev_u32 v235, 3072, v238
VA 237,256,254
v_mul_u32_u24 v238, 3277, v237
v_lshrrev_b32 v238, 16, v238
v_mul_u32_u24 v239, 20, v238
v_sub_u32 v239, v237, v239
v_lshrrev_b32 v240, 1, v239
v_lshrrev_b32 v241, 2, v238
v_and_b32 v241, 1, v241
v_xor_b32 v240, v240, v241
v_and_b32 v239, 1, v239
v_lshlrev_b32 v239, 4, v239
v_lshl_add_u32 v239, v240, 5, v239
v_mul_u32_u24 v238, 1280, v238
v_add_u32 v238, v238, v239
MV 236,238
s_lshl_b32 s21, s12, 8
AS 21,21,20480
v_mov_b32 v247, s21
v_min_u32 v246, 7, v254
v_lshl_add_u32 v247, v246, 2, v247
global_load_dword v246, v247, s[14:15] sc1
s_waitcnt vmcnt(0)
v_cmp_ne_u32 vcc, s79, v246
N 4
s_cbranch_vccnz VP_SPIN%=
VP_OK%=:
B
AM 59,0
N 0
Ds 196,28,57,0
Ds 196,28,57,1024
Ds 196,28,57,2048
Ds 196,28,57,3072
AS 57,57,16384
s_cmp_eq_u32 s13, 0
s_cbranch_scc0 AT0_M1%=
SM 73,0x4000
SM 74,0x0
SM 75,0x0
SM 76,0x0
s_branch AT0_MD%=
AT0_M1%=:
s_cmp_eq_u32 s13, 1
s_cbranch_scc0 AT0_M2%=
SM 73,0x4000
SM 74,0x2
SM 75,0x0
SM 76,0x0
s_branch AT0_MD%=
AT0_M2%=:
s_cmp_eq_u32 s13, 2
s_cbranch_scc0 AT0_M3%=
SM 73,0x4000
SM 74,0x0
SM 75,0x0
SM 76,0x8
s_branch AT0_MD%=
AT0_M3%=:
s_cmp_eq_u32 s13, 3
s_cbranch_scc0 AT0_M4%=
SM 73,0x4000
SM 74,0x0
SM 75,0x0
SM 76,0x20
s_branch AT0_MD%=
AT0_M4%=:
s_cmp_eq_u32 s13, 4
s_cbranch_scc0 AT0_M5%=
SM 73,0x4000
SM 74,0x0
SM 75,0x0
SM 76,0x80
s_branch AT0_MD%=
AT0_M5%=:
s_cmp_eq_u32 s13, 5
s_cbranch_scc0 AT0_M6%=
SM 73,0x4000
SM 74,0x0
SM 75,0x0
SM 76,0x200
s_branch AT0_MD%=
AT0_M6%=:
s_cmp_eq_u32 s13, 6
s_cbranch_scc0 AT0_M7%=
SM 73,0x4000
SM 74,0x0
SM 75,0x0
SM 76,0x800
s_branch AT0_MD%=
AT0_M7%=:
SM 73,0x0
SM 74,0x0
SM 75,0x4000
SM 76,0x2000
AT0_MD%=:
WV 4
B
Rv 144,197,0
Rv 148,197,1024
Rv 152,197,2048
Rv 156,197,3072
Rv 160,197,4096
Rv 164,197,5120
Rv 168,197,6144
W 5
s_cmp_eq_u32 s13, 0
s_cbranch_scc1 AT0_B0%=
s_branch AT0_B7%=
XAT0_B0%=:
AS 60,60,1
AT0_B0%=:
Mvvaz 0,144,128
Mvvaz 8,144,144
Mvvaz 16,144,160
Rv 172,197,7168
Mvvaz 24,144,176
Mvvac 24,148,180
Mvvac 16,148,164
Rv 144,197,8192
Mvvac 8,148,148
Mvvac 0,148,132
W 5
Mvvac 0,152,136
Mvvac 8,152,152
Rv 148,197,9216
Mvvac 16,152,168
Mvvac 24,152,184
Mvvac 24,156,188
Mvvac 16,156,172
Rv 152,197,10240
Mvvac 8,156,156
Mvvac 0,156,140
W 5
Mvvaz 4,160,128
Mvvaz 12,160,144
Rv 156,197,11264
Mvvaz 20,160,160
N 2
v_max3_f32 v200, v0, v1, v2
Fma 200,200,3
v_max3_f32 v202, v8, v9, v10
Fma 202,202,11
Mvvaz 28,160,176
v_max3_f32 v204, v16, v17, v18
Fma 204,204,19
v_max3_f32 v206, v24, v25, v26
Fma 206,206,27
Mvvac 28,164,180
MV 201,200
MV 203,202
MV 205,204
MV 207,206
Mvvac 20,164,164
Rv 160,197,12288
v_permlane16_swap_b32 v200, v201
v_permlane16_swap_b32 v202, v203
v_permlane16_swap_b32 v204, v205
Mvvac 12,164,148
v_permlane16_swap_b32 v206, v207
Fma 200,200,201
Fma 202,202,203
Fma 204,204,205
Mvvac 4,164,132
Fma 206,206,207
MV 201,200
MV 203,202
MV 205,204
W 5
Mvvac 4,168,136
MV 207,206
v_permlane32_swap_b32 v200, v201
v_permlane32_swap_b32 v202, v203
Mvvac 12,168,152
v_permlane32_swap_b32 v204, v205
v_permlane32_swap_b32 v206, v207
Fma 200,200,201
Fma 202,202,203
Mvvac 20,168,168
Rv 164,197,13312
Fma 204,204,205
Fma 206,206,207
v_sub_f32 v128, 0, v200
v_sub_f32 v129, 0, v200
Mvvac 28,168,184
v_sub_f32 v130, 0, v200
v_sub_f32 v131, 0, v200
v_sub_f32 v132, 0, v202
Mvvac 28,172,188
v_sub_f32 v133, 0, v202
v_sub_f32 v134, 0, v202
v_sub_f32 v135, 0, v202
v_sub_f32 v136, 0, v204
Mvvac 20,172,172
v_sub_f32 v137, 0, v204
v_sub_f32 v138, 0, v204
v_sub_f32 v139, 0, v204
v_sub_f32 v140, 0, v206
Mvvac 12,172,156
v_sub_f32 v141, 0, v206
v_sub_f32 v142, 0, v206
v_sub_f32 v143, 0, v206
Mvvac 4,172,140
Rv 168,197,14336
W 5
Mvvan 32,144,128,128
Mvvan 40,144,144,132
Mvvan 48,144,160,136
Mvvan 56,144,176,140
Mvvac 56,148,180
Rv 172,197,15360
Mvvac 48,148,164
Fad 0,0,128
Mvvac 40,148,148
E 0
Mvvac 32,148,132
Fad 1,1,128
W 4
Mvvac 32,152,136
E 1
Mvvac 40,152,152
Fad 2,2,128
Mvvac 48,152,168
E 2
Mvvac 56,152,184
C 0,0,1
Fad 3,3,128
Mvvac 56,156,188
E 3
AS 56,56,0
SM 58,0
Mvvac 48,156,172
Fad 4,4,128
W 0
B
Mvvac 40,156,156
E 4
AM 58,0
s_mul_i32 s21, s10, 160
Mvvac 32,156,140
Fad 5,5,128
Ds 196,24,56,0
W 15
AS 21,21,0
Mvvan 36,160,128,128
E 5
v_min_u32 v249, 63, v254
Mvvan 44,160,144,132
C 1,2,3
Fad 6,6,128
v_add_u32 v249, s21, v249
Mvvan 52,160,160,136
E 6
v_min_u32 v249, 40959, v249
Mvvan 60,160,176,140
C 2,4,5
Fad 7,7,128
v_lshlrev_b32 v249, 7, v249
Mvvac 60,164,180
E 7
global_load_dword v248, v249, s[36:37]
Mvvac 52,164,164
Fad 8,8,132
s_mul_i32 s21, s10, 160
Mvvac 44,164,148
E 8
Ds 196,24,56,1024
AS 21,21,64
Mvvac 36,164,132
Fad 9,9,132
W 15
v_min_u32 v249, 63, v254
Mvvac 36,168,136
E 9
v_add_u32 v249, s21, v249
Mvvac 44,168,152
C 3,6,7
Fad 10,10,132
v_min_u32 v249, 40959, v249
Mvvac 52,168,168
E 10
v_lshlrev_b32 v249, 7, v249
Mvvac 60,168,184
C 8,8,9
Fad 11,11,132
global_load_dword v248, v249, s[36:37]
Mvvac 60,172,188
E 11
s_mul_i32 s21, s10, 160
Mvvac 52,172,172
Fad 12,12,132
Ds 196,24,56,2048
AS 21,21,128
Mvvac 44,172,156
E 12
v_min_u32 v249, 31, v254
Mvvac 36,172,140
Fad 13,13,132
v_add_u32 v249, s21, v249
E 13
v_min_u32 v249, 40959, v249
C 9,10,11
Fad 14,14,132
v_lshlrev_b32 v249, 7, v249
E 14
global_load_dword v248, v249, s[36:37]
C 10,12,13
Fad 15,15,132
E 15
Ds 196,24,56,3072
Fad 16,16,136
E 16
AS 56,56,4096
Fad 17,17,136
E 17
C 11,14,15
Fad 18,18,136
E 18
C 16,16,17
Fad 19,19,136
E 19
Fad 20,20,136
E 20
AM 58,4096
Fad 21,21,136
Ds 196,24,56,0
E 21
C 17,18,19
Fad 22,22,136
E 22
C 18,20,21
Fad 23,23,136
E 23
Fad 24,24,140
E 24
Ds 196,24,56,1024
Fad 25,25,140
E 25
C 19,22,23
Fad 26,26,140
E 26
C 24,24,25
Fad 27,27,140
E 27
Fad 28,28,140
Ds 196,24,56,2048
E 28
Fad 29,29,140
E 29
C 25,26,27
Fad 30,30,140
E 30
Fad 31,31,140
E 31
Ds 196,24,56,3072
C 26,28,29
E 32
AS 56,56,28672
E 33
C 27,30,31
E 34
C 32,32,33
E 35
W 0
B
Rv 144,197,16384
E 36
AM 59,16384
N 0
Ds 196,28,57,0
Rv 148,197,17408
E 37
C 33,34,35
E 38
Rv 152,197,18432
C 34,36,37
Ds 196,28,57,1024
E 39
Rv 156,197,19456
E 40
Ds 196,28,57,2048
E 41
Rv 160,197,20480
C 35,38,39
E 42
C 40,40,41
Ds 196,28,57,3072
Rv 164,197,21504
E 43
AS 57,57,16384
E 44
Rv 168,197,22528
W 5
XAT0_B1%=:
AS 60,60,1
AT0_B1%=:
Mvvan 64,144,128,128
E 45
Mvvan 72,144,144,132
C 41,42,43
Mvvan 80,144,160,136
Rv 172,197,23552
E 46
Mvvan 88,144,176,140
C 42,44,45
Mvvac 88,148,180
E 47
Mvvac 80,148,164
Rv 144,197,24576
Mvvac 72,148,148
E 48
Mvvac 64,148,132
W 5
Mvvac 64,152,136
E 49
Mvvac 72,152,152
Rv 148,197,25600
C 43,46,47
Mvvac 80,152,168
E 50
Mvvac 88,152,184
C 48,48,49
Mvvac 88,156,188
E 51
Mvvac 80,156,172
Rv 152,197,26624
Mvvac 72,156,156
E 52
Mvvac 64,156,140
W 5
Mvvan 68,160,128,128
E 53
Mvvan 76,160,144,132
Rv 156,197,27648
C 49,50,51
Mvvan 84,160,160,136
E 54
Mvvan 92,160,176,140
C 50,52,53
Mvvac 92,164,180
E 55
Mvvac 84,164,164
Rv 160,197,28672
Mvvac 76,164,148
E 56
Mvvac 68,164,132
W 5
Mvvac 68,168,136
E 57
Mvvac 76,168,152
C 51,54,55
Mvvac 84,168,168
Rv 164,197,29696
E 58
Mvvac 92,168,184
C 56,56,57
Mvvac 92,172,188
E 59
Mvvac 84,172,172
Mvvac 76,172,156
E 60
Mvvac 68,172,140
Rv 168,197,30720
W 5
Mvvan 96,144,128,128
E 61
Mvvan 104,144,144,132
C 57,58,59
Mvvan 112,144,160,136
E 62
Mvvan 120,144,176,140
C 58,60,61
Mvvac 120,148,180
Rv 172,197,31744
E 63
Mvvac 112,148,164
Mvvac 104,148,148
E 64
Mvvac 96,148,132
W 4
Mvvac 96,152,136
E 65
Mvvac 104,152,152
C 59,62,63
Mvvac 112,152,168
E 66
Mvvac 120,152,184
C 64,64,65
Mvvac 120,156,188
E 67
Mvvac 112,156,172
WV 15
B
Mvvac 104,156,156
Rv 144,198,0
E 68
AM 58,16384
s_mul_i32 s21, s10, 68
Mvvac 96,156,140
Ds 196,24,56,0
W 15
AS 21,21,0
Mvvan 100,160,128,128
Rv 148,198,1024
E 69
v_min_u32 v249, 63, v254
Mvvan 108,160,144,132
C 65,66,67
v_add_u32 v249, s21, v249
Mvvan 116,160,160,136
E 70
v_min_u32 v249, 17279, v249
Mvvan 124,160,176,140
Rv 152,198,2048
C 66,68,69
v_lshlrev_b32 v249, 7, v249
Mvvac 124,164,180
E 71
global_load_dword v248, v249, s[38:39]
Mvvac 116,164,164
s_mul_i32 s21, s10, 68
Mvvac 108,164,148
Rv 156,198,3072
E 72
Ds 196,24,56,1024
AS 21,21,64
Mvvac 100,164,132
W 15
v_min_u32 v249, 3, v254
Mvvac 100,168,136
E 73
v_add_u32 v249, s21, v249
Mvvac 108,168,152
Rv 160,198,4096
C 67,70,71
v_min_u32 v249, 17279, v249
Mvvac 116,168,168
E 74
v_lshlrev_b32 v249, 7, v249
Mvvac 124,168,184
C 72,72,73
global_load_dword v248, v249, s[38:39]
Mvvac 124,172,188
Rv 164,198,5120
E 75
Mvvac 116,172,172
Ds 196,24,56,2048
Mvvac 108,172,156
E 76
Mvvac 100,172,140
Rv 168,198,6144
W 5
Mavvz 0,144,0
E 77
Mavvz 4,144,8
C 73,74,75
Mavvz 8,144,16
Rv 172,198,7168
E 78
Mavvz 12,144,24
C 74,76,77
Mavvz 28,148,24
E 79
Ds 196,24,56,3072
Mavvz 24,148,16
Rv 144,198,8192
Mvvvz 176,192,0
Mavvz 20,148,8
E 80
AS 56,56,4096
Mavvz 16,148,0
W 5
Mavvz 32,152,0
E 81
Mavvz 36,152,8
Rv 148,198,9216
C 75,78,79
Mavvz 40,152,16
E 82
Mavvz 44,152,24
C 80,80,81
Mavvz 60,156,24
E 83
Mavvz 56,156,16
Rv 152,198,10240
Mvvvz 180,192,8
Mavvz 52,156,8
E 84
AM 58,20480
Mavvz 48,156,0
Ds 196,24,56,0
W 5
Mavvz 64,160,0
E 85
Mavvz 68,160,8
Rv 156,198,11264
Mvvvz 184,192,16
C 81,82,83
Mavvz 72,160,16
E 86
Mavvz 76,160,24
C 82,84,85
Mavvz 92,164,24
E 87
Mavvz 88,164,16
Rv 160,198,12288
Mavvz 84,164,8
E 88
Ds 196,24,56,1024
Mavvz 80,164,0
Mvvvz 188,192,24
W 5
Mavvz 96,168,0
E 89
Mavvz 100,168,8
C 83,86,87
Mavvz 104,168,16
Rv 164,198,13312
E 90
Mavvz 108,168,24
C 88,88,89
Mavvz 124,172,24
E 91
Mavvz 120,172,16
Ds 196,24,56,2048
Mavvz 116,172,8
E 92
Mavvz 112,172,0
Rv 168,198,14336
W 5
Mavvc 0,144,32
E 93
Mavvc 4,144,40
C 89,90,91
Mavvc 8,144,48
E 94
Mavvc 12,144,56
C 90,92,93
Mavvc 28,148,56
Rv 172,198,15360
E 95
Ds 196,24,56,3072
Mavvc 24,148,48
Mvvvc 176,192,32
Mavvc 20,148,40
E 96
AS 56,56,28672
Mavvc 16,148,32
W 4
Mavvc 32,152,32
E 97
Mavvc 36,152,40
Mvvvc 180,192,40
C 91,94,95
Mavvc 40,152,48
E 98
Mavvc 44,152,56
C 96,96,97
Mavvc 60,156,56
E 99
Mavvc 56,156,48
WV 14
B
AS 57,57,0
SM 59,65536
Mavvc 52,156,40
Rv 144,197,0
E 100
AM 59,0
Mavvc 48,156,32
Ds 196,28,57,0
W 15
Mavvc 64,160,32
Rv 148,197,1024
E 101
Mavvc 68,160,40
Mvvvc 184,192,48
C 97,98,99
Mavvc 72,160,48
E 102
Mavvc 76,160,56
Rv 152,197,2048
C 98,100,101
Ds 196,28,57,1024
Mavvc 92,164,56
E 103
Mavvc 88,164,48
Mavvc 84,164,40
Rv 156,197,3072
E 104
Mavvc 80,164,32
Mvvvc 188,192,56
Ds 196,28,57,2048
W 15
Mavvc 96,168,32
E 105
Mavvc 100,168,40
Rv 160,197,4096
C 99,102,103
Mavvc 104,168,48
E 106
Mavvc 108,168,56
C 104,104,105
Ds 196,28,57,3072
Mavvc 124,172,56
Rv 164,197,5120
E 107
Mavvc 120,172,48
Mavvc 116,172,40
E 108
Mavvc 112,172,32
Rv 168,197,6144
W 5
XAT0_B2%=:
AS 60,60,1
AT0_B2%=:
Mvvan 0,144,128,128
AS 57,57,4096
E 109
Mvvan 8,144,144,132
C 105,106,107
AM 59,4096
Mvvan 16,144,160,136
Rv 172,197,7168
E 110
Ds 196,28,57,0
Mvvan 24,144,176,140
C 106,108,109
Mvvac 24,148,180
E 111
Mvvac 16,148,164
Rv 144,197,8192
Mvvac 8,148,148
E 112
Mvvac 0,148,132
W 5
Mvvac 0,152,136
E 113
Mvvac 8,152,152
Rv 148,197,9216
C 107,110,111
Mvvac 16,152,168
)A"
R"A(E 114
Mvvac 24,152,184
C 112,112,113
Ds 196,28,57,1024
Mvvac 24,156,188
E 115
Mvvac 16,156,172
Rv 152,197,10240
Mvvac 8,156,156
E 116
Mvvac 0,156,140
W 5
Mvvan 4,160,128,128
E 117
Mvvan 12,160,144,132
Rv 156,197,11264
C 113,114,115
Mvvan 20,160,160,136
E 118
Mvvan 28,160,176,140
C 114,116,117
Mvvac 28,164,180
E 119
Ds 196,28,57,2048
Mvvac 20,164,164
Rv 160,197,12288
Mvvac 12,164,148
E 120
Mvvac 4,164,132
W 5
Mvvac 4,168,136
E 121
Mvvac 12,168,152
C 115,118,119
Mvvac 20,168,168
Rv 164,197,13312
E 122
Mvvac 28,168,184
C 120,120,121
Mvvac 28,172,188
E 123
Mvvac 20,172,172
Ds 196,28,57,3072
Mvvac 12,172,156
E 124
Mvvac 4,172,140
Rv 168,197,14336
W 5
AS 57,57,28672
Mvvan 32,144,128,128
E 125
Mvvan 40,144,144,132
C 121,122,123
Mvvan 48,144,160,136
E 126
Mvvan 56,144,176,140
C 122,124,125
Mvvac 56,148,180
Rv 172,197,15360
E 127
Mvvac 48,148,164
Mvvac 40,148,148
E 0
Mvvac 32,148,132
W 4
Mvvac 32,152,136
E 1
Mvvac 40,152,152
C 123,126,127
Mvvac 48,152,168
E 2
Mvvac 56,152,184
C 0,0,1
Mvvac 56,156,188
E 3
Mvvac 48,156,172
WV 16
B
Mvvac 40,156,156
Rv 144,198,16384
E 4
AM 58,0
Mvvac 32,156,140
Ds 196,24,56,0
W 15
Mvvan 36,160,128,128
Rv 148,198,17408
E 5
Mvvan 44,160,144,132
C 1,2,3
Mvvan 52,160,160,136
E 6
Mvvan 60,160,176,140
Rv 152,198,18432
C 2,4,5
Mvvac 60,164,180
E 7
Mvvac 52,164,164
Mvvac 44,164,148
Rv 156,198,19456
E 8
Ds 196,24,56,1024
Mvvac 36,164,132
W 15
Mvvac 36,168,136
E 9
Mvvac 44,168,152
Rv 160,198,20480
C 3,6,7
Mvvac 52,168,168
E 10
Mvvac 60,168,184
C 8,8,9
Mvvac 60,172,188
Rv 164,198,21504
E 11
Mvvac 52,172,172
Ds 196,24,56,2048
Mvvac 44,172,156
E 12
Mvvac 36,172,140
Rv 168,198,22528
W 5
Mavvc 0,144,64
E 13
Mavvc 4,144,72
C 9,10,11
Mavvc 8,144,80
Rv 172,198,23552
E 14
Mavvc 12,144,88
C 10,12,13
Mavvc 28,148,88
E 15
Ds 196,24,56,3072
Mavvc 24,148,80
Rv 144,198,24576
Mvvvc 176,192,64
Mavvc 20,148,72
E 16
AS 56,56,4096
Mavvc 16,148,64
W 5
Mavvc 32,152,64
E 17
Mavvc 36,152,72
Rv 148,198,25600
C 11,14,15
Mavvc 40,152,80
E 18
Mavvc 44,152,88
C 16,16,17
Mavvc 60,156,88
E 19
Mavvc 56,156,80
Rv 152,198,26624
Mvvvc 180,192,72
Mavvc 52,156,72
E 20
AM 58,4096
Mavvc 48,156,64
Ds 196,24,56,0
W 5
Mavvc 64,160,64
E 21
Mavvc 68,160,72
Rv 156,198,27648
Mvvvc 184,192,80
C 17,18,19
Mavvc 72,160,80
E 22
Mavvc 76,160,88
C 18,20,21
Mavvc 92,164,88
E 23
Mavvc 88,164,80
Rv 160,198,28672
Mavvc 84,164,72
E 24
Ds 196,24,56,1024
Mavvc 80,164,64
Mvvvc 188,192,88
W 5
Mavvc 96,168,64
E 25
Mavvc 100,168,72
C 19,22,23
Mavvc 104,168,80
Rv 164,198,29696
E 26
Mavvc 108,168,88
C 24,24,25
Mavvc 124,172,88
E 27
Mavvc 120,172,80
Ds 196,24,56,2048
Mavvc 116,172,72
E 28
Mavvc 112,172,64
Rv 168,198,30720
W 5
Mavvc 0,144,96
E 29
Mavvc 4,144,104
C 25,26,27
Mavvc 8,144,112
E 30
Mavvc 12,144,120
C 26,28,29
Mavvc 28,148,120
Rv 172,198,31744
E 31
Ds 196,24,56,3072
Mavvc 24,148,112
Mvvvc 176,192,96
Mavvc 20,148,104
E 32
AS 56,56,28672
Mavvc 16,148,96
W 4
Mavvc 32,152,96
E 33
Mavvc 36,152,104
Mvvvc 180,192,104
C 27,30,31
Mavvc 40,152,112
E 34
Mavvc 44,152,120
C 32,32,33
Mavvc 60,156,120
E 35
Mavvc 56,156,112
WV 16
B
Mavvc 52,156,104
Rv 144,197,16384
E 36
AM 59,16384
Mavvc 48,156,96
Ds 196,28,57,0
W 15
Mavvc 64,160,96
Rv 148,197,17408
E 37
Mavvc 68,160,104
Mvvvc 184,192,112
C 33,34,35
Mavvc 72,160,112
E 38
Mavvc 76,160,120
Rv 152,197,18432
C 34,36,37
Ds 196,28,57,1024
Mavvc 92,164,120
E 39
Mavvc 88,164,112
Mavvc 84,164,104
Rv 156,197,19456
E 40
Mavvc 80,164,96
Mvvvc 188,192,120
Ds 196,28,57,2048
W 15
Mavvc 96,168,96
E 41
Mavvc 100,168,104
Rv 160,197,20480
C 35,38,39
Mavvc 104,168,112
E 42
Mavvc 108,168,120
C 40,40,41
Ds 196,28,57,3072
Mavvc 124,172,120
Rv 164,197,21504
E 43
Mavvc 120,172,112
Mavvc 116,172,104
E 44
Mavvc 112,172,96
Rv 168,197,22528
W 5
s_bitcmp1_b32 s73, s60
s_cbranch_scc1 XAT0_B4%=
XAT0_B3%=:
AS 60,60,1
AT0_B3%=:
Mvvan 64,144,128,128
AS 57,57,4096
E 45
Mvvan 72,144,144,132
C 41,42,43
AM 59,20480
Mvvan 80,144,160,136
Rv 172,197,23552
E 46
Ds 196,28,57,0
Mvvan 88,144,176,140
C 42,44,45
Mvvac 88,148,180
E 47
Mvvac 80,148,164
Rv 144,197,24576
Mvvac 72,148,148
E 48
Mvvac 64,148,132
W 5
Mvvac 64,152,136
E 49
Mvvac 72,152,152
Rv 148,197,25600
C 43,46,47
Mvvac 80,152,168
E 50
Mvvac 88,152,184
C 48,48,49
Ds 196,28,57,1024
Mvvac 88,156,188
E 51
Mvvac 80,156,172
Rv 152,197,26624
Mvvac 72,156,156
E 52
Mvvac 64,156,140
W 5
Mvvan 68,160,128,128
E 53
Mvvan 76,160,144,132
Rv 156,197,27648
C 49,50,51
Mvvan 84,160,160,136
E 54
Mvvan 92,160,176,140
C 50,52,53
Mvvac 92,164,180
E 55
Ds 196,28,57,2048
Mvvac 84,164,164
Rv 160,197,28672
Mvvac 76,164,148
E 56
Mvvac 68,164,132
W 5
Mvvac 68,168,136
E 57
Mvvac 76,168,152
C 51,54,55
Mvvac 84,168,168
Rv 164,197,29696
E 58
Mvvac 92,168,184
C 56,56,57
Mvvac 92,172,188
E 59
Mvvac 84,172,172
Ds 196,28,57,3072
Mvvac 76,172,156
E 60
Mvvac 68,172,140
Rv 168,197,30720
W 5
AS 57,57,28672
Mvvan 96,144,128,128
E 61
Mvvan 104,144,144,132
C 57,58,59
Mvvan 112,144,160,136
E 62
Mvvan 120,144,176,140
C 58,60,61
Mvvac 120,148,180
Rv 172,197,31744
E 63
Mvvac 112,148,164
Mvvac 104,148,148
E 64
Mvvac 96,148,132
W 4
Mvvac 96,152,136
E 65
Mvvac 104,152,152
C 59,62,63
Mvvac 112,152,168
E 66
Mvvac 120,152,184
C 64,64,65
Mvvac 120,156,188
E 67
Mvvac 112,156,172
WV 16
B
Mvvac 104,156,156
Rv 144,198,0
E 68
AM 58,16384
Mvvac 96,156,140
Ds 196,24,56,0
W 15
Mvvan 100,160,128,128
Rv 148,198,1024
E 69
Mvvan 108,160,144,132
C 65,66,67
Mvvan 116,160,160,136
E 70
Mvvan 124,160,176,140
Rv 152,198,2048
C 66,68,69
Mvvac 124,164,180
E 71
Mvvac 116,164,164
Mvvac 108,164,148
Rv 156,198,3072
E 72
Ds 196,24,56,1024
Mvvac 100,164,132
W 15
Mvvac 100,168,136
E 73
Mvvac 108,168,152
Rv 160,198,4096
C 67,70,71
Mvvac 116,168,168
E 74
Mvvac 124,168,184
C 72,72,73
Mvvac 124,172,188
Rv 164,198,5120
E 75
Mvvac 116,172,172
Ds 196,24,56,2048
Mvvac 108,172,156
E 76
Mvvac 100,172,140
Rv 168,198,6144
W 5
Mavvc 0,144,0
E 77
Mavvc 4,144,8
C 73,74,75
Mavvc 8,144,16
Rv 172,198,7168
E 78
Mavvc 12,144,24
C 74,76,77
Mavvc 28,148,24
E 79
Ds 196,24,56,3072
Mavvc 24,148,16
Rv 144,198,8192
Mvvvc 176,192,0
Mavvc 20,148,8
E 80
AS 56,56,4096
Mavvc 16,148,0
W 5
Mavvc 32,152,0
E 81
Mavvc 36,152,8
Rv 148,198,9216
C 75,78,79
Mavvc 40,152,16
E 82
Mavvc 44,152,24
C 80,80,81
Mavvc 60,156,24
E 83
Mavvc 56,156,16
Rv 152,198,10240
Mvvvc 180,192,8
Mavvc 52,156,8
E 84
AM 58,20480
Mavvc 48,156,0
Ds 196,24,56,0
W 5
Mavvc 64,160,0
E 85
Mavvc 68,160,8
Rv 156,198,11264
Mvvvc 184,192,16
C 81,82,83
Mavvc 72,160,16
E 86
Mavvc 76,160,24
C 82,84,85
Mavvc 92,164,24
E 87
Mavvc 88,164,16
Rv 160,198,12288
Mavvc 84,164,8
E 88
Ds 196,24,56,1024
Mavvc 80,164,0
Mvvvc 188,192,24
W 5
Mavvc 96,168,0
E 89
Mavvc 100,168,8
C 83,86,87
Mavvc 104,168,16
Rv 164,198,13312
E 90
Mavvc 108,168,24
C 88,88,89
Mavvc 124,172,24
E 91
Mavvc 120,172,16
Ds 196,24,56,2048
Mavvc 116,172,8
E 92
Mavvc 112,172,0
Rv 168,198,14336
W 5
Mavvc 0,144,32
E 93
Mavvc 4,144,40
C 89,90,91
Mavvc 8,144,48
E 94
Mavvc 12,144,56
C 90,92,93
Mavvc 28,148,56
Rv 172,198,15360
E 95
Ds 196,24,56,3072
Mavvc 24,148,48
Mvvvc 176,192,32
Mavvc 20,148,40
E 96
AS 56,56,28672
Mavvc 16,148,32
W 4
Mavvc 32,152,32
E 97
Mavvc 36,152,40
Mvvvc 180,192,40
C 91,94,95
Mavvc 40,152,48
E 98
Mavvc 44,152,56
C 96,96,97
Mavvc 60,156,56
E 99
Mavvc 56,156,48
WV 16
B
Mavvc 52,156,40
Rv 144,197,0
E 100
AM 59,0
Mavvc 48,156,32
Ds 196,28,57,0
W 15
Mavvc 64,160,32
Rv 148,197,1024
E 101
Mavvc 68,160,40
Mvvvc 184,192,48
C 97,98,99
Mavvc 72,160,48
E 102
Mavvc 76,160,56
Rv 152,197,2048
C 98,100,101
Ds 196,28,57,1024
Mavvc 92,164,56
E 103
Mavvc 88,164,48
Mavvc 84,164,40
Rv 156,197,3072
E 104
Mavvc 80,164,32
Mvvvc 188,192,56
Ds 196,28,57,2048
W 15
Mavvc 96,168,32
E 105
Mavvc 100,168,40
Rv 160,197,4096
C 99,102,103
Mavvc 104,168,48
E 106
Mavvc 108,168,56
C 104,104,105
Ds 196,28,57,3072
Mavvc 124,172,56
Rv 164,197,5120
E 107
Mavvc 120,172,48
Mavvc 116,172,40
E 108
Mavvc 112,172,32
Rv 168,197,6144
W 5
s_branch XAT0_B2%=
XAT0_B4%=:
AS 60,60,1
AT0_B4%=:
Mvvan 64,144,128,128
AS 57,57,4096
E 45
Mvvan 72,144,144,132
C 41,42,43
AM 59,20480
Mvvan 80,144,160,136
Rv 172,197,23552
E 46
Ds 196,28,57,0
Mvvan 88,144,176,140
C 42,44,45
Mvvac 88,148,180
E 47
Mvvac 80,148,164
Rv 144,197,24576
Mvvac 72,148,148
E 48
Mvvac 64,148,132
W 5
Mvvac 64,152,136
E 49
Mvvac 72,152,152
Rv 148,197,25600
C 43,46,47
Mvvac 80,152,168
E 50
Mvvac 88,152,184
C 48,48,49
Ds 196,28,57,1024
Mvvac 88,156,188
E 51
Mvvac 80,156,172
Rv 152,197,26624
Mvvac 72,156,156
E 52
Mvvac 64,156,140
W 5
Mvvan 68,160,128,128
E 53
Mvvan 76,160,144,132
Rv 156,197,27648
C 49,50,51
Mvvan 84,160,160,136
E 54
Mvvan 92,160,176,140
C 50,52,53
Mvvac 92,164,180
E 55
Ds 196,28,57,2048
Mvvac 84,164,164
Rv 160,197,28672
Mvvac 76,164,148
E 56
Mvvac 68,164,132
W 5
Mvvac 68,168,136
E 57
Mvvac 76,168,152
C 51,54,55
Mvvac 84,168,168
Rv 164,197,29696
E 58
Mvvac 92,168,184
C 56,56,57
Mvvac 92,172,188
E 59
Mvvac 84,172,172
Ds 196,28,57,3072
Mvvac 76,172,156
E 60
Mvvac 68,172,140
Rv 168,197,30720
W 5
AS 57,57,28672
Mvvan 96,144,128,128
E 61
Mvvan 104,144,144,132
C 57,58,59
Mvvan 112,144,160,136
E 62
Mvvan 120,144,176,140
C 58,60,61
Mvvac 120,148,180
Rv 172,197,31744
E 63
Mvvac 112,148,164
Mvvac 104,148,148
E 64
Mvvac 96,148,132
W 4
Mvvac 96,152,136
E 65
Mvvac 104,152,152
C 59,62,63
Mvvac 112,152,168
E 66
Mvvac 120,152,184
C 64,64,65
Mvvac 120,156,188
E 67
Mvvac 112,156,172
WV 16
B
Mvvac 104,156,156
Rv 144,198,0
E 68
Mvvac 96,156,140
W 15
Mvvan 100,160,128,128
Rv 148,198,1024
E 69
Mvvan 108,160,144,132
C 65,66,67
Mvvan 116,160,160,136
E 70
Mvvan 124,160,176,140
Rv 152,198,2048
C 66,68,69
Mvvac 124,164,180
E 71
Mvvac 116,164,164
Mvvac 108,164,148
Rv 156,198,3072
E 72
Mvvac 100,164,132
W 15
Mvvac 100,168,136
E 73
Mvvac 108,168,152
Rv 160,198,4096
C 67,70,71
Mvvac 116,168,168
E 74
Mvvac 124,168,184
C 72,72,73
Mvvac 124,172,188
Rv 164,198,5120
E 75
Mvvac 116,172,172
Mvvac 108,172,156
E 76
Mvvac 100,172,140
Rv 168,198,6144
W 5
Mavvc 0,144,0
E 77
Mavvc 4,144,8
C 73,74,75
Mavvc 8,144,16
Rv 172,198,7168
E 78
Mavvc 12,144,24
C 74,76,77
Mavvc 28,148,24
E 79
Mavvc 24,148,16
Rv 144,198,8192
Mvvvc 176,192,0
Mavvc 20,148,8
E 80
Mavvc 16,148,0
W 5
Mavvc 32,152,0
E 81
Mavvc 36,152,8
Rv 148,198,9216
C 75,78,79
Mavvc 40,152,16
E 82
Mavvc 44,152,24
C 80,80,81
Mavvc 60,156,24
E 83
Mavvc 56,156,16
Rv 152,198,10240
Mvvvc 180,192,8
Mavvc 52,156,8
E 84
Mavvc 48,156,0
W 5
Mavvc 64,160,0
E 85
Mavvc 68,160,8
Rv 156,198,11264
Mvvvc 184,192,16
C 81,82,83
Mavvc 72,160,16
E 86
Mavvc 76,160,24
C 82,84,85
Mavvc 92,164,24
E 87
Mavvc 88,164,16
Rv 160,198,12288
Mavvc 84,164,8
E 88
Mavvc 80,164,0
Mvvvc 188,192,24
W 5
Mavvc 96,168,0
E 89
Mavvc 100,168,8
C 83,86,87
Mavvc 104,168,16
Rv 164,198,13312
E 90
Mavvc 108,168,24
C 88,88,89
Mavvc 124,172,24
E 91
Mavvc 120,172,16
Mavvc 116,172,8
E 92
Mavvc 112,172,0
Rv 168,198,14336
W 5
Mavvc 0,144,32
E 93
Mavvc 4,144,40
C 89,90,91
Mavvc 8,144,48
E 94
Mavvc 12,144,56
C 90,92,93
Mavvc 28,148,56
Rv 172,198,15360
E 95
Mavvc 24,148,48
Mvvvc 176,192,32
Mavvc 20,148,40
E 96
Mavvc 16,148,32
W 4
Mavvc 32,152,32
E 97
Mavvc 36,152,40
Mvvvc 180,192,40
C 91,94,95
Mavvc 40,152,48
E 98
Mavvc 44,152,56
C 96,96,97
Mavvc 60,156,56
E 99
Mavvc 56,156,48
WV 8
B
Mavvc 52,156,40
Rv 144,197,0
E 100
AM 59,0
Mavvc 48,156,32
Ds 196,28,57,0
W 15
Mavvc 64,160,32
Rv 148,197,1024
E 101
Mavvc 68,160,40
Mvvvc 184,192,48
C 97,98,99
Mavvc 72,160,48
E 102
Mavvc 76,160,56
Rv 152,197,2048
C 98,100,101
Ds 196,28,57,1024
Mavvc 92,164,56
E 103
Mavvc 88,164,48
Mavvc 84,164,40
Rv 156,197,3072
E 104
Mavvc 80,164,32
Mvvvc 188,192,56
Ds 196,28,57,2048
W 15
Mavvc 96,168,32
E 105
Mavvc 100,168,40
Rv 160,197,4096
C 99,102,103
Mavvc 104,168,48
E 106
Mavvc 108,168,56
C 104,104,105
Ds 196,28,57,3072
Mavvc 124,172,56
Rv 164,197,5120
E 107
Mavvc 120,172,48
Mavvc 116,172,40
E 108
Mavvc 112,172,32
Rv 168,197,6144
W 5
XAT0_B5%=:
AS 60,60,1
AT0_B5%=:
Mvvan 0,144,128,128
AS 57,57,4096
E 109
Mvvan 8,144,144,132
C 105,106,107
AM 59,4096
Mvvan 16,144,160,136
Rv 172,197,7168
E 110
Ds 196,28,57,0
Mvvan 24,144,176,140
C 106,108,109
Mvvac 24,148,180
E 111
Mvvac 16,148,164
Rv 144,197,8192
Mvvac 8,148,148
E 112
Mvvac 0,148,132
W 5
Mvvac 0,152,136
E 113
Mvvac 8,152,152
Rv 148,197,9216
C 107,110,111
Mvvac 16,152,168
)A"
R"A(E 114
Mvvac 24,152,184
C 112,112,113
Ds 196,28,57,1024
Mvvac 24,156,188
E 115
Mvvac 16,156,172
Rv 152,197,10240
Mvvac 8,156,156
E 116
Mvvac 0,156,140
W 5
E 117
C 113,114,115
Mvvan 12,160,144,132
Rv 156,197,11264
Mvvan 20,160,160,136
E 118
Mvvan 28,160,176,140
C 114,116,117
Mvvac 28,164,180
E 119
Ds 196,28,57,2048
Mvvac 20,164,164
Rv 160,197,12288
Mvvac 12,164,148
E 120
C 115,118,119
W 5
E 121
Mvvac 12,168,152
Mvvac 20,168,168
Rv 164,197,13312
E 122
Mvvac 28,168,184
C 120,120,121
Mvvac 28,172,188
E 123
Mvvac 20,172,172
Ds 196,28,57,3072
Mvvac 12,172,156
E 124
Rv 168,197,14336
W 5
AS 57,57,28672
E 125
C 121,122,123
Mvvan 48,144,160,136
E 126
Mvvan 56,144,176,140
C 122,124,125
Mvvac 56,148,180
Rv 172,197,15360
E 127
Mvvac 48,148,164
K 0,199,0,62
E 0
K 1,199,1,64
W 4
E 1
C 123,126,127
K 2,199,2,66
Mvvac 48,152,168
E 2
Mvvac 56,152,184
K 3,199,3,68
Mvvac 56,156,188
E 3
Mvvac 48,156,172
WV 8
B
Rv 144,198,16384
C 0,0,1
W 15
Rv 148,198,17408
C 1,2,3
v_mov_b32 v2, 0
Mvvan 60,160,176,140
Rv 152,198,18432
Mvvac 60,164,180
Rv 156,198,19456
E 8
W 15
E 9
Rv 160,198,20480
v_mov_b32 v3, 0
E 10
Mvvac 60,168,184
Mvvac 60,172,188
Rv 164,198,21504
E 11
C 8,8,9
K 12,199,12,62
E 12
C 9,10,11
Rv 168,198,22528
K 13,199,13,64
W 5
Mavvc 0,144,64
E 13
Mavvc 4,144,72
K 14,199,14,66
Mavvc 8,144,80
Rv 172,198,23552
E 14
Mavvc 12,144,88
Mvvvc 176,192,64
K 15,199,15,68
Mavvc 28,148,88
E 15
Mavvc 24,148,80
Rv 144,198,24576
C 10,12,13
Mavvc 20,148,72
E 16
Mavvc 16,148,64
W 5
Mavvc 32,152,64
E 17
Mavvc 36,152,72
Rv 148,198,25600
C 11,14,15
Mavvc 40,152,80
E 18
Mavvc 44,152,88
C 16,16,17
Mavvc 60,156,88
E 19
Mavvc 56,156,80
Rv 152,198,26624
Mvvvc 180,192,72
Mavvc 52,156,72
E 20
Mavvc 48,156,64
W 5
Mavvc 64,160,64
E 21
Mavvc 68,160,72
Rv 156,198,27648
Mvvvc 184,192,80
C 17,18,19
Mavvc 72,160,80
E 22
Mavvc 76,160,88
C 18,20,21
Mavvc 92,164,88
E 23
Mavvc 88,164,80
Rv 160,198,28672
Mavvc 84,164,72
E 24
Mavvc 80,164,64
Mvvvc 188,192,88
W 5
Mavvc 96,168,64
E 25
Mavvc 100,168,72
C 19,22,23
Mavvc 104,168,80
Rv 164,198,29696
E 26
Mavvc 108,168,88
C 24,24,25
Mavvc 124,172,88
E 27
Mavvc 120,172,80
Mavvc 116,172,72
E 28
Mavvc 112,172,64
Rv 168,198,30720
W 5
Mavvc 0,144,96
E 29
Mavvc 4,144,104
C 25,26,27
Mavvc 8,144,112
E 30
Mavvc 12,144,120
C 26,28,29
Mavvc 28,148,120
Rv 172,198,31744
E 31
Mavvc 24,148,112
Mvvvc 176,192,96
Mavvc 20,148,104
C 27,30,31
Mavvc 16,148,96
W 4
Mavvc 32,152,96
Mavvc 36,152,104
Mvvvc 180,192,104
Mavvc 40,152,112
Mavvc 44,152,120
Mavvc 60,156,120
Mavvc 56,156,112
W 0
B
Mavvc 52,156,104
Mavvc 48,156,96
W 15
Mavvc 64,160,96
Mvvvc 184,192,112
Mavvc 68,160,104
Mavvc 72,160,112
Mavvc 76,160,120
Mavvc 92,164,120
Mavvc 88,164,112
Mavvc 84,164,104
Mavvc 80,164,96
W 15
Mavvc 96,168,96
Mvvvc 188,192,120
Mavvc 100,168,104
Mavvc 104,168,112
Mavvc 108,168,120
Mavvc 124,172,120
Mavvc 120,172,112
Mavvc 116,172,104
Mavvc 112,172,96
XAT0_B6%=:
AS 60,60,1
AT0_B6%=:
K 48,199,48,62
E 48
K 49,199,49,64
E 49
K 50,199,50,66
E 50
K 51,199,51,68
E 51
C 48,48,49
C 49,50,51
v_mov_b32 v50, 0
E 56
E 57
v_mov_b32 v51, 0
E 58
C 56,56,57
E 59
K 60,199,60,62
E 60
K 61,199,61,64
E 61
C 57,58,59
K 62,199,62,66
E 62
K 63,199,63,68
E 63
C 58,60,61
C 59,62,63
WV 0
B
v_mov_b32 v247, 149520
ds_read_b64 v[244:245], v247
W 0
v_or_b32 v244, v244, v245
N 0
v_readfirstlane_b32 s22, v244
N 3
s_cmp_eq_u32 s22, 0
s_cbranch_scc0 NOEB%=
s_mov_b32 s23, s13
s_lshl_b32 s23, s23, 2
s_lshr_b32 s22, s12, 3
s_add_u32 s23, s23, s22
s_lshl_b32 s21, s23, 8
AS 21,21,32768
s_and_b32 s22, s12, 7
s_lshl_b32 s22, s22, 4
s_add_u32 s21, s21, s22
v_lshlrev_b32 v247, 2, v254
v_add_u32 v247, s21, v247
v_mov_b32 v246, s79
s_mov_b64 exec, 15
global_store_dword v247, v246, s[14:15] sc1
s_mov_b64 exec, -1
N 3
NOEB%=:
Rv 144,198,0
Rv 148,198,1024
Rv 152,198,2048
Rv 156,198,3072
Rv 160,198,4096
Rv 164,198,5120
Rv 168,198,6144
W 5
Mavvc 0,144,0
Mavvc 4,144,8
Mavvc 8,144,16
Rv 172,198,7168
Mvvvc 176,192,0
Mavvc 12,144,24
Mavvc 28,148,24
Mavvc 24,148,16
Rv 144,198,8192
Mavvc 20,148,8
Mavvc 16,148,0
W 5
Mavvc 32,152,0
Mavvc 36,152,8
Rv 148,198,9216
Mvvvc 180,192,8
Mavvc 40,152,16
Mavvc 44,152,24
Mavvc 60,156,24
Mavvc 56,156,16
Rv 152,198,10240
Mavvc 52,156,8
Mavvc 48,156,0
W 5
Mavvc 64,160,0
Mvvvc 184,192,16
Mavvc 68,160,8
Rv 156,198,11264
Mavvc 72,160,16
Mavvc 76,160,24
Mavvc 92,164,24
Mavvc 88,164,16
Rv 160,198,12288
Mavvc 84,164,8
Mavvc 80,164,0
W 5
Mavvc 96,168,0
Mvvvc 188,192,24
Mavvc 100,168,8
Mavvc 104,168,16
Rv 164,198,13312
Mavvc 108,168,24
Mavvc 124,172,24
Mavvc 120,172,16
Mavvc 116,172,8
Mavvc 112,172,0
Rv 168,198,14336
W 5
Mavvc 8,144,48
Mavvc 12,144,56
Mavvc 28,148,56
Rv 172,198,15360
Mavvc 24,148,48
W 4
Mavvc 40,152,48
Mavvc 44,152,56
Mavvc 60,156,56
Mavvc 56,156,48
W 0
B
W 15
Mvvvc 184,192,48
Mavvc 72,160,48
Mavvc 76,160,56
Mavvc 92,164,56
Mavvc 88,164,48
W 15
Mvvvc 188,192,56
Mavvc 104,168,48
Mavvc 108,168,56
Mavvc 124,172,56
Mavvc 120,172,48
s_branch XAT0_FIN%=
XAT0_B7%=:
AS 60,60,1
AT0_B7%=:
Mvvaz 0,144,128
Mvvaz 8,144,144
Mvvaz 16,144,160
Rv 172,197,7168
Mvvaz 24,144,176
Mvvac 24,148,180
Mvvac 16,148,164
Rv 144,197,8192
Mvvac 8,148,148
Mvvac 0,148,132
W 5
Mvvac 0,152,136
Mvvac 8,152,152
Rv 148,197,9216
Mvvac 16,152,168
Mvvac 24,152,184
Mvvac 24,156,188
Mvvac 16,156,172
Rv 152,197,10240
Mvvac 8,156,156
Mvvac 0,156,140
W 5
Mvvaz 4,160,128
Mvvaz 12,160,144
Rv 156,197,11264
Mvvaz 20,160,160
N 2
v_max3_f32 v200, v0, v1, v2
Fma 200,200,3
v_max3_f32 v202, v8, v9, v10
Fma 202,202,11
Mvvaz 28,160,176
v_max3_f32 v204, v16, v17, v18
Fma 204,204,19
v_max3_f32 v206, v24, v25, v26
Fma 206,206,27
Mvvac 28,164,180
MV 201,200
MV 203,202
MV 205,204
MV 207,206
Mvvac 20,164,164
Rv 160,197,12288
v_permlane16_swap_b32 v200, v201
v_permlane16_swap_b32 v202, v203
v_permlane16_swap_b32 v204, v205
Mvvac 12,164,148
v_permlane16_swap_b32 v206, v207
Fma 200,200,201
Fma 202,202,203
Fma 204,204,205
Mvvac 4,164,132
Fma 206,206,207
MV 201,200
MV 203,202
MV 205,204
W 5
Mvvac 4,168,136
MV 207,206
v_permlane32_swap_b32 v200, v201
v_permlane32_swap_b32 v202, v203
Mvvac 12,168,152
v_permlane32_swap_b32 v204, v205
v_permlane32_swap_b32 v206, v207
Fma 200,200,201
Fma 202,202,203
Mvvac 20,168,168
Rv 164,197,13312
Fma 204,204,205
Fma 206,206,207
v_sub_f32 v128, 0, v200
v_sub_f32 v129, 0, v200
Mvvac 28,168,184
v_sub_f32 v130, 0, v200
v_sub_f32 v131, 0, v200
v_sub_f32 v132, 0, v202
Mvvac 28,172,188
v_sub_f32 v133, 0, v202
v_sub_f32 v134, 0, v202
v_sub_f32 v135, 0, v202
v_sub_f32 v136, 0, v204
Mvvac 20,172,172
v_sub_f32 v137, 0, v204
v_sub_f32 v138, 0, v204
v_sub_f32 v139, 0, v204
v_sub_f32 v140, 0, v206
Mvvac 12,172,156
v_sub_f32 v141, 0, v206
v_sub_f32 v142, 0, v206
v_sub_f32 v143, 0, v206
Mvvac 4,172,140
Rv 168,197,14336
W 5
Mvvan 32,144,128,128
Mvvan 40,144,144,132
Mvvan 48,144,160,136
Mvvan 56,144,176,140
Mvvac 56,148,180
Rv 172,197,15360
Mvvac 48,148,164
Fad 0,0,128
Mvvac 40,148,148
E 0
Mvvac 32,148,132
Fad 1,1,128
W 4
Mvvac 32,152,136
E 1
Mvvac 40,152,152
Fad 2,2,128
Mvvac 48,152,168
E 2
Mvvac 56,152,184
C 0,0,1
Fad 3,3,128
Mvvac 56,156,188
E 3
Mvvac 48,156,172
Fad 4,4,128
W 0
B
Mvvac 40,156,156
E 4
AM 58,0
Mvvac 32,156,140
Fad 5,5,128
Ds 196,24,56,0
W 15
Mvvan 36,160,128,128
E 5
Mvvan 44,160,144,132
C 1,2,3
Fad 6,6,128
Mvvan 52,160,160,136
E 6
Mvvan 60,160,176,140
C 2,4,5
Fad 7,7,128
Mvvac 60,164,180
E 7
Mvvac 52,164,164
Fad 8,8,132
Mvvac 44,164,148
E 8
Mvvac 36,164,132
Fad 9,9,132
W 15
Mvvac 36,168,136
E 9
Mvvac 44,168,152
C 3,6,7
Fad 10,10,132
Mvvac 52,168,168
E 10
Mvvac 60,168,184
C 8,8,9
Fad 11,11,132
Mvvac 60,172,188
E 11
Mvvac 52,172,172
Fad 12,12,132
Ds 196,24,56,1024
Mvvac 44,172,156
E 12
Mvvac 36,172,140
Fad 13,13,132
E 13
C 9,10,11
Fad 14,14,132
E 14
C 10,12,13
Fad 15,15,132
E 15
Fad 16,16,136
E 16
Fad 17,17,136
E 17
C 11,14,15
Fad 18,18,136
E 18
C 16,16,17
Fad 19,19,136
Ds 196,24,56,2048
E 19
Fad 20,20,136
E 20
Fad 21,21,136
E 21
C 17,18,19
Fad 22,22,136
E 22
C 18,20,21
Fad 23,23,136
E 23
Fad 24,24,140
E 24
Fad 25,25,140
E 25
C 19,22,23
Fad 26,26,140
Ds 196,24,56,3072
E 26
C 24,24,25
Fad 27,27,140
AS 56,56,16384
E 27
Fad 28,28,140
E 28
Fad 29,29,140
E 29
C 25,26,27
Fad 30,30,140
E 30
Fad 31,31,140
E 31
C 26,28,29
E 32
E 33
C 27,30,31
E 34
C 32,32,33
E 35
W 0
B
Rv 144,197,16384
E 36
AM 59,16384
N 0
Ds 196,28,57,0
Rv 148,197,17408
E 37
C 33,34,35
E 38
Rv 152,197,18432
C 34,36,37
Ds 196,28,57,1024
E 39
Rv 156,197,19456
E 40
Ds 196,28,57,2048
E 41
Rv 160,197,20480
C 35,38,39
E 42
C 40,40,41
Ds 196,28,57,3072
Rv 164,197,21504
E 43
AS 57,57,16384
E 44
Rv 168,197,22528
W 5
XAT0_B8%=:
AS 60,60,1
AT0_B8%=:
Mvvan 64,144,128,128
E 45
Mvvan 72,144,144,132
C 41,42,43
Mvvan 80,144,160,136
Rv 172,197,23552
E 46
Mvvan 88,144,176,140
C 42,44,45
Mvvac 88,148,180
E 47
Mvvac 80,148,164
Rv 144,197,24576
Mvvac 72,148,148
E 48
Mvvac 64,148,132
W 5
Mvvac 64,152,136
E 49
Mvvac 72,152,152
Rv 148,197,25600
C 43,46,47
Mvvac 80,152,168
E 50
Mvvac 88,152,184
C 48,48,49
Mvvac 88,156,188
E 51
Mvvac 80,156,172
Rv 152,197,26624
Mvvac 72,156,156
E 52
Mvvac 64,156,140
W 5
Mvvan 68,160,128,128
E 53
Mvvan 76,160,144,132
Rv 156,197,27648
C 49,50,51
Mvvan 84,160,160,136
E 54
Mvvan 92,160,176,140
C 50,52,53
Mvvac 92,164,180
E 55
Mvvac 84,164,164
Rv 160,197,28672
Mvvac 76,164,148
E 56
Mvvac 68,164,132
W 5
Mvvac 68,168,136
E 57
Mvvac 76,168,152
C 51,54,55
Mvvac 84,168,168
Rv 164,197,29696
E 58
Mvvac 92,168,184
C 56,56,57
Mvvac 92,172,188
E 59
Mvvac 84,172,172
Mvvac 76,172,156
E 60
Mvvac 68,172,140
Rv 168,197,30720
W 5
Mvvan 96,144,128,128
E 61
Mvvan 104,144,144,132
C 57,58,59
Mvvan 112,144,160,136
E 62
Mvvan 120,144,176,140
C 58,60,61
Mvvac 120,148,180
Rv 172,197,31744
E 63
Mvvac 112,148,164
Mvvac 104,148,148
E 64
Mvvac 96,148,132
W 4
Mvvac 96,152,136
E 65
Mvvac 104,152,152
C 59,62,63
Mvvac 112,152,168
E 66
Mvvac 120,152,184
C 64,64,65
Mvvac 120,156,188
E 67
Mvvac 112,156,172
WV 8
B
Mvvac 104,156,156
Rv 144,198,0
E 68
AM 58,16384
Mvvac 96,156,140
Ds 196,24,56,0
W 15
Mvvan 100,160,128,128
Rv 148,198,1024
E 69
Mvvan 108,160,144,132
C 65,66,67
Mvvan 116,160,160,136
E 70
Mvvan 124,160,176,140
Rv 152,198,2048
C 66,68,69
Mvvac 124,164,180
E 71
Mvvac 116,164,164
Mvvac 108,164,148
Rv 156,198,3072
E 72
Mvvac 100,164,132
W 15
Mvvac 100,168,136
E 73
Mvvac 108,168,152
Rv 160,198,4096
C 67,70,71
Mvvac 116,168,168
E 74
Mvvac 124,168,184
C 72,72,73
Mvvac 124,172,188
Rv 164,198,5120
E 75
Mvvac 116,172,172
Ds 196,24,56,1024
Mvvac 108,172,156
E 76
Mvvac 100,172,140
Rv 168,198,6144
W 5
Mavvz 0,144,0
E 77
Mavvz 4,144,8
C 73,74,75
Mavvz 8,144,16
Rv 172,198,7168
E 78
Mavvz 12,144,24
C 74,76,77
Mavvz 28,148,24
E 79
Mavvz 24,148,16
Rv 144,198,8192
Mvvvz 176,192,0
Mavvz 20,148,8
E 80
Mavvz 16,148,0
W 5
Mavvz 32,152,0
E 81
Mavvz 36,152,8
Rv 148,198,9216
C 75,78,79
Mavvz 40,152,16
E 82
Mavvz 44,152,24
C 80,80,81
Ds 196,24,56,2048
Mavvz 60,156,24
E 83
Mavvz 56,156,16
Rv 152,198,10240
Mvvvz 180,192,8
Mavvz 52,156,8
E 84
Mavvz 48,156,0
W 5
Mavvz 64,160,0
E 85
Mavvz 68,160,8
Rv 156,198,11264
Mvvvz 184,192,16
C 81,82,83
Mavvz 72,160,16
E 86
Mavvz 76,160,24
C 82,84,85
Mavvz 92,164,24
E 87
Mavvz 88,164,16
Rv 160,198,12288
Mavvz 84,164,8
E 88
Mavvz 80,164,0
Mvvvz 188,192,24
W 5
Mavvz 96,168,0
E 89
Mavvz 100,168,8
C 83,86,87
Ds 196,24,56,3072
Mavvz 104,168,16
Rv 164,198,13312
E 90
Mavvz 108,168,24
C 88,88,89
AS 56,56,16384
Mavvz 124,172,24
E 91
Mavvz 120,172,16
Mavvz 116,172,8
E 92
Mavvz 112,172,0
Rv 168,198,14336
W 5
Mavvc 0,144,32
E 93
Mavvc 4,144,40
C 89,90,91
Mavvc 8,144,48
E 94
Mavvc 12,144,56
C 90,92,93
Mavvc 28,148,56
Rv 172,198,15360
E 95
Mavvc 24,148,48
Mvvvc 176,192,32
Mavvc 20,148,40
E 96
Mavvc 16,148,32
W 4
Mavvc 32,152,32
E 97
Mavvc 36,152,40
Mvvvc 180,192,40
C 91,94,95
Mavvc 40,152,48
E 98
Mavvc 44,152,56
C 96,96,97
Mavvc 60,156,56
E 99
Mavvc 56,156,48
WV 8
B
Mavvc 52,156,40
Rv 144,197,0
E 100
AM 59,0
Mavvc 48,156,32
Ds 196,28,57,0
W 15
Mavvc 64,160,32
Rv 148,197,1024
E 101
Mavvc 68,160,40
Mvvvc 184,192,48
C 97,98,99
Mavvc 72,160,48
E 102
Mavvc 76,160,56
Rv 152,197,2048
C 98,100,101
Ds 196,28,57,1024
Mavvc 92,164,56
E 103
Mavvc 88,164,48
Mavvc 84,164,40
Rv 156,197,3072
E 104
Mavvc 80,164,32
Mvvvc 188,192,56
Ds 196,28,57,2048
W 15
Mavvc 96,168,32
)A"
R"A(E 105
Mavvc 100,168,40
Rv 160,197,4096
C 99,102,103
Mavvc 104,168,48
E 106
Mavvc 108,168,56
C 104,104,105
Ds 196,28,57,3072
Mavvc 124,172,56
Rv 164,197,5120
E 107
Mavvc 120,172,48
AS 57,57,16384
Mavvc 116,172,40
E 108
Mavvc 112,172,32
Rv 168,197,6144
W 5
s_bitcmp1_b32 s74, s60
s_cbranch_scc1 XAT0_B9%=
s_branch XAT0_B11%=
XAT0_B9%=:
AS 60,60,1
AT0_B9%=:
Mvvan 0,144,128,128
E 109
Mvvan 8,144,144,132
C 105,106,107
Mvvan 16,144,160,136
Rv 172,197,7168
E 110
Mvvan 24,144,176,140
C 106,108,109
Mvvac 24,148,180
E 111
Mvvac 16,148,164
Rv 144,197,8192
Mvvac 8,148,148
E 112
Mvvac 0,148,132
W 5
Mvvac 0,152,136
E 113
Mvvac 8,152,152
Rv 148,197,9216
C 107,110,111
Mvvac 16,152,168
E 114
Mvvac 24,152,184
C 112,112,113
Mvvac 24,156,188
E 115
Mvvac 16,156,172
Rv 152,197,10240
Mvvac 8,156,156
E 116
Mvvac 0,156,140
W 5
Mvvan 4,160,128,128
E 117
Mvvan 12,160,144,132
Rv 156,197,11264
C 113,114,115
Mvvan 20,160,160,136
E 118
Mvvan 28,160,176,140
C 114,116,117
Mvvac 28,164,180
E 119
Mvvac 20,164,164
Rv 160,197,12288
Mvvac 12,164,148
E 120
Mvvac 4,164,132
W 5
Mvvac 4,168,136
E 121
Mvvac 12,168,152
C 115,118,119
Mvvac 20,168,168
Rv 164,197,13312
E 122
Mvvac 28,168,184
C 120,120,121
Mvvac 28,172,188
E 123
Mvvac 20,172,172
Mvvac 12,172,156
E 124
Mvvac 4,172,140
Rv 168,197,14336
W 5
Mvvan 32,144,128,128
E 125
Mvvan 40,144,144,132
C 121,122,123
Mvvan 48,144,160,136
E 126
Mvvan 56,144,176,140
C 122,124,125
Mvvac 56,148,180
Rv 172,197,15360
E 127
Mvvac 48,148,164
Mvvac 40,148,148
E 0
Mvvac 32,148,132
W 4
Mvvac 32,152,136
E 1
Mvvac 40,152,152
C 123,126,127
Mvvac 48,152,168
E 2
Mvvac 56,152,184
C 0,0,1
Mvvac 56,156,188
E 3
AS 56,56,0
SM 58,0
Mvvac 48,156,172
WV 8
B
Mvvac 40,156,156
Rv 144,198,16384
E 4
AM 58,0
s_mul_i32 s21, s10, 160
Mvvac 32,156,140
Ds 196,24,56,0
W 15
AS 21,21,0
Mvvan 36,160,128,128
Rv 148,198,17408
E 5
v_min_u32 v249, 63, v254
Mvvan 44,160,144,132
C 1,2,3
v_add_u32 v249, s21, v249
Mvvan 52,160,160,136
E 6
v_min_u32 v249, 40959, v249
Mvvan 60,160,176,140
Rv 152,198,18432
C 2,4,5
v_lshlrev_b32 v249, 7, v249
Mvvac 60,164,180
E 7
global_load_dword v248, v249, s[36:37]
Mvvac 52,164,164
s_mul_i32 s21, s10, 160
Mvvac 44,164,148
Rv 156,198,19456
E 8
Ds 196,24,56,1024
AS 21,21,64
Mvvac 36,164,132
W 15
v_min_u32 v249, 63, v254
Mvvac 36,168,136
E 9
v_add_u32 v249, s21, v249
Mvvac 44,168,152
Rv 160,198,20480
C 3,6,7
v_min_u32 v249, 40959, v249
Mvvac 52,168,168
E 10
v_lshlrev_b32 v249, 7, v249
Mvvac 60,168,184
C 8,8,9
global_load_dword v248, v249, s[36:37]
Mvvac 60,172,188
Rv 164,198,21504
E 11
s_mul_i32 s21, s10, 160
Mvvac 52,172,172
Ds 196,24,56,2048
AS 21,21,128
Mvvac 44,172,156
E 12
v_min_u32 v249, 31, v254
Mvvac 36,172,140
Rv 168,198,22528
W 5
v_add_u32 v249, s21, v249
Mavvc 0,144,64
E 13
v_min_u32 v249, 40959, v249
Mavvc 4,144,72
C 9,10,11
v_lshlrev_b32 v249, 7, v249
Mavvc 8,144,80
Rv 172,198,23552
E 14
global_load_dword v248, v249, s[36:37]
Mavvc 12,144,88
C 10,12,13
Mavvc 28,148,88
E 15
Ds 196,24,56,3072
Mavvc 24,148,80
Rv 144,198,24576
Mvvvc 176,192,64
Mavvc 20,148,72
E 16
AS 56,56,4096
Mavvc 16,148,64
W 5
Mavvc 32,152,64
E 17
Mavvc 36,152,72
Rv 148,198,25600
C 11,14,15
Mavvc 40,152,80
E 18
Mavvc 44,152,88
C 16,16,17
Mavvc 60,156,88
E 19
Mavvc 56,156,80
Rv 152,198,26624
Mvvvc 180,192,72
Mavvc 52,156,72
E 20
AM 58,4096
Mavvc 48,156,64
Ds 196,24,56,0
W 5
Mavvc 64,160,64
E 21
Mavvc 68,160,72
Rv 156,198,27648
Mvvvc 184,192,80
C 17,18,19
Mavvc 72,160,80
E 22
Mavvc 76,160,88
C 18,20,21
Mavvc 92,164,88
E 23
Mavvc 88,164,80
Rv 160,198,28672
Mavvc 84,164,72
E 24
Ds 196,24,56,1024
Mavvc 80,164,64
Mvvvc 188,192,88
W 5
Mavvc 96,168,64
E 25
Mavvc 100,168,72
C 19,22,23
Mavvc 104,168,80
Rv 164,198,29696
E 26
Mavvc 108,168,88
C 24,24,25
Mavvc 124,172,88
E 27
Mavvc 120,172,80
Ds 196,24,56,2048
Mavvc 116,172,72
E 28
Mavvc 112,172,64
Rv 168,198,30720
W 5
Mavvc 0,144,96
E 29
Mavvc 4,144,104
C 25,26,27
Mavvc 8,144,112
E 30
Mavvc 12,144,120
C 26,28,29
Mavvc 28,148,120
Rv 172,198,31744
E 31
Ds 196,24,56,3072
Mavvc 24,148,112
Mvvvc 176,192,96
Mavvc 20,148,104
E 32
AS 56,56,28672
Mavvc 16,148,96
W 4
Mavvc 32,152,96
E 33
Mavvc 36,152,104
Mvvvc 180,192,104
C 27,30,31
Mavvc 40,152,112
E 34
Mavvc 44,152,120
C 32,32,33
Mavvc 60,156,120
E 35
Mavvc 56,156,112
WV 15
B
Mavvc 52,156,104
Rv 144,197,16384
E 36
AM 59,16384
Mavvc 48,156,96
Ds 196,28,57,0
W 15
Mavvc 64,160,96
Rv 148,197,17408
E 37
Mavvc 68,160,104
Mvvvc 184,192,112
C 33,34,35
Mavvc 72,160,112
E 38
Mavvc 76,160,120
Rv 152,197,18432
C 34,36,37
Ds 196,28,57,1024
Mavvc 92,164,120
E 39
Mavvc 88,164,112
Mavvc 84,164,104
Rv 156,197,19456
E 40
Mavvc 80,164,96
Mvvvc 188,192,120
Ds 196,28,57,2048
W 15
Mavvc 96,168,96
E 41
Mavvc 100,168,104
Rv 160,197,20480
C 35,38,39
Mavvc 104,168,112
E 42
Mavvc 108,168,120
C 40,40,41
Ds 196,28,57,3072
Mavvc 124,172,120
Rv 164,197,21504
E 43
Mavvc 120,172,112
AS 57,57,16384
Mavvc 116,172,104
E 44
Mavvc 112,172,96
Rv 168,197,22528
W 5
s_bitcmp1_b32 s75, s60
s_cbranch_scc1 XAT0_B13%=
XAT0_B10%=:
AS 60,60,1
AT0_B10%=:
Mvvan 64,144,128,128
E 45
Mvvan 72,144,144,132
C 41,42,43
Mvvan 80,144,160,136
Rv 172,197,23552
E 46
Mvvan 88,144,176,140
C 42,44,45
Mvvac 88,148,180
E 47
Mvvac 80,148,164
Rv 144,197,24576
Mvvac 72,148,148
E 48
Mvvac 64,148,132
W 5
Mvvac 64,152,136
E 49
Mvvac 72,152,152
Rv 148,197,25600
C 43,46,47
Mvvac 80,152,168
E 50
Mvvac 88,152,184
C 48,48,49
Mvvac 88,156,188
E 51
Mvvac 80,156,172
Rv 152,197,26624
Mvvac 72,156,156
E 52
Mvvac 64,156,140
W 5
Mvvan 68,160,128,128
E 53
Mvvan 76,160,144,132
Rv 156,197,27648
C 49,50,51
Mvvan 84,160,160,136
E 54
Mvvan 92,160,176,140
C 50,52,53
Mvvac 92,164,180
E 55
Mvvac 84,164,164
Rv 160,197,28672
Mvvac 76,164,148
E 56
Mvvac 68,164,132
W 5
Mvvac 68,168,136
E 57
Mvvac 76,168,152
C 51,54,55
Mvvac 84,168,168
Rv 164,197,29696
E 58
Mvvac 92,168,184
C 56,56,57
Mvvac 92,172,188
E 59
Mvvac 84,172,172
Mvvac 76,172,156
E 60
Mvvac 68,172,140
Rv 168,197,30720
W 5
Mvvan 96,144,128,128
E 61
Mvvan 104,144,144,132
C 57,58,59
Mvvan 112,144,160,136
E 62
Mvvan 120,144,176,140
C 58,60,61
Mvvac 120,148,180
Rv 172,197,31744
E 63
Mvvac 112,148,164
Mvvac 104,148,148
E 64
Mvvac 96,148,132
W 4
Mvvac 96,152,136
E 65
Mvvac 104,152,152
C 59,62,63
Mvvac 112,152,168
E 66
Mvvac 120,152,184
C 64,64,65
Mvvac 120,156,188
E 67
Mvvac 112,156,172
WV 15
B
Mvvac 104,156,156
Rv 144,198,0
E 68
AM 58,16384
s_mul_i32 s21, s10, 68
Mvvac 96,156,140
Ds 196,24,56,0
W 15
AS 21,21,0
Mvvan 100,160,128,128
Rv 148,198,1024
E 69
v_min_u32 v249, 63, v254
Mvvan 108,160,144,132
C 65,66,67
v_add_u32 v249, s21, v249
Mvvan 116,160,160,136
E 70
v_min_u32 v249, 17279, v249
Mvvan 124,160,176,140
Rv 152,198,2048
C 66,68,69
v_lshlrev_b32 v249, 7, v249
Mvvac 124,164,180
E 71
global_load_dword v248, v249, s[38:39]
Mvvac 116,164,164
s_mul_i32 s21, s10, 68
Mvvac 108,164,148
Rv 156,198,3072
E 72
Ds 196,24,56,1024
AS 21,21,64
Mvvac 100,164,132
W 15
v_min_u32 v249, 3, v254
Mvvac 100,168,136
E 73
v_add_u32 v249, s21, v249
Mvvac 108,168,152
Rv 160,198,4096
C 67,70,71
v_min_u32 v249, 17279, v249
Mvvac 116,168,168
E 74
v_lshlrev_b32 v249, 7, v249
Mvvac 124,168,184
C 72,72,73
global_load_dword v248, v249, s[38:39]
Mvvac 124,172,188
Rv 164,198,5120
E 75
Mvvac 116,172,172
Ds 196,24,56,2048
Mvvac 108,172,156
E 76
Mvvac 100,172,140
Rv 168,198,6144
W 5
Mavvc 0,144,0
E 77
Mavvc 4,144,8
C 73,74,75
Mavvc 8,144,16
Rv 172,198,7168
E 78
Mavvc 12,144,24
C 74,76,77
Mavvc 28,148,24
E 79
Ds 196,24,56,3072
Mavvc 24,148,16
Rv 144,198,8192
Mvvvc 176,192,0
Mavvc 20,148,8
E 80
AS 56,56,4096
Mavvc 16,148,0
W 5
Mavvc 32,152,0
E 81
Mavvc 36,152,8
Rv 148,198,9216
C 75,78,79
Mavvc 40,152,16
E 82
Mavvc 44,152,24
C 80,80,81
Mavvc 60,156,24
E 83
Mavvc 56,156,16
Rv 152,198,10240
Mvvvc 180,192,8
Mavvc 52,156,8
E 84
AM 58,20480
Mavvc 48,156,0
Ds 196,24,56,0
W 5
Mavvc 64,160,0
E 85
Mavvc 68,160,8
Rv 156,198,11264
Mvvvc 184,192,16
C 81,82,83
Mavvc 72,160,16
E 86
Mavvc 76,160,24
C 82,84,85
Mavvc 92,164,24
E 87
Mavvc 88,164,16
Rv 160,198,12288
Mavvc 84,164,8
E 88
Ds 196,24,56,1024
Mavvc 80,164,0
Mvvvc 188,192,24
W 5
Mavvc 96,168,0
E 89
Mavvc 100,168,8
C 83,86,87
Mavvc 104,168,16
Rv 164,198,13312
E 90
Mavvc 108,168,24
C 88,88,89
Mavvc 124,172,24
E 91
Mavvc 120,172,16
Ds 196,24,56,2048
Mavvc 116,172,8
E 92
Mavvc 112,172,0
Rv 168,198,14336
W 5
Mavvc 0,144,32
E 93
Mavvc 4,144,40
C 89,90,91
Mavvc 8,144,48
E 94
Mavvc 12,144,56
C 90,92,93
Mavvc 28,148,56
Rv 172,198,15360
E 95
Ds 196,24,56,3072
Mavvc 24,148,48
Mvvvc 176,192,32
Mavvc 20,148,40
E 96
AS 56,56,28672
Mavvc 16,148,32
W 4
Mavvc 32,152,32
E 97
Mavvc 36,152,40
Mvvvc 180,192,40
C 91,94,95
Mavvc 40,152,48
E 98
Mavvc 44,152,56
C 96,96,97
Mavvc 60,156,56
E 99
Mavvc 56,156,48
WV 14
B
AS 57,57,0
SM 59,65536
Mavvc 52,156,40
Rv 144,197,0
E 100
AM 59,0
Mavvc 48,156,32
Ds 196,28,57,0
W 15
Mavvc 64,160,32
Rv 148,197,1024
E 101
Mavvc 68,160,40
Mvvvc 184,192,48
C 97,98,99
Mavvc 72,160,48
E 102
Mavvc 76,160,56
Rv 152,197,2048
C 98,100,101
Ds 196,28,57,1024
Mavvc 92,164,56
E 103
Mavvc 88,164,48
Mavvc 84,164,40
Rv 156,197,3072
E 104
Mavvc 80,164,32
Mvvvc 188,192,56
Ds 196,28,57,2048
W 15
Mavvc 96,168,32
E 105
Mavvc 100,168,40
Rv 160,197,4096
C 99,102,103
Mavvc 104,168,48
E 106
Mavvc 108,168,56
C 104,104,105
Ds 196,28,57,3072
Mavvc 124,172,56
Rv 164,197,5120
E 107
Mavvc 120,172,48
Mavvc 116,172,40
E 108
Mavvc 112,172,32
Rv 168,197,6144
W 5
s_branch XAT0_B2%=
XAT0_B11%=:
AS 60,60,1
AT0_B11%=:
Mvvan 0,144,128,128
E 109
Mvvan 8,144,144,132
C 105,106,107
Mvvan 16,144,160,136
Rv 172,197,7168
E 110
Mvvan 24,144,176,140
C 106,108,109
Mvvac 24,148,180
E 111
Mvvac 16,148,164
Rv 144,197,8192
Mvvac 8,148,148
E 112
Mvvac 0,148,132
W 5
Mvvac 0,152,136
E 113
Mvvac 8,152,152
Rv 148,197,9216
C 107,110,111
Mvvac 16,152,168
E 114
Mvvac 24,152,184
C 112,112,113
Mvvac 24,156,188
E 115
Mvvac 16,156,172
Rv 152,197,10240
Mvvac 8,156,156
E 116
Mvvac 0,156,140
W 5
Mvvan 4,160,128,128
E 117
Mvvan 12,160,144,132
Rv 156,197,11264
C 113,114,115
Mvvan 20,160,160,136
E 118
Mvvan 28,160,176,140
C 114,116,117
Mvvac 28,164,180
E 119
Mvvac 20,164,164
Rv 160,197,12288
Mvvac 12,164,148
E 120
Mvvac 4,164,132
W 5
Mvvac 4,168,136
E 121
Mvvac 12,168,152
C 115,118,119
Mvvac 20,168,168
Rv 164,197,13312
E 122
Mvvac 28,168,184
C 120,120,121
Mvvac 28,172,188
E 123
Mvvac 20,172,172
Mvvac 12,172,156
E 124
Mvvac 4,172,140
Rv 168,197,14336
W 5
Mvvan 32,144,128,128
E 125
Mvvan 40,144,144,132
C 121,122,123
Mvvan 48,144,160,136
E 126
Mvvan 56,144,176,140
C 122,124,125
Mvvac 56,148,180
Rv 172,197,15360
E 127
Mvvac 48,148,164
Mvvac 40,148,148
E 0
Mvvac 32,148,132
W 4
Mvvac 32,152,136
E 1
Mvvac 40,152,152
C 123,126,127
Mvvac 48,152,168
E 2
Mvvac 56,152,184
C 0,0,1
Mvvac 56,156,188
E 3
Mvvac 48,156,172
WV 8
B
Mvvac 40,156,156
Rv 144,198,16384
E 4
AM 58,0
Mvvac 32,156,140
Ds 196,24,56,0
W 15
Mvvan 36,160,128,128
Rv 148,198,17408
E 5
Mvvan 44,160,144,132
C 1,2,3
Mvvan 52,160,160,136
E 6
Mvvan 60,160,176,140
Rv 152,198,18432
C 2,4,5
Mvvac 60,164,180
E 7
Mvvac 52,164,164
Mvvac 44,164,148
Rv 156,198,19456
E 8
Mvvac 36,164,132
W 15
Mvvac 36,168,136
E 9
Mvvac 44,168,152
Rv 160,198,20480
C 3,6,7
Mvvac 52,168,168
E 10
Mvvac 60,168,184
C 8,8,9
Mvvac 60,172,188
Rv 164,198,21504
E 11
Mvvac 52,172,172
Ds 196,24,56,1024
Mvvac 44,172,156
E 12
Mvvac 36,172,140
Rv 168,198,22528
W 5
Mavvc 0,144,64
E 13
Mavvc 4,144,72
C 9,10,11
Mavvc 8,144,80
Rv 172,198,23552
E 14
Mavvc 12,144,88
C 10,12,13
Mavvc 28,148,88
E 15
Mavvc 24,148,80
Rv 144,198,24576
Mvvvc 176,192,64
Mavvc 20,148,72
E 16
Mavvc 16,148,64
W 5
Mavvc 32,152,64
E 17
Mavvc 36,152,72
Rv 148,198,25600
C 11,14,15
Mavvc 40,152,80
E 18
Mavvc 44,152,88
C 16,16,17
Ds 196,24,56,2048
Mavvc 60,156,88
E 19
Mavvc 56,156,80
Rv 152,198,26624
Mvvvc 180,192,72
Mavvc 52,156,72
E 20
Mavvc 48,156,64
W 5
Mavvc 64,160,64
E 21
Mavvc 68,160,72
Rv 156,198,27648
Mvvvc 184,192,80
C 17,18,19
Mavvc 72,160,80
E 22
Mavvc 76,160,88
C 18,20,21
Mavvc 92,164,88
E 23
Mavvc 88,164,80
Rv 160,198,28672
Mavvc 84,164,72
E 24
Mavvc 80,164,64
Mvvvc 188,192,88
W 5
Mavvc 96,168,64
E 25
Mavvc 100,168,72
C 19,22,23
Ds 196,24,56,3072
Mavvc 104,168,80
Rv 164,198,29696
E 26
Mavvc 108,168,88
C 24,24,25
AS 56,56,16384
Mavvc 124,172,88
E 27
Mavvc 120,172,80
)A"
R"A(Mavvc 116,172,72
E 28
Mavvc 112,172,64
Rv 168,198,30720
W 5
Mavvc 0,144,96
E 29
Mavvc 4,144,104
C 25,26,27
Mavvc 8,144,112
E 30
Mavvc 12,144,120
C 26,28,29
Mavvc 28,148,120
Rv 172,198,31744
E 31
Mavvc 24,148,112
Mvvvc 176,192,96
Mavvc 20,148,104
E 32
Mavvc 16,148,96
W 4
Mavvc 32,152,96
E 33
Mavvc 36,152,104
Mvvvc 180,192,104
C 27,30,31
Mavvc 40,152,112
E 34
Mavvc 44,152,120
C 32,32,33
Mavvc 60,156,120
E 35
Mavvc 56,156,112
WV 8
B
Mavvc 52,156,104
Rv 144,197,16384
E 36
AM 59,16384
Mavvc 48,156,96
Ds 196,28,57,0
W 15
Mavvc 64,160,96
Rv 148,197,17408
E 37
Mavvc 68,160,104
Mvvvc 184,192,112
C 33,34,35
Mavvc 72,160,112
E 38
Mavvc 76,160,120
Rv 152,197,18432
C 34,36,37
Ds 196,28,57,1024
Mavvc 92,164,120
E 39
Mavvc 88,164,112
Mavvc 84,164,104
Rv 156,197,19456
E 40
Mavvc 80,164,96
Mvvvc 188,192,120
Ds 196,28,57,2048
W 15
Mavvc 96,168,96
E 41
Mavvc 100,168,104
Rv 160,197,20480
C 35,38,39
Mavvc 104,168,112
E 42
Mavvc 108,168,120
C 40,40,41
Ds 196,28,57,3072
Mavvc 124,172,120
Rv 164,197,21504
E 43
Mavvc 120,172,112
AS 57,57,16384
Mavvc 116,172,104
E 44
Mavvc 112,172,96
Rv 168,197,22528
W 5
XAT0_B12%=:
AS 60,60,1
AT0_B12%=:
Mvvan 64,144,128,128
E 45
Mvvan 72,144,144,132
C 41,42,43
Mvvan 80,144,160,136
Rv 172,197,23552
E 46
Mvvan 88,144,176,140
C 42,44,45
Mvvac 88,148,180
E 47
Mvvac 80,148,164
Rv 144,197,24576
Mvvac 72,148,148
E 48
Mvvac 64,148,132
W 5
Mvvac 64,152,136
E 49
Mvvac 72,152,152
Rv 148,197,25600
C 43,46,47
Mvvac 80,152,168
E 50
Mvvac 88,152,184
C 48,48,49
Mvvac 88,156,188
E 51
Mvvac 80,156,172
Rv 152,197,26624
Mvvac 72,156,156
E 52
Mvvac 64,156,140
W 5
Mvvan 68,160,128,128
E 53
Mvvan 76,160,144,132
Rv 156,197,27648
C 49,50,51
Mvvan 84,160,160,136
E 54
Mvvan 92,160,176,140
C 50,52,53
Mvvac 92,164,180
E 55
Mvvac 84,164,164
Rv 160,197,28672
Mvvac 76,164,148
E 56
Mvvac 68,164,132
W 5
Mvvac 68,168,136
E 57
Mvvac 76,168,152
C 51,54,55
Mvvac 84,168,168
Rv 164,197,29696
E 58
Mvvac 92,168,184
C 56,56,57
Mvvac 92,172,188
E 59
Mvvac 84,172,172
Mvvac 76,172,156
E 60
Mvvac 68,172,140
Rv 168,197,30720
W 5
Mvvan 96,144,128,128
E 61
Mvvan 104,144,144,132
C 57,58,59
Mvvan 112,144,160,136
E 62
Mvvan 120,144,176,140
C 58,60,61
Mvvac 120,148,180
Rv 172,197,31744
E 63
Mvvac 112,148,164
Mvvac 104,148,148
E 64
Mvvac 96,148,132
W 4
Mvvac 96,152,136
E 65
Mvvac 104,152,152
C 59,62,63
Mvvac 112,152,168
E 66
Mvvac 120,152,184
C 64,64,65
Mvvac 120,156,188
E 67
Mvvac 112,156,172
WV 8
B
Mvvac 104,156,156
Rv 144,198,0
E 68
AM 58,16384
Mvvac 96,156,140
Ds 196,24,56,0
W 15
Mvvan 100,160,128,128
Rv 148,198,1024
E 69
Mvvan 108,160,144,132
C 65,66,67
Mvvan 116,160,160,136
E 70
Mvvan 124,160,176,140
Rv 152,198,2048
C 66,68,69
Mvvac 124,164,180
E 71
Mvvac 116,164,164
Mvvac 108,164,148
Rv 156,198,3072
E 72
Mvvac 100,164,132
W 15
Mvvac 100,168,136
E 73
Mvvac 108,168,152
Rv 160,198,4096
C 67,70,71
Mvvac 116,168,168
E 74
Mvvac 124,168,184
C 72,72,73
Mvvac 124,172,188
Rv 164,198,5120
E 75
Mvvac 116,172,172
Ds 196,24,56,1024
Mvvac 108,172,156
E 76
Mvvac 100,172,140
Rv 168,198,6144
W 5
Mavvc 0,144,0
E 77
Mavvc 4,144,8
C 73,74,75
Mavvc 8,144,16
Rv 172,198,7168
E 78
Mavvc 12,144,24
C 74,76,77
Mavvc 28,148,24
E 79
Mavvc 24,148,16
Rv 144,198,8192
Mvvvc 176,192,0
Mavvc 20,148,8
E 80
Mavvc 16,148,0
W 5
Mavvc 32,152,0
E 81
Mavvc 36,152,8
Rv 148,198,9216
C 75,78,79
Mavvc 40,152,16
E 82
Mavvc 44,152,24
C 80,80,81
Ds 196,24,56,2048
Mavvc 60,156,24
E 83
Mavvc 56,156,16
Rv 152,198,10240
Mvvvc 180,192,8
Mavvc 52,156,8
E 84
Mavvc 48,156,0
W 5
Mavvc 64,160,0
E 85
Mavvc 68,160,8
Rv 156,198,11264
Mvvvc 184,192,16
C 81,82,83
Mavvc 72,160,16
E 86
Mavvc 76,160,24
C 82,84,85
Mavvc 92,164,24
E 87
Mavvc 88,164,16
Rv 160,198,12288
Mavvc 84,164,8
E 88
Mavvc 80,164,0
Mvvvc 188,192,24
W 5
Mavvc 96,168,0
E 89
Mavvc 100,168,8
C 83,86,87
Ds 196,24,56,3072
Mavvc 104,168,16
Rv 164,198,13312
E 90
Mavvc 108,168,24
C 88,88,89
AS 56,56,16384
Mavvc 124,172,24
E 91
Mavvc 120,172,16
Mavvc 116,172,8
E 92
Mavvc 112,172,0
Rv 168,198,14336
W 5
Mavvc 0,144,32
E 93
Mavvc 4,144,40
C 89,90,91
Mavvc 8,144,48
E 94
Mavvc 12,144,56
C 90,92,93
Mavvc 28,148,56
Rv 172,198,15360
E 95
Mavvc 24,148,48
Mvvvc 176,192,32
Mavvc 20,148,40
E 96
Mavvc 16,148,32
W 4
Mavvc 32,152,32
E 97
Mavvc 36,152,40
Mvvvc 180,192,40
C 91,94,95
Mavvc 40,152,48
E 98
Mavvc 44,152,56
C 96,96,97
Mavvc 60,156,56
E 99
Mavvc 56,156,48
WV 8
B
Mavvc 52,156,40
Rv 144,197,0
E 100
AM 59,0
Mavvc 48,156,32
Ds 196,28,57,0
W 15
Mavvc 64,160,32
Rv 148,197,1024
E 101
Mavvc 68,160,40
Mvvvc 184,192,48
C 97,98,99
Mavvc 72,160,48
E 102
Mavvc 76,160,56
Rv 152,197,2048
C 98,100,101
Ds 196,28,57,1024
Mavvc 92,164,56
E 103
Mavvc 88,164,48
Mavvc 84,164,40
Rv 156,197,3072
E 104
Mavvc 80,164,32
Mvvvc 188,192,56
Ds 196,28,57,2048
W 15
Mavvc 96,168,32
E 105
Mavvc 100,168,40
Rv 160,197,4096
C 99,102,103
Mavvc 104,168,48
E 106
Mavvc 108,168,56
C 104,104,105
Ds 196,28,57,3072
Mavvc 124,172,56
Rv 164,197,5120
E 107
Mavvc 120,172,48
AS 57,57,16384
Mavvc 116,172,40
E 108
Mavvc 112,172,32
Rv 168,197,6144
W 5
s_bitcmp1_b32 s76, s60
s_cbranch_scc1 XAT0_B9%=
s_branch XAT0_B11%=
XAT0_B13%=:
AS 60,60,1
AT0_B13%=:
Mvvan 64,144,128,128
E 45
Mvvan 72,144,144,132
C 41,42,43
Mvvan 80,144,160,136
Rv 172,197,23552
E 46
Mvvan 88,144,176,140
C 42,44,45
Mvvac 88,148,180
E 47
Mvvac 80,148,164
Rv 144,197,24576
Mvvac 72,148,148
E 48
Mvvac 64,148,132
W 5
Mvvac 64,152,136
E 49
Mvvac 72,152,152
Rv 148,197,25600
C 43,46,47
Mvvac 80,152,168
E 50
Mvvac 88,152,184
C 48,48,49
Mvvac 88,156,188
E 51
Mvvac 80,156,172
Rv 152,197,26624
Mvvac 72,156,156
E 52
Mvvac 64,156,140
W 5
Mvvan 68,160,128,128
E 53
Mvvan 76,160,144,132
Rv 156,197,27648
C 49,50,51
Mvvan 84,160,160,136
E 54
Mvvan 92,160,176,140
C 50,52,53
Mvvac 92,164,180
E 55
Mvvac 84,164,164
Rv 160,197,28672
Mvvac 76,164,148
E 56
Mvvac 68,164,132
W 5
Mvvac 68,168,136
E 57
Mvvac 76,168,152
C 51,54,55
Mvvac 84,168,168
Rv 164,197,29696
E 58
Mvvac 92,168,184
C 56,56,57
Mvvac 92,172,188
E 59
Mvvac 84,172,172
Mvvac 76,172,156
E 60
Mvvac 68,172,140
Rv 168,197,30720
W 5
Mvvan 96,144,128,128
E 61
Mvvan 104,144,144,132
C 57,58,59
Mvvan 112,144,160,136
E 62
Mvvan 120,144,176,140
C 58,60,61
Mvvac 120,148,180
Rv 172,197,31744
E 63
Mvvac 112,148,164
Mvvac 104,148,148
E 64
Mvvac 96,148,132
W 4
Mvvac 96,152,136
E 65
Mvvac 104,152,152
C 59,62,63
Mvvac 112,152,168
E 66
Mvvac 120,152,184
C 64,64,65
Mvvac 120,156,188
E 67
Mvvac 112,156,172
WV 15
B
Mvvac 104,156,156
Rv 144,198,0
E 68
s_mul_i32 s21, s10, 68
Mvvac 96,156,140
W 15
AS 21,21,0
Mvvan 100,160,128,128
Rv 148,198,1024
E 69
v_min_u32 v249, 63, v254
Mvvan 108,160,144,132
C 65,66,67
v_add_u32 v249, s21, v249
Mvvan 116,160,160,136
E 70
v_min_u32 v249, 17279, v249
Mvvan 124,160,176,140
Rv 152,198,2048
C 66,68,69
v_lshlrev_b32 v249, 7, v249
Mvvac 124,164,180
E 71
global_load_dword v248, v249, s[38:39]
Mvvac 116,164,164
s_mul_i32 s21, s10, 68
Mvvac 108,164,148
Rv 156,198,3072
E 72
AS 21,21,64
Mvvac 100,164,132
W 15
v_min_u32 v249, 3, v254
Mvvac 100,168,136
E 73
v_add_u32 v249, s21, v249
Mvvac 108,168,152
Rv 160,198,4096
C 67,70,71
v_min_u32 v249, 17279, v249
Mvvac 116,168,168
E 74
v_lshlrev_b32 v249, 7, v249
Mvvac 124,168,184
C 72,72,73
global_load_dword v248, v249, s[38:39]
Mvvac 124,172,188
Rv 164,198,5120
E 75
Mvvac 116,172,172
Mvvac 108,172,156
E 76
Mvvac 100,172,140
Rv 168,198,6144
W 5
Mavvc 0,144,0
E 77
Mavvc 4,144,8
C 73,74,75
Mavvc 8,144,16
Rv 172,198,7168
E 78
Mavvc 12,144,24
C 74,76,77
Mavvc 28,148,24
E 79
Mavvc 24,148,16
Rv 144,198,8192
Mvvvc 176,192,0
Mavvc 20,148,8
E 80
Mavvc 16,148,0
W 5
Mavvc 32,152,0
E 81
Mavvc 36,152,8
Rv 148,198,9216
C 75,78,79
Mavvc 40,152,16
E 82
Mavvc 44,152,24
C 80,80,81
Mavvc 60,156,24
E 83
Mavvc 56,156,16
Rv 152,198,10240
Mvvvc 180,192,8
Mavvc 52,156,8
E 84
Mavvc 48,156,0
W 5
Mavvc 64,160,0
E 85
Mavvc 68,160,8
Rv 156,198,11264
Mvvvc 184,192,16
C 81,82,83
Mavvc 72,160,16
E 86
Mavvc 76,160,24
C 82,84,85
Mavvc 92,164,24
E 87
Mavvc 88,164,16
Rv 160,198,12288
Mavvc 84,164,8
E 88
Mavvc 80,164,0
Mvvvc 188,192,24
W 5
Mavvc 96,168,0
E 89
Mavvc 100,168,8
C 83,86,87
Mavvc 104,168,16
Rv 164,198,13312
E 90
Mavvc 108,168,24
C 88,88,89
Mavvc 124,172,24
E 91
Mavvc 120,172,16
Mavvc 116,172,8
E 92
Mavvc 112,172,0
Rv 168,198,14336
W 5
Mavvc 0,144,32
E 93
Mavvc 4,144,40
C 89,90,91
Mavvc 8,144,48
E 94
Mavvc 12,144,56
C 90,92,93
Mavvc 28,148,56
Rv 172,198,15360
E 95
Mavvc 24,148,48
Mvvvc 176,192,32
Mavvc 20,148,40
E 96
Mavvc 16,148,32
W 4
Mavvc 32,152,32
E 97
Mavvc 36,152,40
Mvvvc 180,192,40
C 91,94,95
Mavvc 40,152,48
E 98
Mavvc 44,152,56
C 96,96,97
Mavvc 60,156,56
E 99
Mavvc 56,156,48
WV 6
B
AS 57,57,0
SM 59,65536
Mavvc 52,156,40
Rv 144,197,0
E 100
AM 59,0
Mavvc 48,156,32
Ds 196,28,57,0
W 15
Mavvc 64,160,32
Rv 148,197,1024
E 101
Mavvc 68,160,40
Mvvvc 184,192,48
C 97,98,99
Mavvc 72,160,48
E 102
Mavvc 76,160,56
Rv 152,197,2048
C 98,100,101
Ds 196,28,57,1024
Mavvc 92,164,56
E 103
Mavvc 88,164,48
Mavvc 84,164,40
Rv 156,197,3072
E 104
Mavvc 80,164,32
Mvvvc 188,192,56
Ds 196,28,57,2048
W 15
Mavvc 96,168,32
E 105
Mavvc 100,168,40
Rv 160,197,4096
C 99,102,103
Mavvc 104,168,48
E 106
Mavvc 108,168,56
C 104,104,105
Ds 196,28,57,3072
Mavvc 124,172,56
Rv 164,197,5120
E 107
Mavvc 120,172,48
Mavvc 116,172,40
E 108
Mavvc 112,172,32
Rv 168,197,6144
W 5
s_branch XAT0_B5%=
XAT0_FIN%=:
AS 60,60,1
AT0_FIN%=:
N 7
WV 0
B
VA 200,0,196
DWa 200,64,0
DWa 200,68,1024
DWa 200,72,2048
DWa 200,76,3072
DWa 200,80,4096
DWa 200,84,5120
DWa 200,88,6144
DWa 200,92,7168
DWa 200,96,8192
DWa 200,100,9216
DWa 200,104,10240
DWa 200,108,11264
DWa 200,112,12288
DWa 200,116,13312
DWa 200,120,14336
DWa 200,124,15360
v_mov_b32 v231, 0x5d800000
v_cmp_nle_f32 vcc, v176, v231
N 3
s_or_b64 s[70:71], s[70:71], vcc
v_cmp_nle_f32 vcc, v180, v231
N 3
s_or_b64 s[70:71], s[70:71], vcc
v_cmp_nle_f32 vcc, v184, v231
N 3
s_or_b64 s[70:71], s[70:71], vcc
v_cmp_nle_f32 vcc, v188, v231
N 3
s_or_b64 s[70:71], s[70:71], vcc
s_cmp_lg_u64 s[70:71], 0
s_cselect_b32 s16, 1, 0
v_mov_b32 v202, s16
v_mov_b32 v203, 149504
ds_write_b32 v203, v202
MV 204,128
MV 208,176
MV 205,132
MV 209,180
MV 206,136
MV 210,184
MV 207,140
MV 211,188
VA 201,155648,196
ds_write_b128 v201, v[204:207]
DWv 201,208,1024
s_sub_u32 s16, 15, s13
s_lshr_b32 s17, s16, 1
s_lshl_b32 s17, s17, 21
s_and_b32 s16, s16, 1
s_lshl_b32 s16, s16, 13
s_add_u32 s17, s17, s16
s_lshl_b32 s16, s12, 16
s_add_u32 s17, s17, s16
AS 72,17,0
W 0
B
VA 201,159744,196
ds_read_b128 v[212:215], v201
Rv 216,201,1024
v_mov_b32 v203, 149504
ds_read_b128 v[64:67], v203
ds_read_b64 v[68:69], v203 offset:16
VA 200,32768,196
Rv 0,200,0
Rv 4,200,1024
Rv 8,200,2048
Rv 12,200,3072
Rv 16,200,4096
Rv 20,200,5120
Rv 24,200,6144
Rv 28,200,7168
Rv 32,200,8192
Rv 36,200,9216
Rv 40,200,10240
Rv 44,200,11264
Rv 48,200,12288
Rv 52,200,13312
Rv 56,200,14336
Rv 60,200,15360
v_mov_b32 v231, 0x5d800000
W 15
v_min_f32 v201, v204, v212
Fsu 204,201,204
Fsu 212,201,212
E 204
E 212
Fmu 202,208,204
W 15
Ffm 202,216,212
v_rcp_f32 v202, v202
N 0
Fmu 220,204,202
Fmu 222,212,202
v_min_f32 v201, v205, v213
Fsu 205,201,205
Fsu 213,201,213
E 205
E 213
Fmu 202,209,205
Ffm 202,217,213
v_rcp_f32 v202, v202
N 0
Fmu 230,205,202
Fmu 224,213,202
v_min_f32 v201, v206, v214
Fsu 206,201,206
Fsu 214,201,214
E 206
E 214
Fmu 202,210,206
Ffm 202,218,214
v_rcp_f32 v202, v202
N 0
Fmu 128,206,202
Fmu 226,214,202
v_min_f32 v201, v207, v215
Fsu 207,201,207
Fsu 215,201,215
E 207
E 215
Fmu 202,211,207
Ffm 202,219,215
v_rcp_f32 v202, v202
N 0
Fmu 130,207,202
Fmu 228,215,202
W 15
v_or_b32 v64, v64, v65
v_or_b32 v66, v66, v67
v_or_b32 v64, v64, v66
W 15
v_or_b32 v68, v68, v69
v_or_b32 v65, v64, v68
N 0
v_readfirstlane_b32 s16, v65
v_readfirstlane_b32 s17, v64
N 3
s_cmp_lg_u32 s16, 0
s_cbranch_scc1 GOSLOW0%=
FASTM0%=:
G 64,0
G 68,16
G 65,1
G 69,17
G 66,2
G 70,18
G 67,3
G 71,19
PM 64,64,220
PM 66,66,220
PM 68,68,220
PM 70,70,220
W 15
v_pk_fma_f32 v[64:65], v[0:1], v[222:223], v[64:65] op_sel_hi:[1,0,1]
W 11
v_pk_fma_f32 v[68:69], v[16:17], v[222:223], v[68:69] op_sel_hi:[1,0,1]
v_pk_fma_f32 v[66:67], v[2:3], v[222:223], v[66:67] op_sel_hi:[1,0,1]
v_pk_fma_f32 v[70:71], v[18:19], v[222:223], v[70:71] op_sel_hi:[1,0,1]
C 72,64,65
C 73,66,67
C 74,68,69
)A"
R"A(C 75,70,71
Svs 72,196,32,72,0
G 76,4
G 80,20
G 77,5
G 81,21
G 78,6
G 82,22
G 79,7
G 83,23
PM 76,76,230
PM 78,78,230
PM 80,80,230
PM 82,82,230
v_pk_fma_f32 v[76:77], v[4:5], v[224:225], v[76:77] op_sel_hi:[1,0,1]
W 10
v_pk_fma_f32 v[80:81], v[20:21], v[224:225], v[80:81] op_sel_hi:[1,0,1]
v_pk_fma_f32 v[78:79], v[6:7], v[224:225], v[78:79] op_sel_hi:[1,0,1]
v_pk_fma_f32 v[82:83], v[22:23], v[224:225], v[82:83] op_sel_hi:[1,0,1]
C 84,76,77
C 85,78,79
C 86,80,81
C 87,82,83
Svs 84,196,32,72,1024
G 88,8
G 92,24
G 89,9
G 93,25
G 90,10
G 94,26
G 91,11
G 95,27
PM 88,88,128
PM 90,90,128
PM 92,92,128
PM 94,94,128
v_pk_fma_f32 v[88:89], v[8:9], v[226:227], v[88:89] op_sel_hi:[1,0,1]
W 9
v_pk_fma_f32 v[92:93], v[24:25], v[226:227], v[92:93] op_sel_hi:[1,0,1]
v_pk_fma_f32 v[90:91], v[10:11], v[226:227], v[90:91] op_sel_hi:[1,0,1]
v_pk_fma_f32 v[94:95], v[26:27], v[226:227], v[94:95] op_sel_hi:[1,0,1]
C 96,88,89
C 97,90,91
C 98,92,93
C 99,94,95
Svs 96,196,32,72,2048
G 100,12
G 104,28
G 101,13
G 105,29
G 102,14
G 106,30
G 103,15
G 107,31
PM 100,100,130
PM 102,102,130
PM 104,104,130
PM 106,106,130
v_pk_fma_f32 v[100:101], v[12:13], v[228:229], v[100:101] op_sel_hi:[1,0,1]
W 8
v_pk_fma_f32 v[104:105], v[28:29], v[228:229], v[104:105] op_sel_hi:[1,0,1]
v_pk_fma_f32 v[102:103], v[14:15], v[228:229], v[102:103] op_sel_hi:[1,0,1]
v_pk_fma_f32 v[106:107], v[30:31], v[228:229], v[106:107] op_sel_hi:[1,0,1]
C 108,100,101
C 109,102,103
C 110,104,105
C 111,106,107
Svs 108,196,32,72,3072
AS 72,72,16384
G 64,32
G 68,48
G 65,33
G 69,49
G 66,34
G 70,50
G 67,35
G 71,51
PM 64,64,220
PM 66,66,220
PM 68,68,220
PM 70,70,220
W 7
v_pk_fma_f32 v[64:65], v[32:33], v[222:223], v[64:65] op_sel_hi:[1,0,1]
W 3
v_pk_fma_f32 v[68:69], v[48:49], v[222:223], v[68:69] op_sel_hi:[1,0,1]
v_pk_fma_f32 v[66:67], v[34:35], v[222:223], v[66:67] op_sel_hi:[1,0,1]
v_pk_fma_f32 v[70:71], v[50:51], v[222:223], v[70:71] op_sel_hi:[1,0,1]
C 72,64,65
C 73,66,67
C 74,68,69
C 75,70,71
Svs 72,196,32,72,0
G 76,36
G 80,52
G 77,37
G 81,53
G 78,38
G 82,54
G 79,39
G 83,55
PM 76,76,230
PM 78,78,230
PM 80,80,230
PM 82,82,230
v_pk_fma_f32 v[76:77], v[36:37], v[224:225], v[76:77] op_sel_hi:[1,0,1]
W 2
v_pk_fma_f32 v[80:81], v[52:53], v[224:225], v[80:81] op_sel_hi:[1,0,1]
v_pk_fma_f32 v[78:79], v[38:39], v[224:225], v[78:79] op_sel_hi:[1,0,1]
v_pk_fma_f32 v[82:83], v[54:55], v[224:225], v[82:83] op_sel_hi:[1,0,1]
C 84,76,77
C 85,78,79
C 86,80,81
C 87,82,83
Svs 84,196,32,72,1024
G 88,40
G 92,56
G 89,41
G 93,57
G 90,42
G 94,58
G 91,43
G 95,59
PM 88,88,128
PM 90,90,128
PM 92,92,128
PM 94,94,128
v_pk_fma_f32 v[88:89], v[40:41], v[226:227], v[88:89] op_sel_hi:[1,0,1]
W 1
v_pk_fma_f32 v[92:93], v[56:57], v[226:227], v[92:93] op_sel_hi:[1,0,1]
v_pk_fma_f32 v[90:91], v[42:43], v[226:227], v[90:91] op_sel_hi:[1,0,1]
v_pk_fma_f32 v[94:95], v[58:59], v[226:227], v[94:95] op_sel_hi:[1,0,1]
C 96,88,89
C 97,90,91
C 98,92,93
C 99,94,95
Svs 96,196,32,72,2048
G 100,44
G 104,60
G 101,45
G 105,61
G 102,46
G 106,62
G 103,47
G 107,63
PM 100,100,130
PM 102,102,130
PM 104,104,130
PM 106,106,130
v_pk_fma_f32 v[100:101], v[44:45], v[228:229], v[100:101] op_sel_hi:[1,0,1]
W 0
v_pk_fma_f32 v[104:105], v[60:61], v[228:229], v[104:105] op_sel_hi:[1,0,1]
v_pk_fma_f32 v[102:103], v[46:47], v[228:229], v[102:103] op_sel_hi:[1,0,1]
v_pk_fma_f32 v[106:107], v[62:63], v[228:229], v[106:107] op_sel_hi:[1,0,1]
C 108,100,101
C 109,102,103
C 110,104,105
C 111,106,107
Svs 108,196,32,72,3072
s_getpc_b64 s[16:17]
1:
s_add_u32 s16, s16, AE0%=-1b
s_addc_u32 s17, s17, 0
s_lshr_b32 s21, s10, 3
v_lshlrev_b32 v249, 5, v254
v_add_u32 v249, s21, v249
v_min_u32 v249, ((OE0%=-AE0%=)>>7), v249
v_lshlrev_b32 v249, 7, v249
global_load_dword v248, v249, s[16:17]
s_and_b32 s16, s10, 7
s_lshr_b32 s41, s16, 1
s_and_b32 s16, s16, 1
s_lshl_b32 s16, s16, 3
s_lshr_b32 s17, s10, 3
s_lshr_b32 s18, s17, 2
s_add_u32 s39, s16, s18
s_and_b32 s40, s17, 3
s_lshl_b32 s16, s41, 2
s_add_u32 s43, s16, s40
s_mov_b64 s[24:25], s[50:51]
SM 26,0x500000
SM 27,0x20000
s_lshl_b32 s16, s41, 10
s_lshl_b32 s17, s11, 5
s_add_u32 s16, s16, s17
s_mul_i32 s16, s16, 1280
s_mul_i32 s17, s40, 320
s_add_u32 s36, s16, s17
AE0%=:
s_waitcnt vmcnt(0)
MDONE0%=:
s_sub_u32 s23, 15, s13
s_lshl_b32 s23, s23, 2
s_lshr_b32 s22, s12, 3
s_add_u32 s23, s23, s22
s_and_b32 s19, s12, 7
s_lshl_b32 s19, s19, 2
AS 19,19,0
s_lshl_b32 s21, s23, 8
AS 21,21,32768
s_lshl_b32 s22, s19, 2
s_add_u32 s21, s21, s22
v_mov_b32 v247, s21
v_mov_b32 v246, s79
s_mov_b64 exec, 1
global_store_dword v247, v246, s[14:15] sc1
s_mov_b64 exec, -1
N 3
s_mov_b32 s23, s13
s_lshl_b32 s23, s23, 2
s_lshr_b32 s22, s12, 3
s_add_u32 s23, s23, s22
s_lshl_b32 s21, s23, 8
AS 21,21,32768
s_and_b32 s22, s12, 7
s_lshl_b32 s22, s22, 4
s_add_u32 s21, s21, s22
v_lshlrev_b32 v247, 2, v254
v_add_u32 v247, s21, v247
v_mov_b32 v246, s79
s_mov_b64 exec, 15
global_store_dword v247, v246, s[14:15] sc1
s_mov_b64 exec, -1
N 3
AS 42,36,0
M0 65536
N 0
D 232,24,42,0
D 233,24,42,1024
D 234,24,42,2048
D 235,24,42,3072
M0 69632
N 0
D 236,24,42,0
AS 42,36,20480
M0 70656
N 0
D 232,24,42,0
D 233,24,42,1024
D 234,24,42,2048
D 235,24,42,3072
M0 74752
N 0
D 236,24,42,0
s_add_u32 s28, s14, 0x2400000
s_addc_u32 s29, s15, 0
SM 30,0x1000000
SM 31,0x20000
s_add_u32 s32, s14, 0x3400000
s_addc_u32 s33, s15, 0
SM 34,0xa00000
SM 35,0x20000
v_lshrrev_b32 v117, 4, v254
v_and_b32 v118, 15, v254
v_lshrrev_b32 v118, 2, v118
v_lshl_add_u32 v118, v117, 2, v118
v_mul_u32_u24 v118, 320, v118
v_and_b32 v119, 3, v254
v_lshl_add_u32 v118, v119, 3, v118
v_and_b32 v117, 1, v117
v_lshlrev_b32 v117, 5, v117
v_add_u32 v119, v118, v117
v_sub_u32 v120, v118, v117
VA 122,65536,119
VA 123,65536,120
VA 124,106496,119
VA 125,106496,120
VA 126,0,119
VA 127,0,120
s_lshr_b32 s16, s39, 1
s_lshl_b32 s16, s16, 21
s_lshl_b32 s17, s41, 19
s_add_u32 s16, s16, s17
s_and_b32 s17, s39, 1
s_lshl_b32 s17, s17, 13
s_add_u32 s16, s16, s17
s_lshl_b32 s17, s11, 11
s_add_u32 s37, s16, s17
s_lshl_b32 s16, s41, 4
s_add_u32 s16, s16, s39
s_lshl_b32 s16, s16, 3
s_lshl_b32 s17, s11, 1
s_add_u32 s16, s16, s17
s_mul_i32 s16, s16, 20480
s_mul_i32 s17, s40, 5120
s_add_u32 s38, s16, s17
s_load_dwordx2 s[44:45], s[8:9], 0
SM 46,0x280000
SM 47,0x20000
s_load_dwordx2 s[48:49], s[8:9], 48
SM 50,0x500
SM 51,0x20000
s_load_dwordx2 s[52:53], s[8:9], 80
SM 54,0x280000
SM 55,0x20000
v_lshrrev_b32 v104, 5, v254
v_lshlrev_b32 v104, 5, v104
v_lshrrev_b32 v105, 3, v254
v_and_b32 v105, 3, v105
v_lshl_add_u32 v101, v105, 3, v104
v_and_b32 v106, 7, v254
v_mul_u32_u24 v106, 1280, v106
v_add_u32 v100, v106, v101
s_lshl_b32 s16, s39, 7
s_lshl_b32 s17, s43, 3
s_add_u32 s16, s16, s17
s_mov_b32 s66, s16
s_mul_i32 s16, s16, 1280
s_mul_i32 s17, s11, 320
s_add_u32 s65, s16, s17
s_mov_b32 s67, s17
v_lshrrev_b32 v104, 5, v254
v_mul_u32_u24 v104, 81920, v104
v_lshlrev_b32 v104, 6, v104
v_and_b32 v106, 7, v254
v_lshl_add_u32 v106, v105, 4, v106
s_and_b32 s16, s43, 1
s_lshl_b32 s16, s16, 3
v_add_u32 v106, s16, v106
v_lshl_add_u32 v102, v106, 4, v104
s_lshl_b32 s16, s39, 3
s_lshr_b32 s17, s43, 1
s_add_u32 s16, s16, s17
s_mul_i32 s16, s16, 20480
s_mul_i32 s17, s11, 5120
s_add_u32 s64, s16, s17
W 0
s_load_dwordx2 s[70:71], s[8:9], 56
s_load_dwordx2 s[76:77], s[8:9], 64
s_load_dwordx2 s[72:73], s[8:9], 72
SM 74,0x21c000
SM 75,0x20000
W 0
s_lshl_b32 s23, s39, 2
s_add_u32 s23, s23, s41
s_lshl_b32 s21, s23, 8
AS 21,21,32768
v_mov_b32 v247, s21
v_min_u32 v246, 31, v254
v_lshl_add_u32 v247, v246, 2, v247
global_load_dword v246, v247, s[14:15] sc1
s_waitcnt vmcnt(0)
v_cmp_ne_u32 vcc, s79, v246
N 4
s_cbranch_vccnz CX_SPIN%=
CX_OK%=:
B
s_lshr_b32 s78, s10, 1
s_and_b32 s16, s10, 1
s_cselect_b32 s16, 54, s78
s_cmp_lt_u32 s16, 54
s_cbranch_scc0 OMN0%=
s_mov_b64 s[56:57], s[70:71]
SM 58,0x21c000
SM 59,0x20000
s_mov_b64 s[60:61], s[76:77]
SM 62,0x21c000
SM 63,0x20000
v_lshrrev_b32 v99, 2, v254
v_mul_u32_u24 v99, 3456, v99
v_and_b32 v98, 3, v254
v_lshl_add_u32 v99, v98, 4, v99
s_lshl_b32 s22, s78, 6
AS 22,22,0
AS 21,37,0
Las 80,196,28,21,0
Las 84,196,28,21,1024
AS 21,37,16384
Las 88,196,28,21,0
Las 92,196,28,21,1024
AS 21,37,32768
Las 96,196,28,21,0
Las 100,196,28,21,1024
AS 21,37,49152
Las 104,196,28,21,0
Las 108,196,28,21,1024
AS 21,37,65536
Las 112,196,28,21,0
Las 116,196,28,21,1024
AS 21,37,81920
Las 120,196,28,21,0
Las 124,196,28,21,1024
AS 42,36,163840
M0 106496
N 0
D 232,24,42,0
D 233,24,42,1024
D 234,24,42,2048
D 235,24,42,3072
M0 110592
N 0
D 236,24,42,0
AS 42,36,184320
M0 111616
N 0
D 232,24,42,0
D 233,24,42,1024
D 234,24,42,2048
D 235,24,42,3072
M0 115712
N 0
D 236,24,42,0
W 0
B
T 0,122,0
T 2,122,5120
T 4,123,32
T 6,123,5152
T 8,122,64
T 10,122,5184
T 12,123,96
T 14,123,5216
T 16,122,128
T 18,122,5248
T 20,123,160
T 22,123,5280
T 24,122,192
T 26,122,5312
T 28,123,224
T 30,123,5344
T 32,122,256
T 34,122,5376
T 36,123,288
T 38,123,5408
WV 20
Mavaz 0,0,80
AS 42,36,327680
M0 0
Mavaz 4,0,84
T 40,122,10240
D 232,24,42,0
Mavaz 12,4,84
T 42,122,15360
T 44,123,10272
Mavaz 8,4,80
T 46,123,15392
D 233,24,42,1024
Mavaz 16,8,80
T 48,122,10304
Mavaz 20,8,84
T 50,122,15424
T 52,123,10336
Mavaz 28,12,84
T 54,123,15456
D 234,24,42,2048
Mavaz 24,12,80
T 56,122,10368
T 58,122,15488
Mavaz 32,16,80
D 235,24,42,3072
Mavaz 36,16,84
T 60,123,10400
T 62,123,15520
Mavaz 44,20,84
T 64,122,10432
M0 4096
Mavaz 40,20,80
T 66,122,15552
D 236,24,42,0
Mavaz 48,24,80
T 68,123,10464
AS 21,37,98304
Mavaz 52,24,84
T 70,123,15584
Las 128,196,28,21,0
Mavaz 60,28,84
T 72,122,10496
Mavaz 56,28,80
T 74,122,15616
T 76,123,10528
Mavaz 64,32,80
T 78,123,15648
Las 132,196,28,21,1024
Mavaz 68,32,84
Mavaz 76,36,84
Mavaz 72,36,80
WV 25
Mavac 0,40,88
AS 42,36,348160
M0 5120
Mavac 4,40,92
T 0,122,20480
D 232,24,42,0
Mavac 12,44,92
T 2,122,25600
T 4,123,20512
Mavac 8,44,88
T 6,123,25632
D 233,24,42,1024
Mavac 16,48,88
T 8,122,20544
Mavac 20,48,92
T 10,122,25664
T 12,123,20576
Mavac 28,52,92
T 14,123,25696
D 234,24,42,2048
Mavac 24,52,88
T 16,122,20608
T 18,122,25728
Mavac 32,56,88
D 235,24,42,3072
Mavac 36,56,92
T 20,123,20640
T 22,123,25760
Mavac 44,60,92
T 24,122,20672
M0 9216
Mavac 40,60,88
T 26,122,25792
D 236,24,42,0
Mavac 48,64,88
T 28,123,20704
AS 21,37,114688
Mavac 52,64,92
T 30,123,25824
Las 136,196,28,21,0
Mavac 60,68,92
T 32,122,20736
Mavac 56,68,88
T 34,122,25856
T 36,123,20768
Mavac 64,72,88
T 38,123,25888
Las 140,196,28,21,1024
Mavac 68,72,92
Mavac 76,76,92
Mavac 72,76,88
WV 30
Mavac 0,0,96
T 40,122,30720
AS 21,37,131072
Mavac 4,0,100
T 42,122,35840
Las 80,196,28,21,0
Mavac 12,4,100
T 44,123,30752
Mavac 8,4,96
T 46,123,35872
Mavac 16,8,96
T 48,122,30784
Mavac 20,8,100
T 50,122,35904
Mavac 28,12,100
T 52,123,30816
T 54,123,35936
Mavac 24,12,96
T 56,122,30848
Las 84,196,28,21,1024
Mavac 32,16,96
T 58,122,35968
Mavac 36,16,100
T 60,123,30880
Mavac 44,20,100
T 62,123,36000
Mavac 40,20,96
T 64,122,30912
Mavac 48,24,96
T 66,122,36032
T 68,123,30944
Mavac 52,24,100
T 70,123,36064
La 144,99,56,22,0
Mavac 60,28,100
T 72,122,30976
Mavac 56,28,96
T 74,122,36096
Mavac 64,32,96
T 76,123,31008
Mavac 68,32,100
T 78,123,36128
Mavac 76,36,100
Mavac 72,36,96
WV 17
B
Mavac 0,40,104
AS 42,36,491520
M0 65536
Mavac 4,40,108
T 0,124,0
D 232,24,42,0
Mavac 12,44,108
T 2,124,5120
T 4,125,32
Mavac 8,44,104
T 6,125,5152
D 233,24,42,1024
Mavac 16,48,104
T 8,124,64
T 10,124,5184
Mavac 20,48,108
T 12,125,96
D 234,24,42,2048
Mavac 28,52,108
T 14,125,5216
T 16,124,128
Mavac 24,52,104
D 235,24,42,3072
Mavac 32,56,104
T 18,124,5248
T 20,125,160
Mavac 36,56,108
T 22,125,5280
M0 69632
Mavac 44,60,108
T 24,124,192
D 236,24,42,0
Mavac 40,60,104
T 26,124,5312
AS 21,37,147456
Mavac 48,64,104
T 28,125,224
Las 88,196,28,21,0
Mavac 52,64,108
T 30,125,5344
T 32,124,256
Mavac 60,68,108
T 34,124,5376
Las 92,196,28,21,1024
Mavac 56,68,104
T 36,125,288
T 38,125,5408
Mavac 64,72,104
La 148,99,60,22,0
Mavac 68,72,108
Mavac 76,76,108
Mavac 72,76,104
W 0
Mavac 0,0,112
T 40,124,10240
AS 21,37,163840
Mavac 4,0,116
T 42,124,15360
Las 96,196,28,21,0
Mavac 12,4,116
T 44,125,10272
Mavac 8,4,112
T 46,125,15392
Mavac 16,8,112
T 48,124,10304
Mavac 20,8,116
T 50,124,15424
Mavac 28,12,116
T 52,125,10336
T 54,125,15456
)A"
R"A(Mavac 24,12,112
T 56,124,10368
Las 100,196,28,21,1024
Mavac 32,16,112
T 58,124,15488
Mavac 36,16,116
T 60,125,10400
Mavac 44,20,116
T 62,125,15520
Mavac 40,20,112
T 64,124,10432
T 66,124,15552
Mavac 48,24,112
T 68,125,10464
AS 22,22,55296
Mavac 52,24,116
T 70,125,15584
La 152,99,56,22,0
Mavac 60,28,116
T 72,124,10496
Mavac 56,28,112
T 74,124,15616
Mavac 64,32,112
T 76,125,10528
Mavac 68,32,116
T 78,125,15648
Mavac 76,36,116
Mavac 72,36,112
W 0
Mavac 0,40,120
AS 42,36,512000
M0 70656
Mavac 4,40,124
T 0,124,20480
D 232,24,42,0
Mavac 12,44,124
T 2,124,25600
T 4,125,20512
Mavac 8,44,120
T 6,125,25632
D 233,24,42,1024
Mavac 16,48,120
T 8,124,20544
T 10,124,25664
Mavac 20,48,124
T 12,125,20576
D 234,24,42,2048
Mavac 28,52,124
T 14,125,25696
T 16,124,20608
Mavac 24,52,120
D 235,24,42,3072
Mavac 32,56,120
T 18,124,25728
T 20,125,20640
Mavac 36,56,124
T 22,125,25760
M0 74752
Mavac 44,60,124
T 24,124,20672
D 236,24,42,0
Mavac 40,60,120
T 26,124,25792
AS 21,37,180224
Mavac 48,64,120
T 28,125,20704
Las 104,196,28,21,0
Mavac 52,64,124
T 30,125,25824
T 32,124,20736
Mavac 60,68,124
T 34,124,25856
Las 108,196,28,21,1024
Mavac 56,68,120
T 36,125,20768
T 38,125,25888
Mavac 64,72,120
La 156,99,60,22,0
Mavac 68,72,124
Mavac 76,76,124
Mavac 72,76,120
WV 29
Mavac 0,0,128
T 40,124,30720
AS 21,37,196608
Mavac 4,0,132
T 42,124,35840
Las 112,196,28,21,0
Mavac 12,4,132
T 44,125,30752
Mavac 8,4,128
T 46,125,35872
Mavac 16,8,128
T 48,124,30784
Mavac 20,8,132
T 50,124,35904
Mavac 28,12,132
T 52,125,30816
Mavac 24,12,128
T 54,125,35936
Mavac 32,16,128
T 56,124,30848
Mavac 36,16,132
T 58,124,35968
T 60,125,30880
Mavac 44,20,132
T 62,125,36000
Las 116,196,28,21,1024
Mavac 40,20,128
T 64,124,30912
Mavac 48,24,128
T 66,124,36032
Mavac 52,24,132
T 68,125,30944
Mavac 60,28,132
T 70,125,36064
Mavac 56,28,128
T 72,124,30976
Mavac 64,32,128
T 74,124,36096
Mavac 68,32,132
T 76,125,31008
Mavac 76,36,132
T 78,125,36128
Mavac 72,36,128
WV 24
B
Mavac 0,40,136
AS 42,36,655360
M0 106496
Mavac 4,40,140
T 0,126,0
D 232,24,42,0
Mavac 12,44,140
T 2,126,5120
T 4,127,32
Mavac 8,44,136
T 6,127,5152
D 233,24,42,1024
Mavac 16,48,136
T 8,126,64
T 10,126,5184
Mavac 20,48,140
T 12,127,96
D 234,24,42,2048
Mavac 28,52,140
T 14,127,5216
T 16,126,128
Mavac 24,52,136
D 235,24,42,3072
Mavac 32,56,136
T 18,126,5248
T 20,127,160
Mavac 36,56,140
T 22,127,5280
M0 110592
Mavac 44,60,140
T 24,126,192
D 236,24,42,0
Mavac 40,60,136
T 26,126,5312
AS 21,37,212992
Mavac 48,64,136
T 28,127,224
Las 120,196,28,21,0
Mavac 52,64,140
T 30,127,5344
T 32,126,256
Mavac 60,68,140
T 34,126,5376
Las 124,196,28,21,1024
Mavac 56,68,136
T 36,127,288
AS 22,22,55296
Mavac 64,72,136
T 38,127,5408
La 160,99,56,22,0
Mavac 68,72,140
Mavac 76,76,140
Mavac 72,76,136
WV 30
Mavac 0,0,80
T 40,126,10240
AS 21,37,229376
Mavac 4,0,84
T 42,126,15360
Las 128,196,28,21,0
Mavac 12,4,84
T 44,127,10272
Mavac 8,4,80
T 46,127,15392
Mavac 16,8,80
T 48,126,10304
Mavac 20,8,84
T 50,126,15424
Mavac 28,12,84
T 52,127,10336
T 54,127,15456
Mavac 24,12,80
T 56,126,10368
Las 132,196,28,21,1024
Mavac 32,16,80
T 58,126,15488
Mavac 36,16,84
T 60,127,10400
Mavac 44,20,84
T 62,127,15520
Mavac 40,20,80
T 64,126,10432
Mavac 48,24,80
T 66,126,15552
T 68,127,10464
Mavac 52,24,84
T 70,127,15584
La 164,99,60,22,0
Mavac 60,28,84
T 72,126,10496
Mavac 56,28,80
T 74,126,15616
Mavac 64,32,80
T 76,127,10528
Mavac 68,32,84
T 78,127,15648
Mavac 76,36,84
Mavac 72,36,80
WV 25
Mavac 0,40,88
AS 42,36,675840
M0 111616
Mavac 4,40,92
T 0,126,20480
D 232,24,42,0
Mavac 12,44,92
T 2,126,25600
T 4,127,20512
Mavac 8,44,88
T 6,127,25632
D 233,24,42,1024
Mavac 16,48,88
T 8,126,20544
T 10,126,25664
Mavac 20,48,92
T 12,127,20576
D 234,24,42,2048
Mavac 28,52,92
T 14,127,25696
T 16,126,20608
Mavac 24,52,88
D 235,24,42,3072
Mavac 32,56,88
T 18,126,25728
T 20,127,20640
Mavac 36,56,92
T 22,127,25760
M0 115712
Mavac 44,60,92
T 24,126,20672
D 236,24,42,0
Mavac 40,60,88
T 26,126,25792
AS 21,37,245760
Mavac 48,64,88
T 28,127,20704
Las 136,196,28,21,0
Mavac 52,64,92
T 30,127,25824
T 32,126,20736
Mavac 60,68,92
T 34,126,25856
Las 140,196,28,21,1024
Mavac 56,68,88
T 36,127,20768
AS 22,22,55296
Mavac 64,72,88
T 38,127,25888
La 168,99,56,22,0
Mavac 68,72,92
Mavac 76,76,92
Mavac 72,76,88
WV 30
Mavac 0,0,96
T 40,126,30720
AS 21,37,262144
Mavac 4,0,100
T 42,126,35840
Las 80,196,28,21,0
Mavac 12,4,100
T 44,127,30752
Mavac 8,4,96
T 46,127,35872
Mavac 16,8,96
T 48,126,30784
Mavac 20,8,100
T 50,126,35904
Mavac 28,12,100
T 52,127,30816
Mavac 24,12,96
T 54,127,35936
Mavac 32,16,96
T 56,126,30848
Mavac 36,16,100
T 58,126,35968
T 60,127,30880
Mavac 44,20,100
T 62,127,36000
Las 84,196,28,21,1024
Mavac 40,20,96
T 64,126,30912
Mavac 48,24,96
T 66,126,36032
Mavac 52,24,100
T 68,127,30944
Mavac 60,28,100
T 70,127,36064
Mavac 56,28,96
T 72,126,30976
Mavac 64,32,96
T 74,126,36096
Mavac 68,32,100
T 76,127,31008
Mavac 76,36,100
T 78,127,36128
Mavac 72,36,96
WV 24
B
Mavac 0,40,104
AS 42,36,819200
M0 0
Mavac 4,40,108
T 0,122,0
D 232,24,42,0
Mavac 12,44,108
T 2,122,5120
T 4,123,32
Mavac 8,44,104
T 6,123,5152
D 233,24,42,1024
Mavac 16,48,104
T 8,122,64
T 10,122,5184
Mavac 20,48,108
T 12,123,96
D 234,24,42,2048
Mavac 28,52,108
T 14,123,5216
T 16,122,128
Mavac 24,52,104
D 235,24,42,3072
Mavac 32,56,104
T 18,122,5248
T 20,123,160
Mavac 36,56,108
T 22,123,5280
M0 4096
Mavac 44,60,108
T 24,122,192
D 236,24,42,0
Mavac 40,60,104
T 26,122,5312
AS 21,37,278528
Mavac 48,64,104
T 28,123,224
Las 88,196,28,21,0
Mavac 52,64,108
T 30,123,5344
T 32,122,256
Mavac 60,68,108
T 34,122,5376
Las 92,196,28,21,1024
Mavac 56,68,104
T 36,123,288
T 38,123,5408
Mavac 64,72,104
La 172,99,60,22,0
Mavac 68,72,108
Mavac 76,76,108
Mavac 72,76,104
WV 29
Mavac 0,0,112
T 40,122,10240
AS 21,37,294912
Mavac 4,0,116
T 42,122,15360
Las 96,196,28,21,0
Mavac 12,4,116
T 44,123,10272
Mavac 8,4,112
T 46,123,15392
Mavac 16,8,112
T 48,122,10304
Mavac 20,8,116
T 50,122,15424
Mavac 28,12,116
T 52,123,10336
T 54,123,15456
Mavac 24,12,112
T 56,122,10368
Las 100,196,28,21,1024
Mavac 32,16,112
T 58,122,15488
Mavac 36,16,116
T 60,123,10400
Mavac 44,20,116
T 62,123,15520
Mavac 40,20,112
T 64,122,10432
T 66,122,15552
Mavac 48,24,112
T 68,123,10464
AS 22,22,55296
Mavac 52,24,116
T 70,123,15584
La 176,99,56,22,0
Mavac 60,28,116
T 72,122,10496
Mavac 56,28,112
T 74,122,15616
Mavac 64,32,112
T 76,123,10528
Mavac 68,32,116
T 78,123,15648
Mavac 76,36,116
Mavac 72,36,112
WV 25
Mavac 0,40,120
AS 42,36,839680
M0 5120
Mavac 4,40,124
T 0,122,20480
D 232,24,42,0
Mavac 12,44,124
T 2,122,25600
T 4,123,20512
Mavac 8,44,120
T 6,123,25632
D 233,24,42,1024
Mavac 16,48,120
T 8,122,20544
T 10,122,25664
Mavac 20,48,124
T 12,123,20576
D 234,24,42,2048
Mavac 28,52,124
T 14,123,25696
T 16,122,20608
Mavac 24,52,120
D 235,24,42,3072
Mavac 32,56,120
T 18,122,25728
T 20,123,20640
Mavac 36,56,124
T 22,123,25760
M0 9216
Mavac 44,60,124
T 24,122,20672
D 236,24,42,0
Mavac 40,60,120
T 26,122,25792
AS 21,37,311296
Mavac 48,64,120
T 28,123,20704
Las 104,196,28,21,0
Mavac 52,64,124
T 30,123,25824
T 32,122,20736
Mavac 60,68,124
T 34,122,25856
Las 108,196,28,21,1024
Mavac 56,68,120
T 36,123,20768
T 38,123,25888
Mavac 64,72,120
La 180,99,60,22,0
Mavac 68,72,124
Mavac 76,76,124
Mavac 72,76,120
WV 30
Mavac 0,0,128
T 40,122,30720
AS 21,37,327680
Mavac 4,0,132
T 42,122,35840
Las 112,196,28,21,0
Mavac 12,4,132
T 44,123,30752
Mavac 8,4,128
T 46,123,35872
Mavac 16,8,128
T 48,122,30784
Mavac 20,8,132
T 50,122,35904
Mavac 28,12,132
T 52,123,30816
Mavac 24,12,128
T 54,123,35936
Mavac 32,16,128
T 56,122,30848
Mavac 36,16,132
T 58,122,35968
T 60,123,30880
Mavac 44,20,132
T 62,123,36000
Las 116,196,28,21,1024
Mavac 40,20,128
T 64,122,30912
Mavac 48,24,128
T 66,122,36032
Mavac 52,24,132
T 68,123,30944
Mavac 60,28,132
T 70,123,36064
Mavac 56,28,128
T 72,122,30976
Mavac 64,32,128
T 74,122,36096
Mavac 68,32,132
T 76,123,31008
Mavac 76,36,132
T 78,123,36128
Mavac 72,36,128
WV 24
B
Mavac 0,40,136
AS 42,36,983040
M0 65536
Mavac 4,40,140
T 0,124,0
D 232,24,42,0
Mavac 12,44,140
T 2,124,5120
T 4,125,32
Mavac 8,44,136
T 6,125,5152
D 233,24,42,1024
Mavac 16,48,136
T 8,124,64
T 10,124,5184
Mavac 20,48,140
T 12,125,96
D 234,24,42,2048
Mavac 28,52,140
T 14,125,5216
T 16,124,128
Mavac 24,52,136
D 235,24,42,3072
Mavac 32,56,136
T 18,124,5248
T 20,125,160
Mavac 36,56,140
T 22,125,5280
M0 69632
Mavac 44,60,140
T 24,124,192
D 236,24,42,0
Mavac 40,60,136
T 26,124,5312
AS 21,37,344064
Mavac 48,64,136
T 28,125,224
Las 120,196,28,21,0
Mavac 52,64,140
T 30,125,5344
T 32,124,256
Mavac 60,68,140
T 34,124,5376
Las 124,196,28,21,1024
Mavac 56,68,136
T 36,125,288
AS 22,22,55296
Mavac 64,72,136
T 38,125,5408
La 184,99,56,22,0
Mavac 68,72,140
Mavac 76,76,140
Mavac 72,76,136
WV 29
Mavac 0,0,80
T 40,124,10240
AS 21,37,360448
Mavac 4,0,84
T 42,124,15360
Las 128,196,28,21,0
Mavac 12,4,84
T 44,125,10272
Mavac 8,4,80
T 46,125,15392
Mavac 16,8,80
T 48,124,10304
Mavac 20,8,84
T 50,124,15424
Mavac 28,12,84
T 52,125,10336
T 54,125,15456
Mavac 24,12,80
T 56,124,10368
Las 132,196,28,21,1024
Mavac 32,16,80
T 58,124,15488
Mavac 36,16,84
T 60,125,10400
Mavac 44,20,84
T 62,125,15520
Mavac 40,20,80
T 64,124,10432
Mavac 48,24,80
T 66,124,15552
T 68,125,10464
Mavac 52,24,84
T 70,125,15584
La 188,99,60,22,0
Mavac 60,28,84
T 72,124,10496
Mavac 56,28,80
T 74,124,15616
Mavac 64,32,80
T 76,125,10528
Mavac 68,32,84
T 78,125,15648
Mavac 76,36,84
Mavac 72,36,80
WV 25
Mavac 0,40,88
AS 42,36,1003520
M0 70656
Mavac 4,40,92
T 0,124,20480
D 232,24,42,0
Mavac 12,44,92
T 2,124,25600
T 4,125,20512
Mavac 8,44,88
T 6,125,25632
D 233,24,42,1024
Mavac 16,48,88
T 8,124,20544
T 10,124,25664
Mavac 20,48,92
T 12,125,20576
D 234,24,42,2048
Mavac 28,52,92
T 14,125,25696
T 16,124,20608
Mavac 24,52,88
D 235,24,42,3072
Mavac 32,56,88
T 18,124,25728
T 20,125,20640
Mavac 36,56,92
T 22,125,25760
M0 74752
Mavac 44,60,92
T 24,124,20672
D 236,24,42,0
Mavac 40,60,88
T 26,124,25792
AS 21,37,376832
Mavac 48,64,88
T 28,125,20704
Las 136,196,28,21,0
Mavac 52,64,92
T 30,125,25824
T 32,124,20736
Mavac 60,68,92
T 34,124,25856
Las 140,196,28,21,1024
Mavac 56,68,88
T 36,125,20768
AS 22,22,55296
Mavac 64,72,88
T 38,125,25888
La 192,99,56,22,0
Mavac 68,72,92
Mavac 76,76,92
Mavac 72,76,88
WV 30
Mavac 0,0,96
T 40,124,30720
AS 21,37,393216
Mavac 4,0,100
T 42,124,35840
Las 80,196,28,21,0
Mavac 12,4,100
T 44,125,30752
Mavac 8,4,96
T 46,125,35872
Mavac 16,8,96
T 48,124,30784
Mavac 20,8,100
T 50,124,35904
Mavac 28,12,100
T 52,125,30816
T 54,125,35936
Mavac 24,12,96
T 56,124,30848
Las 84,196,28,21,1024
Mavac 32,16,96
T 58,124,35968
Mavac 36,16,100
T 60,125,30880
Mavac 44,20,100
T 62,125,36000
Mavac 40,20,96
T 64,124,30912
Mavac 48,24,96
T 66,124,36032
T 68,125,30944
Mavac 52,24,100
T 70,125,36064
La 196,99,60,22,0
Mavac 60,28,100
T 72,124,30976
Mavac 56,28,96
T 74,124,36096
Mavac 64,32,96
T 76,125,31008
Mavac 68,32,100
T 78,125,36128
Mavac 76,36,100
Mavac 72,36,96
WV 25
B
Mavac 0,40,104
AS 42,36,1146880
M0 106496
Mavac 4,40,108
T 0,126,0
D 232,24,42,0
Mavac 12,44,108
T 2,126,5120
T 4,127,32
Mavac 8,44,104
T 6,127,5152
D 233,24,42,1024
Mavac 16,48,104
T 8,126,64
Mavac 20,48,108
T 10,126,5184
T 12,127,96
Mavac 28,52,108
T 14,127,5216
D 234,24,42,2048
Mavac 24,52,104
T 16,126,128
T 18,126,5248
Mavac 32,56,104
D 235,24,42,3072
Mavac 36,56,108
T 20,127,160
T 22,127,5280
Mavac 44,60,108
T 24,126,192
M0 110592
Mavac 40,60,104
T 26,126,5312
D 236,24,42,0
Mavac 48,64,104
T 28,127,224
AS 21,37,409600
Mavac 52,64,108
T 30,127,5344
Las 88,196,28,21,0
Mavac 60,68,108
T 32,126,256
Mavac 56,68,104
T 34,126,5376
T 36,127,288
Mavac 64,72,104
T 38,127,5408
Las 92,196,28,21,1024
Mavac 68,72,108
Mavac 76,76,108
Mavac 72,76,104
WV 29
Mavac 0,0,112
T 40,126,10240
AS 21,37,425984
Mavac 4,0,116
T 42,126,15360
Las 96,196,28,21,0
Mavac 12,4,116
T 44,127,10272
Mavac 8,4,112
T 46,127,15392
Mavac 16,8,112
T 48,126,10304
Mavac 20,8,116
T 50,126,15424
Mavac 28,12,116
T 52,127,10336
T 54,127,15456
Mavac 24,12,112
T 56,126,10368
Las 100,196,28,21,1024
Mavac 32,16,112
T 58,126,15488
Mavac 36,16,116
T 60,127,10400
Mavac 44,20,116
T 62,127,15520
Mavac 40,20,112
T 64,126,10432
T 66,126,15552
Mavac 48,24,112
T 68,127,10464
)A"
R"A(AS 22,22,55296
Mavac 52,24,116
T 70,127,15584
La 200,99,56,22,0
Mavac 60,28,116
T 72,126,10496
Mavac 56,28,112
T 74,126,15616
Mavac 64,32,112
T 76,127,10528
Mavac 68,32,116
T 78,127,15648
Mavac 76,36,116
Mavac 72,36,112
WV 25
Mavac 0,40,120
AS 42,36,1167360
M0 111616
Mavac 4,40,124
T 0,126,20480
D 232,24,42,0
Mavac 12,44,124
T 2,126,25600
T 4,127,20512
Mavac 8,44,120
T 6,127,25632
D 233,24,42,1024
Mavac 16,48,120
T 8,126,20544
T 10,126,25664
Mavac 20,48,124
T 12,127,20576
D 234,24,42,2048
Mavac 28,52,124
T 14,127,25696
T 16,126,20608
Mavac 24,52,120
D 235,24,42,3072
Mavac 32,56,120
T 18,126,25728
T 20,127,20640
Mavac 36,56,124
T 22,127,25760
M0 115712
Mavac 44,60,124
T 24,126,20672
D 236,24,42,0
Mavac 40,60,120
T 26,126,25792
AS 21,37,442368
Mavac 48,64,120
T 28,127,20704
Las 104,196,28,21,0
Mavac 52,64,124
T 30,127,25824
T 32,126,20736
Mavac 60,68,124
T 34,126,25856
Las 108,196,28,21,1024
Mavac 56,68,120
T 36,127,20768
T 38,127,25888
Mavac 64,72,120
La 204,99,60,22,0
Mavac 68,72,124
Mavac 76,76,124
Mavac 72,76,120
WV 30
Mavac 0,0,128
T 40,126,30720
AS 21,37,458752
Mavac 4,0,132
T 42,126,35840
Las 112,196,28,21,0
Mavac 12,4,132
T 44,127,30752
Mavac 8,4,128
T 46,127,35872
Mavac 16,8,128
T 48,126,30784
Mavac 20,8,132
T 50,126,35904
Mavac 28,12,132
T 52,127,30816
T 54,127,35936
Mavac 24,12,128
T 56,126,30848
Las 116,196,28,21,1024
Mavac 32,16,128
T 58,126,35968
Mavac 36,16,132
T 60,127,30880
Mavac 44,20,132
T 62,127,36000
Mavac 40,20,128
T 64,126,30912
T 66,126,36032
Mavac 48,24,128
T 68,127,30944
AS 22,22,55296
Mavac 52,24,132
T 70,127,36064
La 208,99,56,22,0
Mavac 60,28,132
T 72,126,30976
Mavac 56,28,128
T 74,126,36096
Mavac 64,32,128
T 76,127,31008
Mavac 68,32,132
T 78,127,36128
Mavac 76,36,132
Mavac 72,36,128
WV 25
B
Mavac 0,40,136
T 0,122,0
AS 21,37,475136
Mavac 4,40,140
T 2,122,5120
Las 120,196,28,21,0
Mavac 12,44,140
T 4,123,32
Mavac 8,44,136
T 6,123,5152
Mavac 16,48,136
T 8,122,64
Mavac 20,48,140
T 10,122,5184
Mavac 28,52,140
T 12,123,96
Mavac 24,52,136
T 14,123,5216
Mavac 32,56,136
T 16,122,128
Mavac 36,56,140
T 18,122,5248
T 20,123,160
Mavac 44,60,140
T 22,123,5280
Las 124,196,28,21,1024
Mavac 40,60,136
T 24,122,192
Mavac 48,64,136
T 26,122,5312
Mavac 52,64,140
T 28,123,224
Mavac 60,68,140
T 30,123,5344
Mavac 56,68,136
T 32,122,256
Mavac 64,72,136
T 34,122,5376
Mavac 68,72,140
T 36,123,288
Mavac 76,76,140
T 38,123,5408
Mavac 72,76,136
WV 24
Mavac 0,0,80
T 40,122,10240
AS 21,37,491520
Mavac 4,0,84
T 42,122,15360
Las 128,196,28,21,0
Mavac 12,4,84
T 44,123,10272
Mavac 8,4,80
T 46,123,15392
Mavac 16,8,80
T 48,122,10304
Mavac 20,8,84
T 50,122,15424
Mavac 28,12,84
T 52,123,10336
T 54,123,15456
Mavac 24,12,80
T 56,122,10368
Las 132,196,28,21,1024
Mavac 32,16,80
T 58,122,15488
Mavac 36,16,84
T 60,123,10400
Mavac 44,20,84
T 62,123,15520
Mavac 40,20,80
T 64,122,10432
Mavac 48,24,80
T 66,122,15552
T 68,123,10464
Mavac 52,24,84
T 70,123,15584
La 212,99,60,22,0
Mavac 60,28,84
T 72,122,10496
Mavac 56,28,80
T 74,122,15616
Mavac 64,32,80
T 76,123,10528
Mavac 68,32,84
T 78,123,15648
Mavac 76,36,84
Mavac 72,36,80
WV 19
Mavac 0,40,88
T 0,122,20480
AS 21,37,507904
Mavac 4,40,92
T 2,122,25600
Las 136,196,28,21,0
Mavac 12,44,92
T 4,123,20512
Mavac 8,44,88
T 6,123,25632
Mavac 16,48,88
T 8,122,20544
Mavac 20,48,92
T 10,122,25664
Mavac 28,52,92
T 12,123,20576
T 14,123,25696
Mavac 24,52,88
T 16,122,20608
Las 140,196,28,21,1024
Mavac 32,56,88
T 18,122,25728
Mavac 36,56,92
T 20,123,20640
Mavac 44,60,92
T 22,123,25760
Mavac 40,60,88
T 24,122,20672
T 26,122,25792
Mavac 48,64,88
T 28,123,20704
AS 22,22,55296
Mavac 52,64,92
T 30,123,25824
La 216,99,56,22,0
Mavac 60,68,92
T 32,122,20736
Mavac 56,68,88
T 34,122,25856
Mavac 64,72,88
T 36,123,20768
Mavac 68,72,92
T 38,123,25888
Mavac 76,76,92
Mavac 72,76,88
WV 20
Mavac 0,0,96
T 40,122,30720
T 42,122,35840
Mavac 4,0,100
La 220,99,60,22,0
Mavac 12,4,100
T 44,123,30752
Mavac 8,4,96
T 46,123,35872
Mavac 16,8,96
T 48,122,30784
Mavac 20,8,100
T 50,122,35904
Mavac 28,12,100
T 52,123,30816
Mavac 24,12,96
T 54,123,35936
Mavac 32,16,96
T 56,122,30848
Mavac 36,16,100
T 58,122,35968
Mavac 44,20,100
T 60,123,30880
Mavac 40,20,96
T 62,123,36000
Mavac 48,24,96
T 64,122,30912
Mavac 52,24,100
T 66,122,36032
Mavac 60,28,100
T 68,123,30944
Mavac 56,28,96
T 70,123,36064
Mavac 64,32,96
T 72,122,30976
Mavac 68,32,100
T 74,122,36096
Mavac 76,36,100
T 76,123,31008
Mavac 72,36,96
T 78,123,36128
WV 13
B
Mavac 0,40,104
T 0,124,0
Mavac 4,40,108
T 2,124,5120
Mavac 12,44,108
T 4,125,32
Mavac 8,44,104
T 6,125,5152
Mavac 16,48,104
T 8,124,64
Mavac 20,48,108
T 10,124,5184
Mavac 28,52,108
T 12,125,96
Mavac 24,52,104
T 14,125,5216
Mavac 32,56,104
T 16,124,128
Mavac 36,56,108
T 18,124,5248
Mavac 44,60,108
T 20,125,160
Mavac 40,60,104
T 22,125,5280
Mavac 48,64,104
T 24,124,192
Mavac 52,64,108
T 26,124,5312
Mavac 60,68,108
T 28,125,224
Mavac 56,68,104
T 30,125,5344
Mavac 64,72,104
T 32,124,256
Mavac 68,72,108
T 34,124,5376
Mavac 76,76,108
T 36,125,288
Mavac 72,76,104
T 38,125,5408
WV 10
Mavac 0,0,112
T 40,124,10240
Mavac 4,0,116
T 42,124,15360
Mavac 12,4,116
T 44,125,10272
Mavac 8,4,112
T 46,125,15392
Mavac 16,8,112
T 48,124,10304
Mavac 20,8,116
T 50,124,15424
Mavac 28,12,116
T 52,125,10336
Mavac 24,12,112
T 54,125,15456
Mavac 32,16,112
T 56,124,10368
Mavac 36,16,116
T 58,124,15488
Mavac 44,20,116
T 60,125,10400
Mavac 40,20,112
T 62,125,15520
Mavac 48,24,112
T 64,124,10432
Mavac 52,24,116
T 66,124,15552
Mavac 60,28,116
T 68,125,10464
Mavac 56,28,112
T 70,125,15584
Mavac 64,32,112
T 72,124,10496
Mavac 68,32,116
T 74,124,15616
Mavac 76,36,116
T 76,125,10528
Mavac 72,36,112
T 78,125,15648
WV 7
Mavac 0,40,120
T 0,124,20480
Mavac 4,40,124
T 2,124,25600
Mavac 12,44,124
T 4,125,20512
Mavac 8,44,120
T 6,125,25632
Mavac 16,48,120
T 8,124,20544
Mavac 20,48,124
T 10,124,25664
Mavac 28,52,124
T 12,125,20576
Mavac 24,52,120
T 14,125,25696
Mavac 32,56,120
T 16,124,20608
Mavac 36,56,124
T 18,124,25728
Mavac 44,60,124
T 20,125,20640
Mavac 40,60,120
T 22,125,25760
Mavac 48,64,120
T 24,124,20672
Mavac 52,64,124
T 26,124,25792
Mavac 60,68,124
T 28,125,20704
Mavac 56,68,120
T 30,125,25824
Mavac 64,72,120
T 32,124,20736
Mavac 68,72,124
T 34,124,25856
Mavac 76,76,124
T 36,125,20768
Mavac 72,76,120
T 38,125,25888
WV 5
Mavac 0,0,128
T 40,124,30720
Mavac 4,0,132
T 42,124,35840
Mavac 12,4,132
T 44,125,30752
Mavac 8,4,128
T 46,125,35872
Mavac 16,8,128
T 48,124,30784
Mavac 20,8,132
T 50,124,35904
Mavac 28,12,132
T 52,125,30816
Mavac 24,12,128
T 54,125,35936
Mavac 32,16,128
T 56,124,30848
Mavac 36,16,132
T 58,124,35968
Mavac 44,20,132
T 60,125,30880
Mavac 40,20,128
T 62,125,36000
Mavac 48,24,128
T 64,124,30912
Mavac 52,24,132
T 66,124,36032
Mavac 60,28,132
T 68,125,30944
Mavac 56,28,128
T 70,125,36064
Mavac 64,32,128
T 72,124,30976
Mavac 68,32,132
T 74,124,36096
Mavac 76,36,132
T 76,125,31008
Mavac 72,36,128
T 78,125,36128
WV 2
B
Mavac 0,40,136
Mavac 4,40,140
Mavac 12,44,140
Mavac 8,44,136
Mavac 16,48,136
Mavac 20,48,140
Mavac 28,52,140
Mavac 24,52,136
Mavac 32,56,136
Mavac 36,56,140
Mavac 44,60,140
Mavac 40,60,136
Mavac 48,64,136
Mavac 52,64,140
Mavac 60,68,140
Mavac 56,68,136
Mavac 64,72,136
Mavac 68,72,140
Mavac 76,76,140
Mavac 72,76,136
s_branch OMD0%=
OMN0%=:
AS 21,37,0
Las 80,196,28,21,0
Las 84,196,28,21,1024
AS 21,37,16384
Las 88,196,28,21,0
Las 92,196,28,21,1024
AS 21,37,32768
Las 96,196,28,21,0
Las 100,196,28,21,1024
AS 21,37,49152
Las 104,196,28,21,0
Las 108,196,28,21,1024
AS 21,37,65536
Las 112,196,28,21,0
Las 116,196,28,21,1024
AS 21,37,81920
Las 120,196,28,21,0
Las 124,196,28,21,1024
AS 42,36,163840
M0 106496
N 0
D 232,24,42,0
D 233,24,42,1024
D 234,24,42,2048
D 235,24,42,3072
M0 110592
N 0
D 236,24,42,0
AS 42,36,184320
M0 111616
N 0
D 232,24,42,0
D 233,24,42,1024
D 234,24,42,2048
D 235,24,42,3072
M0 115712
N 0
D 236,24,42,0
W 0
B
T 0,122,0
T 2,122,5120
T 4,123,32
T 6,123,5152
T 8,122,64
T 10,122,5184
T 12,123,96
T 14,123,5216
T 16,122,128
T 18,122,5248
T 20,123,160
T 22,123,5280
T 24,122,192
T 26,122,5312
T 28,123,224
T 30,123,5344
T 32,122,256
T 34,122,5376
T 36,123,288
T 38,123,5408
WV 20
Mavaz 0,0,80
AS 42,36,327680
M0 0
Mavaz 4,0,84
T 40,122,10240
D 232,24,42,0
Mavaz 12,4,84
T 42,122,15360
T 44,123,10272
Mavaz 8,4,80
T 46,123,15392
D 233,24,42,1024
Mavaz 16,8,80
T 48,122,10304
Mavaz 20,8,84
T 50,122,15424
T 52,123,10336
Mavaz 28,12,84
T 54,123,15456
D 234,24,42,2048
Mavaz 24,12,80
T 56,122,10368
T 58,122,15488
Mavaz 32,16,80
D 235,24,42,3072
Mavaz 36,16,84
T 60,123,10400
T 62,123,15520
Mavaz 44,20,84
T 64,122,10432
M0 4096
Mavaz 40,20,80
T 66,122,15552
D 236,24,42,0
Mavaz 48,24,80
T 68,123,10464
AS 21,37,98304
Mavaz 52,24,84
T 70,123,15584
Las 128,196,28,21,0
Mavaz 60,28,84
T 72,122,10496
Mavaz 56,28,80
T 74,122,15616
T 76,123,10528
Mavaz 64,32,80
T 78,123,15648
Las 132,196,28,21,1024
Mavaz 68,32,84
Mavaz 76,36,84
Mavaz 72,36,80
WV 25
Mavac 0,40,88
AS 42,36,348160
M0 5120
Mavac 4,40,92
T 0,122,20480
D 232,24,42,0
Mavac 12,44,92
T 2,122,25600
T 4,123,20512
Mavac 8,44,88
T 6,123,25632
D 233,24,42,1024
Mavac 16,48,88
T 8,122,20544
Mavac 20,48,92
T 10,122,25664
T 12,123,20576
Mavac 28,52,92
T 14,123,25696
D 234,24,42,2048
Mavac 24,52,88
T 16,122,20608
T 18,122,25728
Mavac 32,56,88
D 235,24,42,3072
Mavac 36,56,92
T 20,123,20640
T 22,123,25760
Mavac 44,60,92
T 24,122,20672
M0 9216
Mavac 40,60,88
T 26,122,25792
D 236,24,42,0
Mavac 48,64,88
T 28,123,20704
AS 21,37,114688
Mavac 52,64,92
T 30,123,25824
Las 136,196,28,21,0
Mavac 60,68,92
T 32,122,20736
Mavac 56,68,88
T 34,122,25856
T 36,123,20768
Mavac 64,72,88
T 38,123,25888
Las 140,196,28,21,1024
Mavac 68,72,92
Mavac 76,76,92
Mavac 72,76,88
WV 30
Mavac 0,0,96
T 40,122,30720
AS 21,37,131072
Mavac 4,0,100
T 42,122,35840
Las 80,196,28,21,0
Mavac 12,4,100
T 44,123,30752
Mavac 8,4,96
T 46,123,35872
Mavac 16,8,96
T 48,122,30784
Mavac 20,8,100
T 50,122,35904
Mavac 28,12,100
T 52,123,30816
Mavac 24,12,96
T 54,123,35936
Mavac 32,16,96
T 56,122,30848
Mavac 36,16,100
T 58,122,35968
T 60,123,30880
Mavac 44,20,100
T 62,123,36000
Las 84,196,28,21,1024
Mavac 40,20,96
T 64,122,30912
Mavac 48,24,96
T 66,122,36032
Mavac 52,24,100
T 68,123,30944
Mavac 60,28,100
T 70,123,36064
Mavac 56,28,96
T 72,122,30976
Mavac 64,32,96
T 74,122,36096
Mavac 68,32,100
T 76,123,31008
Mavac 76,36,100
T 78,123,36128
Mavac 72,36,96
WV 16
B
Mavac 0,40,104
AS 42,36,491520
M0 65536
Mavac 4,40,108
T 0,124,0
D 232,24,42,0
Mavac 12,44,108
T 2,124,5120
T 4,125,32
Mavac 8,44,104
T 6,125,5152
D 233,24,42,1024
Mavac 16,48,104
T 8,124,64
Mavac 20,48,108
T 10,124,5184
T 12,125,96
Mavac 28,52,108
T 14,125,5216
D 234,24,42,2048
Mavac 24,52,104
T 16,124,128
T 18,124,5248
Mavac 32,56,104
D 235,24,42,3072
Mavac 36,56,108
T 20,125,160
T 22,125,5280
Mavac 44,60,108
T 24,124,192
M0 69632
Mavac 40,60,104
T 26,124,5312
D 236,24,42,0
Mavac 48,64,104
T 28,125,224
AS 21,37,147456
Mavac 52,64,108
T 30,125,5344
Las 88,196,28,21,0
Mavac 60,68,108
T 32,124,256
Mavac 56,68,104
T 34,124,5376
T 36,125,288
Mavac 64,72,104
T 38,125,5408
Las 92,196,28,21,1024
Mavac 68,72,108
Mavac 76,76,108
Mavac 72,76,104
W 0
Mavac 0,0,112
T 40,124,10240
AS 21,37,163840
Mavac 4,0,116
T 42,124,15360
Las 96,196,28,21,0
Mavac 12,4,116
T 44,125,10272
Mavac 8,4,112
T 46,125,15392
Mavac 16,8,112
T 48,124,10304
Mavac 20,8,116
T 50,124,15424
Mavac 28,12,116
T 52,125,10336
Mavac 24,12,112
T 54,125,15456
Mavac 32,16,112
T 56,124,10368
Mavac 36,16,116
T 58,124,15488
T 60,125,10400
Mavac 44,20,116
T 62,125,15520
Las 100,196,28,21,1024
Mavac 40,20,112
T 64,124,10432
Mavac 48,24,112
T 66,124,15552
Mavac 52,24,116
T 68,125,10464
Mavac 60,28,116
T 70,125,15584
Mavac 56,28,112
T 72,124,10496
Mavac 64,32,112
T 74,124,15616
Mavac 68,32,116
T 76,125,10528
Mavac 76,36,116
T 78,125,15648
Mavac 72,36,112
W 0
Mavac 0,40,120
AS 42,36,512000
M0 70656
Mavac 4,40,124
T 0,124,20480
D 232,24,42,0
Mavac 12,44,124
T 2,124,25600
T 4,125,20512
Mavac 8,44,120
T 6,125,25632
D 233,24,42,1024
Mavac 16,48,120
T 8,124,20544
Mavac 20,48,124
T 10,124,25664
T 12,125,20576
Mavac 28,52,124
T 14,125,25696
D 234,24,42,2048
Mavac 24,52,120
T 16,124,20608
T 18,124,25728
Mavac 32,56,120
D 235,24,42,3072
)A"
R"A(Mavac 36,56,124
T 20,125,20640
T 22,125,25760
Mavac 44,60,124
T 24,124,20672
M0 74752
Mavac 40,60,120
T 26,124,25792
D 236,24,42,0
Mavac 48,64,120
T 28,125,20704
AS 21,37,180224
Mavac 52,64,124
T 30,125,25824
Las 104,196,28,21,0
Mavac 60,68,124
T 32,124,20736
Mavac 56,68,120
T 34,124,25856
T 36,125,20768
Mavac 64,72,120
T 38,125,25888
Las 108,196,28,21,1024
Mavac 68,72,124
Mavac 76,76,124
Mavac 72,76,120
WV 25
Mavac 0,0,128
T 40,124,30720
AS 21,37,196608
Mavac 4,0,132
T 42,124,35840
Las 112,196,28,21,0
Mavac 12,4,132
T 44,125,30752
Mavac 8,4,128
T 46,125,35872
Mavac 16,8,128
T 48,124,30784
Mavac 20,8,132
T 50,124,35904
Mavac 28,12,132
T 52,125,30816
Mavac 24,12,128
T 54,125,35936
Mavac 32,16,128
T 56,124,30848
Mavac 36,16,132
T 58,124,35968
T 60,125,30880
Mavac 44,20,132
T 62,125,36000
Las 116,196,28,21,1024
Mavac 40,20,128
T 64,124,30912
Mavac 48,24,128
T 66,124,36032
Mavac 52,24,132
T 68,125,30944
Mavac 60,28,132
T 70,125,36064
Mavac 56,28,128
T 72,124,30976
Mavac 64,32,128
T 74,124,36096
Mavac 68,32,132
T 76,125,31008
Mavac 76,36,132
T 78,125,36128
Mavac 72,36,128
WV 20
B
Mavac 0,40,136
AS 42,36,655360
M0 106496
Mavac 4,40,140
T 0,126,0
D 232,24,42,0
Mavac 12,44,140
T 2,126,5120
T 4,127,32
Mavac 8,44,136
T 6,127,5152
D 233,24,42,1024
Mavac 16,48,136
T 8,126,64
Mavac 20,48,140
T 10,126,5184
T 12,127,96
Mavac 28,52,140
T 14,127,5216
D 234,24,42,2048
Mavac 24,52,136
T 16,126,128
T 18,126,5248
Mavac 32,56,136
D 235,24,42,3072
Mavac 36,56,140
T 20,127,160
T 22,127,5280
Mavac 44,60,140
T 24,126,192
M0 110592
Mavac 40,60,136
T 26,126,5312
D 236,24,42,0
Mavac 48,64,136
T 28,127,224
AS 21,37,212992
Mavac 52,64,140
T 30,127,5344
Las 120,196,28,21,0
Mavac 60,68,140
T 32,126,256
Mavac 56,68,136
T 34,126,5376
T 36,127,288
Mavac 64,72,136
T 38,127,5408
Las 124,196,28,21,1024
Mavac 68,72,140
Mavac 76,76,140
Mavac 72,76,136
WV 25
Mavac 0,0,80
T 40,126,10240
AS 21,37,229376
Mavac 4,0,84
T 42,126,15360
Las 128,196,28,21,0
Mavac 12,4,84
T 44,127,10272
Mavac 8,4,80
T 46,127,15392
Mavac 16,8,80
T 48,126,10304
Mavac 20,8,84
T 50,126,15424
Mavac 28,12,84
T 52,127,10336
Mavac 24,12,80
T 54,127,15456
Mavac 32,16,80
T 56,126,10368
Mavac 36,16,84
T 58,126,15488
T 60,127,10400
Mavac 44,20,84
T 62,127,15520
Las 132,196,28,21,1024
Mavac 40,20,80
T 64,126,10432
Mavac 48,24,80
T 66,126,15552
Mavac 52,24,84
T 68,127,10464
Mavac 60,28,84
T 70,127,15584
Mavac 56,28,80
T 72,126,10496
Mavac 64,32,80
T 74,126,15616
Mavac 68,32,84
T 76,127,10528
Mavac 76,36,84
T 78,127,15648
Mavac 72,36,80
WV 20
Mavac 0,40,88
AS 42,36,675840
M0 111616
Mavac 4,40,92
T 0,126,20480
D 232,24,42,0
Mavac 12,44,92
T 2,126,25600
T 4,127,20512
Mavac 8,44,88
T 6,127,25632
D 233,24,42,1024
Mavac 16,48,88
T 8,126,20544
Mavac 20,48,92
T 10,126,25664
T 12,127,20576
Mavac 28,52,92
T 14,127,25696
D 234,24,42,2048
Mavac 24,52,88
T 16,126,20608
T 18,126,25728
Mavac 32,56,88
D 235,24,42,3072
Mavac 36,56,92
T 20,127,20640
T 22,127,25760
Mavac 44,60,92
T 24,126,20672
M0 115712
Mavac 40,60,88
T 26,126,25792
D 236,24,42,0
Mavac 48,64,88
T 28,127,20704
AS 21,37,245760
Mavac 52,64,92
T 30,127,25824
Las 136,196,28,21,0
Mavac 60,68,92
T 32,126,20736
Mavac 56,68,88
T 34,126,25856
T 36,127,20768
Mavac 64,72,88
T 38,127,25888
Las 140,196,28,21,1024
Mavac 68,72,92
Mavac 76,76,92
Mavac 72,76,88
WV 25
Mavac 0,0,96
T 40,126,30720
AS 21,37,262144
Mavac 4,0,100
T 42,126,35840
Las 80,196,28,21,0
Mavac 12,4,100
T 44,127,30752
Mavac 8,4,96
T 46,127,35872
Mavac 16,8,96
T 48,126,30784
Mavac 20,8,100
T 50,126,35904
Mavac 28,12,100
T 52,127,30816
Mavac 24,12,96
T 54,127,35936
Mavac 32,16,96
T 56,126,30848
Mavac 36,16,100
T 58,126,35968
T 60,127,30880
Mavac 44,20,100
T 62,127,36000
Las 84,196,28,21,1024
Mavac 40,20,96
T 64,126,30912
Mavac 48,24,96
T 66,126,36032
Mavac 52,24,100
T 68,127,30944
Mavac 60,28,100
T 70,127,36064
Mavac 56,28,96
T 72,126,30976
Mavac 64,32,96
T 74,126,36096
Mavac 68,32,100
T 76,127,31008
Mavac 76,36,100
T 78,127,36128
Mavac 72,36,96
WV 20
B
Mavac 0,40,104
AS 42,36,819200
M0 0
Mavac 4,40,108
T 0,122,0
D 232,24,42,0
Mavac 12,44,108
T 2,122,5120
T 4,123,32
Mavac 8,44,104
T 6,123,5152
D 233,24,42,1024
Mavac 16,48,104
T 8,122,64
Mavac 20,48,108
T 10,122,5184
T 12,123,96
Mavac 28,52,108
T 14,123,5216
D 234,24,42,2048
Mavac 24,52,104
T 16,122,128
T 18,122,5248
Mavac 32,56,104
D 235,24,42,3072
Mavac 36,56,108
T 20,123,160
T 22,123,5280
Mavac 44,60,108
T 24,122,192
M0 4096
Mavac 40,60,104
T 26,122,5312
D 236,24,42,0
Mavac 48,64,104
T 28,123,224
AS 21,37,278528
Mavac 52,64,108
T 30,123,5344
Las 88,196,28,21,0
Mavac 60,68,108
T 32,122,256
Mavac 56,68,104
T 34,122,5376
T 36,123,288
Mavac 64,72,104
T 38,123,5408
Las 92,196,28,21,1024
Mavac 68,72,108
Mavac 76,76,108
Mavac 72,76,104
WV 25
Mavac 0,0,112
T 40,122,10240
AS 21,37,294912
Mavac 4,0,116
T 42,122,15360
Las 96,196,28,21,0
Mavac 12,4,116
T 44,123,10272
Mavac 8,4,112
T 46,123,15392
Mavac 16,8,112
T 48,122,10304
Mavac 20,8,116
T 50,122,15424
Mavac 28,12,116
T 52,123,10336
Mavac 24,12,112
T 54,123,15456
Mavac 32,16,112
T 56,122,10368
Mavac 36,16,116
T 58,122,15488
T 60,123,10400
Mavac 44,20,116
T 62,123,15520
Las 100,196,28,21,1024
Mavac 40,20,112
T 64,122,10432
Mavac 48,24,112
T 66,122,15552
Mavac 52,24,116
T 68,123,10464
Mavac 60,28,116
T 70,123,15584
Mavac 56,28,112
T 72,122,10496
Mavac 64,32,112
T 74,122,15616
Mavac 68,32,116
T 76,123,10528
Mavac 76,36,116
T 78,123,15648
Mavac 72,36,112
WV 20
Mavac 0,40,120
AS 42,36,839680
M0 5120
Mavac 4,40,124
T 0,122,20480
D 232,24,42,0
Mavac 12,44,124
T 2,122,25600
T 4,123,20512
Mavac 8,44,120
T 6,123,25632
D 233,24,42,1024
Mavac 16,48,120
T 8,122,20544
Mavac 20,48,124
T 10,122,25664
T 12,123,20576
Mavac 28,52,124
T 14,123,25696
D 234,24,42,2048
Mavac 24,52,120
T 16,122,20608
T 18,122,25728
Mavac 32,56,120
D 235,24,42,3072
Mavac 36,56,124
T 20,123,20640
T 22,123,25760
Mavac 44,60,124
T 24,122,20672
M0 9216
Mavac 40,60,120
T 26,122,25792
D 236,24,42,0
Mavac 48,64,120
T 28,123,20704
AS 21,37,311296
Mavac 52,64,124
T 30,123,25824
Las 104,196,28,21,0
Mavac 60,68,124
T 32,122,20736
Mavac 56,68,120
T 34,122,25856
T 36,123,20768
Mavac 64,72,120
T 38,123,25888
Las 108,196,28,21,1024
Mavac 68,72,124
Mavac 76,76,124
Mavac 72,76,120
WV 25
Mavac 0,0,128
T 40,122,30720
AS 21,37,327680
Mavac 4,0,132
T 42,122,35840
Las 112,196,28,21,0
Mavac 12,4,132
T 44,123,30752
Mavac 8,4,128
T 46,123,35872
Mavac 16,8,128
T 48,122,30784
Mavac 20,8,132
T 50,122,35904
Mavac 28,12,132
T 52,123,30816
Mavac 24,12,128
T 54,123,35936
Mavac 32,16,128
T 56,122,30848
Mavac 36,16,132
T 58,122,35968
T 60,123,30880
Mavac 44,20,132
T 62,123,36000
Las 116,196,28,21,1024
Mavac 40,20,128
T 64,122,30912
Mavac 48,24,128
T 66,122,36032
Mavac 52,24,132
T 68,123,30944
Mavac 60,28,132
T 70,123,36064
Mavac 56,28,128
T 72,122,30976
Mavac 64,32,128
T 74,122,36096
Mavac 68,32,132
T 76,123,31008
Mavac 76,36,132
T 78,123,36128
Mavac 72,36,128
WV 20
B
Mavac 0,40,136
AS 42,36,983040
M0 65536
Mavac 4,40,140
T 0,124,0
D 232,24,42,0
Mavac 12,44,140
T 2,124,5120
T 4,125,32
Mavac 8,44,136
T 6,125,5152
D 233,24,42,1024
Mavac 16,48,136
T 8,124,64
Mavac 20,48,140
T 10,124,5184
T 12,125,96
Mavac 28,52,140
T 14,125,5216
D 234,24,42,2048
Mavac 24,52,136
T 16,124,128
T 18,124,5248
Mavac 32,56,136
D 235,24,42,3072
Mavac 36,56,140
T 20,125,160
T 22,125,5280
Mavac 44,60,140
T 24,124,192
M0 69632
Mavac 40,60,136
T 26,124,5312
D 236,24,42,0
Mavac 48,64,136
T 28,125,224
AS 21,37,344064
Mavac 52,64,140
T 30,125,5344
Las 120,196,28,21,0
Mavac 60,68,140
T 32,124,256
Mavac 56,68,136
T 34,124,5376
T 36,125,288
Mavac 64,72,136
T 38,125,5408
Las 124,196,28,21,1024
Mavac 68,72,140
Mavac 76,76,140
Mavac 72,76,136
WV 25
Mavac 0,0,80
T 40,124,10240
AS 21,37,360448
Mavac 4,0,84
T 42,124,15360
Las 128,196,28,21,0
Mavac 12,4,84
T 44,125,10272
Mavac 8,4,80
T 46,125,15392
Mavac 16,8,80
T 48,124,10304
Mavac 20,8,84
T 50,124,15424
Mavac 28,12,84
T 52,125,10336
Mavac 24,12,80
T 54,125,15456
Mavac 32,16,80
T 56,124,10368
Mavac 36,16,84
T 58,124,15488
T 60,125,10400
Mavac 44,20,84
T 62,125,15520
Las 132,196,28,21,1024
Mavac 40,20,80
T 64,124,10432
Mavac 48,24,80
T 66,124,15552
Mavac 52,24,84
T 68,125,10464
Mavac 60,28,84
T 70,125,15584
Mavac 56,28,80
T 72,124,10496
Mavac 64,32,80
T 74,124,15616
Mavac 68,32,84
T 76,125,10528
Mavac 76,36,84
T 78,125,15648
Mavac 72,36,80
WV 20
Mavac 0,40,88
AS 42,36,1003520
M0 70656
Mavac 4,40,92
T 0,124,20480
D 232,24,42,0
Mavac 12,44,92
T 2,124,25600
T 4,125,20512
Mavac 8,44,88
T 6,125,25632
D 233,24,42,1024
Mavac 16,48,88
T 8,124,20544
Mavac 20,48,92
T 10,124,25664
T 12,125,20576
Mavac 28,52,92
T 14,125,25696
D 234,24,42,2048
Mavac 24,52,88
T 16,124,20608
T 18,124,25728
Mavac 32,56,88
D 235,24,42,3072
Mavac 36,56,92
T 20,125,20640
T 22,125,25760
Mavac 44,60,92
T 24,124,20672
M0 74752
Mavac 40,60,88
T 26,124,25792
D 236,24,42,0
Mavac 48,64,88
T 28,125,20704
AS 21,37,376832
Mavac 52,64,92
T 30,125,25824
Las 136,196,28,21,0
Mavac 60,68,92
T 32,124,20736
Mavac 56,68,88
T 34,124,25856
T 36,125,20768
Mavac 64,72,88
T 38,125,25888
Las 140,196,28,21,1024
Mavac 68,72,92
Mavac 76,76,92
Mavac 72,76,88
WV 25
Mavac 0,0,96
T 40,124,30720
AS 21,37,393216
Mavac 4,0,100
T 42,124,35840
Las 80,196,28,21,0
Mavac 12,4,100
T 44,125,30752
Mavac 8,4,96
T 46,125,35872
Mavac 16,8,96
T 48,124,30784
Mavac 20,8,100
T 50,124,35904
Mavac 28,12,100
T 52,125,30816
Mavac 24,12,96
T 54,125,35936
Mavac 32,16,96
T 56,124,30848
Mavac 36,16,100
T 58,124,35968
T 60,125,30880
Mavac 44,20,100
T 62,125,36000
Las 84,196,28,21,1024
Mavac 40,20,96
T 64,124,30912
Mavac 48,24,96
T 66,124,36032
Mavac 52,24,100
T 68,125,30944
Mavac 60,28,100
T 70,125,36064
Mavac 56,28,96
T 72,124,30976
Mavac 64,32,96
T 74,124,36096
Mavac 68,32,100
T 76,125,31008
Mavac 76,36,100
T 78,125,36128
Mavac 72,36,96
WV 20
B
Mavac 0,40,104
AS 42,36,1146880
M0 106496
Mavac 4,40,108
T 0,126,0
D 232,24,42,0
Mavac 12,44,108
T 2,126,5120
T 4,127,32
Mavac 8,44,104
T 6,127,5152
D 233,24,42,1024
Mavac 16,48,104
T 8,126,64
Mavac 20,48,108
T 10,126,5184
T 12,127,96
Mavac 28,52,108
T 14,127,5216
D 234,24,42,2048
Mavac 24,52,104
T 16,126,128
T 18,126,5248
Mavac 32,56,104
D 235,24,42,3072
Mavac 36,56,108
T 20,127,160
T 22,127,5280
Mavac 44,60,108
T 24,126,192
M0 110592
Mavac 40,60,104
T 26,126,5312
D 236,24,42,0
Mavac 48,64,104
T 28,127,224
AS 21,37,409600
Mavac 52,64,108
T 30,127,5344
Las 88,196,28,21,0
Mavac 60,68,108
T 32,126,256
Mavac 56,68,104
T 34,126,5376
T 36,127,288
Mavac 64,72,104
T 38,127,5408
Las 92,196,28,21,1024
Mavac 68,72,108
Mavac 76,76,108
Mavac 72,76,104
WV 25
Mavac 0,0,112
T 40,126,10240
AS 21,37,425984
Mavac 4,0,116
T 42,126,15360
Las 96,196,28,21,0
Mavac 12,4,116
T 44,127,10272
Mavac 8,4,112
T 46,127,15392
Mavac 16,8,112
T 48,126,10304
Mavac 20,8,116
T 50,126,15424
Mavac 28,12,116
T 52,127,10336
Mavac 24,12,112
T 54,127,15456
Mavac 32,16,112
T 56,126,10368
Mavac 36,16,116
T 58,126,15488
T 60,127,10400
Mavac 44,20,116
T 62,127,15520
Las 100,196,28,21,1024
Mavac 40,20,112
T 64,126,10432
Mavac 48,24,112
T 66,126,15552
Mavac 52,24,116
T 68,127,10464
Mavac 60,28,116
T 70,127,15584
Mavac 56,28,112
T 72,126,10496
Mavac 64,32,112
T 74,126,15616
Mavac 68,32,116
T 76,127,10528
Mavac 76,36,116
T 78,127,15648
Mavac 72,36,112
WV 20
Mavac 0,40,120
AS 42,36,1167360
M0 111616
Mavac 4,40,124
T 0,126,20480
D 232,24,42,0
Mavac 12,44,124
T 2,126,25600
T 4,127,20512
Mavac 8,44,120
T 6,127,25632
D 233,24,42,1024
Mavac 16,48,120
T 8,126,20544
Mavac 20,48,124
T 10,126,25664
T 12,127,20576
Mavac 28,52,124
T 14,127,25696
D 234,24,42,2048
Mavac 24,52,120
T 16,126,20608
T 18,126,25728
Mavac 32,56,120
D 235,24,42,3072
Mavac 36,56,124
T 20,127,20640
T 22,127,25760
Mavac 44,60,124
T 24,126,20672
M0 115712
Mavac 40,60,120
T 26,126,25792
D 236,24,42,0
Mavac 48,64,120
T 28,127,20704
AS 21,37,442368
Mavac 52,64,124
T 30,127,25824
Las 104,196,28,21,0
Mavac 60,68,124
T 32,126,20736
Mavac 56,68,120
T 34,126,25856
T 36,127,20768
Mavac 64,72,120
T 38,127,25888
Las 108,196,28,21,1024
Mavac 68,72,124
Mavac 76,76,124
Mavac 72,76,120
WV 25
Mavac 0,0,128
T 40,126,30720
AS 21,37,458752
Mavac 4,0,132
T 42,126,35840
Las 112,196,28,21,0
Mavac 12,4,132
)A"
R"A(T 44,127,30752
Mavac 8,4,128
T 46,127,35872
Mavac 16,8,128
T 48,126,30784
Mavac 20,8,132
T 50,126,35904
Mavac 28,12,132
T 52,127,30816
Mavac 24,12,128
T 54,127,35936
Mavac 32,16,128
T 56,126,30848
Mavac 36,16,132
T 58,126,35968
T 60,127,30880
Mavac 44,20,132
T 62,127,36000
Las 116,196,28,21,1024
Mavac 40,20,128
T 64,126,30912
Mavac 48,24,128
T 66,126,36032
Mavac 52,24,132
T 68,127,30944
Mavac 60,28,132
T 70,127,36064
Mavac 56,28,128
T 72,126,30976
Mavac 64,32,128
T 74,126,36096
Mavac 68,32,132
T 76,127,31008
Mavac 76,36,132
T 78,127,36128
Mavac 72,36,128
WV 20
B
Mavac 0,40,136
T 0,122,0
AS 21,37,475136
Mavac 4,40,140
T 2,122,5120
Las 120,196,28,21,0
Mavac 12,44,140
T 4,123,32
Mavac 8,44,136
T 6,123,5152
Mavac 16,48,136
T 8,122,64
Mavac 20,48,140
T 10,122,5184
Mavac 28,52,140
T 12,123,96
Mavac 24,52,136
T 14,123,5216
Mavac 32,56,136
T 16,122,128
Mavac 36,56,140
T 18,122,5248
T 20,123,160
Mavac 44,60,140
T 22,123,5280
Las 124,196,28,21,1024
Mavac 40,60,136
T 24,122,192
Mavac 48,64,136
T 26,122,5312
Mavac 52,64,140
T 28,123,224
Mavac 60,68,140
T 30,123,5344
Mavac 56,68,136
T 32,122,256
Mavac 64,72,136
T 34,122,5376
Mavac 68,72,140
T 36,123,288
Mavac 76,76,140
T 38,123,5408
Mavac 72,76,136
WV 20
Mavac 0,0,80
T 40,122,10240
AS 21,37,491520
Mavac 4,0,84
T 42,122,15360
Las 128,196,28,21,0
Mavac 12,4,84
T 44,123,10272
Mavac 8,4,80
T 46,123,15392
Mavac 16,8,80
T 48,122,10304
Mavac 20,8,84
T 50,122,15424
Mavac 28,12,84
T 52,123,10336
Mavac 24,12,80
T 54,123,15456
Mavac 32,16,80
T 56,122,10368
Mavac 36,16,84
T 58,122,15488
T 60,123,10400
Mavac 44,20,84
T 62,123,15520
Las 132,196,28,21,1024
Mavac 40,20,80
T 64,122,10432
Mavac 48,24,80
T 66,122,15552
Mavac 52,24,84
T 68,123,10464
Mavac 60,28,84
T 70,123,15584
Mavac 56,28,80
T 72,122,10496
Mavac 64,32,80
T 74,122,15616
Mavac 68,32,84
T 76,123,10528
Mavac 76,36,84
T 78,123,15648
Mavac 72,36,80
WV 15
Mavac 0,40,88
T 0,122,20480
AS 21,37,507904
Mavac 4,40,92
T 2,122,25600
Las 136,196,28,21,0
Mavac 12,44,92
T 4,123,20512
Mavac 8,44,88
T 6,123,25632
Mavac 16,48,88
T 8,122,20544
Mavac 20,48,92
T 10,122,25664
Mavac 28,52,92
T 12,123,20576
Mavac 24,52,88
T 14,123,25696
Mavac 32,56,88
T 16,122,20608
Mavac 36,56,92
T 18,122,25728
T 20,123,20640
Mavac 44,60,92
T 22,123,25760
Las 140,196,28,21,1024
Mavac 40,60,88
T 24,122,20672
Mavac 48,64,88
T 26,122,25792
Mavac 52,64,92
T 28,123,20704
Mavac 60,68,92
T 30,123,25824
Mavac 56,68,88
T 32,122,20736
Mavac 64,72,88
T 34,122,25856
Mavac 68,72,92
T 36,123,20768
Mavac 76,76,92
T 38,123,25888
Mavac 72,76,88
WV 15
Mavac 0,0,96
T 40,122,30720
Mavac 4,0,100
T 42,122,35840
Mavac 12,4,100
T 44,123,30752
Mavac 8,4,96
T 46,123,35872
Mavac 16,8,96
T 48,122,30784
Mavac 20,8,100
T 50,122,35904
Mavac 28,12,100
T 52,123,30816
Mavac 24,12,96
T 54,123,35936
Mavac 32,16,96
T 56,122,30848
Mavac 36,16,100
T 58,122,35968
Mavac 44,20,100
T 60,123,30880
Mavac 40,20,96
T 62,123,36000
Mavac 48,24,96
T 64,122,30912
Mavac 52,24,100
T 66,122,36032
Mavac 60,28,100
T 68,123,30944
Mavac 56,28,96
T 70,123,36064
Mavac 64,32,96
T 72,122,30976
Mavac 68,32,100
T 74,122,36096
Mavac 76,36,100
T 76,123,31008
Mavac 72,36,96
T 78,123,36128
WV 8
B
Mavac 0,40,104
T 0,124,0
Mavac 4,40,108
T 2,124,5120
Mavac 12,44,108
T 4,125,32
Mavac 8,44,104
T 6,125,5152
Mavac 16,48,104
T 8,124,64
Mavac 20,48,108
T 10,124,5184
Mavac 28,52,108
T 12,125,96
Mavac 24,52,104
T 14,125,5216
Mavac 32,56,104
T 16,124,128
Mavac 36,56,108
T 18,124,5248
Mavac 44,60,108
T 20,125,160
Mavac 40,60,104
T 22,125,5280
Mavac 48,64,104
T 24,124,192
Mavac 52,64,108
T 26,124,5312
Mavac 60,68,108
T 28,125,224
Mavac 56,68,104
T 30,125,5344
Mavac 64,72,104
T 32,124,256
Mavac 68,72,108
T 34,124,5376
Mavac 76,76,108
T 36,125,288
Mavac 72,76,104
T 38,125,5408
WV 6
Mavac 0,0,112
T 40,124,10240
Mavac 4,0,116
T 42,124,15360
Mavac 12,4,116
T 44,125,10272
Mavac 8,4,112
T 46,125,15392
Mavac 16,8,112
T 48,124,10304
Mavac 20,8,116
T 50,124,15424
Mavac 28,12,116
T 52,125,10336
Mavac 24,12,112
T 54,125,15456
Mavac 32,16,112
T 56,124,10368
Mavac 36,16,116
T 58,124,15488
Mavac 44,20,116
T 60,125,10400
Mavac 40,20,112
T 62,125,15520
Mavac 48,24,112
T 64,124,10432
Mavac 52,24,116
T 66,124,15552
Mavac 60,28,116
T 68,125,10464
Mavac 56,28,112
T 70,125,15584
Mavac 64,32,112
T 72,124,10496
Mavac 68,32,116
T 74,124,15616
Mavac 76,36,116
T 76,125,10528
Mavac 72,36,112
T 78,125,15648
WV 4
Mavac 0,40,120
T 0,124,20480
Mavac 4,40,124
T 2,124,25600
Mavac 12,44,124
T 4,125,20512
Mavac 8,44,120
T 6,125,25632
Mavac 16,48,120
T 8,124,20544
Mavac 20,48,124
T 10,124,25664
Mavac 28,52,124
T 12,125,20576
Mavac 24,52,120
T 14,125,25696
Mavac 32,56,120
T 16,124,20608
Mavac 36,56,124
T 18,124,25728
Mavac 44,60,124
T 20,125,20640
Mavac 40,60,120
T 22,125,25760
Mavac 48,64,120
T 24,124,20672
Mavac 52,64,124
T 26,124,25792
Mavac 60,68,124
T 28,125,20704
Mavac 56,68,120
T 30,125,25824
Mavac 64,72,120
T 32,124,20736
Mavac 68,72,124
T 34,124,25856
Mavac 76,76,124
T 36,125,20768
Mavac 72,76,120
T 38,125,25888
WV 2
Mavac 0,0,128
T 40,124,30720
Mavac 4,0,132
T 42,124,35840
Mavac 12,4,132
T 44,125,30752
Mavac 8,4,128
T 46,125,35872
Mavac 16,8,128
T 48,124,30784
Mavac 20,8,132
T 50,124,35904
Mavac 28,12,132
T 52,125,30816
Mavac 24,12,128
T 54,125,35936
Mavac 32,16,128
T 56,124,30848
Mavac 36,16,132
T 58,124,35968
Mavac 44,20,132
T 60,125,30880
Mavac 40,20,128
T 62,125,36000
Mavac 48,24,128
T 64,124,30912
Mavac 52,24,132
T 66,124,36032
Mavac 60,28,132
T 68,125,30944
Mavac 56,28,128
T 70,125,36064
Mavac 64,32,128
T 72,124,30976
Mavac 68,32,132
T 74,124,36096
Mavac 76,36,132
T 76,125,31008
Mavac 72,36,128
T 78,125,36128
WV 0
B
Mavac 0,40,136
Mavac 4,40,140
Mavac 12,44,140
Mavac 8,44,136
Mavac 16,48,136
Mavac 20,48,140
Mavac 28,52,140
Mavac 24,52,136
Mavac 32,56,136
Mavac 36,56,140
Mavac 44,60,140
Mavac 40,60,136
Mavac 48,64,136
Mavac 52,64,140
Mavac 60,68,140
Mavac 56,68,136
Mavac 64,72,136
Mavac 68,72,140
Mavac 76,76,140
Mavac 72,76,136
OMD0%=:
G 0,0
G 4,8
G 1,1
G 5,9
G 2,2
G 6,10
G 3,3
G 7,11
C 8,0,1
C 9,2,3
C 10,4,5
C 11,6,7
Svs 8,196,32,38,0
G 16,16
G 20,24
G 17,17
G 21,25
G 18,18
G 22,26
G 19,19
G 23,27
C 24,16,17
C 25,18,19
C 26,20,21
C 27,22,23
Svs 24,196,32,38,1024
G 32,32
G 36,40
G 33,33
G 37,41
G 34,34
G 38,42
G 35,35
G 39,43
C 40,32,33
C 41,34,35
C 42,36,37
C 43,38,39
Svs 40,196,32,38,2048
G 48,48
G 52,56
G 49,49
G 53,57
G 50,50
G 54,58
G 51,51
G 55,59
C 56,48,49
C 57,50,51
C 58,52,53
C 59,54,55
Svs 56,196,32,38,3072
G 0,64
G 4,72
G 1,65
G 5,73
G 2,66
G 6,74
G 3,67
G 7,75
C 8,0,1
C 9,2,3
C 10,4,5
C 11,6,7
AS 16,38,4096
Svs 8,196,32,16,0
AS 38,38,20480
G 16,4
G 20,12
G 17,5
G 21,13
G 18,6
G 22,14
G 19,7
G 23,15
C 24,16,17
C 25,18,19
C 26,20,21
C 27,22,23
Svs 24,196,32,38,0
G 32,20
G 36,28
G 33,21
G 37,29
G 34,22
G 38,30
G 35,23
G 39,31
C 40,32,33
C 41,34,35
C 42,36,37
C 43,38,39
Svs 40,196,32,38,1024
G 48,36
G 52,44
G 49,37
G 53,45
G 50,38
G 54,46
G 51,39
G 55,47
C 56,48,49
C 57,50,51
C 58,52,53
C 59,54,55
Svs 56,196,32,38,2048
G 0,52
G 4,60
G 1,53
G 5,61
G 2,54
G 6,62
G 3,55
G 7,63
C 8,0,1
C 9,2,3
C 10,4,5
C 11,6,7
Svs 8,196,32,38,3072
G 16,68
G 20,76
G 17,69
G 21,77
G 18,70
G 22,78
G 19,71
G 23,79
C 24,16,17
C 25,18,19
C 26,20,21
C 27,22,23
AS 16,38,4096
Svs 24,196,32,16,0
buffer_load_dwordx2 v[40:41], v100, s[44:47], s65 offen offset:0
buffer_load_dwordx2 v[42:43], v100, s[44:47], s65 offen offset:64
buffer_load_dwordx2 v[44:45], v100, s[44:47], s65 offen offset:128
buffer_load_dwordx2 v[46:47], v100, s[44:47], s65 offen offset:192
buffer_load_dwordx2 v[48:49], v100, s[44:47], s65 offen offset:256
buffer_load_dwordx2 v[50:51], v101, s[48:51], s67 offen offset:0
buffer_load_dwordx2 v[52:53], v101, s[48:51], s67 offen offset:64
buffer_load_dwordx2 v[54:55], v101, s[48:51], s67 offen offset:128
buffer_load_dwordx2 v[56:57], v101, s[48:51], s67 offen offset:192
buffer_load_dwordx2 v[58:59], v101, s[48:51], s67 offen offset:256
s_mov_b64 s[24:25], s[70:71]
SM 26,0x21c000
SM 27,0x20000
s_mov_b64 s[28:29], s[76:77]
SM 30,0x21c000
SM 31,0x20000
s_waitcnt vmcnt(0)
B
s_lshl_b32 s21, s39, 8
AS 21,21,49152
s_lshl_b32 s22, s43, 2
s_add_u32 s21, s21, s22
v_mov_b32 v247, s21
v_mov_b32 v246, s79
s_mov_b64 exec, 1
global_store_dword v247, v246, s[14:15] sc1
s_mov_b64 exec, -1
N 3
s_and_b32 s16, s10, 1
s_cselect_b32 s16, 54, s78
s_cmp_lt_u32 s16, 54
s_cbranch_scc0 NOST0%=
VA 98,0,196
DWa 98,144,0
DWa 98,148,40960
DWa 98,152,1024
DWa 98,156,41984
DWa 98,160,2048
DWa 98,164,43008
DWa 98,168,3072
DWa 98,172,44032
DWa 98,176,4096
DWa 98,180,45056
DWa 98,184,5120
DWa 98,188,46080
DWa 98,192,6144
DWa 98,196,47104
DWa 98,200,7168
DWa 98,204,48128
DWa 98,208,8192
DWa 98,212,49152
DWa 98,216,9216
DWa 98,220,50176
NOST0%=:
s_lshl_b32 s21, s39, 8
AS 21,21,49152
v_mov_b32 v247, s21
v_min_u32 v246, 15, v254
v_lshl_add_u32 v247, v246, 2, v247
global_load_dword v246, v247, s[14:15] sc1
s_waitcnt vmcnt(0)
v_cmp_ne_u32 vcc, s79, v246
N 4
s_cbranch_vccnz PP_SPIN%=
PP_OK%=:
B
s_and_b32 s16, s10, 1
s_cselect_b32 s16, 54, s78
s_cmp_lt_u32 s16, 54
s_cbranch_scc1 PF0%=
Lvs 0,102,32,64,0
Lvs 8,102,32,64,1024
Lvs 16,102,32,64,2048
Lvs 24,102,32,64,3072
AS 21,64,4096
Lvs 32,102,32,21,0
AS 64,64,2621440
Lvs 4,102,32,64,0
Lvs 12,102,32,64,1024
Lvs 20,102,32,64,2048
Lvs 28,102,32,64,3072
AS 21,64,4096
Lvs 36,102,32,21,0
v_and_b32 v108, 7, v254
v_lshlrev_b32 v108, 4, v108
VA 108,152064,108
s_lshl_b32 s16, s11, 7
v_add_u32 v103, s16, v108
LO 110,50
HI 111,50
LO 112,51
HI 113,51
LO 114,52
HI 115,52
LO 116,53
HI 117,53
LO 118,54
HI 119,54
LO 120,55
HI 121,55
LO 122,56
HI 123,56
LO 124,57
HI 125,57
LO 126,58
HI 127,58
LO 128,59
HI 129,59
PA 104,110,112
PA 104,104,114
PA 104,104,116
PA 104,104,118
PA 104,104,120
PA 104,104,122
PA 104,104,124
PA 104,104,126
PA 104,104,128
Fad 99,104,105
s_waitcnt vmcnt(9)
LO 88,0
HI 89,0
s_waitcnt vmcnt(4)
LO 104,4
HI 105,4
PA 88,88,104
LO 90,1
HI 91,1
LO 104,5
HI 105,5
PA 90,90,104
LO 92,2
HI 93,2
LO 104,6
HI 105,6
PA 92,92,104
LO 94,3
HI 95,3
LO 104,7
HI 105,7
PA 94,94,104
v_permlane32_swap_b32 v88, v92
v_permlane32_swap_b32 v89, v93
v_permlane32_swap_b32 v90, v94
v_permlane32_swap_b32 v91, v95
PA 64,88,92
PA 66,90,94
LO 104,40
HI 105,40
LO 106,41
HI 107,41
PA 64,64,104
PA 66,66,106
LO 88,8
HI 89,8
s_waitcnt vmcnt(3)
LO 104,12
HI 105,12
PA 88,88,104
LO 90,9
HI 91,9
LO 104,13
HI 105,13
PA 90,90,104
LO 92,10
HI 93,10
LO 104,14
HI 105,14
PA 92,92,104
LO 94,11
HI 95,11
LO 104,15
HI 105,15
PA 94,94,104
v_permlane32_swap_b32 v88, v92
v_permlane32_swap_b32 v89, v93
v_permlane32_swap_b32 v90, v94
v_permlane32_swap_b32 v91, v95
PA 68,88,92
PA 70,90,94
LO 104,42
HI 105,42
LO 106,43
HI 107,43
PA 68,68,104
PA 70,70,106
LO 88,16
HI 89,16
s_waitcnt vmcnt(2)
LO 104,20
HI 105,20
PA 88,88,104
LO 90,17
HI 91,17
LO 104,21
HI 105,21
PA 90,90,104
LO 92,18
HI 93,18
LO 104,22
HI 105,22
PA 92,92,104
LO 94,19
HI 95,19
LO 104,23
HI 105,23
PA 94,94,104
v_permlane32_swap_b32 v88, v92
v_permlane32_swap_b32 v89, v93
v_permlane32_swap_b32 v90, v94
v_permlane32_swap_b32 v91, v95
PA 72,88,92
PA 74,90,94
LO 104,44
HI 105,44
LO 106,45
HI 107,45
PA 72,72,104
PA 74,74,106
LO 88,24
HI 89,24
s_waitcnt vmcnt(1)
LO 104,28
HI 105,28
PA 88,88,104
LO 90,25
HI 91,25
LO 104,29
HI 105,29
PA 90,90,104
LO 92,26
HI 93,26
LO 104,30
HI 105,30
PA 92,92,104
LO 94,27
HI 95,27
LO 104,31
HI 105,31
PA 94,94,104
v_permlane32_swap_b32 v88, v92
v_permlane32_swap_b32 v89, v93
v_permlane32_swap_b32 v90, v94
v_permlane32_swap_b32 v91, v95
PA 76,88,92
PA 78,90,94
LO 104,46
HI 105,46
LO 106,47
HI 107,47
PA 76,76,104
PA 78,78,106
LO 88,32
HI 89,32
s_waitcnt vmcnt(0)
LO 104,36
HI 105,36
PA 88,88,104
LO 90,33
HI 91,33
LO 104,37
HI 105,37
PA 90,90,104
LO 92,34
HI 93,34
LO 104,38
HI 105,38
PA 92,92,104
LO 94,35
HI 95,35
LO 104,39
HI 105,39
PA 94,94,104
v_permlane32_swap_b32 v88, v92
v_permlane32_swap_b32 v89, v93
v_permlane32_swap_b32 v90, v94
v_permlane32_swap_b32 v91, v95
PA 80,88,92
PA 82,90,94
LO 104,48
HI 105,48
LO 106,49
HI 107,49
PA 80,80,104
PA 82,82,106
PA 104,64,66
v_pk_mul_f32 v[106:107], v[64:65], v[64:65]
v_pk_mul_f32 v[88:89], v[64:65], v[110:111]
v_pk_fma_f32 v[106:107], v[66:67], v[66:67], v[106:107]
)A"
R"A(v_pk_fma_f32 v[88:89], v[66:67], v[112:113], v[88:89]
PA 104,104,68
v_pk_fma_f32 v[106:107], v[68:69], v[68:69], v[106:107]
v_pk_fma_f32 v[88:89], v[68:69], v[114:115], v[88:89]
PA 104,104,70
v_pk_fma_f32 v[106:107], v[70:71], v[70:71], v[106:107]
v_pk_fma_f32 v[88:89], v[70:71], v[116:117], v[88:89]
PA 104,104,72
v_pk_fma_f32 v[106:107], v[72:73], v[72:73], v[106:107]
v_pk_fma_f32 v[88:89], v[72:73], v[118:119], v[88:89]
PA 104,104,74
v_pk_fma_f32 v[106:107], v[74:75], v[74:75], v[106:107]
v_pk_fma_f32 v[88:89], v[74:75], v[120:121], v[88:89]
PA 104,104,76
v_pk_fma_f32 v[106:107], v[76:77], v[76:77], v[106:107]
v_pk_fma_f32 v[88:89], v[76:77], v[122:123], v[88:89]
PA 104,104,78
v_pk_fma_f32 v[106:107], v[78:79], v[78:79], v[106:107]
v_pk_fma_f32 v[88:89], v[78:79], v[124:125], v[88:89]
PA 104,104,80
v_pk_fma_f32 v[106:107], v[80:81], v[80:81], v[106:107]
v_pk_fma_f32 v[88:89], v[80:81], v[126:127], v[88:89]
PA 104,104,82
v_pk_fma_f32 v[106:107], v[82:83], v[82:83], v[106:107]
v_pk_fma_f32 v[88:89], v[82:83], v[128:129], v[88:89]
Fad 96,104,105
Fad 97,106,107
Fad 98,88,89
v_add_f32_dpp v96, v96, v96 row_ror:8 row_mask:0xf bank_mask:0xf
v_add_f32_dpp v97, v97, v97 row_ror:8 row_mask:0xf bank_mask:0xf
v_add_f32_dpp v98, v98, v98 row_ror:8 row_mask:0xf bank_mask:0xf
v_add_f32_dpp v99, v99, v99 row_ror:8 row_mask:0xf bank_mask:0xf
MV 104,96
MV 105,97
MV 106,98
MV 107,99
v_permlane16_swap_b32 v96, v104
v_permlane16_swap_b32 v97, v105
v_permlane16_swap_b32 v98, v106
v_permlane16_swap_b32 v99, v107
Fad 96,96,104
Fad 97,97,105
Fad 98,98,106
Fad 99,99,107
MV 104,96
MV 105,97
MV 106,98
MV 107,99
v_permlane32_swap_b32 v96, v104
v_permlane32_swap_b32 v97, v105
v_permlane32_swap_b32 v98, v106
v_permlane32_swap_b32 v99, v107
Fad 96,96,104
Fad 97,97,105
Fad 98,98,106
Fad 99,99,107
ds_write_b128 v103, v[96:99]
W 0
B
Rv 0,108,0
Rv 4,108,128
Rv 8,108,256
Rv 12,108,384
W 2
PA 0,0,4
PA 2,2,6
W 0
PA 8,8,12
PA 10,10,14
PA 0,0,8
PA 2,2,10
v_mul_f32 v88, 0x3acccccd, v0
v_mul_f32 v90, 0x3acccccd, v1
v_fma_f32 v90, -v88, v88, v90
v_max_f32 v90, 0, v90
v_add_f32 v91, 0x3727c5ac, v90
v_rsq_f32 v91, v91
v_fma_f32 v92, -v88, v3, v2
Fmu 92,92,91
v_add_f32 v104, 1.0, v91
Fmu 105,88,91
v_xor_b32 v105, 0x80000000, v105
FM 64,64,104,105
FM 65,65,104,105
FM 66,66,104,105
FM 67,67,104,105
C 24,64,65
C 25,66,67
FM 68,68,104,105
FM 69,69,104,105
FM 70,70,104,105
FM 71,71,104,105
C 26,68,69
C 27,70,71
FM 72,72,104,105
FM 73,73,104,105
FM 74,74,104,105
FM 75,75,104,105
C 28,72,73
C 29,74,75
FM 76,76,104,105
FM 77,77,104,105
FM 78,78,104,105
FM 79,79,104,105
C 30,76,77
C 31,78,79
FM 80,80,104,105
FM 81,81,104,105
FM 82,82,104,105
FM 83,83,104,105
C 32,80,81
C 33,82,83
s_add_u32 s36, s14, 0x10000
s_addc_u32 s37, s15, 0
SM 38,0x8000
SM 39,0x20000
MV 124,92
MV 125,88
MV 126,91
v_mov_b32 v127, s79
s_lshl_b32 s16, s66, 4
s_mov_b64 exec, 0xff
Svs 124,196,36,16,0
s_mov_b64 exec, -1
s_and_b32 s16, s10, 7
s_lshl_b32 s16, s16, 8
AS 16,16,53248
v_mov_b32 v162, s16
SM 20,0
PS_N0%=:
global_load_dwordx2 v[160:161], v162, s[14:15] sc1
s_waitcnt vmcnt(0)
v_readfirstlane_b32 s17, v161
N 3
s_cmp_eq_u32 s17, s79
s_cbranch_scc1 PSOK_N0%=
AS 20,20,1
s_cmp_lt_u32 s20, 0x10000
s_cbranch_scc0 PSOK_N0%=
s_sleep 8
s_branch PS_N0%=
PSOK_N0%=:
v_mov_b32 v162, 151552
ds_write_b32 v162, v160
W 0
B
v_mov_b32 v162, 151552
ds_read_b32 v160, v162
W 0
v_readfirstlane_b32 s16, v160
N 3
s_and_b32 s68, s16, 0xffff
s_lshr_b32 s69, s16, 16
v_and_b32 v160, 7, v254
v_add_u32 v160, s66, v160
v_cmp_ne_u32 vcc, s68, v160
v_cmp_ne_u32 s[16:17], s69, v160
N 4
s_and_b64 exec, vcc, s[16:17]
buffer_store_dwordx2 v[24:25], v100, s[52:55], s65 offen offset:0 sc1
buffer_store_dwordx2 v[26:27], v100, s[52:55], s65 offen offset:64 sc1
buffer_store_dwordx2 v[28:29], v100, s[52:55], s65 offen offset:128 sc1
buffer_store_dwordx2 v[30:31], v100, s[52:55], s65 offen offset:192 sc1
buffer_store_dwordx2 v[32:33], v100, s[52:55], s65 offen offset:256 sc1
s_mov_b64 exec, -1
s_branch PE0%=
PF0%=:
Lvs 0,102,32,64,0
Lvs 8,102,32,64,1024
Lvs 16,102,32,64,2048
Lvs 24,102,32,64,3072
AS 21,64,4096
Lvs 32,102,32,21,0
AS 64,64,2621440
Lvs 4,102,32,64,0
Lvs 12,102,32,64,1024
Lvs 20,102,32,64,2048
Lvs 28,102,32,64,3072
AS 21,64,4096
Lvs 36,102,32,21,0
v_and_b32 v108, 7, v254
v_lshlrev_b32 v108, 4, v108
VA 108,152064,108
s_lshl_b32 s16, s11, 7
v_add_u32 v103, s16, v108
LO 110,50
HI 111,50
LO 112,51
HI 113,51
LO 114,52
HI 115,52
LO 116,53
HI 117,53
LO 118,54
HI 119,54
LO 120,55
HI 121,55
LO 122,56
HI 123,56
LO 124,57
HI 125,57
LO 126,58
HI 127,58
LO 128,59
HI 129,59
PA 104,110,112
PA 104,104,114
PA 104,104,116
PA 104,104,118
PA 104,104,120
PA 104,104,122
PA 104,104,124
PA 104,104,126
PA 104,104,128
Fad 99,104,105
s_waitcnt vmcnt(9)
LO 88,0
HI 89,0
s_waitcnt vmcnt(4)
LO 104,4
HI 105,4
PA 88,88,104
LO 90,1
HI 91,1
LO 104,5
HI 105,5
PA 90,90,104
LO 92,2
HI 93,2
LO 104,6
HI 105,6
PA 92,92,104
LO 94,3
HI 95,3
LO 104,7
HI 105,7
PA 94,94,104
v_permlane32_swap_b32 v88, v92
v_permlane32_swap_b32 v89, v93
v_permlane32_swap_b32 v90, v94
v_permlane32_swap_b32 v91, v95
PA 64,88,92
PA 66,90,94
LO 104,40
HI 105,40
LO 106,41
HI 107,41
PA 64,64,104
PA 66,66,106
LO 88,8
HI 89,8
s_waitcnt vmcnt(3)
LO 104,12
HI 105,12
PA 88,88,104
LO 90,9
HI 91,9
LO 104,13
HI 105,13
PA 90,90,104
LO 92,10
HI 93,10
LO 104,14
HI 105,14
PA 92,92,104
LO 94,11
HI 95,11
LO 104,15
HI 105,15
PA 94,94,104
v_permlane32_swap_b32 v88, v92
v_permlane32_swap_b32 v89, v93
v_permlane32_swap_b32 v90, v94
v_permlane32_swap_b32 v91, v95
PA 68,88,92
PA 70,90,94
LO 104,42
HI 105,42
LO 106,43
HI 107,43
PA 68,68,104
PA 70,70,106
LO 88,16
HI 89,16
s_waitcnt vmcnt(2)
LO 104,20
HI 105,20
PA 88,88,104
LO 90,17
HI 91,17
LO 104,21
HI 105,21
PA 90,90,104
LO 92,18
HI 93,18
LO 104,22
HI 105,22
PA 92,92,104
LO 94,19
HI 95,19
LO 104,23
HI 105,23
PA 94,94,104
v_permlane32_swap_b32 v88, v92
v_permlane32_swap_b32 v89, v93
v_permlane32_swap_b32 v90, v94
v_permlane32_swap_b32 v91, v95
PA 72,88,92
PA 74,90,94
LO 104,44
HI 105,44
LO 106,45
HI 107,45
PA 72,72,104
PA 74,74,106
LO 88,24
HI 89,24
s_waitcnt vmcnt(1)
LO 104,28
HI 105,28
PA 88,88,104
LO 90,25
HI 91,25
LO 104,29
HI 105,29
PA 90,90,104
LO 92,26
HI 93,26
LO 104,30
HI 105,30
PA 92,92,104
LO 94,27
HI 95,27
LO 104,31
HI 105,31
PA 94,94,104
v_permlane32_swap_b32 v88, v92
v_permlane32_swap_b32 v89, v93
v_permlane32_swap_b32 v90, v94
v_permlane32_swap_b32 v91, v95
PA 76,88,92
PA 78,90,94
LO 104,46
HI 105,46
LO 106,47
HI 107,47
PA 76,76,104
PA 78,78,106
LO 88,32
HI 89,32
s_waitcnt vmcnt(0)
LO 104,36
HI 105,36
PA 88,88,104
LO 90,33
HI 91,33
LO 104,37
HI 105,37
PA 90,90,104
LO 92,34
HI 93,34
LO 104,38
HI 105,38
PA 92,92,104
LO 94,35
HI 95,35
LO 104,39
HI 105,39
PA 94,94,104
v_permlane32_swap_b32 v88, v92
v_permlane32_swap_b32 v89, v93
v_permlane32_swap_b32 v90, v94
v_permlane32_swap_b32 v91, v95
PA 80,88,92
PA 82,90,94
LO 104,48
HI 105,48
LO 106,49
HI 107,49
PA 80,80,104
PA 82,82,106
PA 104,64,66
v_pk_mul_f32 v[106:107], v[64:65], v[64:65]
v_pk_mul_f32 v[88:89], v[64:65], v[110:111]
v_pk_fma_f32 v[106:107], v[66:67], v[66:67], v[106:107]
v_pk_fma_f32 v[88:89], v[66:67], v[112:113], v[88:89]
PA 104,104,68
v_pk_fma_f32 v[106:107], v[68:69], v[68:69], v[106:107]
v_pk_fma_f32 v[88:89], v[68:69], v[114:115], v[88:89]
PA 104,104,70
v_pk_fma_f32 v[106:107], v[70:71], v[70:71], v[106:107]
v_pk_fma_f32 v[88:89], v[70:71], v[116:117], v[88:89]
PA 104,104,72
v_pk_fma_f32 v[106:107], v[72:73], v[72:73], v[106:107]
v_pk_fma_f32 v[88:89], v[72:73], v[118:119], v[88:89]
PA 104,104,74
v_pk_fma_f32 v[106:107], v[74:75], v[74:75], v[106:107]
v_pk_fma_f32 v[88:89], v[74:75], v[120:121], v[88:89]
PA 104,104,76
v_pk_fma_f32 v[106:107], v[76:77], v[76:77], v[106:107]
v_pk_fma_f32 v[88:89], v[76:77], v[122:123], v[88:89]
PA 104,104,78
v_pk_fma_f32 v[106:107], v[78:79], v[78:79], v[106:107]
v_pk_fma_f32 v[88:89], v[78:79], v[124:125], v[88:89]
PA 104,104,80
v_pk_fma_f32 v[106:107], v[80:81], v[80:81], v[106:107]
v_pk_fma_f32 v[88:89], v[80:81], v[126:127], v[88:89]
PA 104,104,82
v_pk_fma_f32 v[106:107], v[82:83], v[82:83], v[106:107]
v_pk_fma_f32 v[88:89], v[82:83], v[128:129], v[88:89]
Fad 96,104,105
Fad 97,106,107
Fad 98,88,89
v_add_f32_dpp v96, v96, v96 row_ror:8 row_mask:0xf bank_mask:0xf
v_add_f32_dpp v97, v97, v97 row_ror:8 row_mask:0xf bank_mask:0xf
v_add_f32_dpp v98, v98, v98 row_ror:8 row_mask:0xf bank_mask:0xf
v_add_f32_dpp v99, v99, v99 row_ror:8 row_mask:0xf bank_mask:0xf
MV 104,96
MV 105,97
MV 106,98
MV 107,99
v_permlane16_swap_b32 v96, v104
v_permlane16_swap_b32 v97, v105
v_permlane16_swap_b32 v98, v106
v_permlane16_swap_b32 v99, v107
Fad 96,96,104
Fad 97,97,105
Fad 98,98,106
Fad 99,99,107
MV 104,96
MV 105,97
MV 106,98
MV 107,99
v_permlane32_swap_b32 v96, v104
v_permlane32_swap_b32 v97, v105
v_permlane32_swap_b32 v98, v106
v_permlane32_swap_b32 v99, v107
Fad 96,96,104
Fad 97,97,105
Fad 98,98,106
Fad 99,99,107
ds_write_b128 v103, v[96:99]
W 0
B
Rv 0,108,0
Rv 4,108,128
Rv 8,108,256
Rv 12,108,384
W 2
PA 0,0,4
PA 2,2,6
W 0
PA 8,8,12
PA 10,10,14
PA 0,0,8
PA 2,2,10
v_mul_f32 v88, 0x3acccccd, v0
v_mul_f32 v90, 0x3acccccd, v1
v_fma_f32 v90, -v88, v88, v90
v_max_f32 v90, 0, v90
v_add_f32 v91, 0x3727c5ac, v90
v_rsq_f32 v91, v91
v_fma_f32 v92, -v88, v3, v2
Fmu 92,92,91
v_add_f32 v104, 1.0, v91
Fmu 105,88,91
v_xor_b32 v105, 0x80000000, v105
FM 64,64,104,105
FM 65,65,104,105
FM 66,66,104,105
FM 67,67,104,105
C 24,64,65
C 25,66,67
FM 68,68,104,105
FM 69,69,104,105
FM 70,70,104,105
FM 71,71,104,105
C 26,68,69
C 27,70,71
FM 72,72,104,105
FM 73,73,104,105
FM 74,74,104,105
FM 75,75,104,105
C 28,72,73
C 29,74,75
FM 76,76,104,105
FM 77,77,104,105
FM 78,78,104,105
FM 79,79,104,105
C 30,76,77
C 31,78,79
FM 80,80,104,105
FM 81,81,104,105
FM 82,82,104,105
FM 83,83,104,105
C 32,80,81
C 33,82,83
s_add_u32 s36, s14, 0x10000
s_addc_u32 s37, s15, 0
SM 38,0x8000
SM 39,0x20000
MV 124,92
MV 125,88
MV 126,91
v_mov_b32 v127, s79
s_lshl_b32 s16, s66, 4
s_mov_b64 exec, 0xff
Svs 124,196,36,16,0
s_mov_b64 exec, -1
GW 224,v24
GW 225,v25
GW 226,v26
GW 227,v27
GW 228,v28
GW 229,v29
GW 230,v30
GW 231,v31
GW 232,v32
GW 233,v33
GW 234,v100
s_add_u32 s36, s14, 0x10000
s_addc_u32 s37, s15, 0
SM 38,0x8000
SM 39,0x20000
s_lshl_b32 s17, s11, 10
SM 20,0
SW_F0%=:
Lvs 128,196,36,17,0
AS 16,17,4096
Lvs 132,196,36,16,0
AS 16,17,8192
Lvs 136,196,36,16,0
AS 16,17,12288
Lvs 140,196,36,16,0
AS 16,17,16384
Lvs 144,196,36,16,0
AS 16,17,20480
Lvs 148,196,36,16,0
AS 16,17,24576
Lvs 152,196,36,16,0
AS 16,17,28672
Lvs 156,196,36,16,0
s_waitcnt vmcnt(0)
v_xor_b32 v160, s79, v131
v_xor_b32 v161, s79, v135
v_or_b32 v160, v160, v161
v_xor_b32 v161, s79, v139
v_or_b32 v160, v160, v161
v_xor_b32 v161, s79, v143
v_or_b32 v160, v160, v161
v_xor_b32 v161, s79, v147
v_or_b32 v160, v160, v161
v_xor_b32 v161, s79, v151
v_or_b32 v160, v160, v161
v_xor_b32 v161, s79, v155
v_or_b32 v160, v160, v161
v_xor_b32 v161, s79, v159
v_or_b32 v160, v160, v161
v_cmp_ne_u32 vcc, 0, v160
N 4
s_cbranch_vccz SWOK_F0%=
AS 20,20,1
s_cmp_lt_u32 s20, 0x10000
s_cbranch_scc0 SWOK_F0%=
s_sleep 1
s_branch SW_F0%=
SWOK_F0%=:
s_lshl_b32 s16, s11, 6
v_add_u32 v161, s16, v254
v_sub_u32 v161, 2047, v161
v_ashrrev_i32 v160, 31, v128
v_or_b32 v160, 0x80000000, v160
v_xor_b32 v160, v160, v128
v_cmp_o_f32 vcc, v128, v128
v_and_b32 v160, 0xfffff800, v160
N 0
v_cndmask_b32 v160, 0, v160, vcc
v_subrev_u32 v162, 0, v161
v_or_b32 v128, v160, v162
v_ashrrev_i32 v160, 31, v132
v_or_b32 v160, 0x80000000, v160
v_xor_b32 v160, v160, v132
v_cmp_o_f32 vcc, v132, v132
v_and_b32 v160, 0xfffff800, v160
N 0
v_cndmask_b32 v160, 0, v160, vcc
v_subrev_u32 v162, 256, v161
v_or_b32 v132, v160, v162
v_ashrrev_i32 v160, 31, v136
v_or_b32 v160, 0x80000000, v160
v_xor_b32 v160, v160, v136
v_cmp_o_f32 vcc, v136, v136
v_and_b32 v160, 0xfffff800, v160
N 0
v_cndmask_b32 v160, 0, v160, vcc
v_subrev_u32 v162, 512, v161
v_or_b32 v136, v160, v162
v_ashrrev_i32 v160, 31, v140
v_or_b32 v160, 0x80000000, v160
v_xor_b32 v160, v160, v140
v_cmp_o_f32 vcc, v140, v140
v_and_b32 v160, 0xfffff800, v160
N 0
v_cndmask_b32 v160, 0, v160, vcc
v_subrev_u32 v162, 768, v161
v_or_b32 v140, v160, v162
)A"
R"A(v_ashrrev_i32 v160, 31, v144
v_or_b32 v160, 0x80000000, v160
v_xor_b32 v160, v160, v144
v_cmp_o_f32 vcc, v144, v144
v_and_b32 v160, 0xfffff800, v160
N 0
v_cndmask_b32 v160, 0, v160, vcc
v_subrev_u32 v162, 1024, v161
v_or_b32 v144, v160, v162
v_ashrrev_i32 v160, 31, v148
v_or_b32 v160, 0x80000000, v160
v_xor_b32 v160, v160, v148
v_cmp_o_f32 vcc, v148, v148
v_and_b32 v160, 0xfffff800, v160
N 0
v_cndmask_b32 v160, 0, v160, vcc
v_subrev_u32 v162, 1280, v161
v_or_b32 v148, v160, v162
v_ashrrev_i32 v160, 31, v152
v_or_b32 v160, 0x80000000, v160
v_xor_b32 v160, v160, v152
v_cmp_o_f32 vcc, v152, v152
v_and_b32 v160, 0xfffff800, v160
N 0
v_cndmask_b32 v160, 0, v160, vcc
v_subrev_u32 v162, 1536, v161
v_or_b32 v152, v160, v162
v_ashrrev_i32 v160, 31, v156
v_or_b32 v160, 0x80000000, v160
v_xor_b32 v160, v160, v156
v_cmp_o_f32 vcc, v156, v156
v_and_b32 v160, 0xfffff800, v160
N 0
v_cndmask_b32 v160, 0, v160, vcc
v_subrev_u32 v162, 1792, v161
v_or_b32 v156, v160, v162
MV 164,128
v_mov_b32 v165, 0
v_mov_b32 v166, 0
v_min_u32 v160, v164, v132
v_max_u32 v164, v164, v132
v_max_u32 v165, v165, v166
v_max_u32 v165, v165, v160
v_min_u32 v160, v164, v136
v_max_u32 v164, v164, v136
v_max_u32 v165, v165, v166
v_max_u32 v165, v165, v160
v_min_u32 v160, v164, v140
v_max_u32 v164, v164, v140
v_max_u32 v165, v165, v166
v_max_u32 v165, v165, v160
v_min_u32 v160, v164, v144
v_max_u32 v164, v164, v144
v_max_u32 v165, v165, v166
v_max_u32 v165, v165, v160
v_min_u32 v160, v164, v148
v_max_u32 v164, v164, v148
v_max_u32 v165, v165, v166
v_max_u32 v165, v165, v160
v_min_u32 v160, v164, v152
v_max_u32 v164, v164, v152
v_max_u32 v165, v165, v166
v_max_u32 v165, v165, v160
v_min_u32 v160, v164, v156
v_max_u32 v164, v164, v156
v_max_u32 v165, v165, v166
v_max_u32 v165, v165, v160
v_mov_b32_dpp v167, v164 quad_perm:[1,0,3,2] row_mask:0xf bank_mask:0xf
N 0
v_mov_b32_dpp v168, v165 quad_perm:[1,0,3,2] row_mask:0xf bank_mask:0xf
v_min_u32 v160, v164, v167
v_max_u32 v164, v164, v167
v_max_u32 v165, v165, v168
v_max_u32 v165, v165, v160
v_mov_b32_dpp v167, v164 quad_perm:[2,3,0,1] row_mask:0xf bank_mask:0xf
N 0
v_mov_b32_dpp v168, v165 quad_perm:[2,3,0,1] row_mask:0xf bank_mask:0xf
v_min_u32 v160, v164, v167
v_max_u32 v164, v164, v167
v_max_u32 v165, v165, v168
v_max_u32 v165, v165, v160
v_mov_b32_dpp v167, v164 row_half_mirror row_mask:0xf bank_mask:0xf
N 0
v_mov_b32_dpp v168, v165 row_half_mirror row_mask:0xf bank_mask:0xf
v_min_u32 v160, v164, v167
v_max_u32 v164, v164, v167
v_max_u32 v165, v165, v168
v_max_u32 v165, v165, v160
v_mov_b32_dpp v167, v164 row_mirror row_mask:0xf bank_mask:0xf
N 0
v_mov_b32_dpp v168, v165 row_mirror row_mask:0xf bank_mask:0xf
v_min_u32 v160, v164, v167
v_max_u32 v164, v164, v167
v_max_u32 v165, v165, v168
v_max_u32 v165, v165, v160
MV 167,164
MV 168,165
N 0
v_permlane16_swap_b32 v164, v167
v_permlane16_swap_b32 v165, v168
v_min_u32 v160, v164, v167
v_max_u32 v164, v164, v167
v_max_u32 v165, v165, v168
v_max_u32 v165, v165, v160
MV 167,164
MV 168,165
N 0
v_permlane32_swap_b32 v164, v167
v_permlane32_swap_b32 v165, v168
v_min_u32 v160, v164, v167
v_max_u32 v164, v164, v167
v_max_u32 v165, v165, v168
v_max_u32 v165, v165, v160
v_mov_b32 v169, 151552
ds_write_b64 v169, v[164:165]
W 0
B
v_mov_b32 v169, 151552
ds_read_b128 v[172:175], v169
Rv 176,169,16
W 1
v_min_u32 v160, v172, v174
v_max_u32 v172, v172, v174
v_max_u32 v173, v173, v175
v_max_u32 v173, v173, v160
W 0
v_min_u32 v160, v172, v176
v_max_u32 v172, v172, v176
v_max_u32 v173, v173, v177
v_max_u32 v173, v173, v160
v_min_u32 v160, v172, v178
v_max_u32 v172, v172, v178
v_max_u32 v173, v173, v179
v_max_u32 v173, v173, v160
v_and_b32 v172, 2047, v172
v_and_b32 v173, 2047, v173
v_sub_u32 v172, 2047, v172
v_sub_u32 v173, 2047, v173
v_readfirstlane_b32 s68, v172
v_readfirstlane_b32 s69, v173
s_cmp_eq_u32 s10, 0
s_cbranch_scc0 NOSEL%=
N 1
s_lshl_b32 s16, s69, 16
s_or_b32 s16, s16, s68
v_mov_b32 v160, s16
v_mov_b32 v161, s79
v_lshlrev_b32 v162, 8, v254
VA 162,53248,162
s_mov_b64 exec, 0xff
global_store_dwordx2 v162, v[160:161], s[14:15] sc1
s_mov_b64 exec, -1
NOSEL%=:
v_and_b32 v208, 1, v254
v_lshrrev_b32 v209, 1, v254
v_and_b32 v209, 3, v209
v_lshrrev_b32 v226, 3, v254
VA 226,0,226
v_mov_b32 v210, s68
v_mov_b32 v211, s69
v_cmp_eq_u32 vcc, 1, v208
N 1
v_cndmask_b32 v210, v210, v211, vcc
v_lshrrev_b32 v212, 4, v210
v_mul_u32_u24 v212, 20480, v212
v_lshl_add_u32 v212, v226, 10, v212
v_and_b32 v213, 15, v210
v_lshl_add_u32 v213, v209, 4, v213
v_lshl_add_u32 v212, v213, 4, v212
v_mul_u32_u24 v213, 1280, v210
v_lshl_add_u32 v213, v209, 3, v213
v_lshl_add_u32 v213, v226, 6, v213
v_lshlrev_b32 v214, 4, v210
s_mov_b32 exec_lo, -1
s_mov_b32 exec_hi, 0xff
buffer_load_dwordx4 v[216:219], v214, s[36:39], 0 offen offset:0 sc1
SM 18,0
Lvs 64,212,32,18,0
SM 18,2621440
Lvs 68,212,32,18,0
SM 18,5242880
Lvs 72,212,32,18,0
SM 18,7864320
Lvs 76,212,32,18,0
buffer_load_dwordx2 v[0:1], v213, s[44:47], 0 offen offset:0
buffer_load_dwordx2 v[2:3], v213, s[44:47], 0 offen offset:32
s_mov_b64 exec, -1
s_mul_i32 s17, s78, 40960
AS 17,17,0
M0 81920
N 0
D 196,72,17,0
D 196,72,17,1024
D 196,72,17,2048
D 196,72,17,3072
M0 86016
AS 17,17,4096
D 196,72,17,0
D 196,72,17,1024
D 196,72,17,2048
D 196,72,17,3072
M0 90112
AS 17,17,4096
D 196,72,17,0
D 196,72,17,1024
v_lshrrev_b32 v222, 4, v254
v_and_b32 v223, 15, v254
v_lshrrev_b32 v223, 2, v223
v_lshl_add_u32 v223, v222, 2, v223
v_and_b32 v222, 3, v254
v_lshlrev_b32 v222, 3, v222
v_lshl_add_u32 v224, v223, 6, v222
VA 224,0,224
v_mul_u32_u24 v225, 1280, v223
v_add_u32 v225, v225, v222
VA 225,81920,225
Ta 0,224,0
Ta 2,224,1024
Ta 4,224,2048
Ta 6,224,3072
Ta 8,224,4096
Ta 10,224,5120
Ta 12,224,6144
Ta 14,224,7168
Ta 16,224,8192
Ta 18,224,9216
Ta 20,224,10240
Ta 22,224,11264
Ta 24,224,12288
Ta 26,224,13312
Ta 28,224,14336
Ta 30,224,15360
Ta 32,224,16384
Ta 34,224,17408
Ta 36,224,18432
Ta 38,224,19456
Ta 40,224,20480
Ta 42,224,21504
Ta 44,224,22528
Ta 46,224,23552
Ta 48,224,24576
Ta 50,224,25600
Ta 52,224,26624
Ta 54,224,27648
Ta 56,224,28672
Ta 58,224,29696
Ta 60,224,30720
Ta 62,224,31744
Ta 64,224,32768
Ta 66,224,33792
Ta 68,224,34816
Ta 70,224,35840
Ta 72,224,36864
Ta 74,224,37888
Ta 76,224,38912
Ta 78,224,39936
v_lshlrev_b32 v221, 10, v226
v_lshl_add_u32 v220, v209, 4, v208
v_lshl_add_u32 v221, v220, 4, v221
VA 227,122880,221
v_mul_u32_u24 v221, 2560, v208
v_lshl_add_u32 v221, v209, 4, v221
v_lshl_add_u32 v221, v226, 7, v221
VA 221,143360,221
VA 220,122880,196
s_waitcnt vmcnt(0)
v_add_f32 v222, 1.0, v218
Fmu 223,217,218
v_xor_b32 v223, 0x80000000, v223
LO 40,64
HI 41,64
LO 228,68
HI 229,68
PA 40,40,228
LO 228,72
HI 229,72
PA 40,40,228
LO 228,76
HI 229,76
PA 40,40,228
LO 228,0
HI 229,0
PA 40,40,228
LO 42,65
HI 43,65
LO 228,69
HI 229,69
PA 42,42,228
LO 228,73
HI 229,73
PA 42,42,228
LO 228,77
HI 229,77
PA 42,42,228
LO 228,1
HI 229,1
PA 42,42,228
LO 44,66
HI 45,66
LO 228,70
HI 229,70
PA 44,44,228
LO 228,74
HI 229,74
PA 44,44,228
LO 228,78
HI 229,78
PA 44,44,228
LO 228,2
HI 229,2
PA 44,44,228
LO 46,67
HI 47,67
LO 228,71
HI 229,71
PA 46,46,228
LO 228,75
HI 229,75
PA 46,46,228
LO 228,79
HI 229,79
PA 46,46,228
LO 228,3
HI 229,3
PA 46,46,228
FM 48,40,218,223
FM 49,41,218,223
FM 50,42,218,223
FM 51,43,218,223
FM 52,44,218,223
FM 53,45,218,223
FM 54,46,218,223
FM 55,47,218,223
C 56,48,49
C 57,50,51
C 58,52,53
C 59,54,55
FM 40,40,222,223
FM 41,41,222,223
FM 42,42,222,223
FM 43,43,222,223
FM 44,44,222,223
FM 45,45,222,223
FM 46,46,222,223
FM 47,47,222,223
s_mov_b32 exec_lo, -1
s_mov_b32 exec_hi, 0xff
ds_write_b128 v227, v[56:59]
ds_write_b128 v221, v[40:43]
DWv 221,44,64
s_mov_b64 exec, -1
v_lshlrev_b32 v228, 2, v254
VA 228,151616,228
s_mov_b64 exec, 3
ds_write_b32 v228, v216
s_mov_b64 exec, -1
W 0
B
Rv 0,220,0
Rv 4,220,1024
Rv 8,220,2048
Rv 12,220,3072
Rv 16,220,4096
Rv 20,220,5120
Rv 24,220,6144
Rv 28,220,7168
Rv 32,220,8192
Rv 36,220,9216
Rv 40,220,10240
Rv 44,220,11264
Rv 48,220,12288
Rv 52,220,13312
Rv 56,220,14336
Rv 60,220,15360
Rv 64,220,16384
Rv 68,220,17408
Rv 72,220,18432
Rv 76,220,19456
Ta 80,225,0
Ta 82,225,20480
Ta 84,225,32
Ta 86,225,20512
Ta 88,225,64
Ta 90,225,20544
Ta 92,225,96
Ta 94,225,20576
Ta 96,225,128
Ta 98,225,20608
Ta 100,225,160
Ta 102,225,20640
Ta 104,225,192
Ta 106,225,20672
Ta 108,225,224
Ta 110,225,20704
Ta 112,225,256
Ta 114,225,20736
Ta 116,225,288
Ta 118,225,20768
W 15
Mvavz 180,0,0
W 15
Mvavz 184,4,4
W 15
Mvavc 180,8,8
W 15
Mvavc 184,12,12
W 15
Mvavc 180,16,16
W 15
Mvavc 184,20,20
W 15
Mvavc 180,24,24
W 15
Mvavc 184,28,28
W 15
Mvavc 180,32,32
W 15
Mvavc 184,36,36
W 15
Mvavc 180,40,40
W 15
Mvavc 184,44,44
W 15
Mvavc 180,48,48
W 15
Mvavc 184,52,52
W 15
Mvavc 180,56,56
W 15
Mvavc 184,60,60
W 15
Mvavc 180,64,64
W 15
Mvavc 184,68,68
W 15
Mvavc 180,72,72
W 15
Mvavc 184,76,76
VA 215,148480,196
N 6
Fad 160,180,184
Fad 161,181,185
Fad 162,182,186
Fad 163,183,187
v_mul_f32 v168, 0xbfb8aa3b, v160
E 168
N 0
v_add_f32 v168, 1.0, v168
v_rcp_f32 v168, v168
N 0
Fmu 168,168,160
v_mul_f32 v169, 0xbfb8aa3b, v161
E 169
N 0
v_add_f32 v169, 1.0, v169
v_rcp_f32 v169, v169
N 0
Fmu 169,169,161
v_mul_f32 v170, 0xbfb8aa3b, v162
E 170
N 0
v_add_f32 v170, 1.0, v170
v_rcp_f32 v170, v170
N 0
Fmu 170,170,162
v_mul_f32 v171, 0xbfb8aa3b, v163
E 171
N 0
v_add_f32 v171, 1.0, v171
v_rcp_f32 v171, v171
N 0
Fmu 171,171,163
W 0
B
ds_read_b128 v[164:167], v215
W 0
Fmu 168,168,164
Fmu 169,169,165
Fmu 170,170,166
Fmu 171,171,167
C 172,168,169
C 173,170,171
VA 215,150528,196
ds_write_b64 v215, v[172:173]
s_add_u32 s40, s14, 0x5420000
s_addc_u32 s41, s15, 0
SM 42,0x43800
SM 43,0x20000
v_and_b32 v208, 15, v254
v_mul_u32_u24 v208, 2560, v208
v_lshrrev_b32 v209, 4, v254
v_lshl_add_u32 v208, v209, 4, v208
s_mul_i32 s16, s78, 5120
AS 16,16,0
W 0
B
VA 215,150528,196
ds_read_b128 v[176:179], v215
W 0
Mvavz 48,80,176
Mvavz 52,84,176
Mvavz 56,88,176
Mvavz 60,92,176
Mvavz 64,96,176
Mvavz 68,100,176
Mvavz 72,104,176
Mvavz 76,108,176
Mvavz 80,112,176
Mvavz 84,116,176
s_mov_b32 exec_lo, 0x00030003
s_mov_b32 exec_hi, 0x00030003
Svs 48,208,40,16,0
Svs 52,208,40,16,64
Svs 56,208,40,16,128
Svs 60,208,40,16,192
Svs 64,208,40,16,256
Svs 68,208,40,16,320
Svs 72,208,40,16,384
Svs 76,208,40,16,448
Svs 80,208,40,16,512
Svs 84,208,40,16,576
s_mov_b64 exec, -1
s_waitcnt vmcnt(0)
B
SM 19,0
s_lshl_b32 s21, s19, 8
AS 21,21,56320
s_lshl_b32 s22, s78, 2
s_add_u32 s21, s21, s22
v_mov_b32 v247, s21
v_mov_b32 v246, s79
s_mov_b64 exec, 1
global_store_dword v247, v246, s[14:15] sc1
s_mov_b64 exec, -1
N 3
G 24,224
G 25,225
G 26,226
G 27,227
G 28,228
G 29,229
G 30,230
G 31,231
G 32,232
G 33,233
G 100,234
v_and_b32 v160, 7, v254
v_add_u32 v160, s66, v160
v_cmp_ne_u32 vcc, s68, v160
v_cmp_ne_u32 s[16:17], s69, v160
N 4
s_and_b64 exec, vcc, s[16:17]
buffer_store_dwordx2 v[24:25], v100, s[52:55], s65 offen offset:0 sc1
buffer_store_dwordx2 v[26:27], v100, s[52:55], s65 offen offset:64 sc1
buffer_store_dwordx2 v[28:29], v100, s[52:55], s65 offen offset:128 sc1
buffer_store_dwordx2 v[30:31], v100, s[52:55], s65 offen offset:192 sc1
buffer_store_dwordx2 v[32:33], v100, s[52:55], s65 offen offset:256 sc1
s_mov_b64 exec, -1
s_cmp_lt_u32 s78, 20
s_cbranch_scc0 PE0%=
v_mov_b32 v247, 56320
v_min_u32 v246, 53, v254
v_lshl_add_u32 v247, v246, 2, v247
global_load_dword v246, v247, s[14:15] sc1
s_waitcnt vmcnt(0)
v_cmp_ne_u32 vcc, s79, v246
N 4
s_cbranch_vccnz FY_SPIN%=
FY_OK%=:
B
s_cmp_eq_u32 s10, 0
s_cbranch_scc0 NOGEN%=
v_mov_b32 v247, 61440
v_mov_b32 v246, s79
s_mov_b64 exec, 1
global_store_dword v247, v246, s[14:15] sc1
s_mov_b64 exec, -1
NOGEN%=:
s_lshl_b32 s16, s78, 8
v_lshlrev_b32 v200, 2, v254
AS 16,16,0
buffer_load_dword v0, v200, s[40:43], s16 offen sc1
AS 16,16,5120
buffer_load_dword v1, v200, s[40:43], s16 offen sc1
AS 16,16,5120
buffer_load_dword v2, v200, s[40:43], s16 offen sc1
AS 16,16,5120
buffer_load_dword v3, v200, s[40:43], s16 offen sc1
AS 16,16,5120
buffer_load_dword v4, v200, s[40:43], s16 offen sc1
AS 16,16,5120
buffer_load_dword v5, v200, s[40:43], s16 offen sc1
AS 16,16,5120
buffer_load_dword v6, v200, s[40:43], s16 offen sc1
AS 16,16,5120
buffer_load_dword v7, v200, s[40:43], s16 offen sc1
)A"
R"A(AS 16,16,5120
buffer_load_dword v8, v200, s[40:43], s16 offen sc1
AS 16,16,5120
buffer_load_dword v9, v200, s[40:43], s16 offen sc1
AS 16,16,5120
buffer_load_dword v10, v200, s[40:43], s16 offen sc1
AS 16,16,5120
buffer_load_dword v11, v200, s[40:43], s16 offen sc1
AS 16,16,5120
buffer_load_dword v12, v200, s[40:43], s16 offen sc1
AS 16,16,5120
buffer_load_dword v13, v200, s[40:43], s16 offen sc1
AS 16,16,5120
s_waitcnt vmcnt(12)
Fad 0,0,1
s_waitcnt vmcnt(11)
Fad 0,0,2
s_waitcnt vmcnt(10)
Fad 0,0,3
s_waitcnt vmcnt(9)
Fad 0,0,4
s_waitcnt vmcnt(8)
Fad 0,0,5
s_waitcnt vmcnt(7)
Fad 0,0,6
s_waitcnt vmcnt(6)
Fad 0,0,7
s_waitcnt vmcnt(5)
Fad 0,0,8
s_waitcnt vmcnt(4)
Fad 0,0,9
s_waitcnt vmcnt(3)
Fad 0,0,10
s_waitcnt vmcnt(2)
Fad 0,0,11
s_waitcnt vmcnt(1)
Fad 0,0,12
s_waitcnt vmcnt(0)
Fad 0,0,13
VA 201,151808,200
ds_write_b32 v201, v0 offset:0
W 0
B
ds_read_b32 v0, v201 offset:0
ds_read_b32 v1, v201 offset:256
ds_read_b32 v2, v201 offset:512
ds_read_b32 v3, v201 offset:768
s_cmp_ge_u32 s78, 10
s_cselect_b32 s17, 1, 0
s_cselect_b32 s18, s69, s68
s_lshl_b32 s16, s78, 8
AS 16,16,143360
v_add_u32 v202, s16, v200
ds_read_b32 v4, v202
s_lshl_b32 s16, s17, 2
AS 16,16,151616
v_mov_b32 v203, s16
ds_read_b32 v5, v203
W 4
Fad 0,0,1
W 2
Fad 2,2,3
Fad 0,0,2
W 0
v_mul_f32 v5, 0xbfb8aa3b, v5
E 5
N 0
v_add_f32 v5, 1.0, v5
v_rcp_f32 v5, v5
N 0
Ffm 4,0,5
C 4,4,4
s_mul_i32 s16, s18, 1280
s_mul_i32 s17, s17, 1280
s_sub_u32 s16, s16, s17
s_lshl_b32 s17, s78, 7
s_add_u32 s16, s16, s17
v_lshlrev_b32 v202, 1, v254
buffer_store_short v4, v202, s[52:55], s16 offen
PE0%=:
OE0%=:
s_branch FM0_DONE%=
XN0_SPIN%=:
SM 20,0
XN0_L%=:
s_sleep 2
global_load_dword v246, v247, s[14:15] sc1
s_waitcnt vmcnt(0)
v_cmp_ne_u32 vcc, s79, v246
N 4
s_cbranch_vccz XN0_OK%=
AS 20,20,1
s_cmp_lt_u32 s20, 65536
s_cbranch_scc1 XN0_L%=
s_branch XN0_OK%=
KP0_SPIN%=:
SM 20,0
KP0_L%=:
s_sleep 2
global_load_dword v246, v247, s[14:15] sc1
s_waitcnt vmcnt(0)
v_cmp_ne_u32 vcc, s79, v246
N 4
s_cbranch_vccz KP0_OK%=
AS 20,20,1
s_cmp_lt_u32 s20, 65536
s_cbranch_scc1 KP0_L%=
s_branch KP0_OK%=
VP_SPIN%=:
SM 20,0
VP_L%=:
s_sleep 2
global_load_dword v246, v247, s[14:15] sc1
s_waitcnt vmcnt(0)
v_cmp_ne_u32 vcc, s79, v246
N 4
s_cbranch_vccz VP_OK%=
AS 20,20,1
s_cmp_lt_u32 s20, 65536
s_cbranch_scc1 VP_L%=
s_branch VP_OK%=
GOSLOW0%=:
s_cmp_eq_u32 s17, 0
s_cbranch_scc1 SBACK0%=
s_getpc_b64 s[22:23]
SRET0%=:
s_add_u32 s22, s22, SBACK0%=-SRET0%=
s_addc_u32 s23, s23, 0
s_getpc_b64 s[16:17]
SJ0%=:
s_add_u32 s16, s16, SLOW%=-SJ0%=
s_addc_u32 s17, s17, 0
s_setpc_b64 s[16:17]
SBACK0%=:
s_and_b32 s16, s10, 7
s_lshr_b32 s41, s16, 1
s_and_b32 s16, s16, 1
s_lshl_b32 s16, s16, 3
s_lshr_b32 s17, s10, 3
s_lshr_b32 s18, s17, 2
s_add_u32 s39, s16, s18
s_and_b32 s40, s17, 3
s_lshl_b32 s16, s41, 2
s_add_u32 s43, s16, s40
s_mov_b64 s[24:25], s[50:51]
SM 26,0x500000
SM 27,0x20000
s_lshl_b32 s16, s41, 10
s_lshl_b32 s17, s11, 5
s_add_u32 s16, s16, s17
s_mul_i32 s16, s16, 1280
s_mul_i32 s17, s40, 320
s_add_u32 s36, s16, s17
s_waitcnt vmcnt(0)
B
s_getpc_b64 s[16:17]
SK0%=:
s_add_u32 s16, s16, MDONE0%=-SK0%=
s_addc_u32 s17, s17, -1
s_setpc_b64 s[16:17]
CX_SPIN%=:
SM 20,0
CX_L%=:
s_sleep 2
global_load_dword v246, v247, s[14:15] sc1
s_waitcnt vmcnt(0)
v_cmp_ne_u32 vcc, s79, v246
N 4
s_cbranch_vccz CX_OK%=
AS 20,20,1
s_cmp_lt_u32 s20, 65536
s_cbranch_scc1 CX_L%=
s_branch CX_OK%=
PP_SPIN%=:
SM 20,0
PP_L%=:
s_sleep 2
global_load_dword v246, v247, s[14:15] sc1
s_waitcnt vmcnt(0)
v_cmp_ne_u32 vcc, s79, v246
N 4
s_cbranch_vccz PP_OK%=
AS 20,20,1
s_cmp_lt_u32 s20, 65536
s_cbranch_scc1 PP_L%=
s_branch PP_OK%=
FY_SPIN%=:
SM 20,0
FY_L%=:
s_sleep 2
global_load_dword v246, v247, s[14:15] sc1
s_waitcnt vmcnt(0)
v_cmp_ne_u32 vcc, s79, v246
N 4
s_cbranch_vccz FY_OK%=
AS 20,20,1
s_cmp_lt_u32 s20, 65536
s_cbranch_scc1 FY_L%=
s_branch FY_OK%=
FM0_DONE%=:
s_getpc_b64 s[16:17]
FJ4%=:
s_add_u32 s16, s16, FM_END%=-FJ4%=
s_addc_u32 s17, s17, 0
s_setpc_b64 s[16:17]
FM_W1%=:
s_waitcnt vmcnt(20)
B
Rv 0,140,0
T 32,144,0
T 34,144,1024
Rv 4,140,2048
T 36,145,0
T 38,145,1024
Rv 8,140,4096
T 40,146,0
T 42,146,1024
Rv 12,140,6144
T 44,147,0
T 46,147,1024
Rv 16,140,8192
T 48,148,0
T 50,148,1024
Rv 20,140,10240
T 52,149,0
T 54,149,1024
Rv 24,140,12288
T 56,150,0
T 58,150,1024
Rv 28,140,14336
T 60,151,0
T 62,151,1024
WV 16
B
AS 79,79,1
Mavvz 0,32,0
Mavvz 4,32,4
Rv 64,141,0
Mavvz 8,32,8
T 96,144,16384
v_dot2c_f32_bf16 v181, v168, v180
Mavvz 12,32,12
v_dot2c_f32_bf16 v182, v168, v168
AM 35,0
Mavvz 16,32,16
v_dot2c_f32_bf16 v181, v169, v180
D 236,28,33,0
Mavvz 20,32,20
T 98,144,17408
v_dot2c_f32_bf16 v182, v169, v169
Mavvz 24,32,24
Rv 68,141,2048
Mavvz 28,32,28
v_dot2c_f32_bf16 v181, v170, v180
Mavvz 60,36,28
T 100,145,16384
Mavvz 56,36,24
v_dot2c_f32_bf16 v182, v170, v170
v_dot2c_f32_bf16 v181, v171, v180
Mavvz 52,36,20
T 102,145,17408
v_dot2c_f32_bf16 v182, v171, v171
Mavvz 48,36,16
v_dot2c_f32_bf16 v181, v172, v180
D 237,28,33,1024
Mavvz 44,36,12
Rv 72,141,4096
Mavvz 40,36,8
v_dot2c_f32_bf16 v182, v172, v172
Mavvz 36,36,4
T 104,146,16384
Mavvz 32,36,0
v_dot2c_f32_bf16 v181, v173, v180
v_dot2c_f32_bf16 v182, v173, v173
Mavvz 64,40,0
T 106,146,17408
Mavvz 68,40,4
v_dot2c_f32_bf16 v181, v174, v180
v_dot2c_f32_bf16 v182, v174, v174
Mavvz 72,40,8
D 238,28,33,2048
Mavvz 76,40,12
Rv 76,141,6144
v_dot2c_f32_bf16 v181, v175, v180
Mavvz 80,40,16
T 108,147,16384
Mavvz 84,40,20
v_dot2c_f32_bf16 v182, v175, v175
v_dot2c_f32_bf16 v181, v176, v180
Mavvz 88,40,24
T 110,147,17408
Mavvz 92,40,28
v_dot2c_f32_bf16 v182, v176, v176
v_dot2c_f32_bf16 v181, v177, v180
Mavvz 124,44,28
Rv 80,141,8192
Mavvz 120,44,24
v_dot2c_f32_bf16 v182, v177, v177
D 239,28,33,3072
Mavvz 116,44,20
v_dot2c_f32_bf16 v181, v178, v180
AS 33,33,262144
Mavvz 112,44,16
T 112,148,16384
v_dot2c_f32_bf16 v182, v178, v178
Mavvz 108,44,12
T 114,148,17408
v_dot2c_f32_bf16 v181, v179, v180
Mavvz 104,44,8
Rv 84,141,10240
Mavvz 100,44,4
v_dot2c_f32_bf16 v182, v179, v179
Mavvz 96,44,0
N 3
AM 34,69632
Mavvz 128,48,0
v_add_f32_dpp v181, v181, v181 row_ror:8 row_mask:0xf bank_mask:0xf
D 132,24,32,0
Mavvz 132,48,4
T 116,149,16384
v_add_f32_dpp v182, v182, v182 row_ror:8 row_mask:0xf bank_mask:0xf
Mavvz 136,48,8
T 118,149,17408
MV 186,181
Mavvz 140,48,12
Rv 88,141,12288
Mavvz 144,48,16
MV 187,182
Mavvz 148,48,20
T 120,150,16384
Mavvz 152,48,24
N 1
Mavvz 156,48,28
v_permlane16_swap_b32 v181, v186
D 133,24,32,1024
Mavvz 188,52,28
T 122,150,17408
v_permlane16_swap_b32 v182, v187
Mavvz 184,52,24
Rv 92,141,14336
N 1
Mavvz 180,52,20
Fad 181,181,186
Mavvz 176,52,16
T 124,151,16384
Mavvz 172,52,12
Fad 182,182,187
MV 186,181
Mavvz 168,52,8
T 126,151,17408
MV 187,182
Mavvz 164,52,4
D 134,24,32,2048
Mavvz 160,52,0
N 1
Mavvz 192,56,0
v_permlane32_swap_b32 v181, v186
Mavvz 196,56,4
v_permlane32_swap_b32 v182, v187
Mavvz 200,56,8
N 1
Mavvz 204,56,12
Fad 181,181,186
Mavvz 208,56,16
Mavvz 212,56,20
Fad 182,182,187
D 135,24,32,3072
Mavvz 216,56,24
AS 32,32,128
Mavvz 220,56,28
MV 186,181
Mavvz 252,60,28
MV 187,182
Mavvz 248,60,24
ds_write_b64 v185, v[186:187]
Mavvz 244,60,20
Mavvz 240,60,16
Mavvz 236,60,12
Mavvz 232,60,8
Mavvz 228,60,4
Mavvz 224,60,0
WV 12
B
Mavvc 0,96,64
Mavvc 4,96,68
Rv 0,140,32768
Mavvc 8,96,72
T 32,144,32768
Mavvc 12,96,76
AM 35,16384
Mavvc 16,96,80
D 236,28,33,0
Mavvc 20,96,84
T 34,144,33792
Mavvc 24,96,88
Rv 4,140,34816
Mavvc 28,96,92
Mavvc 60,100,92
T 36,145,32768
Mavvc 56,100,88
Mavvc 52,100,84
T 38,145,33792
Mavvc 48,100,80
D 237,28,33,1024
Mavvc 44,100,76
Rv 8,140,36864
Mavvc 40,100,72
Mavvc 36,100,68
T 40,146,32768
Mavvc 32,100,64
Mavvc 64,104,64
T 42,146,33792
Mavvc 68,104,68
Mavvc 72,104,72
D 238,28,33,2048
Mavvc 76,104,76
Rv 12,140,38912
Mavvc 80,104,80
T 44,147,32768
Mavvc 84,104,84
Mavvc 88,104,88
T 46,147,33792
Mavvc 92,104,92
Mavvc 124,108,92
Rv 16,140,40960
Mavvc 120,108,88
D 239,28,33,3072
Mavvc 116,108,84
AS 33,33,262144
Mavvc 112,108,80
T 48,148,32768
Mavvc 108,108,76
T 50,148,33792
Mavvc 104,108,72
Rv 20,140,43008
Mavvc 100,108,68
Mavvc 96,108,64
AM 34,0
Mavvc 128,112,64
D 128,24,32,0
Mavvc 132,112,68
T 52,149,32768
Mavvc 136,112,72
T 54,149,33792
Mavvc 140,112,76
Rv 24,140,45056
Mavvc 144,112,80
Mavvc 148,112,84
T 56,150,32768
Mavvc 152,112,88
Mavvc 156,112,92
D 129,24,32,1024
Mavvc 188,116,92
T 58,150,33792
Mavvc 184,116,88
Rv 28,140,47104
Mavvc 180,116,84
Mavvc 176,116,80
T 60,151,32768
Mavvc 172,116,76
Mavvc 168,116,72
T 62,151,33792
Mavvc 164,116,68
D 130,24,32,2048
Mavvc 160,116,64
Mavvc 192,120,64
Mavvc 196,120,68
Mavvc 200,120,72
Mavvc 204,120,76
Mavvc 208,120,80
Mavvc 212,120,84
D 131,24,32,3072
Mavvc 216,120,88
Mavvc 220,120,92
Mavvc 252,124,92
Mavvc 248,124,88
Mavvc 244,124,84
ds_read_b64 v[188:189], v184 offset:0
Mavvc 240,124,80
ds_read_b64 v[190:191], v184 offset:64
Mavvc 236,124,76
ds_read_b64 v[192:193], v184 offset:128
Mavvc 232,124,72
ds_read_b64 v[194:195], v184 offset:192
Mavvc 228,124,68
Mavvc 224,124,64
WV 12
B
Mavvc 0,32,0
Mavvc 4,32,4
Rv 64,141,32768
Fad 181,188,190
Mavvc 8,32,8
T 96,144,0
Fad 182,189,191
Mavvc 12,32,12
Fad 186,192,194
AM 35,32768
Mavvc 16,32,16
Fad 187,193,195
D 236,28,33,0
Mavvc 20,32,20
T 98,144,1024
Fad 181,181,186
Mavvc 24,32,24
Rv 68,141,34816
Fad 182,182,187
Mavvc 28,32,28
v_mul_f32 v181, 0x3acccccd, v181
v_mul_f32 v182, 0x3acccccd, v182
Mavvc 60,36,28
T 100,145,0
v_fma_f32 v182, -v181, v181, v182
s_lshr_b32 s21, s10, 3
Mavvc 56,36,24
v_max_f32 v182, 0, v182
v_add_f32 v182, 0x3727c5ac, v182
Mavvc 52,36,20
T 102,145,1024
v_rsq_f32 v182, v182
Mavvc 48,36,16
N 0
s_mul_i32 s21, s21, 160
D 237,28,33,1024
Mavvc 44,36,12
Rv 72,141,36864
v_mul_f32 v182, 0x3e0293ee, v182
Mavvc 40,36,8
Fmu 181,181,182
v_xor_b32 v181, 0x80000000, v181
Mavvc 36,36,4
T 104,146,0
LO 196,168
AS 21,21,64
Mavvc 32,36,0
HI 197,168
FM 196,196,182,181
Mavvc 64,40,0
T 106,146,1024
FM 197,197,182,181
Mavvc 68,40,4
C 168,196,197
LO 196,169
v_min_u32 v249, 63, v254
Mavvc 72,40,8
HI 197,169
D 238,28,33,2048
Mavvc 76,40,12
Rv 76,141,38912
FM 196,196,182,181
Mavvc 80,40,16
T 108,147,0
FM 197,197,182,181
v_add_u32 v249, s21, v249
Mavvc 84,40,20
C 169,196,197
LO 196,170
Mavvc 88,40,24
T 110,147,1024
HI 197,170
Mavvc 92,40,28
FM 196,196,182,181
FM 197,197,182,181
v_and_b32 v248, 7, v249
Mavvc 124,44,28
Rv 80,141,40960
C 170,196,197
Mavvc 120,44,24
LO 196,171
D 239,28,33,3072
Mavvc 116,44,20
HI 197,171
v_lshrrev_b32 v249, 3, v249
AS 33,33,262144
Mavvc 112,44,16
T 112,148,0
FM 196,196,182,181
Mavvc 108,44,12
T 114,148,1024
FM 197,197,182,181
Mavvc 104,44,8
Rv 84,141,43008
C 171,196,197
v_lshlrev_b32 v249, 13, v249
Mavvc 100,44,4
LO 196,172
HI 197,172
Mavvc 96,44,0
FM 196,196,182,181
AM 34,4096
Mavvc 128,48,0
FM 197,197,182,181
v_lshl_add_u32 v249, v248, 7, v249
D 132,24,32,0
Mavvc 132,48,4
T 116,149,0
C 172,196,197
Mavvc 136,48,8
T 118,149,1024
LO 196,173
Mavvc 140,48,12
Rv 88,141,45056
HI 197,173
s_and_b32 s21, s10, 7
Mavvc 144,48,16
FM 196,196,182,181
FM 197,197,182,181
Mavvc 148,48,20
T 120,150,0
C 173,196,197
Mavvc 152,48,24
LO 196,174
HI 197,174
s_lshl_b32 s21, s21, 10
Mavvc 156,48,28
FM 196,196,182,181
D 133,24,32,1024
Mavvc 188,52,28
T 122,150,1024
FM 197,197,182,181
Mavvc 184,52,24
Rv 92,141,47104
C 174,196,197
buffer_load_dword v248, v249, s[44:47], s21 offen
Mavvc 180,52,20
LO 196,175
HI 197,175
Mavvc 176,52,16
T 124,151,0
FM 196,196,182,181
Mavvc 172,52,12
FM 197,197,182,181
C 175,196,197
Mavvc 168,52,8
T 126,151,1024
LO 196,176
Mavvc 164,52,4
HI 197,176
D 134,24,32,2048
Mavvc 160,52,0
FM 196,196,182,181
Mavvc 192,56,0
FM 197,197,182,181
Mavvc 196,56,4
C 176,196,197
Mavvc 200,56,8
LO 196,177
Mavvc 204,56,12
HI 197,177
Mavvc 208,56,16
FM 196,196,182,181
FM 197,197,182,181
Mavvc 212,56,20
C 177,196,197
D 135,24,32,3072
Mavvc 216,56,24
LO 196,178
AS 32,32,128
Mavvc 220,56,28
HI 197,178
Mavvc 252,60,28
FM 196,196,182,181
Mavvc 248,60,24
FM 197,197,182,181
Mavvc 244,60,20
C 178,196,197
Mavvc 240,60,16
LO 196,179
Mavvc 236,60,12
HI 197,179
Mavvc 232,60,8
FM 196,196,182,181
Mavvc 228,60,4
FM 197,197,182,181
Mavvc 224,60,0
C 179,196,197
s_lshr_b32 s18, s10, 4
s_mul_i32 s18, s18, 163840
s_lshr_b32 s19, s10, 1
s_and_b32 s19, s19, 7
s_lshl_b32 s19, s19, 10
)A"
R"A(s_add_u32 s18, s18, s19
s_and_b32 s19, s10, 1
s_lshl_b32 s19, s19, 7
s_add_u32 s18, s18, s19
AS 19,18,65536
Svs 168,205,48,18,0
Svs 172,205,48,19,0
Svs 176,206,48,18,0
WV 16
B
Mavvc 0,96,64
Mavvc 4,96,68
Rv 0,142,0
Mavvc 8,96,72
T 32,144,16384
Mavvc 12,96,76
AM 35,0
Mavvc 16,96,80
D 236,28,33,0
Mavvc 20,96,84
T 34,144,17408
Mavvc 24,96,88
Rv 4,142,2048
Mavvc 28,96,92
Mavvc 60,100,92
T 36,145,16384
Mavvc 56,100,88
Mavvc 52,100,84
T 38,145,17408
Mavvc 48,100,80
D 237,28,33,1024
Mavvc 44,100,76
Rv 8,142,4096
Mavvc 40,100,72
Mavvc 36,100,68
T 40,146,16384
Mavvc 32,100,64
Mavvc 64,104,64
T 42,146,17408
Mavvc 68,104,68
Mavvc 72,104,72
D 238,28,33,2048
Mavvc 76,104,76
Rv 12,142,6144
Mavvc 80,104,80
T 44,147,16384
Mavvc 84,104,84
Mavvc 88,104,88
T 46,147,17408
Mavvc 92,104,92
Mavvc 124,108,92
Rv 16,142,8192
Mavvc 120,108,88
D 239,28,33,3072
Mavvc 116,108,84
AS 33,33,262144
Mavvc 112,108,80
T 48,148,16384
Mavvc 108,108,76
T 50,148,17408
Mavvc 104,108,72
Rv 20,142,10240
Mavvc 100,108,68
Mavvc 96,108,64
AM 34,32768
Mavvc 128,112,64
D 128,24,32,0
Mavvc 132,112,68
T 52,149,16384
Mavvc 136,112,72
T 54,149,17408
Mavvc 140,112,76
Rv 24,142,12288
Mavvc 144,112,80
Mavvc 148,112,84
T 56,150,16384
Mavvc 152,112,88
Mavvc 156,112,92
D 129,24,32,1024
Mavvc 188,116,92
T 58,150,17408
Mavvc 184,116,88
Rv 28,142,14336
Mavvc 180,116,84
Mavvc 176,116,80
T 60,151,16384
Mavvc 172,116,76
Mavvc 168,116,72
T 62,151,17408
Mavvc 164,116,68
D 130,24,32,2048
Mavvc 160,116,64
Mavvc 192,120,64
Mavvc 196,120,68
Mavvc 200,120,72
Mavvc 204,120,76
Mavvc 208,120,80
Mavvc 212,120,84
D 131,24,32,3072
Mavvc 216,120,88
Mavvc 220,120,92
Mavvc 252,124,92
Mavvc 248,124,88
Mavvc 244,124,84
Mavvc 240,124,80
Mavvc 236,124,76
Mavvc 232,124,72
Mavvc 228,124,68
Mavvc 224,124,64
WV 16
B
Mavvc 0,32,0
Mavvc 4,32,4
Rv 64,143,0
Mavvc 8,32,8
T 96,144,32768
Mavvc 12,32,12
AM 35,16384
Mavvc 16,32,16
D 236,28,33,0
Mavvc 20,32,20
T 98,144,33792
Mavvc 24,32,24
Rv 68,143,2048
Mavvc 28,32,28
Mavvc 60,36,28
T 100,145,32768
Mavvc 56,36,24
Mavvc 52,36,20
T 102,145,33792
Mavvc 48,36,16
D 237,28,33,1024
Mavvc 44,36,12
Rv 72,143,4096
Mavvc 40,36,8
Mavvc 36,36,4
T 104,146,32768
Mavvc 32,36,0
Mavvc 64,40,0
T 106,146,33792
Mavvc 68,40,4
Mavvc 72,40,8
D 238,28,33,2048
Mavvc 76,40,12
Rv 76,143,6144
Mavvc 80,40,16
T 108,147,32768
Mavvc 84,40,20
Mavvc 88,40,24
T 110,147,33792
Mavvc 92,40,28
Mavvc 124,44,28
Rv 80,143,8192
Mavvc 120,44,24
D 239,28,33,3072
Mavvc 116,44,20
AS 33,33,262144
Mavvc 112,44,16
T 112,148,32768
Mavvc 108,44,12
T 114,148,33792
Mavvc 104,44,8
Rv 84,143,10240
Mavvc 100,44,4
Mavvc 96,44,0
AM 34,36864
Mavvc 128,48,0
D 132,24,32,0
Mavvc 132,48,4
T 116,149,32768
Mavvc 136,48,8
T 118,149,33792
Mavvc 140,48,12
Rv 88,143,12288
Mavvc 144,48,16
Mavvc 148,48,20
T 120,150,32768
Mavvc 152,48,24
Mavvc 156,48,28
D 133,24,32,1024
Mavvc 188,52,28
T 122,150,33792
Mavvc 184,52,24
Rv 92,143,14336
Mavvc 180,52,20
Mavvc 176,52,16
T 124,151,32768
Mavvc 172,52,12
Mavvc 168,52,8
T 126,151,33792
Mavvc 164,52,4
D 134,24,32,2048
Mavvc 160,52,0
Mavvc 192,56,0
Mavvc 196,56,4
Mavvc 200,56,8
Mavvc 204,56,12
Mavvc 208,56,16
Mavvc 212,56,20
D 135,24,32,3072
Mavvc 216,56,24
AS 32,32,128
Mavvc 220,56,28
Mavvc 252,60,28
Mavvc 248,60,24
Mavvc 244,60,20
Mavvc 240,60,16
Mavvc 236,60,12
Mavvc 232,60,8
Mavvc 228,60,4
Mavvc 224,60,0
WV 12
B
Mavvc 0,96,64
Mavvc 4,96,68
Rv 0,140,0
Mavvc 8,96,72
T 32,144,0
Mavvc 12,96,76
AM 35,32768
Mavvc 16,96,80
D 236,28,33,0
Mavvc 20,96,84
T 34,144,1024
Mavvc 24,96,88
Rv 4,140,2048
Mavvc 28,96,92
Mavvc 60,100,92
T 36,145,0
Mavvc 56,100,88
Mavvc 52,100,84
T 38,145,1024
Mavvc 48,100,80
D 237,28,33,1024
Mavvc 44,100,76
Rv 8,140,4096
Mavvc 40,100,72
Mavvc 36,100,68
T 40,146,0
Mavvc 32,100,64
Mavvc 64,104,64
T 42,146,1024
Mavvc 68,104,68
Mavvc 72,104,72
D 238,28,33,2048
Mavvc 76,104,76
Rv 12,140,6144
Mavvc 80,104,80
T 44,147,0
Mavvc 84,104,84
Mavvc 88,104,88
T 46,147,1024
Mavvc 92,104,92
Mavvc 124,108,92
Rv 16,140,8192
Mavvc 120,108,88
D 239,28,33,3072
Mavvc 116,108,84
AS 33,33,262144
Mavvc 112,108,80
T 48,148,0
Mavvc 108,108,76
T 50,148,1024
Mavvc 104,108,72
Rv 20,140,10240
Mavvc 100,108,68
Mavvc 96,108,64
AM 34,65536
Mavvc 128,112,64
D 128,24,32,0
Mavvc 132,112,68
T 52,149,0
Mavvc 136,112,72
T 54,149,1024
Mavvc 140,112,76
Rv 24,140,12288
Mavvc 144,112,80
Mavvc 148,112,84
T 56,150,0
Mavvc 152,112,88
Mavvc 156,112,92
D 129,24,32,1024
Mavvc 188,116,92
T 58,150,1024
Mavvc 184,116,88
Rv 28,140,14336
Mavvc 180,116,84
Mavvc 176,116,80
T 60,151,0
Mavvc 172,116,76
Mavvc 168,116,72
T 62,151,1024
Mavvc 164,116,68
D 130,24,32,2048
Mavvc 160,116,64
Mavvc 192,120,64
Mavvc 196,120,68
Mavvc 200,120,72
Mavvc 204,120,76
Mavvc 208,120,80
Mavvc 212,120,84
D 131,24,32,3072
Mavvc 216,120,88
Mavvc 220,120,92
Mavvc 252,124,92
Mavvc 248,124,88
Mavvc 244,124,84
Mavvc 240,124,80
Mavvc 236,124,76
Mavvc 232,124,72
Mavvc 228,124,68
Mavvc 224,124,64
WV 12
B
Mavvc 0,32,0
Mavvc 4,32,4
Rv 64,141,0
Mavvc 8,32,8
T 96,144,16384
Mavvc 12,32,12
AM 35,0
Mavvc 16,32,16
D 236,28,33,0
Mavvc 20,32,20
T 98,144,17408
Mavvc 24,32,24
Rv 68,141,2048
Mavvc 28,32,28
Mavvc 60,36,28
T 100,145,16384
Mavvc 56,36,24
Mavvc 52,36,20
T 102,145,17408
Mavvc 48,36,16
D 237,28,33,1024
Mavvc 44,36,12
Rv 72,141,4096
Mavvc 40,36,8
Mavvc 36,36,4
T 104,146,16384
Mavvc 32,36,0
Mavvc 64,40,0
T 106,146,17408
Mavvc 68,40,4
Mavvc 72,40,8
D 238,28,33,2048
Mavvc 76,40,12
Rv 76,141,6144
Mavvc 80,40,16
T 108,147,16384
Mavvc 84,40,20
Mavvc 88,40,24
T 110,147,17408
Mavvc 92,40,28
Mavvc 124,44,28
Rv 80,141,8192
Mavvc 120,44,24
D 239,28,33,3072
Mavvc 116,44,20
AS 33,33,262144
Mavvc 112,44,16
T 112,148,16384
Mavvc 108,44,12
T 114,148,17408
Mavvc 104,44,8
Rv 84,141,10240
Mavvc 100,44,4
Mavvc 96,44,0
AM 34,69632
Mavvc 128,48,0
D 132,24,32,0
Mavvc 132,48,4
T 116,149,16384
Mavvc 136,48,8
T 118,149,17408
Mavvc 140,48,12
Rv 88,141,12288
Mavvc 144,48,16
Mavvc 148,48,20
T 120,150,16384
Mavvc 152,48,24
Mavvc 156,48,28
D 133,24,32,1024
Mavvc 188,52,28
T 122,150,17408
Mavvc 184,52,24
Rv 92,141,14336
Mavvc 180,52,20
Mavvc 176,52,16
T 124,151,16384
Mavvc 172,52,12
Mavvc 168,52,8
T 126,151,17408
Mavvc 164,52,4
D 134,24,32,2048
Mavvc 160,52,0
Mavvc 192,56,0
Mavvc 196,56,4
Mavvc 200,56,8
Mavvc 204,56,12
Mavvc 208,56,16
Mavvc 212,56,20
D 135,24,32,3072
Mavvc 216,56,24
AS 32,32,128
Mavvc 220,56,28
Mavvc 252,60,28
Mavvc 248,60,24
Mavvc 244,60,20
Mavvc 240,60,16
Mavvc 236,60,12
Mavvc 232,60,8
Mavvc 228,60,4
Mavvc 224,60,0
WV 12
B
s_getpc_b64 s[16:17]
1:
s_add_u32 s16, s16, QS1%=-1b
s_addc_u32 s17, s17, 0
s_lshr_b32 s21, s10, 3
v_lshlrev_b32 v249, 5, v254
v_add_u32 v249, s21, v249
v_min_u32 v249, ((QE1%=-QS1%=)>>7), v249
v_lshlrev_b32 v249, 7, v249
global_load_dword v248, v249, s[16:17]
Mavvc 0,96,64
Mavvc 4,96,68
Rv 0,140,32768
Mavvc 8,96,72
T 32,144,32768
Mavvc 12,96,76
AM 35,16384
Mavvc 16,96,80
D 236,28,33,0
Mavvc 20,96,84
T 34,144,33792
Mavvc 24,96,88
Rv 4,140,34816
Mavvc 28,96,92
Mavvc 60,100,92
T 36,145,32768
Mavvc 56,100,88
Mavvc 52,100,84
T 38,145,33792
Mavvc 48,100,80
D 237,28,33,1024
Mavvc 44,100,76
Rv 8,140,36864
Mavvc 40,100,72
Mavvc 36,100,68
T 40,146,32768
Mavvc 32,100,64
Mavvc 64,104,64
T 42,146,33792
Mavvc 68,104,68
Mavvc 72,104,72
D 238,28,33,2048
Mavvc 76,104,76
Rv 12,140,38912
Mavvc 80,104,80
T 44,147,32768
Mavvc 84,104,84
Mavvc 88,104,88
T 46,147,33792
Mavvc 92,104,92
Mavvc 124,108,92
Rv 16,140,40960
Mavvc 120,108,88
D 239,28,33,3072
Mavvc 116,108,84
AS 33,33,262144
Mavvc 112,108,80
T 48,148,32768
Mavvc 108,108,76
T 50,148,33792
Mavvc 104,108,72
Rv 20,140,43008
Mavvc 100,108,68
Mavvc 96,108,64
AM 34,0
Mavvc 128,112,64
D 128,24,32,0
Mavvc 132,112,68
T 52,149,32768
Mavvc 136,112,72
T 54,149,33792
Mavvc 140,112,76
Rv 24,140,45056
Mavvc 144,112,80
Mavvc 148,112,84
T 56,150,32768
Mavvc 152,112,88
Mavvc 156,112,92
D 129,24,32,1024
Mavvc 188,116,92
T 58,150,33792
Mavvc 184,116,88
Rv 28,140,47104
Mavvc 180,116,84
Mavvc 176,116,80
T 60,151,32768
Mavvc 172,116,76
Mavvc 168,116,72
T 62,151,33792
Mavvc 164,116,68
D 130,24,32,2048
Mavvc 160,116,64
Mavvc 192,120,64
Mavvc 196,120,68
Mavvc 200,120,72
Mavvc 204,120,76
Mavvc 208,120,80
Mavvc 212,120,84
D 131,24,32,3072
Mavvc 216,120,88
Mavvc 220,120,92
Mavvc 252,124,92
Mavvc 248,124,88
Mavvc 244,124,84
Mavvc 240,124,80
Mavvc 236,124,76
Mavvc 232,124,72
Mavvc 228,124,68
Mavvc 224,124,64
WV 13
B
Mavvc 0,32,0
Mavvc 4,32,4
Rv 64,141,32768
Mavvc 8,32,8
T 96,144,0
Mavvc 12,32,12
AM 35,32768
Mavvc 16,32,16
D 236,28,33,0
Mavvc 20,32,20
T 98,144,1024
Mavvc 24,32,24
Rv 68,141,34816
Mavvc 28,32,28
Mavvc 60,36,28
T 100,145,0
Mavvc 56,36,24
Mavvc 52,36,20
T 102,145,1024
Mavvc 48,36,16
D 237,28,33,1024
Mavvc 44,36,12
Rv 72,141,36864
Mavvc 40,36,8
Mavvc 36,36,4
T 104,146,0
Mavvc 32,36,0
Mavvc 64,40,0
T 106,146,1024
Mavvc 68,40,4
Mavvc 72,40,8
D 238,28,33,2048
Mavvc 76,40,12
Rv 76,141,38912
Mavvc 80,40,16
T 108,147,0
Mavvc 84,40,20
Mavvc 88,40,24
T 110,147,1024
Mavvc 92,40,28
Mavvc 124,44,28
Rv 80,141,40960
Mavvc 120,44,24
D 239,28,33,3072
Mavvc 116,44,20
AS 33,33,262144
Mavvc 112,44,16
T 112,148,0
Mavvc 108,44,12
T 114,148,1024
Mavvc 104,44,8
Rv 84,141,43008
Mavvc 100,44,4
Mavvc 96,44,0
AM 34,4096
Mavvc 128,48,0
D 132,24,32,0
Mavvc 132,48,4
T 116,149,0
Mavvc 136,48,8
T 118,149,1024
Mavvc 140,48,12
Rv 88,141,45056
Mavvc 144,48,16
Mavvc 148,48,20
T 120,150,0
Mavvc 152,48,24
Mavvc 156,48,28
D 133,24,32,1024
Mavvc 188,52,28
T 122,150,1024
Mavvc 184,52,24
Rv 92,141,47104
Mavvc 180,52,20
Mavvc 176,52,16
T 124,151,0
Mavvc 172,52,12
Mavvc 168,52,8
T 126,151,1024
Mavvc 164,52,4
D 134,24,32,2048
Mavvc 160,52,0
Mavvc 192,56,0
Mavvc 196,56,4
Mavvc 200,56,8
Mavvc 204,56,12
Mavvc 208,56,16
Mavvc 212,56,20
D 135,24,32,3072
Mavvc 216,56,24
AS 32,32,128
Mavvc 220,56,28
Mavvc 252,60,28
Mavvc 248,60,24
Mavvc 244,60,20
Mavvc 240,60,16
Mavvc 236,60,12
Mavvc 232,60,8
Mavvc 228,60,4
Mavvc 224,60,0
WV 12
B
s_sub_u32 s23, 15, s13
s_cmp_ge_u32 s11, 2
s_cselect_b32 s23, s13, s23
s_lshl_b32 s21, s23, 8
AS 21,21,0
v_mov_b32 v247, s21
v_min_u32 v246, 15, v254
v_lshl_add_u32 v247, v246, 2, v247
global_load_dword v246, v247, s[14:15] sc1
Mavvc 0,96,64
Mavvc 4,96,68
Rv 0,142,0
Mavvc 8,96,72
T 32,144,16384
Mavvc 12,96,76
AM 35,0
Mavvc 16,96,80
D 236,28,33,0
Mavvc 20,96,84
T 34,144,17408
Mavvc 24,96,88
Rv 4,142,2048
Mavvc 28,96,92
Mavvc 60,100,92
T 36,145,16384
Mavvc 56,100,88
Mavvc 52,100,84
T 38,145,17408
Mavvc 48,100,80
D 237,28,33,1024
Mavvc 44,100,76
Rv 8,142,4096
Mavvc 40,100,72
Mavvc 36,100,68
T 40,146,16384
Mavvc 32,100,64
Mavvc 64,104,64
T 42,146,17408
Mavvc 68,104,68
Mavvc 72,104,72
D 238,28,33,2048
Mavvc 76,104,76
Rv 12,142,6144
Mavvc 80,104,80
T 44,147,16384
Mavvc 84,104,84
Mavvc 88,104,88
T 46,147,17408
Mavvc 92,104,92
Mavvc 124,108,92
Rv 16,142,8192
Mavvc 120,108,88
D 239,28,33,3072
Mavvc 116,108,84
AS 33,33,262144
Mavvc 112,108,80
T 48,148,16384
Mavvc 108,108,76
T 50,148,17408
Mavvc 104,108,72
Rv 20,142,10240
Mavvc 100,108,68
Mavvc 96,108,64
AM 34,32768
Mavvc 128,112,64
D 128,24,32,0
Mavvc 132,112,68
T 52,149,16384
Mavvc 136,112,72
T 54,149,17408
Mavvc 140,112,76
Rv 24,142,12288
Mavvc 144,112,80
Mavvc 148,112,84
T 56,150,16384
Mavvc 152,112,88
Mavvc 156,112,92
D 129,24,32,1024
Mavvc 188,116,92
T 58,150,17408
Mavvc 184,116,88
Rv 28,142,14336
Mavvc 180,116,84
Mavvc 176,116,80
T 60,151,16384
Mavvc 172,116,76
Mavvc 168,116,72
T 62,151,17408
Mavvc 164,116,68
D 130,24,32,2048
Mavvc 160,116,64
Mavvc 192,120,64
Mavvc 196,120,68
Mavvc 200,120,72
Mavvc 204,120,76
Mavvc 208,120,80
Mavvc 212,120,84
D 131,24,32,3072
Mavvc 216,120,88
Mavvc 220,120,92
Mavvc 252,124,92
Mavvc 248,124,88
Mavvc 244,124,84
Mavvc 240,124,80
Mavvc 236,124,76
Mavvc 232,124,72
Mavvc 228,124,68
Mavvc 224,124,64
WV 13
B
Mavvc 0,32,0
Mavvc 4,32,4
Rv 64,143,0
Mavvc 8,32,8
T 96,144,32768
Mavvc 12,32,12
AM 35,16384
Mavvc 16,32,16
D 236,28,33,0
Mavvc 20,32,20
T 98,144,33792
Mavvc 24,32,24
Rv 68,143,2048
Mavvc 28,32,28
Mavvc 60,36,28
T 100,145,32768
Mavvc 56,36,24
Mavvc 52,36,20
T 102,145,33792
Mavvc 48,36,16
D 237,28,33,1024
Mavvc 44,36,12
Rv 72,143,4096
Mavvc 40,36,8
Mavvc 36,36,4
T 104,146,32768
Mavvc 32,36,0
Mavvc 64,40,0
T 106,146,33792
Mavvc 68,40,4
Mavvc 72,40,8
D 238,28,33,2048
)A"
R"A(Mavvc 76,40,12
Rv 76,143,6144
Mavvc 80,40,16
T 108,147,32768
Mavvc 84,40,20
Mavvc 88,40,24
T 110,147,33792
Mavvc 92,40,28
Mavvc 124,44,28
Rv 80,143,8192
Mavvc 120,44,24
D 239,28,33,3072
Mavvc 116,44,20
AS 33,33,262144
Mavvc 112,44,16
T 112,148,32768
Mavvc 108,44,12
T 114,148,33792
Mavvc 104,44,8
Rv 84,143,10240
Mavvc 100,44,4
Mavvc 96,44,0
AM 34,36864
Mavvc 128,48,0
D 132,24,32,0
Mavvc 132,48,4
T 116,149,32768
Mavvc 136,48,8
T 118,149,33792
Mavvc 140,48,12
Rv 88,143,12288
Mavvc 144,48,16
Mavvc 148,48,20
T 120,150,32768
Mavvc 152,48,24
Mavvc 156,48,28
D 133,24,32,1024
Mavvc 188,52,28
T 122,150,33792
Mavvc 184,52,24
Rv 92,143,14336
Mavvc 180,52,20
Mavvc 176,52,16
T 124,151,32768
Mavvc 172,52,12
Mavvc 168,52,8
T 126,151,33792
Mavvc 164,52,4
D 134,24,32,2048
Mavvc 160,52,0
Mavvc 192,56,0
Mavvc 196,56,4
Mavvc 200,56,8
Mavvc 204,56,12
Mavvc 208,56,16
Mavvc 212,56,20
D 135,24,32,3072
Mavvc 216,56,24
AS 32,32,128
Mavvc 220,56,28
Mavvc 252,60,28
Mavvc 248,60,24
Mavvc 244,60,20
Mavvc 240,60,16
Mavvc 236,60,12
Mavvc 232,60,8
Mavvc 228,60,4
Mavvc 224,60,0
WV 12
B
v_cmp_ne_u32 vcc, s79, v246
N 4
s_cbranch_vccnz XN1_SPIN%=
XN1_OK%=:
Mavvc 0,96,64
Mavvc 4,96,68
Rv 0,140,0
Mavvc 8,96,72
T 32,144,0
Mavvc 12,96,76
AM 35,32768
Mavvc 16,96,80
D 236,28,33,0
Mavvc 20,96,84
T 34,144,1024
Mavvc 24,96,88
Rv 4,140,2048
Mavvc 28,96,92
s_and_b32 s21, s12, 3
Mavvc 60,100,92
T 36,145,0
Mavvc 56,100,88
s_mul_i32 s21, s21, 40960
Mavvc 52,100,84
T 38,145,1024
Mavvc 48,100,80
s_add_u32 s21, s21, s53
D 237,28,33,1024
Mavvc 44,100,76
Rv 8,140,4096
Mavvc 40,100,72
AS 21,21,16384
Mavvc 36,100,68
T 40,146,0
Mavvc 32,100,64
v_min_u32 v249, 63, v254
Mavvc 64,104,64
T 42,146,1024
Mavvc 68,104,68
v_lshlrev_b32 v249, 7, v249
Mavvc 72,104,72
D 238,28,33,2048
Mavvc 76,104,76
Rv 12,140,6144
buffer_load_dword v248, v249, s[48:51], s21 offen sc1
Mavvc 80,104,80
T 44,147,0
Mavvc 84,104,84
Mavvc 88,104,88
T 46,147,1024
Mavvc 92,104,92
Mavvc 124,108,92
Rv 16,140,8192
Mavvc 120,108,88
D 239,28,33,3072
Mavvc 116,108,84
AS 33,33,262144
Mavvc 112,108,80
T 48,148,0
Mavvc 108,108,76
T 50,148,1024
Mavvc 104,108,72
Rv 20,140,10240
Mavvc 100,108,68
Mavvc 96,108,64
T 52,149,0
Mavvc 128,112,64
Mavvc 132,112,68
T 54,149,1024
Mavvc 136,112,72
Mavvc 140,112,76
Rv 24,140,12288
Mavvc 144,112,80
AS 55,53,0
M0 73728
Mavvc 148,112,84
T 56,150,0
Ds 243,48,55,0
Mavvc 152,112,88
Mavvc 156,112,92
T 58,150,1024
Mavvc 188,116,92
Mavvc 184,116,88
Rv 28,140,14336
Mavvc 180,116,84
Mavvc 176,116,80
T 60,151,0
Ds 243,48,55,1024
Mavvc 172,116,76
Mavvc 168,116,72
T 62,151,1024
Mavvc 164,116,68
Mavvc 160,116,64
Mavvc 192,120,64
Mavvc 196,120,68
Mavvc 200,120,72
Ds 243,48,55,2048
Mavvc 204,120,76
Mavvc 208,120,80
Mavvc 212,120,84
Mavvc 216,120,88
Mavvc 220,120,92
Mavvc 252,124,92
Ds 243,48,55,3072
Mavvc 248,124,88
Mavvc 244,124,84
Mavvc 240,124,80
Mavvc 236,124,76
Mavvc 232,124,72
Mavvc 228,124,68
Mavvc 224,124,64
WV 13
B
Mavvc 0,32,0
Mavvc 4,32,4
Rv 64,141,0
Mavvc 8,32,8
T 96,144,16384
Mavvc 12,32,12
AM 35,0
Mavvc 16,32,16
D 236,28,33,0
Mavvc 20,32,20
T 98,144,17408
Mavvc 24,32,24
Rv 68,141,2048
Mavvc 28,32,28
s_and_b32 s21, s12, 3
Mavvc 60,36,28
T 100,145,16384
Mavvc 56,36,24
s_mul_i32 s21, s21, 40960
Mavvc 52,36,20
T 102,145,17408
Mavvc 48,36,16
s_add_u32 s21, s21, s53
D 237,28,33,1024
Mavvc 44,36,12
Rv 72,141,4096
Mavvc 40,36,8
AS 21,21,24576
Mavvc 36,36,4
T 104,146,16384
Mavvc 32,36,0
v_min_u32 v249, 63, v254
Mavvc 64,40,0
T 106,146,17408
Mavvc 68,40,4
v_lshlrev_b32 v249, 7, v249
Mavvc 72,40,8
D 238,28,33,2048
Mavvc 76,40,12
Rv 76,141,6144
buffer_load_dword v248, v249, s[48:51], s21 offen sc1
Mavvc 80,40,16
T 108,147,16384
Mavvc 84,40,20
Mavvc 88,40,24
T 110,147,17408
Mavvc 92,40,28
Mavvc 124,44,28
Rv 80,141,8192
Mavvc 120,44,24
D 239,28,33,3072
Mavvc 116,44,20
AS 33,33,262144
Mavvc 112,44,16
T 112,148,16384
Mavvc 108,44,12
T 114,148,17408
Mavvc 104,44,8
Rv 84,141,10240
Mavvc 100,44,4
Mavvc 96,44,0
T 116,149,16384
Mavvc 128,48,0
Mavvc 132,48,4
T 118,149,17408
Mavvc 136,48,8
Mavvc 140,48,12
Rv 88,141,12288
Mavvc 144,48,16
AS 55,53,8192
M0 77824
Mavvc 148,48,20
T 120,150,16384
Ds 243,48,55,0
Mavvc 152,48,24
Mavvc 156,48,28
T 122,150,17408
Mavvc 188,52,28
Mavvc 184,52,24
Rv 92,141,14336
Mavvc 180,52,20
Mavvc 176,52,16
T 124,151,16384
Ds 243,48,55,1024
Mavvc 172,52,12
Mavvc 168,52,8
T 126,151,17408
Mavvc 164,52,4
Mavvc 160,52,0
Mavvc 192,56,0
Mavvc 196,56,4
Mavvc 200,56,8
Ds 243,48,55,2048
Mavvc 204,56,12
Mavvc 208,56,16
Mavvc 212,56,20
Mavvc 216,56,24
Mavvc 220,56,28
Mavvc 252,60,28
Ds 243,48,55,3072
Mavvc 248,60,24
Mavvc 244,60,20
Mavvc 240,60,16
Mavvc 236,60,12
Mavvc 232,60,8
Mavvc 228,60,4
Mavvc 224,60,0
WV 13
B
Mavvc 0,96,64
Mavvc 4,96,68
Rv 0,140,32768
Mavvc 8,96,72
T 32,144,32768
Mavvc 12,96,76
s_add_u32 s55, s52, 0x0
M0 118784
Mavvc 16,96,80
T 34,144,33792
D 236,44,55,0
Mavvc 20,96,84
Mavvc 24,96,88
Rv 4,140,34816
Mavvc 28,96,92
s_and_b32 s21, s12, 3
Mavvc 60,100,92
T 36,145,32768
Mavvc 56,100,88
s_mul_i32 s21, s21, 40960
Mavvc 52,100,84
T 38,145,33792
Mavvc 48,100,80
s_add_u32 s21, s21, s53
Mavvc 44,100,76
Rv 8,140,36864
D 237,44,55,1024
Mavvc 40,100,72
AS 21,21,32768
Mavvc 36,100,68
T 40,146,32768
Mavvc 32,100,64
v_min_u32 v249, 31, v254
Mavvc 64,104,64
T 42,146,33792
Mavvc 68,104,68
v_lshlrev_b32 v249, 7, v249
Mavvc 72,104,72
Rv 12,140,38912
Mavvc 76,104,76
buffer_load_dword v248, v249, s[48:51], s21 offen sc1
D 238,44,55,2048
Mavvc 80,104,80
T 44,147,32768
Mavvc 84,104,84
Mavvc 88,104,88
T 46,147,33792
Mavvc 92,104,92
Mavvc 124,108,92
Rv 16,140,40960
Mavvc 120,108,88
T 48,148,32768
Mavvc 116,108,84
D 239,44,55,3072
Mavvc 112,108,80
T 50,148,33792
Mavvc 108,108,76
Mavvc 104,108,72
Rv 20,140,43008
Mavvc 100,108,68
Mavvc 96,108,64
T 52,149,32768
Mavvc 128,112,64
AS 55,53,16384
M0 8192
Mavvc 132,112,68
T 54,149,33792
Ds 243,48,55,0
Mavvc 136,112,72
Mavvc 140,112,76
Rv 24,140,45056
Mavvc 144,112,80
Mavvc 148,112,84
T 56,150,32768
Mavvc 152,112,88
Mavvc 156,112,92
T 58,150,33792
Mavvc 188,116,92
Mavvc 184,116,88
Rv 28,140,47104
Ds 243,48,55,1024
Mavvc 180,116,84
Mavvc 176,116,80
T 60,151,32768
Mavvc 172,116,76
Mavvc 168,116,72
T 62,151,33792
Mavvc 164,116,68
Mavvc 160,116,64
Mavvc 192,120,64
Ds 243,48,55,2048
Mavvc 196,120,68
Mavvc 200,120,72
Mavvc 204,120,76
Mavvc 208,120,80
Mavvc 212,120,84
Mavvc 216,120,88
Mavvc 220,120,92
Ds 243,48,55,3072
Mavvc 252,124,92
Mavvc 248,124,88
Mavvc 244,124,84
Mavvc 240,124,80
Mavvc 236,124,76
Mavvc 232,124,72
Mavvc 228,124,68
Mavvc 224,124,64
WV 13
B
Mavvc 0,32,0
Mavvc 4,32,4
Rv 64,141,32768
Mavvc 8,32,8
T 96,144,0
Mavvc 12,32,12
s_add_u32 s55, s52, 0x80000
M0 135168
Mavvc 16,32,16
T 98,144,1024
D 236,44,55,0
Mavvc 20,32,20
Mavvc 24,32,24
Rv 68,141,34816
Mavvc 28,32,28
Mavvc 60,36,28
T 100,145,0
Mavvc 56,36,24
Mavvc 52,36,20
T 102,145,1024
Mavvc 48,36,16
Mavvc 44,36,12
Rv 72,141,36864
D 237,44,55,1024
Mavvc 40,36,8
Mavvc 36,36,4
T 104,146,0
Mavvc 32,36,0
Mavvc 64,40,0
T 106,146,1024
Mavvc 68,40,4
Mavvc 72,40,8
Rv 76,141,38912
Mavvc 76,40,12
D 238,44,55,2048
Mavvc 80,40,16
T 108,147,0
Mavvc 84,40,20
Mavvc 88,40,24
T 110,147,1024
Mavvc 92,40,28
Mavvc 124,44,28
Rv 80,141,40960
Mavvc 120,44,24
T 112,148,0
Mavvc 116,44,20
D 239,44,55,3072
Mavvc 112,44,16
T 114,148,1024
Mavvc 108,44,12
Mavvc 104,44,8
Rv 84,141,43008
Mavvc 100,44,4
Mavvc 96,44,0
T 116,149,0
Mavvc 128,48,0
AS 55,53,24576
M0 12288
Mavvc 132,48,4
T 118,149,1024
Ds 243,48,55,0
Mavvc 136,48,8
Mavvc 140,48,12
Rv 88,141,45056
Mavvc 144,48,16
Mavvc 148,48,20
T 120,150,0
Mavvc 152,48,24
Mavvc 156,48,28
T 122,150,1024
Mavvc 188,52,28
Mavvc 184,52,24
Rv 92,141,47104
Ds 243,48,55,1024
Mavvc 180,52,20
Mavvc 176,52,16
T 124,151,0
Mavvc 172,52,12
Mavvc 168,52,8
T 126,151,1024
Mavvc 164,52,4
Mavvc 160,52,0
Mavvc 192,56,0
Ds 243,48,55,2048
Mavvc 196,56,4
Mavvc 200,56,8
Mavvc 204,56,12
Mavvc 208,56,16
Mavvc 212,56,20
Mavvc 216,56,24
Mavvc 220,56,28
Ds 243,48,55,3072
Mavvc 252,60,28
Mavvc 248,60,24
Mavvc 244,60,20
Mavvc 240,60,16
Mavvc 236,60,12
Mavvc 232,60,8
Mavvc 228,60,4
Mavvc 224,60,0
WV 12
B
Mavvc 0,96,64
Mavvc 4,96,68
Mavvc 8,96,72
Mavvc 12,96,76
s_add_u32 s55, s52, 0x100000
M0 102400
Mavvc 16,96,80
D 236,44,55,0
Mavvc 20,96,84
Rv 192,245,0
Mavvc 24,96,88
Mavvc 28,96,92
Rv 196,245,1024
Mavvc 60,100,92
Mavvc 56,100,88
D 237,44,55,1024
Mavvc 52,100,84
Rv 200,245,2048
Mavvc 48,100,80
Mavvc 44,100,76
Mavvc 40,100,72
D 238,44,55,2048
Mavvc 36,100,68
Rv 204,245,3072
Mavvc 32,100,64
Mavvc 64,104,64
Mavvc 68,104,68
T 128,240,16384
T 130,240,17408
Mavvc 72,104,72
D 239,44,55,3072
Mavvc 76,104,76
Mavvc 80,104,80
X 242,32,240
Mavvc 84,104,84
T 132,242,16384
T 134,242,17408
Mavvc 88,104,88
AS 55,53,32768
M0 40960
Mavvc 92,104,92
Ds 243,48,55,0
Mavvc 124,108,92
X 241,64,240
Mavvc 120,108,88
T 136,241,16384
Mavvc 116,108,84
T 138,241,17408
Mavvc 112,108,80
X 242,96,240
T 140,242,16384
Mavvc 108,108,76
T 142,242,17408
Ds 243,48,55,1024
Mavvc 104,108,72
Mavvc 100,108,68
X 241,128,240
Mavvc 96,108,64
T 144,241,16384
Mavvc 128,112,64
T 146,241,17408
Mavvc 132,112,68
Ds 243,48,55,2048
Mavvc 136,112,72
X 242,160,240
Mavvc 140,112,76
T 148,242,16384
Mavvc 144,112,80
T 150,242,17408
Mavvc 148,112,84
X 241,192,240
T 152,241,16384
Mavvc 152,112,88
T 154,241,17408
Ds 243,48,55,3072
Mavvc 156,112,92
Mavvc 188,116,92
X 242,224,240
T 156,242,16384
T 158,242,17408
Mavvc 184,116,88
AS 55,53,40960
M0 45056
Mavvc 180,116,84
Ds 243,48,55,0
Mavvc 176,116,80
Mavvc 172,116,76
Mavvc 168,116,72
Mavvc 164,116,68
Mavvc 160,116,64
Ds 243,48,55,1024
Mavvc 192,120,64
Mavvc 196,120,68
Mavvc 200,120,72
Mavvc 204,120,76
Mavvc 208,120,80
Ds 243,48,55,2048
Mavvc 212,120,84
Mavvc 216,120,88
Mavvc 220,120,92
Mavvc 252,124,92
Mavvc 248,124,88
Ds 243,48,55,3072
Mavvc 244,124,84
Mavvc 240,124,80
Mavvc 236,124,76
Mavvc 232,124,72
Mavvc 228,124,68
Mavvc 224,124,64
s_add_u32 s36, s14, 0x400000
s_addc_u32 s37, s15, 0
SM 38,0x1000000
SM 39,0x20000
s_lshl_b32 s16, s12, 19
s_lshl_b32 s17, s13, 16
s_add_u32 s40, s16, s17
AS 40,40,32768
s_add_u32 s24, s14, 0x400000
s_addc_u32 s25, s15, 0
SM 26,0x2000000
SM 27,0x20000
s_add_u32 s28, s14, 0x1400000
s_addc_u32 s29, s15, 0
SM 30,0x1000000
SM 31,0x20000
s_lshl_b32 s16, s12, 19
s_lshl_b32 s17, s11, 12
s_add_u32 s56, s16, s17
s_mov_b32 s57, s56
AS 58,17,0
AS 59,17,65536
AS 56,56,32768
QS1%=:
W 0
Mvvvz 0,128,192
Rv 208,245,4096
G 224,0
Mvvvz 4,128,196
Rv 212,245,5120
G 225,1
Mvvvz 8,128,200
G 226,2
G 227,3
Mvvvz 12,128,204
Rv 216,245,6144
G 228,32
Mvvvz 28,132,204
G 229,33
G 230,34
Mvvvz 24,132,200
Rv 220,245,7168
G 231,35
Mvvvz 20,132,196
C 232,224,225
C 233,226,227
Mvvvz 16,132,192
T 160,240,24576
C 234,228,229
Mvvvz 32,136,192
T 162,240,25600
C 235,230,231
Mvvvz 36,136,196
X 242,32,240
Svs 232,243,36,40,0
Mvvvz 40,136,200
T 164,242,24576
G 224,64
Mvvvz 44,136,204
T 166,242,25600
G 225,65
Mvvvz 60,140,204
X 241,64,240
G 226,66
Mvvvz 56,140,200
T 168,241,24576
G 227,67
Mvvvz 52,140,196
T 170,241,25600
G 228,96
Mvvvz 48,140,192
X 242,96,240
G 229,97
Mvvvz 64,144,192
T 172,242,24576
G 230,98
Mvvvz 68,144,196
T 174,242,25600
G 231,99
Mvvvz 72,144,200
X 241,128,240
C 232,224,225
Mvvvz 76,144,204
T 176,241,24576
C 233,226,227
Mvvvz 92,148,204
T 178,241,25600
C 234,228,229
Mvvvz 88,148,200
X 242,160,240
C 235,230,231
Mvvvz 84,148,196
T 180,242,24576
Svs 232,243,36,40,1024
Mvvvz 80,148,192
T 182,242,25600
G 224,128
Mvvvz 96,152,192
X 241,192,240
G 225,129
Mvvvz 100,152,196
T 184,241,24576
G 226,130
Mvvvz 104,152,200
T 186,241,25600
G 227,131
Mvvvz 108,152,204
X 242,224,240
G 228,160
Mvvvz 124,156,204
T 188,242,24576
G 229,161
Mvvvz 120,156,200
T 190,242,25600
G 230,162
Mvvvz 116,156,196
G 231,163
Mvvvz 112,156,192
C 232,224,225
C 233,226,227
C 234,228,229
C 235,230,231
Svs 232,243,36,40,2048
G 224,192
G 225,193
G 226,194
G 227,195
G 228,224
G 229,225
G 230,226
G 231,227
C 232,224,225
C 233,226,227
C 234,228,229
C 235,230,231
Svs 232,243,36,40,3072
WV 16
B
Mvvvc 0,160,208
Rv 192,244,0
AS 40,40,4096
Mvvvc 4,160,212
Rv 196,244,1024
G 224,4
Mvvvc 8,160,216
G 225,5
G 226,6
Mvvvc 12,160,220
Rv 200,244,2048
G 227,7
Mvvvc 28,164,220
G 228,36
G 229,37
Mvvvc 24,164,216
Rv 204,244,3072
G 230,38
Mvvvc 20,164,212
)A"
R"A(G 231,39
C 232,224,225
Mvvvc 16,164,208
T 128,240,32768
C 233,226,227
Mvvvc 32,168,208
T 130,240,33792
C 234,228,229
Mvvvc 36,168,212
X 242,32,240
C 235,230,231
Mvvvc 40,168,216
T 132,242,32768
Svs 232,243,36,40,0
Mvvvc 44,168,220
T 134,242,33792
G 224,68
Mvvvc 60,172,220
X 241,64,240
G 225,69
Mvvvc 56,172,216
T 136,241,32768
G 226,70
Mvvvc 52,172,212
T 138,241,33792
G 227,71
Mvvvc 48,172,208
X 242,96,240
G 228,100
Mvvvc 64,176,208
T 140,242,32768
G 229,101
Mvvvc 68,176,212
T 142,242,33792
G 230,102
Mvvvc 72,176,216
X 241,128,240
G 231,103
Mvvvc 76,176,220
T 144,241,32768
C 232,224,225
Mvvvc 92,180,220
T 146,241,33792
C 233,226,227
Mvvvc 88,180,216
X 242,160,240
C 234,228,229
Mvvvc 84,180,212
T 148,242,32768
C 235,230,231
Mvvvc 80,180,208
T 150,242,33792
Svs 232,243,36,40,1024
Mvvvc 96,184,208
X 241,192,240
G 224,132
Mvvvc 100,184,212
T 152,241,32768
G 225,133
Mvvvc 104,184,216
T 154,241,33792
G 226,134
Mvvvc 108,184,220
X 242,224,240
G 227,135
Mvvvc 124,188,220
T 156,242,32768
G 228,164
Mvvvc 120,188,216
T 158,242,33792
G 229,165
Mvvvc 116,188,212
G 230,166
Mvvvc 112,188,208
G 231,167
C 232,224,225
C 233,226,227
C 234,228,229
C 235,230,231
Svs 232,243,36,40,2048
G 224,196
G 225,197
G 226,198
G 227,199
G 228,228
G 229,229
G 230,230
G 231,231
C 232,224,225
C 233,226,227
C 234,228,229
C 235,230,231
Svs 232,243,36,40,3072
W 0
Mvvvc 0,128,192
Rv 208,244,4096
AS 40,40,4096
Mvvvc 4,128,196
Rv 212,244,5120
G 224,8
Mvvvc 8,128,200
G 225,9
G 226,10
Mvvvc 12,128,204
Rv 216,244,6144
Mvvvc 28,132,204
G 227,11
G 228,40
Mvvvc 24,132,200
Rv 220,244,7168
G 229,41
Mvvvc 20,132,196
G 230,42
G 231,43
Mvvvc 16,132,192
T 160,240,40960
C 232,224,225
Mvvvc 32,136,192
T 162,240,41984
C 233,226,227
Mvvvc 36,136,196
X 242,32,240
C 234,228,229
Mvvvc 40,136,200
T 164,242,40960
C 235,230,231
Mvvvc 44,136,204
T 166,242,41984
Svs 232,243,36,40,0
Mvvvc 60,140,204
X 241,64,240
G 224,72
Mvvvc 56,140,200
T 168,241,40960
G 225,73
Mvvvc 52,140,196
T 170,241,41984
G 226,74
Mvvvc 48,140,192
X 242,96,240
G 227,75
Mvvvc 64,144,192
T 172,242,40960
G 228,104
Mvvvc 68,144,196
T 174,242,41984
G 229,105
Mvvvc 72,144,200
X 241,128,240
G 230,106
Mvvvc 76,144,204
T 176,241,40960
G 231,107
Mvvvc 92,148,204
T 178,241,41984
C 232,224,225
Mvvvc 88,148,200
X 242,160,240
C 233,226,227
Mvvvc 84,148,196
T 180,242,40960
C 234,228,229
Mvvvc 80,148,192
T 182,242,41984
C 235,230,231
Mvvvc 96,152,192
X 241,192,240
Svs 232,243,36,40,1024
Mvvvc 100,152,196
T 184,241,40960
G 224,136
Mvvvc 104,152,200
T 186,241,41984
G 225,137
Mvvvc 108,152,204
X 242,224,240
G 226,138
Mvvvc 124,156,204
T 188,242,40960
G 227,139
Mvvvc 120,156,200
T 190,242,41984
G 228,168
Mvvvc 116,156,196
G 229,169
Mvvvc 112,156,192
G 230,170
G 231,171
C 232,224,225
C 233,226,227
C 234,228,229
C 235,230,231
Svs 232,243,36,40,2048
WV 11
B
Mvvvc 0,160,208
Rv 192,244,32768
G 224,200
Mvvvc 4,160,212
Rv 196,244,33792
G 225,201
Mvvvc 8,160,216
G 226,202
G 227,203
Mvvvc 12,160,220
Rv 200,244,34816
G 228,232
Mvvvc 28,164,220
G 229,233
G 230,234
Mvvvc 24,164,216
Rv 204,244,35840
G 231,235
Mvvvc 20,164,212
C 232,224,225
C 233,226,227
Mvvvc 16,164,208
T 128,240,0
C 234,228,229
Mvvvc 32,168,208
T 130,240,1024
C 235,230,231
Mvvvc 36,168,212
X 242,32,240
Svs 232,243,36,40,3072
Mvvvc 40,168,216
T 132,242,0
AS 40,40,4096
Mvvvc 44,168,220
T 134,242,1024
G 224,12
Mvvvc 60,172,220
X 241,64,240
G 225,13
Mvvvc 56,172,216
T 136,241,0
G 226,14
Mvvvc 52,172,212
T 138,241,1024
G 227,15
Mvvvc 48,172,208
X 242,96,240
G 228,44
Mvvvc 64,176,208
T 140,242,0
G 229,45
Mvvvc 68,176,212
T 142,242,1024
G 230,46
Mvvvc 72,176,216
X 241,128,240
G 231,47
Mvvvc 76,176,220
T 144,241,0
C 232,224,225
Mvvvc 92,180,220
T 146,241,1024
C 233,226,227
Mvvvc 88,180,216
X 242,160,240
C 234,228,229
Mvvvc 84,180,212
T 148,242,0
C 235,230,231
Mvvvc 80,180,208
T 150,242,1024
Svs 232,243,36,40,0
Mvvvc 96,184,208
X 241,192,240
G 224,76
Mvvvc 100,184,212
T 152,241,0
G 225,77
Mvvvc 104,184,216
T 154,241,1024
G 226,78
Mvvvc 108,184,220
X 242,224,240
G 227,79
Mvvvc 124,188,220
T 156,242,0
G 228,108
Mvvvc 120,188,216
T 158,242,1024
G 229,109
Mvvvc 116,188,212
G 230,110
Mvvvc 112,188,208
G 231,111
C 232,224,225
C 233,226,227
C 234,228,229
C 235,230,231
Svs 232,243,36,40,1024
G 224,140
G 225,141
G 226,142
G 227,143
G 228,172
G 229,173
G 230,174
G 231,175
C 232,224,225
C 233,226,227
C 234,228,229
C 235,230,231
Svs 232,243,36,40,2048
W 0
Mvvvc 0,128,192
Rv 208,244,36864
G 224,204
Mvvvc 4,128,196
Rv 212,244,37888
G 225,205
Mvvvc 8,128,200
G 226,206
G 227,207
Mvvvc 12,128,204
Rv 216,244,38912
Mvvvc 28,132,204
G 228,236
G 229,237
Mvvvc 24,132,200
Rv 220,244,39936
G 230,238
Mvvvc 20,132,196
G 231,239
C 232,224,225
Mvvvc 16,132,192
T 160,240,8192
C 233,226,227
Mvvvc 32,136,192
T 162,240,9216
C 234,228,229
Mvvvc 36,136,196
X 242,32,240
C 235,230,231
Mvvvc 40,136,200
T 164,242,8192
Svs 232,243,36,40,3072
Mvvvc 44,136,204
T 166,242,9216
AS 40,40,4096
Mvvvc 60,140,204
X 241,64,240
G 224,16
Mvvvc 56,140,200
T 168,241,8192
G 225,17
Mvvvc 52,140,196
T 170,241,9216
G 226,18
Mvvvc 48,140,192
X 242,96,240
G 227,19
Mvvvc 64,144,192
T 172,242,8192
G 228,48
Mvvvc 68,144,196
T 174,242,9216
G 229,49
Mvvvc 72,144,200
X 241,128,240
G 230,50
Mvvvc 76,144,204
T 176,241,8192
G 231,51
Mvvvc 92,148,204
T 178,241,9216
C 232,224,225
Mvvvc 88,148,200
X 242,160,240
C 233,226,227
Mvvvc 84,148,196
T 180,242,8192
C 234,228,229
Mvvvc 80,148,192
T 182,242,9216
C 235,230,231
Mvvvc 96,152,192
X 241,192,240
Svs 232,243,36,40,0
Mvvvc 100,152,196
T 184,241,8192
G 224,80
Mvvvc 104,152,200
T 186,241,9216
G 225,81
Mvvvc 108,152,204
X 242,224,240
G 226,82
Mvvvc 124,156,204
T 188,242,8192
G 227,83
Mvvvc 120,156,200
T 190,242,9216
G 228,112
Mvvvc 116,156,196
G 229,113
Mvvvc 112,156,192
G 230,114
G 231,115
C 232,224,225
C 233,226,227
C 234,228,229
C 235,230,231
Svs 232,243,36,40,1024
W 0
B
Mvvvc 0,160,208
Rv 192,245,0
G 224,144
Mvvvc 4,160,212
Rv 196,245,1024
G 225,145
Mvvvc 8,160,216
G 226,146
G 227,147
Mvvvc 12,160,220
Rv 200,245,2048
G 228,176
Mvvvc 28,164,220
G 229,177
G 230,178
Mvvvc 24,164,216
Rv 204,245,3072
G 231,179
Mvvvc 20,164,212
C 232,224,225
C 233,226,227
Mvvvc 16,164,208
T 128,240,16384
C 234,228,229
Mvvvc 32,168,208
T 130,240,17408
C 235,230,231
Mvvvc 36,168,212
X 242,32,240
Svs 232,243,36,40,2048
Mvvvc 40,168,216
T 132,242,16384
G 224,208
Mvvvc 44,168,220
T 134,242,17408
G 225,209
Mvvvc 60,172,220
X 241,64,240
G 226,210
Mvvvc 56,172,216
T 136,241,16384
G 227,211
Mvvvc 52,172,212
T 138,241,17408
G 228,240
Mvvvc 48,172,208
X 242,96,240
G 229,241
Mvvvc 64,176,208
T 140,242,16384
G 230,242
Mvvvc 68,176,212
T 142,242,17408
G 231,243
Mvvvc 72,176,216
X 241,128,240
C 232,224,225
Mvvvc 76,176,220
T 144,241,16384
C 233,226,227
Mvvvc 92,180,220
T 146,241,17408
C 234,228,229
Mvvvc 88,180,216
X 242,160,240
C 235,230,231
Mvvvc 84,180,212
T 148,242,16384
Svs 232,243,36,40,3072
Mvvvc 80,180,208
T 150,242,17408
AS 40,40,4096
Mvvvc 96,184,208
X 241,192,240
G 224,20
Mvvvc 100,184,212
T 152,241,16384
G 225,21
Mvvvc 104,184,216
T 154,241,17408
G 226,22
Mvvvc 108,184,220
X 242,224,240
G 227,23
Mvvvc 124,188,220
T 156,242,16384
G 228,52
Mvvvc 120,188,216
T 158,242,17408
G 229,53
Mvvvc 116,188,212
G 230,54
Mvvvc 112,188,208
G 231,55
C 232,224,225
C 233,226,227
C 234,228,229
C 235,230,231
Svs 232,243,36,40,0
G 224,84
G 225,85
G 226,86
G 227,87
G 228,116
G 229,117
G 230,118
G 231,119
C 232,224,225
C 233,226,227
C 234,228,229
C 235,230,231
Svs 232,243,36,40,1024
W 0
Mvvvc 0,128,192
Rv 208,245,4096
G 224,148
Mvvvc 4,128,196
Rv 212,245,5120
G 225,149
Mvvvc 8,128,200
G 226,150
G 227,151
Mvvvc 12,128,204
Rv 216,245,6144
Mvvvc 28,132,204
G 228,180
G 229,181
Mvvvc 24,132,200
Rv 220,245,7168
G 230,182
Mvvvc 20,132,196
G 231,183
C 232,224,225
Mvvvc 16,132,192
T 160,240,24576
C 233,226,227
Mvvvc 32,136,192
T 162,240,25600
C 234,228,229
Mvvvc 36,136,196
X 242,32,240
C 235,230,231
Mvvvc 40,136,200
T 164,242,24576
Svs 232,243,36,40,2048
Mvvvc 44,136,204
T 166,242,25600
G 224,212
Mvvvc 60,140,204
X 241,64,240
G 225,213
Mvvvc 56,140,200
T 168,241,24576
G 226,214
Mvvvc 52,140,196
T 170,241,25600
G 227,215
Mvvvc 48,140,192
X 242,96,240
G 228,244
Mvvvc 64,144,192
T 172,242,24576
G 229,245
Mvvvc 68,144,196
T 174,242,25600
G 230,246
Mvvvc 72,144,200
X 241,128,240
G 231,247
Mvvvc 76,144,204
T 176,241,24576
C 232,224,225
Mvvvc 92,148,204
T 178,241,25600
C 233,226,227
Mvvvc 88,148,200
X 242,160,240
C 234,228,229
Mvvvc 84,148,196
T 180,242,24576
C 235,230,231
Mvvvc 80,148,192
T 182,242,25600
Svs 232,243,36,40,3072
Mvvvc 96,152,192
X 241,192,240
AS 40,40,4096
Mvvvc 100,152,196
T 184,241,24576
G 224,24
Mvvvc 104,152,200
T 186,241,25600
G 225,25
Mvvvc 108,152,204
X 242,224,240
G 226,26
Mvvvc 124,156,204
T 188,242,24576
G 227,27
Mvvvc 120,156,200
T 190,242,25600
G 228,56
Mvvvc 116,156,196
G 229,57
Mvvvc 112,156,192
G 230,58
G 231,59
C 232,224,225
C 233,226,227
C 234,228,229
C 235,230,231
Svs 232,243,36,40,0
W 0
B
Mvvvc 0,160,208
Rv 192,244,0
G 224,88
Mvvvc 4,160,212
Rv 196,244,1024
G 225,89
Mvvvc 8,160,216
G 226,90
G 227,91
Mvvvc 12,160,220
Rv 200,244,2048
G 228,120
Mvvvc 28,164,220
G 229,121
G 230,122
Mvvvc 24,164,216
Rv 204,244,3072
G 231,123
Mvvvc 20,164,212
C 232,224,225
C 233,226,227
Mvvvc 16,164,208
T 128,240,32768
C 234,228,229
Mvvvc 32,168,208
T 130,240,33792
C 235,230,231
Mvvvc 36,168,212
X 242,32,240
Svs 232,243,36,40,1024
Mvvvc 40,168,216
T 132,242,32768
G 224,152
Mvvvc 44,168,220
T 134,242,33792
G 225,153
Mvvvc 60,172,220
X 241,64,240
G 226,154
Mvvvc 56,172,216
T 136,241,32768
G 227,155
Mvvvc 52,172,212
T 138,241,33792
G 228,184
Mvvvc 48,172,208
X 242,96,240
G 229,185
Mvvvc 64,176,208
T 140,242,32768
G 230,186
Mvvvc 68,176,212
T 142,242,33792
G 231,187
Mvvvc 72,176,216
X 241,128,240
C 232,224,225
Mvvvc 76,176,220
T 144,241,32768
C 233,226,227
Mvvvc 92,180,220
T 146,241,33792
C 234,228,229
Mvvvc 88,180,216
X 242,160,240
C 235,230,231
Mvvvc 84,180,212
T 148,242,32768
Svs 232,243,36,40,2048
Mvvvc 80,180,208
T 150,242,33792
G 224,216
Mvvvc 96,184,208
X 241,192,240
G 225,217
Mvvvc 100,184,212
T 152,241,32768
G 226,218
Mvvvc 104,184,216
T 154,241,33792
G 227,219
Mvvvc 108,184,220
X 242,224,240
G 228,248
Mvvvc 124,188,220
T 156,242,32768
G 229,249
Mvvvc 120,188,216
T 158,242,33792
G 230,250
Mvvvc 116,188,212
G 231,251
Mvvvc 112,188,208
C 232,224,225
C 233,226,227
C 234,228,229
C 235,230,231
Svs 232,243,36,40,3072
AS 40,40,4096
G 224,28
G 225,29
G 226,30
G 227,31
G 228,60
G 229,61
G 230,62
G 231,63
C 232,224,225
C 233,226,227
C 234,228,229
C 235,230,231
Svs 232,243,36,40,0
W 0
Mvvvc 0,128,192
Rv 208,244,4096
G 224,92
Mvvvc 4,128,196
Rv 212,244,5120
G 225,93
Mvvvc 8,128,200
G 226,94
G 227,95
Mvvvc 12,128,204
Rv 216,244,6144
Mvvvc 28,132,204
G 228,124
G 229,125
Mvvvc 24,132,200
Rv 220,244,7168
G 230,126
Mvvvc 20,132,196
G 231,127
C 232,224,225
Mvvvc 16,132,192
T 160,240,40960
C 233,226,227
Mvvvc 32,136,192
T 162,240,41984
C 234,228,229
Mvvvc 36,136,196
X 242,32,240
C 235,230,231
Mvvvc 40,136,200
T 164,242,40960
Svs 232,243,36,40,1024
Mvvvc 44,136,204
T 166,242,41984
G 224,156
Mvvvc 60,140,204
X 241,64,240
G 225,157
Mvvvc 56,140,200
T 168,241,40960
G 226,158
Mvvvc 52,140,196
T 170,241,41984
G 227,159
Mvvvc 48,140,192
X 242,96,240
G 228,188
Mvvvc 64,144,192
T 172,242,40960
G 229,189
Mvvvc 68,144,196
T 174,242,41984
G 230,190
Mvvvc 72,144,200
X 241,128,240
G 231,191
Mvvvc 76,144,204
T 176,241,40960
C 232,224,225
Mvvvc 92,148,204
T 178,241,41984
C 233,226,227
Mvvvc 88,148,200
X 242,160,240
C 234,228,229
Mvvvc 84,148,196
T 180,242,40960
C 235,230,231
Mvvvc 80,148,192
T 182,242,41984
Svs 232,243,36,40,2048
Mvvvc 96,152,192
X 241,192,240
G 224,220
Mvvvc 100,152,196
T 184,241,40960
G 225,221
Mvvvc 104,152,200
T 186,241,41984
G 226,222
Mvvvc 108,152,204
X 242,224,240
G 227,223
Mvvvc 124,156,204
T 188,242,40960
G 228,252
Mvvvc 120,156,200
T 190,242,41984
G 229,253
Mvvvc 116,156,196
G 230,254
Mvvvc 112,156,192
G 231,255
C 232,224,225
C 233,226,227
C 234,228,229
C 235,230,231
Svs 232,243,36,40,3072
W 0
B
Mvvvc 0,160,208
Rv 192,244,32768
Mvvvc 4,160,212
s_add_u32 s55, s52, 0x380000
M0 135168
Mvvvc 8,160,216
Rv 196,244,33792
D 236,44,55,0
)A"
R"A(Mvvvc 12,160,220
Rv 200,244,34816
Mvvvc 28,164,220
D 237,44,55,1024
Mvvvc 24,164,216
Rv 204,244,35840
Mvvvc 20,164,212
Mvvvc 16,164,208
D 238,44,55,2048
Mvvvc 32,168,208
T 128,240,0
T 130,240,1024
Mvvvc 36,168,212
X 242,32,240
T 132,242,0
D 239,44,55,3072
Mvvvc 40,168,216
T 134,242,1024
s_add_u32 s55, s52, 0x3c0000
M0 143360
Mvvvc 44,168,220
D 236,44,55,0
Mvvvc 60,172,220
X 241,64,240
Mvvvc 56,172,216
T 136,241,0
T 138,241,1024
Mvvvc 52,172,212
X 242,96,240
D 237,44,55,1024
Mvvvc 48,172,208
T 140,242,0
T 142,242,1024
Mvvvc 64,176,208
X 241,128,240
D 238,44,55,2048
Mvvvc 68,176,212
T 144,241,0
T 146,241,1024
Mvvvc 72,176,216
X 242,160,240
D 239,44,55,3072
Mvvvc 76,176,220
T 148,242,0
T 150,242,1024
X 241,192,240
Mvvvc 92,180,220
T 152,241,0
AS 55,53,114688
M0 8192
Mvvvc 88,180,216
T 154,241,1024
Ds 243,48,55,0
Mvvvc 84,180,212
X 242,224,240
T 156,242,0
Mvvvc 80,180,208
T 158,242,1024
Ds 243,48,55,1024
Mvvvc 96,184,208
Mvvvc 100,184,212
Ds 243,48,55,2048
Mvvvc 104,184,216
Mvvvc 108,184,220
Mvvvc 124,188,220
Ds 243,48,55,3072
Mvvvc 120,188,216
Mvvvc 116,188,212
Mvvvc 112,188,208
W 0
Mvvvc 0,128,192
Rv 208,244,36864
Mvvvc 4,128,196
AS 55,54,114688
M0 24576
Mvvvc 8,128,200
Rv 212,244,37888
Ds 243,48,55,0
Mvvvc 12,128,204
Rv 216,244,38912
Mvvvc 28,132,204
Ds 243,48,55,1024
Mvvvc 24,132,200
Rv 220,244,39936
Mvvvc 20,132,196
Mvvvc 16,132,192
Ds 243,48,55,2048
Mvvvc 32,136,192
T 160,240,8192
T 162,240,9216
Mvvvc 36,136,196
X 242,32,240
T 164,242,8192
Ds 243,48,55,3072
Mvvvc 40,136,200
T 166,242,9216
AS 55,53,122880
M0 12288
Mvvvc 44,136,204
Ds 243,48,55,0
Mvvvc 60,140,204
X 241,64,240
Mvvvc 56,140,200
T 168,241,8192
T 170,241,9216
Mvvvc 52,140,196
X 242,96,240
Ds 243,48,55,1024
Mvvvc 48,140,192
T 172,242,8192
T 174,242,9216
Mvvvc 64,144,192
X 241,128,240
Ds 243,48,55,2048
Mvvvc 68,144,196
T 176,241,8192
T 178,241,9216
Mvvvc 72,144,200
X 242,160,240
Ds 243,48,55,3072
Mvvvc 76,144,204
T 180,242,8192
T 182,242,9216
X 241,192,240
Mvvvc 92,148,204
T 184,241,8192
AS 55,54,122880
M0 28672
Mvvvc 88,148,200
T 186,241,9216
Ds 243,48,55,0
Mvvvc 84,148,196
X 242,224,240
T 188,242,8192
Mvvvc 80,148,192
T 190,242,9216
Ds 243,48,55,1024
Mvvvc 96,152,192
Mvvvc 100,152,196
Ds 243,48,55,2048
Mvvvc 104,152,200
Mvvvc 108,152,204
Mvvvc 124,156,204
Ds 243,48,55,3072
Mvvvc 120,156,200
Mvvvc 116,156,196
Mvvvc 112,156,192
W 0
B
s_getpc_b64 s[16:17]
1:
s_add_u32 s16, s16, QE1%=-1b
s_addc_u32 s17, s17, 0
s_lshr_b32 s21, s10, 3
v_lshlrev_b32 v249, 5, v254
v_add_u32 v249, s21, v249
v_min_u32 v249, ((AE1%=-QE1%=)>>7), v249
v_lshlrev_b32 v249, 7, v249
global_load_dword v248, v249, s[16:17]
Mvvvc 0,160,208
Rv 192,245,0
Mvvvc 4,160,212
s_add_u32 s55, s52, 0x400000
M0 102400
Mvvvc 8,160,216
Rv 196,245,1024
D 236,44,55,0
Mvvvc 12,160,220
Rv 200,245,2048
Mvvvc 28,164,220
D 237,44,55,1024
Mvvvc 24,164,216
Rv 204,245,3072
Mvvvc 20,164,212
Mvvvc 16,164,208
D 238,44,55,2048
Mvvvc 32,168,208
T 128,240,16384
T 130,240,17408
Mvvvc 36,168,212
X 242,32,240
T 132,242,16384
D 239,44,55,3072
Mvvvc 40,168,216
T 134,242,17408
s_add_u32 s55, s52, 0x440000
M0 110592
Mvvvc 44,168,220
D 236,44,55,0
Mvvvc 60,172,220
X 241,64,240
Mvvvc 56,172,216
T 136,241,16384
T 138,241,17408
Mvvvc 52,172,212
X 242,96,240
D 237,44,55,1024
Mvvvc 48,172,208
T 140,242,16384
T 142,242,17408
Mvvvc 64,176,208
X 241,128,240
D 238,44,55,2048
Mvvvc 68,176,212
T 144,241,16384
T 146,241,17408
Mvvvc 72,176,216
X 242,160,240
D 239,44,55,3072
Mvvvc 76,176,220
T 148,242,16384
T 150,242,17408
X 241,192,240
Mvvvc 92,180,220
T 152,241,16384
AS 55,53,131072
M0 40960
Mvvvc 88,180,216
T 154,241,17408
Ds 243,48,55,0
Mvvvc 84,180,212
X 242,224,240
T 156,242,16384
Mvvvc 80,180,208
T 158,242,17408
Ds 243,48,55,1024
Mvvvc 96,184,208
Mvvvc 100,184,212
Ds 243,48,55,2048
Mvvvc 104,184,216
Mvvvc 108,184,220
Mvvvc 124,188,220
Ds 243,48,55,3072
Mvvvc 120,188,216
Mvvvc 116,188,212
Mvvvc 112,188,208
W 0
s_load_dwordx2 s[42:43], s[8:9], 40
s_load_dwordx2 s[36:37], s[8:9], 56
s_load_dwordx2 s[38:39], s[8:9], 64
Mvvvc 0,128,192
Rv 208,245,4096
Mvvvc 4,128,196
AS 55,54,131072
M0 57344
Mvvvc 8,128,200
Rv 212,245,5120
Ds 243,48,55,0
Mvvvc 12,128,204
Rv 216,245,6144
Mvvvc 28,132,204
Ds 243,48,55,1024
Mvvvc 24,132,200
Rv 220,245,7168
Mvvvc 20,132,196
Mvvvc 16,132,192
Ds 243,48,55,2048
Mvvvc 32,136,192
T 160,240,24576
T 162,240,25600
Mvvvc 36,136,196
X 242,32,240
T 164,242,24576
Ds 243,48,55,3072
Mvvvc 40,136,200
T 166,242,25600
AS 55,53,139264
M0 45056
Mvvvc 44,136,204
Ds 243,48,55,0
Mvvvc 60,140,204
X 241,64,240
Mvvvc 56,140,200
T 168,241,24576
T 170,241,25600
Mvvvc 52,140,196
X 242,96,240
Ds 243,48,55,1024
Mvvvc 48,140,192
T 172,242,24576
T 174,242,25600
Mvvvc 64,144,192
X 241,128,240
Ds 243,48,55,2048
Mvvvc 68,144,196
T 176,241,24576
T 178,241,25600
Mvvvc 72,144,200
X 242,160,240
Ds 243,48,55,3072
Mvvvc 76,144,204
T 180,242,24576
T 182,242,25600
X 241,192,240
Mvvvc 92,148,204
T 184,241,24576
AS 55,54,139264
M0 61440
Mvvvc 88,148,200
T 186,241,25600
Ds 243,48,55,0
Mvvvc 84,148,196
X 242,224,240
T 188,242,24576
Mvvvc 80,148,192
T 190,242,25600
Ds 243,48,55,1024
Mvvvc 96,152,192
Mvvvc 100,152,196
Ds 243,48,55,2048
Mvvvc 104,152,200
Mvvvc 108,152,204
Mvvvc 124,156,204
Ds 243,48,55,3072
Mvvvc 120,156,200
Mvvvc 116,156,196
Mvvvc 112,156,192
WV 25
B
Mvvvc 0,160,208
Rv 192,244,0
Mvvvc 4,160,212
s_add_u32 s55, s52, 0x480000
M0 118784
Mvvvc 8,160,216
Rv 196,244,1024
D 236,44,55,0
Mvvvc 12,160,220
Rv 200,244,2048
Mvvvc 28,164,220
D 237,44,55,1024
Mvvvc 24,164,216
Rv 204,244,3072
Mvvvc 20,164,212
Mvvvc 16,164,208
D 238,44,55,2048
Mvvvc 32,168,208
T 128,240,32768
T 130,240,33792
Mvvvc 36,168,212
X 242,32,240
T 132,242,32768
D 239,44,55,3072
Mvvvc 40,168,216
T 134,242,33792
s_add_u32 s55, s52, 0x4c0000
M0 126976
Mvvvc 44,168,220
D 236,44,55,0
Mvvvc 60,172,220
X 241,64,240
Mvvvc 56,172,216
T 136,241,32768
T 138,241,33792
Mvvvc 52,172,212
X 242,96,240
D 237,44,55,1024
Mvvvc 48,172,208
T 140,242,32768
T 142,242,33792
Mvvvc 64,176,208
X 241,128,240
D 238,44,55,2048
Mvvvc 68,176,212
T 144,241,32768
T 146,241,33792
Mvvvc 72,176,216
X 242,160,240
D 239,44,55,3072
Mvvvc 76,176,220
T 148,242,32768
T 150,242,33792
X 241,192,240
Mvvvc 92,180,220
T 152,241,32768
AS 55,53,147456
M0 73728
Mvvvc 88,180,216
T 154,241,33792
Ds 243,48,55,0
Mvvvc 84,180,212
X 242,224,240
T 156,242,32768
Mvvvc 80,180,208
T 158,242,33792
Ds 243,48,55,1024
Mvvvc 96,184,208
Mvvvc 100,184,212
Ds 243,48,55,2048
Mvvvc 104,184,216
Mvvvc 108,184,220
Mvvvc 124,188,220
Ds 243,48,55,3072
Mvvvc 120,188,216
Mvvvc 116,188,212
Mvvvc 112,188,208
W 0
s_lshl_b32 s21, s12, 8
AS 21,21,4096
v_mov_b32 v247, s21
v_min_u32 v246, 7, v254
v_lshl_add_u32 v247, v246, 2, v247
global_load_dword v246, v247, s[14:15] sc1
Mvvvc 0,128,192
Rv 208,244,4096
Mvvvc 4,128,196
AS 55,54,147456
M0 90112
Mvvvc 8,128,200
Rv 212,244,5120
Ds 243,48,55,0
Mvvvc 12,128,204
Rv 216,244,6144
Mvvvc 28,132,204
Ds 243,48,55,1024
Mvvvc 24,132,200
Rv 220,244,7168
Mvvvc 20,132,196
Mvvvc 16,132,192
Ds 243,48,55,2048
Mvvvc 32,136,192
T 160,240,40960
T 162,240,41984
Mvvvc 36,136,196
X 242,32,240
T 164,242,40960
Ds 243,48,55,3072
Mvvvc 40,136,200
T 166,242,41984
AS 55,53,155648
M0 77824
Mvvvc 44,136,204
Ds 243,48,55,0
Mvvvc 60,140,204
X 241,64,240
Mvvvc 56,140,200
T 168,241,40960
T 170,241,41984
Mvvvc 52,140,196
X 242,96,240
Ds 243,48,55,1024
Mvvvc 48,140,192
T 172,242,40960
T 174,242,41984
Mvvvc 64,144,192
X 241,128,240
Ds 243,48,55,2048
Mvvvc 68,144,196
T 176,241,40960
T 178,241,41984
Mvvvc 72,144,200
X 242,160,240
Ds 243,48,55,3072
Mvvvc 76,144,204
T 180,242,40960
T 182,242,41984
X 241,192,240
Mvvvc 92,148,204
T 184,241,40960
AS 55,54,155648
M0 94208
Mvvvc 88,148,200
T 186,241,41984
Ds 243,48,55,0
Mvvvc 84,148,196
X 242,224,240
T 188,242,40960
Mvvvc 80,148,192
T 190,242,41984
Ds 243,48,55,1024
Mvvvc 96,152,192
Mvvvc 100,152,196
Ds 243,48,55,2048
Mvvvc 104,152,200
Mvvvc 108,152,204
Mvvvc 124,156,204
Ds 243,48,55,3072
Mvvvc 120,156,200
Mvvvc 116,156,196
Mvvvc 112,156,192
WV 12
B
v_cmp_ne_u32 vcc, s79, v246
N 4
s_cbranch_vccnz KP1_SPIN%=
KP1_OK%=:
Mvvvc 0,160,208
Rv 192,244,32768
Mvvvc 4,160,212
Rv 196,244,33792
Mvvvc 8,160,216
s_lshl_b32 s21, s12, 19
AS 21,21,8192
M0 8192
Mvvvc 12,160,220
Rv 200,244,34816
Ds 243,24,21,0
Mvvvc 28,164,220
Mvvvc 24,164,216
Rv 204,244,35840
Mvvvc 20,164,212
Mvvvc 16,164,208
T 128,240,0
Ds 243,24,21,1024
Mvvvc 32,168,208
T 130,240,1024
Mvvvc 36,168,212
X 242,32,240
Mvvvc 40,168,216
T 132,242,0
Ds 243,24,21,2048
Mvvvc 44,168,220
T 134,242,1024
Mvvvc 60,172,220
X 241,64,240
Mvvvc 56,172,216
T 136,241,0
Ds 243,24,21,3072
Mvvvc 52,172,212
T 138,241,1024
Mvvvc 48,172,208
X 242,96,240
Mvvvc 64,176,208
T 140,242,0
s_lshl_b32 s21, s12, 19
AS 21,21,12288
M0 12288
Mvvvc 68,176,212
T 142,242,1024
Ds 243,24,21,0
Mvvvc 72,176,216
X 241,128,240
Mvvvc 76,176,220
T 144,241,0
Mvvvc 92,180,220
T 146,241,1024
Ds 243,24,21,1024
Mvvvc 88,180,216
X 242,160,240
Mvvvc 84,180,212
T 148,242,0
Mvvvc 80,180,208
T 150,242,1024
Mvvvc 96,184,208
X 241,192,240
Ds 243,24,21,2048
Mvvvc 100,184,212
T 152,241,0
Mvvvc 104,184,216
T 154,241,1024
Mvvvc 108,184,220
X 242,224,240
Ds 243,24,21,3072
Mvvvc 124,188,220
T 156,242,0
Mvvvc 120,188,216
T 158,242,1024
Mvvvc 116,188,212
Mvvvc 112,188,208
W 0
Mvvvc 0,128,192
Rv 208,244,36864
Mvvvc 4,128,196
Rv 212,244,37888
Mvvvc 8,128,200
s_lshl_b32 s21, s12, 19
AS 21,21,24576
M0 24576
Mvvvc 12,128,204
Rv 216,244,38912
Ds 243,24,21,0
Mvvvc 28,132,204
Mvvvc 24,132,200
Rv 220,244,39936
Mvvvc 20,132,196
Mvvvc 16,132,192
T 160,240,8192
Ds 243,24,21,1024
Mvvvc 32,136,192
T 162,240,9216
Mvvvc 36,136,196
X 242,32,240
Mvvvc 40,136,200
T 164,242,8192
Ds 243,24,21,2048
Mvvvc 44,136,204
T 166,242,9216
Mvvvc 60,140,204
X 241,64,240
Mvvvc 56,140,200
T 168,241,8192
Ds 243,24,21,3072
Mvvvc 52,140,196
T 170,241,9216
Mvvvc 48,140,192
X 242,96,240
Mvvvc 64,144,192
T 172,242,8192
s_lshl_b32 s21, s12, 19
AS 21,21,28672
M0 28672
Mvvvc 68,144,196
T 174,242,9216
Ds 243,24,21,0
Mvvvc 72,144,200
X 241,128,240
Mvvvc 76,144,204
T 176,241,8192
Mvvvc 92,148,204
T 178,241,9216
Ds 243,24,21,1024
Mvvvc 88,148,200
X 242,160,240
Mvvvc 84,148,196
T 180,242,8192
Mvvvc 80,148,192
T 182,242,9216
Mvvvc 96,152,192
X 241,192,240
Ds 243,24,21,2048
Mvvvc 100,152,196
T 184,241,8192
Mvvvc 104,152,200
T 186,241,9216
Mvvvc 108,152,204
X 242,224,240
Ds 243,24,21,3072
Mvvvc 124,156,204
T 188,242,8192
Mvvvc 120,156,200
T 190,242,9216
Mvvvc 116,156,196
Mvvvc 112,156,192
WV 16
B
Mvvvc 0,160,208
Rv 192,245,0
Mvvvc 4,160,212
Rv 196,245,1024
Mvvvc 8,160,216
Mvvvc 12,160,220
Rv 200,245,2048
Mvvvc 28,164,220
Mvvvc 24,164,216
Rv 204,245,3072
Mvvvc 20,164,212
Mvvvc 16,164,208
T 128,240,16384
Mvvvc 32,168,208
T 130,240,17408
Mvvvc 36,168,212
X 242,32,240
Mvvvc 40,168,216
T 132,242,16384
Mvvvc 44,168,220
T 134,242,17408
Mvvvc 60,172,220
X 241,64,240
Mvvvc 56,172,216
T 136,241,16384
Mvvvc 52,172,212
T 138,241,17408
Mvvvc 48,172,208
X 242,96,240
Mvvvc 64,176,208
T 140,242,16384
Mvvvc 68,176,212
T 142,242,17408
Mvvvc 72,176,216
X 241,128,240
Mvvvc 76,176,220
T 144,241,16384
Mvvvc 92,180,220
T 146,241,17408
Mvvvc 88,180,216
X 242,160,240
Mvvvc 84,180,212
T 148,242,16384
Mvvvc 80,180,208
T 150,242,17408
Mvvvc 96,184,208
X 241,192,240
Mvvvc 100,184,212
T 152,241,16384
Mvvvc 104,184,216
T 154,241,17408
Mvvvc 108,184,220
X 242,224,240
Mvvvc 124,188,220
T 156,242,16384
Mvvvc 120,188,216
T 158,242,17408
Mvvvc 116,188,212
Mvvvc 112,188,208
W 0
Mvvvc 0,128,192
Rv 208,245,4096
Mvvvc 4,128,196
Rv 212,245,5120
Mvvvc 8,128,200
Mvvvc 12,128,204
Rv 216,245,6144
Mvvvc 28,132,204
Mvvvc 24,132,200
Rv 220,245,7168
Mvvvc 20,132,196
Mvvvc 16,132,192
T 160,240,24576
Mvvvc 32,136,192
T 162,240,25600
Mvvvc 36,136,196
X 242,32,240
Mvvvc 40,136,200
T 164,242,24576
Mvvvc 44,136,204
T 166,242,25600
Mvvvc 60,140,204
X 241,64,240
Mvvvc 56,140,200
T 168,241,24576
Mvvvc 52,140,196
T 170,241,25600
Mvvvc 48,140,192
X 242,96,240
Mvvvc 64,144,192
T 172,242,24576
Mvvvc 68,144,196
T 174,242,25600
Mvvvc 72,144,200
X 241,128,240
Mvvvc 76,144,204
T 176,241,24576
Mvvvc 92,148,204
T 178,241,25600
Mvvvc 88,148,200
X 242,160,240
Mvvvc 84,148,196
T 180,242,24576
Mvvvc 80,148,192
)A"
R"A(T 182,242,25600
Mvvvc 96,152,192
X 241,192,240
Mvvvc 100,152,196
T 184,241,24576
Mvvvc 104,152,200
T 186,241,25600
Mvvvc 108,152,204
X 242,224,240
Mvvvc 124,156,204
T 188,242,24576
Mvvvc 120,156,200
T 190,242,25600
Mvvvc 116,156,196
Mvvvc 112,156,192
W 0
B
Mvvvc 0,160,208
Mvvvc 4,160,212
Mvvvc 8,160,216
Mvvvc 12,160,220
Mvvvc 28,164,220
Mvvvc 24,164,216
Mvvvc 20,164,212
Mvvvc 16,164,208
Mvvvc 32,168,208
Mvvvc 36,168,212
Mvvvc 40,168,216
Mvvvc 44,168,220
Mvvvc 60,172,220
Mvvvc 56,172,216
Mvvvc 52,172,212
Mvvvc 48,172,208
Mvvvc 64,176,208
Mvvvc 68,176,212
Mvvvc 72,176,216
Mvvvc 76,176,220
Mvvvc 92,180,220
Mvvvc 88,180,216
Mvvvc 84,180,212
Mvvvc 80,180,208
Mvvvc 96,184,208
Mvvvc 100,184,212
Mvvvc 104,184,216
Mvvvc 108,184,220
Mvvvc 124,188,220
Mvvvc 120,188,216
Mvvvc 116,188,212
Mvvvc 112,188,208
QE1%=:
WV 0
B
VA 224,114688,243
C 128,0,1
C 129,2,3
C 130,16,17
C 131,18,19
DWv 224,128,0
C 132,32,33
C 133,34,35
C 134,48,49
C 135,50,51
DWv 224,132,1024
C 136,64,65
C 137,66,67
C 138,80,81
C 139,82,83
DWv 224,136,2048
C 140,96,97
C 141,98,99
C 142,112,113
C 143,114,115
DWv 224,140,3072
C 144,4,5
C 145,6,7
C 146,20,21
C 147,22,23
DWv 224,144,4096
C 148,36,37
C 149,38,39
C 150,52,53
C 151,54,55
DWv 224,148,5120
C 152,68,69
C 153,70,71
C 154,84,85
C 155,86,87
DWv 224,152,6144
C 156,100,101
C 157,102,103
C 158,116,117
C 159,118,119
DWv 224,156,7168
C 128,8,9
C 129,10,11
C 130,24,25
C 131,26,27
DWv 224,128,8192
C 132,40,41
C 133,42,43
C 134,56,57
C 135,58,59
DWv 224,132,9216
C 136,72,73
C 137,74,75
C 138,88,89
C 139,90,91
DWv 224,136,10240
C 140,104,105
C 141,106,107
C 142,120,121
C 143,122,123
DWv 224,140,11264
C 144,12,13
C 145,14,15
C 146,28,29
C 147,30,31
DWv 224,144,12288
C 148,44,45
C 149,46,47
C 150,60,61
C 151,62,63
DWv 224,148,13312
C 152,76,77
C 153,78,79
C 154,92,93
C 155,94,95
DWv 224,152,14336
C 156,108,109
C 157,110,111
C 158,124,125
C 159,126,127
DWv 224,156,15360
W 0
B
Ra 128,224,0
Ra 132,224,1024
Ra 136,224,2048
Ra 140,224,3072
Ra 144,224,4096
Ra 148,224,5120
Ra 152,224,6144
Ra 156,224,7168
Ra 160,224,8192
Ra 164,224,9216
Ra 168,224,10240
Ra 172,224,11264
Ra 176,224,12288
Ra 180,224,13312
Ra 184,224,14336
Ra 188,224,15360
s_add_u32 s32, s14, 0x2400000
s_addc_u32 s33, s15, 0
SM 34,0x1000000
SM 35,0x20000
v_lshlrev_b32 v196, 4, v254
VA 197,0,196
VA 198,65536,196
v_mov_b32 v199, 0xff800000
v_mov_b32 v192, 0x3f803f80
v_mov_b32 v193, 0x3f803f80
v_mov_b32 v194, 0x3f803f80
v_mov_b32 v195, 0x3f803f80
v_and_b32 v200, 15, v254
v_lshrrev_b32 v201, 4, v254
v_lshlrev_b32 v201, 2, v201
VA 202,0,201
v_cmp_le_u32 s[62:63], v202, v200
VA 202,1,201
v_cmp_le_u32 s[64:65], v202, v200
VA 202,2,201
v_cmp_le_u32 s[66:67], v202, v200
VA 202,3,201
v_cmp_le_u32 s[68:69], v202, v200
s_lshl_b32 s61, s13, 1
AS 61,61,2
SM 60,0
s_mov_b64 s[70:71], 0
s_mov_b64 s[50:51], s[42:43]
VA 237,0,254
v_mul_u32_u24 v238, 3277, v237
v_lshrrev_b32 v238, 16, v238
v_mul_u32_u24 v239, 20, v238
v_sub_u32 v239, v237, v239
v_lshrrev_b32 v240, 1, v239
v_lshrrev_b32 v241, 2, v238
v_and_b32 v241, 1, v241
v_xor_b32 v240, v240, v241
v_and_b32 v239, 1, v239
v_lshlrev_b32 v239, 4, v239
v_lshl_add_u32 v239, v240, 5, v239
v_mul_u32_u24 v238, 1280, v238
v_add_u32 v238, v238, v239
MV 232,238
VA 237,64,254
v_mul_u32_u24 v238, 3277, v237
v_lshrrev_b32 v238, 16, v238
v_mul_u32_u24 v239, 20, v238
v_sub_u32 v239, v237, v239
v_lshrrev_b32 v240, 1, v239
v_lshrrev_b32 v241, 2, v238
v_and_b32 v241, 1, v241
v_xor_b32 v240, v240, v241
v_and_b32 v239, 1, v239
v_lshlrev_b32 v239, 4, v239
v_lshl_add_u32 v239, v240, 5, v239
v_mul_u32_u24 v238, 1280, v238
v_add_u32 v238, v238, v239
v_subrev_u32 v233, 1024, v238
VA 237,128,254
v_mul_u32_u24 v238, 3277, v237
v_lshrrev_b32 v238, 16, v238
v_mul_u32_u24 v239, 20, v238
v_sub_u32 v239, v237, v239
v_lshrrev_b32 v240, 1, v239
v_lshrrev_b32 v241, 2, v238
v_and_b32 v241, 1, v241
v_xor_b32 v240, v240, v241
v_and_b32 v239, 1, v239
v_lshlrev_b32 v239, 4, v239
v_lshl_add_u32 v239, v240, 5, v239
v_mul_u32_u24 v238, 1280, v238
v_add_u32 v238, v238, v239
v_subrev_u32 v234, 2048, v238
VA 237,192,254
v_mul_u32_u24 v238, 3277, v237
v_lshrrev_b32 v238, 16, v238
v_mul_u32_u24 v239, 20, v238
v_sub_u32 v239, v237, v239
v_lshrrev_b32 v240, 1, v239
v_lshrrev_b32 v241, 2, v238
v_and_b32 v241, 1, v241
v_xor_b32 v240, v240, v241
v_and_b32 v239, 1, v239
v_lshlrev_b32 v239, 4, v239
v_lshl_add_u32 v239, v240, 5, v239
v_mul_u32_u24 v238, 1280, v238
v_add_u32 v238, v238, v239
v_subrev_u32 v235, 3072, v238
VA 237,256,254
v_mul_u32_u24 v238, 3277, v237
v_lshrrev_b32 v238, 16, v238
v_mul_u32_u24 v239, 20, v238
v_sub_u32 v239, v237, v239
v_lshrrev_b32 v240, 1, v239
v_lshrrev_b32 v241, 2, v238
v_and_b32 v241, 1, v241
v_xor_b32 v240, v240, v241
v_and_b32 v239, 1, v239
v_lshlrev_b32 v239, 4, v239
v_lshl_add_u32 v239, v240, 5, v239
v_mul_u32_u24 v238, 1280, v238
v_add_u32 v238, v238, v239
MV 236,238
B
AM 59,0
N 0
Ds 196,28,57,0
Ds 196,28,57,1024
Ds 196,28,57,2048
Ds 196,28,57,3072
AS 57,57,16384
s_cmp_eq_u32 s13, 0
s_cbranch_scc0 AT1_M1%=
SM 73,0x4000
SM 74,0x0
SM 75,0x0
SM 76,0x0
s_branch AT1_MD%=
AT1_M1%=:
s_cmp_eq_u32 s13, 1
s_cbranch_scc0 AT1_M2%=
SM 73,0x4000
SM 74,0x2
SM 75,0x0
SM 76,0x0
s_branch AT1_MD%=
AT1_M2%=:
s_cmp_eq_u32 s13, 2
s_cbranch_scc0 AT1_M3%=
SM 73,0x4000
SM 74,0x0
SM 75,0x0
SM 76,0x8
s_branch AT1_MD%=
AT1_M3%=:
s_cmp_eq_u32 s13, 3
s_cbranch_scc0 AT1_M4%=
SM 73,0x4000
SM 74,0x0
SM 75,0x0
SM 76,0x20
s_branch AT1_MD%=
AT1_M4%=:
s_cmp_eq_u32 s13, 4
s_cbranch_scc0 AT1_M5%=
SM 73,0x4000
SM 74,0x0
SM 75,0x0
SM 76,0x80
s_branch AT1_MD%=
AT1_M5%=:
s_cmp_eq_u32 s13, 5
s_cbranch_scc0 AT1_M6%=
SM 73,0x4000
SM 74,0x0
SM 75,0x0
SM 76,0x200
s_branch AT1_MD%=
AT1_M6%=:
s_cmp_eq_u32 s13, 6
s_cbranch_scc0 AT1_M7%=
SM 73,0x4000
SM 74,0x0
SM 75,0x0
SM 76,0x800
s_branch AT1_MD%=
AT1_M7%=:
SM 73,0x0
SM 74,0x0
SM 75,0x4000
SM 76,0x2000
AT1_MD%=:
WV 4
B
Rv 144,197,0
Rv 148,197,1024
Rv 152,197,2048
Rv 156,197,3072
Rv 160,197,4096
Rv 164,197,5120
Rv 168,197,6144
W 5
s_cmp_eq_u32 s13, 0
s_cbranch_scc1 AT1_B0%=
s_branch AT1_B7%=
XAT1_B0%=:
AS 60,60,1
AT1_B0%=:
Mvvaz 0,144,128
Mvvaz 8,144,144
Mvvaz 16,144,160
Rv 172,197,7168
Mvvaz 24,144,176
Mvvac 24,148,180
Mvvac 16,148,164
Rv 144,197,8192
Mvvac 8,148,148
Mvvac 0,148,132
W 5
Mvvac 0,152,136
Mvvac 8,152,152
Rv 148,197,9216
Mvvac 16,152,168
Mvvac 24,152,184
Mvvac 24,156,188
Mvvac 16,156,172
Rv 152,197,10240
Mvvac 8,156,156
Mvvac 0,156,140
W 5
Mvvaz 4,160,128
Mvvaz 12,160,144
Rv 156,197,11264
Mvvaz 20,160,160
N 2
v_max3_f32 v200, v0, v1, v2
Fma 200,200,3
v_max3_f32 v202, v8, v9, v10
Fma 202,202,11
Mvvaz 28,160,176
v_max3_f32 v204, v16, v17, v18
Fma 204,204,19
v_max3_f32 v206, v24, v25, v26
Fma 206,206,27
Mvvac 28,164,180
MV 201,200
MV 203,202
MV 205,204
MV 207,206
Mvvac 20,164,164
Rv 160,197,12288
v_permlane16_swap_b32 v200, v201
v_permlane16_swap_b32 v202, v203
v_permlane16_swap_b32 v204, v205
Mvvac 12,164,148
v_permlane16_swap_b32 v206, v207
Fma 200,200,201
Fma 202,202,203
Fma 204,204,205
Mvvac 4,164,132
Fma 206,206,207
MV 201,200
MV 203,202
MV 205,204
W 5
Mvvac 4,168,136
MV 207,206
v_permlane32_swap_b32 v200, v201
v_permlane32_swap_b32 v202, v203
Mvvac 12,168,152
v_permlane32_swap_b32 v204, v205
v_permlane32_swap_b32 v206, v207
Fma 200,200,201
Fma 202,202,203
Mvvac 20,168,168
Rv 164,197,13312
Fma 204,204,205
Fma 206,206,207
v_sub_f32 v128, 0, v200
v_sub_f32 v129, 0, v200
Mvvac 28,168,184
v_sub_f32 v130, 0, v200
v_sub_f32 v131, 0, v200
v_sub_f32 v132, 0, v202
Mvvac 28,172,188
v_sub_f32 v133, 0, v202
v_sub_f32 v134, 0, v202
v_sub_f32 v135, 0, v202
v_sub_f32 v136, 0, v204
Mvvac 20,172,172
v_sub_f32 v137, 0, v204
v_sub_f32 v138, 0, v204
v_sub_f32 v139, 0, v204
v_sub_f32 v140, 0, v206
Mvvac 12,172,156
v_sub_f32 v141, 0, v206
v_sub_f32 v142, 0, v206
v_sub_f32 v143, 0, v206
Mvvac 4,172,140
Rv 168,197,14336
W 5
Mvvan 32,144,128,128
Mvvan 40,144,144,132
Mvvan 48,144,160,136
Mvvan 56,144,176,140
Mvvac 56,148,180
Rv 172,197,15360
Mvvac 48,148,164
Fad 0,0,128
Mvvac 40,148,148
E 0
Mvvac 32,148,132
Fad 1,1,128
W 4
Mvvac 32,152,136
E 1
Mvvac 40,152,152
Fad 2,2,128
Mvvac 48,152,168
E 2
Mvvac 56,152,184
C 0,0,1
Fad 3,3,128
Mvvac 56,156,188
E 3
AS 56,56,4096
SM 58,8192
Mvvac 48,156,172
Fad 4,4,128
W 0
B
Mvvac 40,156,156
E 4
s_mul_i32 s21, s10, 68
Mvvac 32,156,140
Fad 5,5,128
AM 58,0
W 15
AS 21,21,0
Mvvan 36,160,128,128
E 5
Ds 196,24,56,0
v_min_u32 v249, 63, v254
Mvvan 44,160,144,132
C 1,2,3
Fad 6,6,128
v_add_u32 v249, s21, v249
Mvvan 52,160,160,136
E 6
v_min_u32 v249, 17279, v249
Mvvan 60,160,176,140
C 2,4,5
Fad 7,7,128
v_lshlrev_b32 v249, 7, v249
Mvvac 60,164,180
E 7
global_load_dword v248, v249, s[36:37]
Mvvac 52,164,164
Fad 8,8,132
s_mul_i32 s21, s10, 68
Mvvac 44,164,148
E 8
AS 21,21,64
Mvvac 36,164,132
Fad 9,9,132
Ds 196,24,56,1024
W 15
v_min_u32 v249, 3, v254
Mvvac 36,168,136
E 9
v_add_u32 v249, s21, v249
Mvvac 44,168,152
C 3,6,7
Fad 10,10,132
v_min_u32 v249, 17279, v249
Mvvac 52,168,168
E 10
v_lshlrev_b32 v249, 7, v249
Mvvac 60,168,184
C 8,8,9
Fad 11,11,132
global_load_dword v248, v249, s[36:37]
Mvvac 60,172,188
E 11
Mvvac 52,172,172
Fad 12,12,132
Mvvac 44,172,156
E 12
Ds 196,24,56,2048
Mvvac 36,172,140
Fad 13,13,132
E 13
C 9,10,11
Fad 14,14,132
E 14
C 10,12,13
Fad 15,15,132
E 15
Fad 16,16,136
Ds 196,24,56,3072
E 16
Fad 17,17,136
AS 56,56,4096
E 17
C 11,14,15
Fad 18,18,136
E 18
C 16,16,17
Fad 19,19,136
E 19
Fad 20,20,136
E 20
Fad 21,21,136
AM 58,4096
E 21
Ds 196,24,56,0
C 17,18,19
Fad 22,22,136
E 22
C 18,20,21
Fad 23,23,136
E 23
Fad 24,24,140
E 24
Fad 25,25,140
Ds 196,24,56,1024
E 25
C 19,22,23
Fad 26,26,140
E 26
C 24,24,25
Fad 27,27,140
E 27
Fad 28,28,140
E 28
Ds 196,24,56,2048
Fad 29,29,140
E 29
C 25,26,27
Fad 30,30,140
E 30
Fad 31,31,140
E 31
C 26,28,29
Ds 196,24,56,3072
E 32
AS 56,56,28672
E 33
C 27,30,31
E 34
C 32,32,33
E 35
W 0
B
Rv 144,197,16384
E 36
AM 59,16384
Rv 148,197,17408
E 37
Ds 196,28,57,0
C 33,34,35
E 38
Rv 152,197,18432
C 34,36,37
E 39
Ds 196,28,57,1024
Rv 156,197,19456
E 40
E 41
Ds 196,28,57,2048
Rv 160,197,20480
C 35,38,39
E 42
C 40,40,41
Rv 164,197,21504
E 43
Ds 196,28,57,3072
E 44
AS 57,57,16384
Rv 168,197,22528
W 5
XAT1_B1%=:
AS 60,60,1
AT1_B1%=:
Mvvan 64,144,128,128
E 45
Mvvan 72,144,144,132
C 41,42,43
Mvvan 80,144,160,136
Rv 172,197,23552
E 46
Mvvan 88,144,176,140
C 42,44,45
Mvvac 88,148,180
E 47
Mvvac 80,148,164
Rv 144,197,24576
Mvvac 72,148,148
E 48
Mvvac 64,148,132
W 5
Mvvac 64,152,136
E 49
Mvvac 72,152,152
Rv 148,197,25600
C 43,46,47
Mvvac 80,152,168
E 50
Mvvac 88,152,184
C 48,48,49
Mvvac 88,156,188
E 51
Mvvac 80,156,172
Rv 152,197,26624
Mvvac 72,156,156
E 52
Mvvac 64,156,140
W 5
Mvvan 68,160,128,128
E 53
Mvvan 76,160,144,132
Rv 156,197,27648
C 49,50,51
Mvvan 84,160,160,136
E 54
Mvvan 92,160,176,140
C 50,52,53
Mvvac 92,164,180
E 55
Mvvac 84,164,164
Rv 160,197,28672
Mvvac 76,164,148
E 56
Mvvac 68,164,132
W 5
Mvvac 68,168,136
E 57
Mvvac 76,168,152
C 51,54,55
Mvvac 84,168,168
Rv 164,197,29696
E 58
Mvvac 92,168,184
C 56,56,57
Mvvac 92,172,188
E 59
Mvvac 84,172,172
Mvvac 76,172,156
E 60
Mvvac 68,172,140
Rv 168,197,30720
W 5
Mvvan 96,144,128,128
E 61
Mvvan 104,144,144,132
C 57,58,59
Mvvan 112,144,160,136
E 62
Mvvan 120,144,176,140
C 58,60,61
Mvvac 120,148,180
Rv 172,197,31744
E 63
Mvvac 112,148,164
Mvvac 104,148,148
E 64
Mvvac 96,148,132
W 4
Mvvac 96,152,136
E 65
Mvvac 104,152,152
C 59,62,63
Mvvac 112,152,168
E 66
Mvvac 120,152,184
C 64,64,65
Mvvac 120,156,188
E 67
Mvvac 112,156,172
WV 14
B
Mvvac 104,156,156
Rv 144,198,0
E 68
s_mul_i32 s21, s10, 68
Mvvac 96,156,140
AM 58,16384
W 15
AS 21,21,0
Mvvan 100,160,128,128
Rv 148,198,1024
E 69
Ds 196,24,56,0
v_min_u32 v249, 63, v254
Mvvan 108,160,144,132
C 65,66,67
v_add_u32 v249, s21, v249
Mvvan 116,160,160,136
E 70
v_min_u32 v249, 17279, v249
Mvvan 124,160,176,140
Rv 152,198,2048
C 66,68,69
v_lshlrev_b32 v249, 7, v249
Mvvac 124,164,180
E 71
global_load_dword v248, v249, s[38:39]
Mvvac 116,164,164
s_mul_i32 s21, s10, 68
Mvvac 108,164,148
Rv 156,198,3072
E 72
AS 21,21,64
Mvvac 100,164,132
Ds 196,24,56,1024
W 15
v_min_u32 v249, 3, v254
Mvvac 100,168,136
E 73
v_add_u32 v249, s21, v249
Mvvac 108,168,152
Rv 160,198,4096
C 67,70,71
v_min_u32 v249, 17279, v249
)A"
R"A(Mvvac 116,168,168
E 74
v_lshlrev_b32 v249, 7, v249
Mvvac 124,168,184
C 72,72,73
global_load_dword v248, v249, s[38:39]
Mvvac 124,172,188
Rv 164,198,5120
E 75
Mvvac 116,172,172
Mvvac 108,172,156
E 76
Ds 196,24,56,2048
Mvvac 100,172,140
Rv 168,198,6144
W 5
Mavvz 0,144,0
E 77
Mavvz 4,144,8
C 73,74,75
Mavvz 8,144,16
Rv 172,198,7168
E 78
Mavvz 12,144,24
C 74,76,77
Mavvz 28,148,24
E 79
Mavvz 24,148,16
Rv 144,198,8192
Mvvvz 176,192,0
Ds 196,24,56,3072
Mavvz 20,148,8
E 80
Mavvz 16,148,0
W 5
AS 56,56,4096
Mavvz 32,152,0
E 81
Mavvz 36,152,8
Rv 148,198,9216
C 75,78,79
Mavvz 40,152,16
E 82
Mavvz 44,152,24
C 80,80,81
Mavvz 60,156,24
E 83
Mavvz 56,156,16
Rv 152,198,10240
Mvvvz 180,192,8
Mavvz 52,156,8
E 84
Mavvz 48,156,0
AM 58,20480
W 5
Mavvz 64,160,0
E 85
Ds 196,24,56,0
Mavvz 68,160,8
Rv 156,198,11264
Mvvvz 184,192,16
C 81,82,83
Mavvz 72,160,16
E 86
Mavvz 76,160,24
C 82,84,85
Mavvz 92,164,24
E 87
Mavvz 88,164,16
Rv 160,198,12288
Mavvz 84,164,8
E 88
Mavvz 80,164,0
Mvvvz 188,192,24
Ds 196,24,56,1024
W 5
Mavvz 96,168,0
E 89
Mavvz 100,168,8
C 83,86,87
Mavvz 104,168,16
Rv 164,198,13312
E 90
Mavvz 108,168,24
C 88,88,89
Mavvz 124,172,24
E 91
Mavvz 120,172,16
Mavvz 116,172,8
E 92
Ds 196,24,56,2048
Mavvz 112,172,0
Rv 168,198,14336
W 5
Mavvc 0,144,32
E 93
Mavvc 4,144,40
C 89,90,91
Mavvc 8,144,48
E 94
Mavvc 12,144,56
C 90,92,93
Mavvc 28,148,56
Rv 172,198,15360
E 95
Mavvc 24,148,48
Mvvvc 176,192,32
Ds 196,24,56,3072
Mavvc 20,148,40
E 96
Mavvc 16,148,32
W 4
AS 56,56,28672
Mavvc 32,152,32
E 97
Mavvc 36,152,40
Mvvvc 180,192,40
C 91,94,95
Mavvc 40,152,48
E 98
Mavvc 44,152,56
C 96,96,97
Mavvc 60,156,56
E 99
Mavvc 56,156,48
WV 14
B
AS 57,57,4096
SM 59,73728
Mavvc 52,156,40
Rv 144,197,0
E 100
Mavvc 48,156,32
AM 59,0
W 15
Mavvc 64,160,32
Rv 148,197,1024
E 101
Ds 196,28,57,0
Mavvc 68,160,40
Mvvvc 184,192,48
C 97,98,99
Mavvc 72,160,48
E 102
Mavvc 76,160,56
Rv 152,197,2048
C 98,100,101
Mavvc 92,164,56
E 103
Ds 196,28,57,1024
Mavvc 88,164,48
Mavvc 84,164,40
Rv 156,197,3072
E 104
Mavvc 80,164,32
Mvvvc 188,192,56
W 15
Mavvc 96,168,32
E 105
Ds 196,28,57,2048
Mavvc 100,168,40
Rv 160,197,4096
C 99,102,103
Mavvc 104,168,48
E 106
Mavvc 108,168,56
C 104,104,105
Mavvc 124,172,56
Rv 164,197,5120
E 107
Ds 196,28,57,3072
Mavvc 120,172,48
Mavvc 116,172,40
E 108
Mavvc 112,172,32
Rv 168,197,6144
W 5
XAT1_B2%=:
AS 60,60,1
AT1_B2%=:
Mvvan 0,144,128,128
AS 57,57,4096
E 109
Mvvan 8,144,144,132
C 105,106,107
Mvvan 16,144,160,136
Rv 172,197,7168
E 110
Mvvan 24,144,176,140
C 106,108,109
AM 59,4096
Mvvac 24,148,180
E 111
Ds 196,28,57,0
Mvvac 16,148,164
Rv 144,197,8192
Mvvac 8,148,148
E 112
Mvvac 0,148,132
W 5
Mvvac 0,152,136
E 113
Mvvac 8,152,152
Rv 148,197,9216
C 107,110,111
Mvvac 16,152,168
E 114
Mvvac 24,152,184
C 112,112,113
Mvvac 24,156,188
E 115
Mvvac 16,156,172
Rv 152,197,10240
Ds 196,28,57,1024
Mvvac 8,156,156
E 116
Mvvac 0,156,140
W 5
Mvvan 4,160,128,128
E 117
Mvvan 12,160,144,132
Rv 156,197,11264
C 113,114,115
Mvvan 20,160,160,136
E 118
Mvvan 28,160,176,140
C 114,116,117
Mvvac 28,164,180
E 119
Mvvac 20,164,164
Rv 160,197,12288
Mvvac 12,164,148
E 120
Ds 196,28,57,2048
Mvvac 4,164,132
W 5
Mvvac 4,168,136
E 121
Mvvac 12,168,152
C 115,118,119
Mvvac 20,168,168
Rv 164,197,13312
E 122
Mvvac 28,168,184
C 120,120,121
Mvvac 28,172,188
E 123
Mvvac 20,172,172
Mvvac 12,172,156
E 124
Mvvac 4,172,140
Rv 168,197,14336
Ds 196,28,57,3072
W 5
Mvvan 32,144,128,128
E 125
Mvvan 40,144,144,132
C 121,122,123
AS 57,57,28672
Mvvan 48,144,160,136
E 126
Mvvan 56,144,176,140
C 122,124,125
Mvvac 56,148,180
Rv 172,197,15360
E 127
Mvvac 48,148,164
Mvvac 40,148,148
E 0
Mvvac 32,148,132
W 4
Mvvac 32,152,136
E 1
Mvvac 40,152,152
C 123,126,127
Mvvac 48,152,168
E 2
Mvvac 56,152,184
C 0,0,1
Mvvac 56,156,188
E 3
Mvvac 48,156,172
WV 16
B
Mvvac 40,156,156
Rv 144,198,16384
E 4
Mvvac 32,156,140
AM 58,0
W 15
Mvvan 36,160,128,128
Rv 148,198,17408
E 5
Ds 196,24,56,0
Mvvan 44,160,144,132
C 1,2,3
Mvvan 52,160,160,136
E 6
Mvvan 60,160,176,140
Rv 152,198,18432
C 2,4,5
Mvvac 60,164,180
E 7
Mvvac 52,164,164
Mvvac 44,164,148
Rv 156,198,19456
E 8
Mvvac 36,164,132
Ds 196,24,56,1024
W 15
Mvvac 36,168,136
E 9
Mvvac 44,168,152
Rv 160,198,20480
C 3,6,7
Mvvac 52,168,168
E 10
Mvvac 60,168,184
C 8,8,9
Mvvac 60,172,188
Rv 164,198,21504
E 11
Mvvac 52,172,172
Mvvac 44,172,156
E 12
Ds 196,24,56,2048
Mvvac 36,172,140
Rv 168,198,22528
W 5
Mavvc 0,144,64
E 13
Mavvc 4,144,72
C 9,10,11
Mavvc 8,144,80
Rv 172,198,23552
E 14
Mavvc 12,144,88
C 10,12,13
Mavvc 28,148,88
E 15
Mavvc 24,148,80
Rv 144,198,24576
Mvvvc 176,192,64
Ds 196,24,56,3072
Mavvc 20,148,72
E 16
Mavvc 16,148,64
W 5
AS 56,56,4096
Mavvc 32,152,64
E 17
Mavvc 36,152,72
Rv 148,198,25600
C 11,14,15
Mavvc 40,152,80
E 18
Mavvc 44,152,88
C 16,16,17
Mavvc 60,156,88
E 19
Mavvc 56,156,80
Rv 152,198,26624
Mvvvc 180,192,72
Mavvc 52,156,72
E 20
Mavvc 48,156,64
AM 58,4096
W 5
Mavvc 64,160,64
E 21
Ds 196,24,56,0
Mavvc 68,160,72
Rv 156,198,27648
Mvvvc 184,192,80
C 17,18,19
Mavvc 72,160,80
E 22
Mavvc 76,160,88
C 18,20,21
Mavvc 92,164,88
E 23
Mavvc 88,164,80
Rv 160,198,28672
Mavvc 84,164,72
E 24
Mavvc 80,164,64
Mvvvc 188,192,88
Ds 196,24,56,1024
W 5
Mavvc 96,168,64
E 25
Mavvc 100,168,72
C 19,22,23
Mavvc 104,168,80
Rv 164,198,29696
E 26
Mavvc 108,168,88
C 24,24,25
Mavvc 124,172,88
E 27
Mavvc 120,172,80
Mavvc 116,172,72
E 28
Ds 196,24,56,2048
Mavvc 112,172,64
Rv 168,198,30720
W 5
Mavvc 0,144,96
E 29
Mavvc 4,144,104
C 25,26,27
Mavvc 8,144,112
E 30
Mavvc 12,144,120
C 26,28,29
Mavvc 28,148,120
Rv 172,198,31744
E 31
Mavvc 24,148,112
Mvvvc 176,192,96
Ds 196,24,56,3072
Mavvc 20,148,104
E 32
Mavvc 16,148,96
W 4
AS 56,56,28672
Mavvc 32,152,96
E 33
Mavvc 36,152,104
Mvvvc 180,192,104
C 27,30,31
Mavvc 40,152,112
E 34
Mavvc 44,152,120
C 32,32,33
Mavvc 60,156,120
E 35
Mavvc 56,156,112
WV 16
B
Mavvc 52,156,104
Rv 144,197,16384
E 36
Mavvc 48,156,96
AM 59,16384
W 15
Mavvc 64,160,96
Rv 148,197,17408
E 37
Ds 196,28,57,0
Mavvc 68,160,104
Mvvvc 184,192,112
C 33,34,35
Mavvc 72,160,112
E 38
Mavvc 76,160,120
Rv 152,197,18432
C 34,36,37
Mavvc 92,164,120
E 39
Ds 196,28,57,1024
Mavvc 88,164,112
Mavvc 84,164,104
Rv 156,197,19456
E 40
Mavvc 80,164,96
Mvvvc 188,192,120
W 15
Mavvc 96,168,96
E 41
Ds 196,28,57,2048
Mavvc 100,168,104
Rv 160,197,20480
C 35,38,39
Mavvc 104,168,112
E 42
Mavvc 108,168,120
C 40,40,41
Mavvc 124,172,120
Rv 164,197,21504
E 43
Ds 196,28,57,3072
Mavvc 120,172,112
Mavvc 116,172,104
E 44
Mavvc 112,172,96
Rv 168,197,22528
W 5
s_bitcmp1_b32 s73, s60
s_cbranch_scc1 XAT1_B4%=
XAT1_B3%=:
AS 60,60,1
AT1_B3%=:
Mvvan 64,144,128,128
AS 57,57,4096
E 45
Mvvan 72,144,144,132
C 41,42,43
Mvvan 80,144,160,136
Rv 172,197,23552
E 46
Mvvan 88,144,176,140
C 42,44,45
AM 59,20480
Mvvac 88,148,180
E 47
Ds 196,28,57,0
Mvvac 80,148,164
Rv 144,197,24576
Mvvac 72,148,148
E 48
Mvvac 64,148,132
W 5
Mvvac 64,152,136
E 49
Mvvac 72,152,152
Rv 148,197,25600
C 43,46,47
Mvvac 80,152,168
E 50
Mvvac 88,152,184
C 48,48,49
Mvvac 88,156,188
E 51
Mvvac 80,156,172
Rv 152,197,26624
Ds 196,28,57,1024
Mvvac 72,156,156
E 52
Mvvac 64,156,140
W 5
Mvvan 68,160,128,128
E 53
Mvvan 76,160,144,132
Rv 156,197,27648
C 49,50,51
Mvvan 84,160,160,136
E 54
Mvvan 92,160,176,140
C 50,52,53
Mvvac 92,164,180
E 55
Mvvac 84,164,164
Rv 160,197,28672
Mvvac 76,164,148
E 56
Ds 196,28,57,2048
Mvvac 68,164,132
W 5
Mvvac 68,168,136
E 57
Mvvac 76,168,152
C 51,54,55
Mvvac 84,168,168
Rv 164,197,29696
E 58
Mvvac 92,168,184
C 56,56,57
Mvvac 92,172,188
E 59
Mvvac 84,172,172
Mvvac 76,172,156
E 60
Mvvac 68,172,140
Rv 168,197,30720
Ds 196,28,57,3072
W 5
Mvvan 96,144,128,128
E 61
Mvvan 104,144,144,132
C 57,58,59
AS 57,57,28672
Mvvan 112,144,160,136
E 62
Mvvan 120,144,176,140
C 58,60,61
Mvvac 120,148,180
Rv 172,197,31744
E 63
Mvvac 112,148,164
Mvvac 104,148,148
E 64
Mvvac 96,148,132
W 4
Mvvac 96,152,136
E 65
Mvvac 104,152,152
C 59,62,63
Mvvac 112,152,168
E 66
Mvvac 120,152,184
C 64,64,65
Mvvac 120,156,188
E 67
Mvvac 112,156,172
WV 16
B
Mvvac 104,156,156
Rv 144,198,0
E 68
Mvvac 96,156,140
AM 58,16384
W 15
Mvvan 100,160,128,128
Rv 148,198,1024
E 69
Ds 196,24,56,0
Mvvan 108,160,144,132
C 65,66,67
Mvvan 116,160,160,136
E 70
Mvvan 124,160,176,140
Rv 152,198,2048
C 66,68,69
Mvvac 124,164,180
E 71
Mvvac 116,164,164
Mvvac 108,164,148
Rv 156,198,3072
E 72
Mvvac 100,164,132
Ds 196,24,56,1024
W 15
Mvvac 100,168,136
E 73
Mvvac 108,168,152
Rv 160,198,4096
C 67,70,71
Mvvac 116,168,168
E 74
Mvvac 124,168,184
C 72,72,73
Mvvac 124,172,188
Rv 164,198,5120
E 75
Mvvac 116,172,172
Mvvac 108,172,156
E 76
Ds 196,24,56,2048
Mvvac 100,172,140
Rv 168,198,6144
W 5
Mavvc 0,144,0
E 77
Mavvc 4,144,8
C 73,74,75
Mavvc 8,144,16
Rv 172,198,7168
E 78
Mavvc 12,144,24
C 74,76,77
Mavvc 28,148,24
E 79
Mavvc 24,148,16
Rv 144,198,8192
Mvvvc 176,192,0
Ds 196,24,56,3072
Mavvc 20,148,8
E 80
Mavvc 16,148,0
W 5
AS 56,56,4096
Mavvc 32,152,0
E 81
Mavvc 36,152,8
Rv 148,198,9216
C 75,78,79
Mavvc 40,152,16
E 82
Mavvc 44,152,24
C 80,80,81
Mavvc 60,156,24
E 83
Mavvc 56,156,16
Rv 152,198,10240
Mvvvc 180,192,8
Mavvc 52,156,8
E 84
Mavvc 48,156,0
AM 58,20480
W 5
Mavvc 64,160,0
E 85
Ds 196,24,56,0
Mavvc 68,160,8
Rv 156,198,11264
Mvvvc 184,192,16
C 81,82,83
Mavvc 72,160,16
E 86
Mavvc 76,160,24
C 82,84,85
Mavvc 92,164,24
E 87
Mavvc 88,164,16
Rv 160,198,12288
Mavvc 84,164,8
E 88
Mavvc 80,164,0
Mvvvc 188,192,24
Ds 196,24,56,1024
W 5
Mavvc 96,168,0
E 89
Mavvc 100,168,8
C 83,86,87
Mavvc 104,168,16
Rv 164,198,13312
E 90
Mavvc 108,168,24
C 88,88,89
Mavvc 124,172,24
E 91
Mavvc 120,172,16
Mavvc 116,172,8
E 92
Ds 196,24,56,2048
Mavvc 112,172,0
Rv 168,198,14336
W 5
Mavvc 0,144,32
E 93
Mavvc 4,144,40
C 89,90,91
Mavvc 8,144,48
E 94
Mavvc 12,144,56
C 90,92,93
Mavvc 28,148,56
Rv 172,198,15360
E 95
Mavvc 24,148,48
Mvvvc 176,192,32
Ds 196,24,56,3072
Mavvc 20,148,40
E 96
Mavvc 16,148,32
W 4
AS 56,56,28672
Mavvc 32,152,32
E 97
Mavvc 36,152,40
Mvvvc 180,192,40
C 91,94,95
Mavvc 40,152,48
E 98
Mavvc 44,152,56
C 96,96,97
Mavvc 60,156,56
E 99
Mavvc 56,156,48
WV 16
B
Mavvc 52,156,40
Rv 144,197,0
E 100
Mavvc 48,156,32
AM 59,0
W 15
Mavvc 64,160,32
Rv 148,197,1024
E 101
Ds 196,28,57,0
Mavvc 68,160,40
Mvvvc 184,192,48
C 97,98,99
Mavvc 72,160,48
E 102
Mavvc 76,160,56
Rv 152,197,2048
C 98,100,101
Mavvc 92,164,56
E 103
Ds 196,28,57,1024
Mavvc 88,164,48
Mavvc 84,164,40
Rv 156,197,3072
E 104
Mavvc 80,164,32
Mvvvc 188,192,56
W 15
Mavvc 96,168,32
E 105
Ds 196,28,57,2048
Mavvc 100,168,40
Rv 160,197,4096
C 99,102,103
Mavvc 104,168,48
E 106
Mavvc 108,168,56
C 104,104,105
Mavvc 124,172,56
Rv 164,197,5120
E 107
Ds 196,28,57,3072
Mavvc 120,172,48
Mavvc 116,172,40
E 108
Mavvc 112,172,32
Rv 168,197,6144
W 5
s_branch XAT1_B2%=
XAT1_B4%=:
AS 60,60,1
AT1_B4%=:
Mvvan 64,144,128,128
AS 57,57,4096
E 45
Mvvan 72,144,144,132
C 41,42,43
Mvvan 80,144,160,136
Rv 172,197,23552
E 46
Mvvan 88,144,176,140
C 42,44,45
AM 59,20480
Mvvac 88,148,180
E 47
Ds 196,28,57,0
Mvvac 80,148,164
Rv 144,197,24576
Mvvac 72,148,148
E 48
Mvvac 64,148,132
W 5
Mvvac 64,152,136
E 49
Mvvac 72,152,152
Rv 148,197,25600
C 43,46,47
Mvvac 80,152,168
E 50
Mvvac 88,152,184
C 48,48,49
Mvvac 88,156,188
E 51
Mvvac 80,156,172
Rv 152,197,26624
Ds 196,28,57,1024
Mvvac 72,156,156
E 52
Mvvac 64,156,140
W 5
Mvvan 68,160,128,128
E 53
Mvvan 76,160,144,132
Rv 156,197,27648
C 49,50,51
Mvvan 84,160,160,136
E 54
Mvvan 92,160,176,140
C 50,52,53
Mvvac 92,164,180
E 55
Mvvac 84,164,164
Rv 160,197,28672
Mvvac 76,164,148
E 56
Ds 196,28,57,2048
Mvvac 68,164,132
W 5
Mvvac 68,168,136
E 57
Mvvac 76,168,152
C 51,54,55
Mvvac 84,168,168
Rv 164,197,29696
E 58
Mvvac 92,168,184
C 56,56,57
Mvvac 92,172,188
E 59
Mvvac 84,172,172
Mvvac 76,172,156
E 60
Mvvac 68,172,140
Rv 168,197,30720
Ds 196,28,57,3072
W 5
Mvvan 96,144,128,128
E 61
Mvvan 104,144,144,132
C 57,58,59
AS 57,57,28672
Mvvan 112,144,160,136
E 62
Mvvan 120,144,176,140
C 58,60,61
Mvvac 120,148,180
Rv 172,197,31744
E 63
Mvvac 112,148,164
Mvvac 104,148,148
E 64
Mvvac 96,148,132
W 4
Mvvac 96,152,136
E 65
Mvvac 104,152,152
C 59,62,63
Mvvac 112,152,168
E 66
Mvvac 120,152,184
C 64,64,65
Mvvac 120,156,188
E 67
Mvvac 112,156,172
WV 16
B
Mvvac 104,156,156
Rv 144,198,0
E 68
Mvvac 96,156,140
W 15
Mvvan 100,160,128,128
Rv 148,198,1024
E 69
Mvvan 108,160,144,132
C 65,66,67
Mvvan 116,160,160,136
)A"
R"A(E 70
Mvvan 124,160,176,140
Rv 152,198,2048
C 66,68,69
Mvvac 124,164,180
E 71
Mvvac 116,164,164
Mvvac 108,164,148
Rv 156,198,3072
E 72
Mvvac 100,164,132
W 15
Mvvac 100,168,136
E 73
Mvvac 108,168,152
Rv 160,198,4096
C 67,70,71
Mvvac 116,168,168
E 74
Mvvac 124,168,184
C 72,72,73
Mvvac 124,172,188
Rv 164,198,5120
E 75
Mvvac 116,172,172
Mvvac 108,172,156
E 76
Mvvac 100,172,140
Rv 168,198,6144
W 5
Mavvc 0,144,0
E 77
Mavvc 4,144,8
C 73,74,75
Mavvc 8,144,16
Rv 172,198,7168
E 78
Mavvc 12,144,24
C 74,76,77
Mavvc 28,148,24
E 79
Mavvc 24,148,16
Rv 144,198,8192
Mvvvc 176,192,0
Mavvc 20,148,8
E 80
Mavvc 16,148,0
W 5
Mavvc 32,152,0
E 81
Mavvc 36,152,8
Rv 148,198,9216
C 75,78,79
Mavvc 40,152,16
E 82
Mavvc 44,152,24
C 80,80,81
Mavvc 60,156,24
E 83
Mavvc 56,156,16
Rv 152,198,10240
Mvvvc 180,192,8
Mavvc 52,156,8
E 84
Mavvc 48,156,0
W 5
Mavvc 64,160,0
E 85
Mavvc 68,160,8
Rv 156,198,11264
Mvvvc 184,192,16
C 81,82,83
Mavvc 72,160,16
E 86
Mavvc 76,160,24
C 82,84,85
Mavvc 92,164,24
E 87
Mavvc 88,164,16
Rv 160,198,12288
Mavvc 84,164,8
E 88
Mavvc 80,164,0
Mvvvc 188,192,24
W 5
Mavvc 96,168,0
E 89
Mavvc 100,168,8
C 83,86,87
Mavvc 104,168,16
Rv 164,198,13312
E 90
Mavvc 108,168,24
C 88,88,89
Mavvc 124,172,24
E 91
Mavvc 120,172,16
Mavvc 116,172,8
E 92
Mavvc 112,172,0
Rv 168,198,14336
W 5
Mavvc 0,144,32
E 93
Mavvc 4,144,40
C 89,90,91
Mavvc 8,144,48
E 94
Mavvc 12,144,56
C 90,92,93
Mavvc 28,148,56
Rv 172,198,15360
E 95
Mavvc 24,148,48
Mvvvc 176,192,32
Mavvc 20,148,40
E 96
Mavvc 16,148,32
W 4
Mavvc 32,152,32
E 97
Mavvc 36,152,40
Mvvvc 180,192,40
C 91,94,95
Mavvc 40,152,48
E 98
Mavvc 44,152,56
C 96,96,97
Mavvc 60,156,56
E 99
Mavvc 56,156,48
WV 8
B
Mavvc 52,156,40
Rv 144,197,0
E 100
Mavvc 48,156,32
AM 59,0
W 15
Mavvc 64,160,32
Rv 148,197,1024
E 101
Ds 196,28,57,0
Mavvc 68,160,40
Mvvvc 184,192,48
C 97,98,99
Mavvc 72,160,48
E 102
Mavvc 76,160,56
Rv 152,197,2048
C 98,100,101
Mavvc 92,164,56
E 103
Ds 196,28,57,1024
Mavvc 88,164,48
Mavvc 84,164,40
Rv 156,197,3072
E 104
Mavvc 80,164,32
Mvvvc 188,192,56
W 15
Mavvc 96,168,32
E 105
Ds 196,28,57,2048
Mavvc 100,168,40
Rv 160,197,4096
C 99,102,103
Mavvc 104,168,48
E 106
Mavvc 108,168,56
C 104,104,105
Mavvc 124,172,56
Rv 164,197,5120
E 107
Ds 196,28,57,3072
Mavvc 120,172,48
Mavvc 116,172,40
E 108
Mavvc 112,172,32
Rv 168,197,6144
W 5
XAT1_B5%=:
AS 60,60,1
AT1_B5%=:
Mvvan 0,144,128,128
AS 57,57,4096
E 109
Mvvan 8,144,144,132
C 105,106,107
Mvvan 16,144,160,136
Rv 172,197,7168
E 110
Mvvan 24,144,176,140
C 106,108,109
AM 59,4096
Mvvac 24,148,180
E 111
Ds 196,28,57,0
Mvvac 16,148,164
Rv 144,197,8192
Mvvac 8,148,148
E 112
Mvvac 0,148,132
W 5
Mvvac 0,152,136
E 113
Mvvac 8,152,152
Rv 148,197,9216
C 107,110,111
Mvvac 16,152,168
E 114
Mvvac 24,152,184
C 112,112,113
Mvvac 24,156,188
E 115
Mvvac 16,156,172
Rv 152,197,10240
Ds 196,28,57,1024
Mvvac 8,156,156
E 116
Mvvac 0,156,140
W 5
Mvvan 4,160,128,128
E 117
Mvvan 12,160,144,132
Rv 156,197,11264
C 113,114,115
Mvvan 20,160,160,136
E 118
Mvvan 28,160,176,140
C 114,116,117
Mvvac 28,164,180
E 119
Mvvac 20,164,164
Rv 160,197,12288
Mvvac 12,164,148
E 120
Ds 196,28,57,2048
Mvvac 4,164,132
W 5
Mvvac 4,168,136
E 121
Mvvac 12,168,152
C 115,118,119
Mvvac 20,168,168
Rv 164,197,13312
E 122
Mvvac 28,168,184
C 120,120,121
Mvvac 28,172,188
E 123
Mvvac 20,172,172
Mvvac 12,172,156
E 124
Mvvac 4,172,140
Rv 168,197,14336
Ds 196,28,57,3072
W 5
Mvvan 32,144,128,128
E 125
Mvvan 40,144,144,132
C 121,122,123
AS 57,57,28672
Mvvan 48,144,160,136
E 126
Mvvan 56,144,176,140
C 122,124,125
Mvvac 56,148,180
Rv 172,197,15360
E 127
Mvvac 48,148,164
Mvvac 40,148,148
E 0
Mvvac 32,148,132
W 4
Mvvac 32,152,136
E 1
Mvvac 40,152,152
C 123,126,127
Mvvac 48,152,168
E 2
Mvvac 56,152,184
C 0,0,1
Mvvac 56,156,188
E 3
Mvvac 48,156,172
WV 8
B
Mvvac 40,156,156
Rv 144,198,16384
E 4
Mvvac 32,156,140
W 15
Mvvan 36,160,128,128
Rv 148,198,17408
E 5
Mvvan 44,160,144,132
C 1,2,3
Mvvan 52,160,160,136
E 6
Mvvan 60,160,176,140
Rv 152,198,18432
C 2,4,5
Mvvac 60,164,180
E 7
Mvvac 52,164,164
Mvvac 44,164,148
Rv 156,198,19456
E 8
Mvvac 36,164,132
W 15
Mvvac 36,168,136
E 9
Mvvac 44,168,152
Rv 160,198,20480
C 3,6,7
Mvvac 52,168,168
E 10
Mvvac 60,168,184
C 8,8,9
Mvvac 60,172,188
Rv 164,198,21504
E 11
Mvvac 52,172,172
Mvvac 44,172,156
E 12
Mvvac 36,172,140
Rv 168,198,22528
W 5
Mavvc 0,144,64
E 13
Mavvc 4,144,72
C 9,10,11
Mavvc 8,144,80
Rv 172,198,23552
E 14
Mavvc 12,144,88
C 10,12,13
Mavvc 28,148,88
E 15
Mavvc 24,148,80
Rv 144,198,24576
Mvvvc 176,192,64
Mavvc 20,148,72
E 16
Mavvc 16,148,64
W 5
Mavvc 32,152,64
E 17
Mavvc 36,152,72
Rv 148,198,25600
C 11,14,15
Mavvc 40,152,80
E 18
Mavvc 44,152,88
C 16,16,17
Mavvc 60,156,88
E 19
Mavvc 56,156,80
Rv 152,198,26624
Mvvvc 180,192,72
Mavvc 52,156,72
E 20
Mavvc 48,156,64
W 5
Mavvc 64,160,64
E 21
Mavvc 68,160,72
Rv 156,198,27648
Mvvvc 184,192,80
C 17,18,19
Mavvc 72,160,80
E 22
Mavvc 76,160,88
C 18,20,21
Mavvc 92,164,88
E 23
Mavvc 88,164,80
Rv 160,198,28672
Mavvc 84,164,72
E 24
Mavvc 80,164,64
Mvvvc 188,192,88
W 5
Mavvc 96,168,64
E 25
Mavvc 100,168,72
C 19,22,23
Mavvc 104,168,80
Rv 164,198,29696
E 26
Mavvc 108,168,88
C 24,24,25
Mavvc 124,172,88
E 27
Mavvc 120,172,80
Mavvc 116,172,72
E 28
Mavvc 112,172,64
Rv 168,198,30720
W 5
Mavvc 0,144,96
E 29
Mavvc 4,144,104
C 25,26,27
Mavvc 8,144,112
E 30
Mavvc 12,144,120
C 26,28,29
Mavvc 28,148,120
Rv 172,198,31744
E 31
Mavvc 24,148,112
Mvvvc 176,192,96
Mavvc 20,148,104
E 32
Mavvc 16,148,96
W 4
Mavvc 32,152,96
E 33
Mavvc 36,152,104
Mvvvc 180,192,104
C 27,30,31
Mavvc 40,152,112
E 34
Mavvc 44,152,120
C 32,32,33
Mavvc 60,156,120
E 35
Mavvc 56,156,112
W 0
B
Mavvc 52,156,104
E 36
Mavvc 48,156,96
W 15
Mavvc 64,160,96
E 37
Mavvc 68,160,104
Mvvvc 184,192,112
C 33,34,35
Mavvc 72,160,112
E 38
Mavvc 76,160,120
C 34,36,37
Mavvc 92,164,120
E 39
Mavvc 88,164,112
Mavvc 84,164,104
E 40
Mavvc 80,164,96
Mvvvc 188,192,120
W 15
Mavvc 96,168,96
E 41
Mavvc 100,168,104
C 35,38,39
Mavvc 104,168,112
E 42
Mavvc 108,168,120
C 40,40,41
Mavvc 124,172,120
E 43
Mavvc 120,172,112
Mavvc 116,172,104
E 44
Mavvc 112,172,96
XAT1_B6%=:
AS 60,60,1
AT1_B6%=:
E 45
C 41,42,43
E 46
C 42,44,45
E 47
E 48
E 49
C 43,46,47
E 50
C 48,48,49
E 51
E 52
E 53
C 49,50,51
E 54
C 50,52,53
E 55
E 56
E 57
C 51,54,55
E 58
C 56,56,57
E 59
E 60
E 61
C 57,58,59
E 62
C 58,60,61
E 63
N 0
C 59,62,63
WV 0
B
Rv 144,198,0
Rv 148,198,1024
Rv 152,198,2048
Rv 156,198,3072
Rv 160,198,4096
Rv 164,198,5120
Rv 168,198,6144
W 5
Mavvc 0,144,0
Mavvc 4,144,8
Mavvc 8,144,16
Rv 172,198,7168
Mvvvc 176,192,0
Mavvc 12,144,24
Mavvc 28,148,24
Mavvc 24,148,16
Rv 144,198,8192
Mavvc 20,148,8
Mavvc 16,148,0
W 5
Mavvc 32,152,0
Mavvc 36,152,8
Rv 148,198,9216
Mvvvc 180,192,8
Mavvc 40,152,16
Mavvc 44,152,24
Mavvc 60,156,24
Mavvc 56,156,16
Rv 152,198,10240
Mavvc 52,156,8
Mavvc 48,156,0
W 5
Mavvc 64,160,0
Mvvvc 184,192,16
Mavvc 68,160,8
Rv 156,198,11264
Mavvc 72,160,16
Mavvc 76,160,24
Mavvc 92,164,24
Mavvc 88,164,16
Rv 160,198,12288
Mavvc 84,164,8
Mavvc 80,164,0
W 5
Mavvc 96,168,0
Mvvvc 188,192,24
Mavvc 100,168,8
Mavvc 104,168,16
Rv 164,198,13312
Mavvc 108,168,24
Mavvc 124,172,24
Mavvc 120,172,16
Mavvc 116,172,8
Mavvc 112,172,0
Rv 168,198,14336
W 5
Mavvc 0,144,32
Mavvc 4,144,40
Mavvc 8,144,48
Mvvvc 176,192,32
Mavvc 12,144,56
Mavvc 28,148,56
Rv 172,198,15360
Mavvc 24,148,48
Mavvc 20,148,40
Mavvc 16,148,32
W 4
Mavvc 32,152,32
Mavvc 36,152,40
Mvvvc 180,192,40
Mavvc 40,152,48
Mavvc 44,152,56
Mavvc 60,156,56
Mavvc 56,156,48
W 0
B
Mavvc 52,156,40
Mavvc 48,156,32
W 15
Mavvc 64,160,32
Mvvvc 184,192,48
Mavvc 68,160,40
Mavvc 72,160,48
Mavvc 76,160,56
Mavvc 92,164,56
Mavvc 88,164,48
Mavvc 84,164,40
Mavvc 80,164,32
W 15
Mavvc 96,168,32
Mvvvc 188,192,56
Mavvc 100,168,40
Mavvc 104,168,48
Mavvc 108,168,56
Mavvc 124,172,56
Mavvc 120,172,48
Mavvc 116,172,40
Mavvc 112,172,32
s_branch XAT1_FIN%=
XAT1_B7%=:
AS 60,60,1
AT1_B7%=:
Mvvaz 0,144,128
Mvvaz 8,144,144
Mvvaz 16,144,160
Rv 172,197,7168
Mvvaz 24,144,176
Mvvac 24,148,180
Mvvac 16,148,164
Rv 144,197,8192
Mvvac 8,148,148
Mvvac 0,148,132
W 5
Mvvac 0,152,136
Mvvac 8,152,152
Rv 148,197,9216
Mvvac 16,152,168
Mvvac 24,152,184
Mvvac 24,156,188
Mvvac 16,156,172
Rv 152,197,10240
Mvvac 8,156,156
Mvvac 0,156,140
W 5
Mvvaz 4,160,128
Mvvaz 12,160,144
Rv 156,197,11264
Mvvaz 20,160,160
N 2
v_max3_f32 v200, v0, v1, v2
Fma 200,200,3
v_max3_f32 v202, v8, v9, v10
Fma 202,202,11
Mvvaz 28,160,176
v_max3_f32 v204, v16, v17, v18
Fma 204,204,19
v_max3_f32 v206, v24, v25, v26
Fma 206,206,27
Mvvac 28,164,180
MV 201,200
MV 203,202
MV 205,204
MV 207,206
Mvvac 20,164,164
Rv 160,197,12288
v_permlane16_swap_b32 v200, v201
v_permlane16_swap_b32 v202, v203
v_permlane16_swap_b32 v204, v205
Mvvac 12,164,148
v_permlane16_swap_b32 v206, v207
Fma 200,200,201
Fma 202,202,203
Fma 204,204,205
Mvvac 4,164,132
Fma 206,206,207
MV 201,200
MV 203,202
MV 205,204
W 5
Mvvac 4,168,136
MV 207,206
v_permlane32_swap_b32 v200, v201
v_permlane32_swap_b32 v202, v203
Mvvac 12,168,152
v_permlane32_swap_b32 v204, v205
v_permlane32_swap_b32 v206, v207
Fma 200,200,201
Fma 202,202,203
Mvvac 20,168,168
Rv 164,197,13312
Fma 204,204,205
Fma 206,206,207
v_sub_f32 v128, 0, v200
v_sub_f32 v129, 0, v200
Mvvac 28,168,184
v_sub_f32 v130, 0, v200
v_sub_f32 v131, 0, v200
v_sub_f32 v132, 0, v202
Mvvac 28,172,188
v_sub_f32 v133, 0, v202
v_sub_f32 v134, 0, v202
v_sub_f32 v135, 0, v202
v_sub_f32 v136, 0, v204
Mvvac 20,172,172
v_sub_f32 v137, 0, v204
v_sub_f32 v138, 0, v204
v_sub_f32 v139, 0, v204
v_sub_f32 v140, 0, v206
Mvvac 12,172,156
v_sub_f32 v141, 0, v206
v_sub_f32 v142, 0, v206
v_sub_f32 v143, 0, v206
Mvvac 4,172,140
Rv 168,197,14336
W 5
Mvvan 32,144,128,128
Mvvan 40,144,144,132
Mvvan 48,144,160,136
Mvvan 56,144,176,140
Mvvac 56,148,180
Rv 172,197,15360
Mvvac 48,148,164
Fad 0,0,128
Mvvac 40,148,148
E 0
Mvvac 32,148,132
Fad 1,1,128
W 4
Mvvac 32,152,136
E 1
Mvvac 40,152,152
Fad 2,2,128
Mvvac 48,152,168
E 2
Mvvac 56,152,184
C 0,0,1
Fad 3,3,128
Mvvac 56,156,188
E 3
Mvvac 48,156,172
Fad 4,4,128
W 0
B
Mvvac 40,156,156
E 4
Mvvac 32,156,140
Fad 5,5,128
W 15
Mvvan 36,160,128,128
E 5
Mvvan 44,160,144,132
C 1,2,3
Fad 6,6,128
AM 58,0
Mvvan 52,160,160,136
E 6
Ds 196,24,56,0
Mvvan 60,160,176,140
C 2,4,5
Fad 7,7,128
Mvvac 60,164,180
E 7
Mvvac 52,164,164
Fad 8,8,132
Mvvac 44,164,148
E 8
Mvvac 36,164,132
Fad 9,9,132
W 15
Mvvac 36,168,136
E 9
Mvvac 44,168,152
C 3,6,7
Fad 10,10,132
Mvvac 52,168,168
E 10
Mvvac 60,168,184
C 8,8,9
Fad 11,11,132
Mvvac 60,172,188
E 11
Mvvac 52,172,172
Fad 12,12,132
Mvvac 44,172,156
E 12
Mvvac 36,172,140
Fad 13,13,132
E 13
Ds 196,24,56,1024
C 9,10,11
Fad 14,14,132
E 14
C 10,12,13
Fad 15,15,132
E 15
Fad 16,16,136
E 16
Fad 17,17,136
E 17
C 11,14,15
Fad 18,18,136
E 18
C 16,16,17
Fad 19,19,136
E 19
Fad 20,20,136
E 20
Ds 196,24,56,2048
Fad 21,21,136
E 21
C 17,18,19
Fad 22,22,136
E 22
C 18,20,21
Fad 23,23,136
E 23
Fad 24,24,140
E 24
Fad 25,25,140
E 25
C 19,22,23
Fad 26,26,140
E 26
C 24,24,25
Fad 27,27,140
E 27
Ds 196,24,56,3072
Fad 28,28,140
E 28
AS 56,56,16384
Fad 29,29,140
E 29
C 25,26,27
Fad 30,30,140
E 30
Fad 31,31,140
E 31
C 26,28,29
E 32
E 33
C 27,30,31
E 34
C 32,32,33
E 35
W 0
B
Rv 144,197,16384
E 36
AM 59,16384
Rv 148,197,17408
E 37
Ds 196,28,57,0
C 33,34,35
E 38
Rv 152,197,18432
C 34,36,37
E 39
Ds 196,28,57,1024
Rv 156,197,19456
E 40
E 41
Ds 196,28,57,2048
Rv 160,197,20480
C 35,38,39
E 42
C 40,40,41
Rv 164,197,21504
E 43
Ds 196,28,57,3072
E 44
AS 57,57,16384
Rv 168,197,22528
W 5
XAT1_B8%=:
AS 60,60,1
AT1_B8%=:
Mvvan 64,144,128,128
E 45
Mvvan 72,144,144,132
C 41,42,43
Mvvan 80,144,160,136
Rv 172,197,23552
E 46
Mvvan 88,144,176,140
C 42,44,45
Mvvac 88,148,180
E 47
Mvvac 80,148,164
Rv 144,197,24576
Mvvac 72,148,148
E 48
Mvvac 64,148,132
W 5
Mvvac 64,152,136
E 49
Mvvac 72,152,152
Rv 148,197,25600
C 43,46,47
Mvvac 80,152,168
E 50
Mvvac 88,152,184
C 48,48,49
Mvvac 88,156,188
E 51
Mvvac 80,156,172
Rv 152,197,26624
Mvvac 72,156,156
E 52
Mvvac 64,156,140
W 5
Mvvan 68,160,128,128
E 53
Mvvan 76,160,144,132
Rv 156,197,27648
C 49,50,51
Mvvan 84,160,160,136
E 54
Mvvan 92,160,176,140
C 50,52,53
Mvvac 92,164,180
E 55
Mvvac 84,164,164
Rv 160,197,28672
Mvvac 76,164,148
E 56
Mvvac 68,164,132
W 5
Mvvac 68,168,136
E 57
Mvvac 76,168,152
C 51,54,55
Mvvac 84,168,168
)A"
R"A(Rv 164,197,29696
E 58
Mvvac 92,168,184
C 56,56,57
Mvvac 92,172,188
E 59
Mvvac 84,172,172
Mvvac 76,172,156
E 60
Mvvac 68,172,140
Rv 168,197,30720
W 5
Mvvan 96,144,128,128
E 61
Mvvan 104,144,144,132
C 57,58,59
Mvvan 112,144,160,136
E 62
Mvvan 120,144,176,140
C 58,60,61
Mvvac 120,148,180
Rv 172,197,31744
E 63
Mvvac 112,148,164
Mvvac 104,148,148
E 64
Mvvac 96,148,132
W 4
Mvvac 96,152,136
E 65
Mvvac 104,152,152
C 59,62,63
Mvvac 112,152,168
E 66
Mvvac 120,152,184
C 64,64,65
Mvvac 120,156,188
E 67
Mvvac 112,156,172
WV 8
B
Mvvac 104,156,156
Rv 144,198,0
E 68
Mvvac 96,156,140
W 15
Mvvan 100,160,128,128
Rv 148,198,1024
E 69
Mvvan 108,160,144,132
C 65,66,67
AM 58,16384
Mvvan 116,160,160,136
E 70
Ds 196,24,56,0
Mvvan 124,160,176,140
Rv 152,198,2048
C 66,68,69
Mvvac 124,164,180
E 71
Mvvac 116,164,164
Mvvac 108,164,148
Rv 156,198,3072
E 72
Mvvac 100,164,132
W 15
Mvvac 100,168,136
E 73
Mvvac 108,168,152
Rv 160,198,4096
C 67,70,71
Mvvac 116,168,168
E 74
Mvvac 124,168,184
C 72,72,73
Mvvac 124,172,188
Rv 164,198,5120
E 75
Mvvac 116,172,172
Mvvac 108,172,156
E 76
Mvvac 100,172,140
Rv 168,198,6144
W 5
Mavvz 0,144,0
E 77
Ds 196,24,56,1024
Mavvz 4,144,8
C 73,74,75
Mavvz 8,144,16
Rv 172,198,7168
E 78
Mavvz 12,144,24
C 74,76,77
Mavvz 28,148,24
E 79
Mavvz 24,148,16
Rv 144,198,8192
Mvvvz 176,192,0
Mavvz 20,148,8
E 80
Mavvz 16,148,0
W 5
Mavvz 32,152,0
E 81
Mavvz 36,152,8
Rv 148,198,9216
C 75,78,79
Mavvz 40,152,16
E 82
Mavvz 44,152,24
C 80,80,81
Mavvz 60,156,24
E 83
Mavvz 56,156,16
Rv 152,198,10240
Mvvvz 180,192,8
Mavvz 52,156,8
E 84
Ds 196,24,56,2048
Mavvz 48,156,0
W 5
Mavvz 64,160,0
E 85
Mavvz 68,160,8
Rv 156,198,11264
Mvvvz 184,192,16
C 81,82,83
Mavvz 72,160,16
E 86
Mavvz 76,160,24
C 82,84,85
Mavvz 92,164,24
E 87
Mavvz 88,164,16
Rv 160,198,12288
Mavvz 84,164,8
E 88
Mavvz 80,164,0
Mvvvz 188,192,24
W 5
Mavvz 96,168,0
E 89
Mavvz 100,168,8
C 83,86,87
Mavvz 104,168,16
Rv 164,198,13312
E 90
Mavvz 108,168,24
C 88,88,89
Mavvz 124,172,24
E 91
Ds 196,24,56,3072
Mavvz 120,172,16
Mavvz 116,172,8
E 92
AS 56,56,16384
Mavvz 112,172,0
Rv 168,198,14336
W 5
Mavvc 0,144,32
E 93
Mavvc 4,144,40
C 89,90,91
Mavvc 8,144,48
E 94
Mavvc 12,144,56
C 90,92,93
Mavvc 28,148,56
Rv 172,198,15360
E 95
Mavvc 24,148,48
Mvvvc 176,192,32
Mavvc 20,148,40
E 96
Mavvc 16,148,32
W 4
Mavvc 32,152,32
E 97
Mavvc 36,152,40
Mvvvc 180,192,40
C 91,94,95
Mavvc 40,152,48
E 98
Mavvc 44,152,56
C 96,96,97
Mavvc 60,156,56
E 99
Mavvc 56,156,48
WV 8
B
Mavvc 52,156,40
Rv 144,197,0
E 100
Mavvc 48,156,32
AM 59,0
W 15
Mavvc 64,160,32
Rv 148,197,1024
E 101
Ds 196,28,57,0
Mavvc 68,160,40
Mvvvc 184,192,48
C 97,98,99
Mavvc 72,160,48
E 102
Mavvc 76,160,56
Rv 152,197,2048
C 98,100,101
Mavvc 92,164,56
E 103
Ds 196,28,57,1024
Mavvc 88,164,48
Mavvc 84,164,40
Rv 156,197,3072
E 104
Mavvc 80,164,32
Mvvvc 188,192,56
W 15
Mavvc 96,168,32
E 105
Ds 196,28,57,2048
Mavvc 100,168,40
Rv 160,197,4096
C 99,102,103
Mavvc 104,168,48
E 106
Mavvc 108,168,56
C 104,104,105
Mavvc 124,172,56
Rv 164,197,5120
E 107
Ds 196,28,57,3072
Mavvc 120,172,48
Mavvc 116,172,40
E 108
AS 57,57,16384
Mavvc 112,172,32
Rv 168,197,6144
W 5
s_bitcmp1_b32 s74, s60
s_cbranch_scc1 XAT1_B9%=
s_branch XAT1_B11%=
XAT1_B9%=:
AS 60,60,1
AT1_B9%=:
Mvvan 0,144,128,128
E 109
Mvvan 8,144,144,132
C 105,106,107
Mvvan 16,144,160,136
Rv 172,197,7168
E 110
Mvvan 24,144,176,140
C 106,108,109
Mvvac 24,148,180
E 111
Mvvac 16,148,164
Rv 144,197,8192
Mvvac 8,148,148
E 112
Mvvac 0,148,132
W 5
Mvvac 0,152,136
E 113
Mvvac 8,152,152
Rv 148,197,9216
C 107,110,111
Mvvac 16,152,168
E 114
Mvvac 24,152,184
C 112,112,113
Mvvac 24,156,188
E 115
Mvvac 16,156,172
Rv 152,197,10240
Mvvac 8,156,156
E 116
Mvvac 0,156,140
W 5
Mvvan 4,160,128,128
E 117
Mvvan 12,160,144,132
Rv 156,197,11264
C 113,114,115
Mvvan 20,160,160,136
E 118
Mvvan 28,160,176,140
C 114,116,117
Mvvac 28,164,180
E 119
Mvvac 20,164,164
Rv 160,197,12288
Mvvac 12,164,148
E 120
Mvvac 4,164,132
W 5
Mvvac 4,168,136
E 121
Mvvac 12,168,152
C 115,118,119
Mvvac 20,168,168
Rv 164,197,13312
E 122
Mvvac 28,168,184
C 120,120,121
Mvvac 28,172,188
E 123
Mvvac 20,172,172
Mvvac 12,172,156
E 124
Mvvac 4,172,140
Rv 168,197,14336
W 5
Mvvan 32,144,128,128
E 125
Mvvan 40,144,144,132
C 121,122,123
Mvvan 48,144,160,136
E 126
Mvvan 56,144,176,140
C 122,124,125
Mvvac 56,148,180
Rv 172,197,15360
E 127
Mvvac 48,148,164
Mvvac 40,148,148
E 0
Mvvac 32,148,132
W 4
Mvvac 32,152,136
E 1
Mvvac 40,152,152
C 123,126,127
Mvvac 48,152,168
E 2
Mvvac 56,152,184
C 0,0,1
Mvvac 56,156,188
E 3
AS 56,56,4096
SM 58,8192
Mvvac 48,156,172
WV 8
B
Mvvac 40,156,156
Rv 144,198,16384
E 4
s_mul_i32 s21, s10, 68
Mvvac 32,156,140
AM 58,0
W 15
AS 21,21,0
Mvvan 36,160,128,128
Rv 148,198,17408
E 5
Ds 196,24,56,0
v_min_u32 v249, 63, v254
Mvvan 44,160,144,132
C 1,2,3
v_add_u32 v249, s21, v249
Mvvan 52,160,160,136
E 6
v_min_u32 v249, 17279, v249
Mvvan 60,160,176,140
Rv 152,198,18432
C 2,4,5
v_lshlrev_b32 v249, 7, v249
Mvvac 60,164,180
E 7
global_load_dword v248, v249, s[36:37]
Mvvac 52,164,164
s_mul_i32 s21, s10, 68
Mvvac 44,164,148
Rv 156,198,19456
E 8
AS 21,21,64
Mvvac 36,164,132
Ds 196,24,56,1024
W 15
v_min_u32 v249, 3, v254
Mvvac 36,168,136
E 9
v_add_u32 v249, s21, v249
Mvvac 44,168,152
Rv 160,198,20480
C 3,6,7
v_min_u32 v249, 17279, v249
Mvvac 52,168,168
E 10
v_lshlrev_b32 v249, 7, v249
Mvvac 60,168,184
C 8,8,9
global_load_dword v248, v249, s[36:37]
Mvvac 60,172,188
Rv 164,198,21504
E 11
Mvvac 52,172,172
Mvvac 44,172,156
E 12
Ds 196,24,56,2048
Mvvac 36,172,140
Rv 168,198,22528
W 5
Mavvc 0,144,64
E 13
Mavvc 4,144,72
C 9,10,11
Mavvc 8,144,80
Rv 172,198,23552
E 14
Mavvc 12,144,88
C 10,12,13
Mavvc 28,148,88
E 15
Mavvc 24,148,80
Rv 144,198,24576
Mvvvc 176,192,64
Ds 196,24,56,3072
Mavvc 20,148,72
E 16
Mavvc 16,148,64
W 5
AS 56,56,4096
Mavvc 32,152,64
E 17
Mavvc 36,152,72
Rv 148,198,25600
C 11,14,15
Mavvc 40,152,80
E 18
Mavvc 44,152,88
C 16,16,17
Mavvc 60,156,88
E 19
Mavvc 56,156,80
Rv 152,198,26624
Mvvvc 180,192,72
Mavvc 52,156,72
E 20
Mavvc 48,156,64
AM 58,4096
W 5
Mavvc 64,160,64
E 21
Ds 196,24,56,0
Mavvc 68,160,72
Rv 156,198,27648
Mvvvc 184,192,80
C 17,18,19
Mavvc 72,160,80
E 22
Mavvc 76,160,88
C 18,20,21
Mavvc 92,164,88
E 23
Mavvc 88,164,80
Rv 160,198,28672
Mavvc 84,164,72
E 24
Mavvc 80,164,64
Mvvvc 188,192,88
Ds 196,24,56,1024
W 5
Mavvc 96,168,64
E 25
Mavvc 100,168,72
C 19,22,23
Mavvc 104,168,80
Rv 164,198,29696
E 26
Mavvc 108,168,88
C 24,24,25
Mavvc 124,172,88
E 27
Mavvc 120,172,80
Mavvc 116,172,72
E 28
Ds 196,24,56,2048
Mavvc 112,172,64
Rv 168,198,30720
W 5
Mavvc 0,144,96
E 29
Mavvc 4,144,104
C 25,26,27
Mavvc 8,144,112
E 30
Mavvc 12,144,120
C 26,28,29
Mavvc 28,148,120
Rv 172,198,31744
E 31
Mavvc 24,148,112
Mvvvc 176,192,96
Ds 196,24,56,3072
Mavvc 20,148,104
E 32
Mavvc 16,148,96
W 4
AS 56,56,28672
Mavvc 32,152,96
E 33
Mavvc 36,152,104
Mvvvc 180,192,104
C 27,30,31
Mavvc 40,152,112
E 34
Mavvc 44,152,120
C 32,32,33
Mavvc 60,156,120
E 35
Mavvc 56,156,112
WV 14
B
Mavvc 52,156,104
Rv 144,197,16384
E 36
Mavvc 48,156,96
AM 59,16384
W 15
Mavvc 64,160,96
Rv 148,197,17408
E 37
Ds 196,28,57,0
Mavvc 68,160,104
Mvvvc 184,192,112
C 33,34,35
Mavvc 72,160,112
E 38
Mavvc 76,160,120
Rv 152,197,18432
C 34,36,37
Mavvc 92,164,120
E 39
Ds 196,28,57,1024
Mavvc 88,164,112
Mavvc 84,164,104
Rv 156,197,19456
E 40
Mavvc 80,164,96
Mvvvc 188,192,120
W 15
Mavvc 96,168,96
E 41
Ds 196,28,57,2048
Mavvc 100,168,104
Rv 160,197,20480
C 35,38,39
Mavvc 104,168,112
E 42
Mavvc 108,168,120
C 40,40,41
Mavvc 124,172,120
Rv 164,197,21504
E 43
Ds 196,28,57,3072
Mavvc 120,172,112
Mavvc 116,172,104
E 44
AS 57,57,16384
Mavvc 112,172,96
Rv 168,197,22528
W 5
s_bitcmp1_b32 s75, s60
s_cbranch_scc1 XAT1_B13%=
XAT1_B10%=:
AS 60,60,1
AT1_B10%=:
Mvvan 64,144,128,128
E 45
Mvvan 72,144,144,132
C 41,42,43
Mvvan 80,144,160,136
Rv 172,197,23552
E 46
Mvvan 88,144,176,140
C 42,44,45
Mvvac 88,148,180
E 47
Mvvac 80,148,164
Rv 144,197,24576
Mvvac 72,148,148
E 48
Mvvac 64,148,132
W 5
Mvvac 64,152,136
E 49
Mvvac 72,152,152
Rv 148,197,25600
C 43,46,47
Mvvac 80,152,168
E 50
Mvvac 88,152,184
C 48,48,49
Mvvac 88,156,188
E 51
Mvvac 80,156,172
Rv 152,197,26624
Mvvac 72,156,156
E 52
Mvvac 64,156,140
W 5
Mvvan 68,160,128,128
E 53
Mvvan 76,160,144,132
Rv 156,197,27648
C 49,50,51
Mvvan 84,160,160,136
E 54
Mvvan 92,160,176,140
C 50,52,53
Mvvac 92,164,180
E 55
Mvvac 84,164,164
Rv 160,197,28672
Mvvac 76,164,148
E 56
Mvvac 68,164,132
W 5
Mvvac 68,168,136
E 57
Mvvac 76,168,152
C 51,54,55
Mvvac 84,168,168
Rv 164,197,29696
E 58
Mvvac 92,168,184
C 56,56,57
Mvvac 92,172,188
E 59
Mvvac 84,172,172
Mvvac 76,172,156
E 60
Mvvac 68,172,140
Rv 168,197,30720
W 5
Mvvan 96,144,128,128
E 61
Mvvan 104,144,144,132
C 57,58,59
Mvvan 112,144,160,136
E 62
Mvvan 120,144,176,140
C 58,60,61
Mvvac 120,148,180
Rv 172,197,31744
E 63
Mvvac 112,148,164
Mvvac 104,148,148
E 64
Mvvac 96,148,132
W 4
Mvvac 96,152,136
E 65
Mvvac 104,152,152
C 59,62,63
Mvvac 112,152,168
E 66
Mvvac 120,152,184
C 64,64,65
Mvvac 120,156,188
E 67
Mvvac 112,156,172
WV 14
B
Mvvac 104,156,156
Rv 144,198,0
E 68
s_mul_i32 s21, s10, 68
Mvvac 96,156,140
AM 58,16384
W 15
AS 21,21,0
Mvvan 100,160,128,128
Rv 148,198,1024
E 69
Ds 196,24,56,0
v_min_u32 v249, 63, v254
Mvvan 108,160,144,132
C 65,66,67
v_add_u32 v249, s21, v249
Mvvan 116,160,160,136
E 70
v_min_u32 v249, 17279, v249
Mvvan 124,160,176,140
Rv 152,198,2048
C 66,68,69
v_lshlrev_b32 v249, 7, v249
Mvvac 124,164,180
E 71
global_load_dword v248, v249, s[38:39]
Mvvac 116,164,164
s_mul_i32 s21, s10, 68
Mvvac 108,164,148
Rv 156,198,3072
E 72
AS 21,21,64
Mvvac 100,164,132
Ds 196,24,56,1024
W 15
v_min_u32 v249, 3, v254
Mvvac 100,168,136
E 73
v_add_u32 v249, s21, v249
Mvvac 108,168,152
Rv 160,198,4096
C 67,70,71
v_min_u32 v249, 17279, v249
Mvvac 116,168,168
E 74
v_lshlrev_b32 v249, 7, v249
Mvvac 124,168,184
C 72,72,73
global_load_dword v248, v249, s[38:39]
Mvvac 124,172,188
Rv 164,198,5120
E 75
Mvvac 116,172,172
Mvvac 108,172,156
E 76
Ds 196,24,56,2048
Mvvac 100,172,140
Rv 168,198,6144
W 5
Mavvc 0,144,0
E 77
Mavvc 4,144,8
C 73,74,75
Mavvc 8,144,16
Rv 172,198,7168
E 78
Mavvc 12,144,24
C 74,76,77
Mavvc 28,148,24
E 79
Mavvc 24,148,16
Rv 144,198,8192
Mvvvc 176,192,0
Ds 196,24,56,3072
Mavvc 20,148,8
E 80
Mavvc 16,148,0
W 5
AS 56,56,4096
Mavvc 32,152,0
E 81
Mavvc 36,152,8
Rv 148,198,9216
C 75,78,79
Mavvc 40,152,16
E 82
Mavvc 44,152,24
C 80,80,81
Mavvc 60,156,24
E 83
Mavvc 56,156,16
Rv 152,198,10240
Mvvvc 180,192,8
Mavvc 52,156,8
E 84
Mavvc 48,156,0
AM 58,20480
W 5
Mavvc 64,160,0
E 85
Ds 196,24,56,0
Mavvc 68,160,8
Rv 156,198,11264
Mvvvc 184,192,16
C 81,82,83
Mavvc 72,160,16
E 86
Mavvc 76,160,24
C 82,84,85
Mavvc 92,164,24
E 87
Mavvc 88,164,16
Rv 160,198,12288
Mavvc 84,164,8
E 88
Mavvc 80,164,0
Mvvvc 188,192,24
Ds 196,24,56,1024
W 5
Mavvc 96,168,0
E 89
Mavvc 100,168,8
C 83,86,87
Mavvc 104,168,16
Rv 164,198,13312
E 90
Mavvc 108,168,24
C 88,88,89
Mavvc 124,172,24
E 91
Mavvc 120,172,16
Mavvc 116,172,8
E 92
Ds 196,24,56,2048
Mavvc 112,172,0
Rv 168,198,14336
W 5
Mavvc 0,144,32
E 93
Mavvc 4,144,40
C 89,90,91
Mavvc 8,144,48
E 94
Mavvc 12,144,56
C 90,92,93
Mavvc 28,148,56
Rv 172,198,15360
E 95
Mavvc 24,148,48
Mvvvc 176,192,32
Ds 196,24,56,3072
Mavvc 20,148,40
E 96
Mavvc 16,148,32
W 4
AS 56,56,28672
Mavvc 32,152,32
E 97
Mavvc 36,152,40
Mvvvc 180,192,40
C 91,94,95
Mavvc 40,152,48
E 98
Mavvc 44,152,56
C 96,96,97
Mavvc 60,156,56
E 99
Mavvc 56,156,48
WV 14
B
AS 57,57,4096
SM 59,73728
Mavvc 52,156,40
Rv 144,197,0
E 100
Mavvc 48,156,32
AM 59,0
W 15
Mavvc 64,160,32
Rv 148,197,1024
E 101
Ds 196,28,57,0
Mavvc 68,160,40
Mvvvc 184,192,48
C 97,98,99
Mavvc 72,160,48
E 102
Mavvc 76,160,56
Rv 152,197,2048
C 98,100,101
Mavvc 92,164,56
E 103
Ds 196,28,57,1024
Mavvc 88,164,48
Mavvc 84,164,40
Rv 156,197,3072
E 104
Mavvc 80,164,32
Mvvvc 188,192,56
W 15
Mavvc 96,168,32
E 105
Ds 196,28,57,2048
Mavvc 100,168,40
Rv 160,197,4096
C 99,102,103
Mavvc 104,168,48
E 106
Mavvc 108,168,56
C 104,104,105
Mavvc 124,172,56
Rv 164,197,5120
E 107
Ds 196,28,57,3072
Mavvc 120,172,48
Mavvc 116,172,40
E 108
Mavvc 112,172,32
Rv 168,197,6144
W 5
s_branch XAT1_B2%=
XAT1_B11%=:
AS 60,60,1
AT1_B11%=:
Mvvan 0,144,128,128
E 109
Mvvan 8,144,144,132
C 105,106,107
Mvvan 16,144,160,136
Rv 172,197,7168
E 110
)A"
R"A(Mvvan 24,144,176,140
C 106,108,109
Mvvac 24,148,180
E 111
Mvvac 16,148,164
Rv 144,197,8192
Mvvac 8,148,148
E 112
Mvvac 0,148,132
W 5
Mvvac 0,152,136
E 113
Mvvac 8,152,152
Rv 148,197,9216
C 107,110,111
Mvvac 16,152,168
E 114
Mvvac 24,152,184
C 112,112,113
Mvvac 24,156,188
E 115
Mvvac 16,156,172
Rv 152,197,10240
Mvvac 8,156,156
E 116
Mvvac 0,156,140
W 5
Mvvan 4,160,128,128
E 117
Mvvan 12,160,144,132
Rv 156,197,11264
C 113,114,115
Mvvan 20,160,160,136
E 118
Mvvan 28,160,176,140
C 114,116,117
Mvvac 28,164,180
E 119
Mvvac 20,164,164
Rv 160,197,12288
Mvvac 12,164,148
E 120
Mvvac 4,164,132
W 5
Mvvac 4,168,136
E 121
Mvvac 12,168,152
C 115,118,119
Mvvac 20,168,168
Rv 164,197,13312
E 122
Mvvac 28,168,184
C 120,120,121
Mvvac 28,172,188
E 123
Mvvac 20,172,172
Mvvac 12,172,156
E 124
Mvvac 4,172,140
Rv 168,197,14336
W 5
Mvvan 32,144,128,128
E 125
Mvvan 40,144,144,132
C 121,122,123
Mvvan 48,144,160,136
E 126
Mvvan 56,144,176,140
C 122,124,125
Mvvac 56,148,180
Rv 172,197,15360
E 127
Mvvac 48,148,164
Mvvac 40,148,148
E 0
Mvvac 32,148,132
W 4
Mvvac 32,152,136
E 1
Mvvac 40,152,152
C 123,126,127
Mvvac 48,152,168
E 2
Mvvac 56,152,184
C 0,0,1
Mvvac 56,156,188
E 3
Mvvac 48,156,172
WV 8
B
Mvvac 40,156,156
Rv 144,198,16384
E 4
Mvvac 32,156,140
W 15
Mvvan 36,160,128,128
Rv 148,198,17408
E 5
Mvvan 44,160,144,132
C 1,2,3
AM 58,0
Mvvan 52,160,160,136
E 6
Ds 196,24,56,0
Mvvan 60,160,176,140
Rv 152,198,18432
C 2,4,5
Mvvac 60,164,180
E 7
Mvvac 52,164,164
Mvvac 44,164,148
Rv 156,198,19456
E 8
Mvvac 36,164,132
W 15
Mvvac 36,168,136
E 9
Mvvac 44,168,152
Rv 160,198,20480
C 3,6,7
Mvvac 52,168,168
E 10
Mvvac 60,168,184
C 8,8,9
Mvvac 60,172,188
Rv 164,198,21504
E 11
Mvvac 52,172,172
Mvvac 44,172,156
E 12
Mvvac 36,172,140
Rv 168,198,22528
W 5
Mavvc 0,144,64
E 13
Ds 196,24,56,1024
Mavvc 4,144,72
C 9,10,11
Mavvc 8,144,80
Rv 172,198,23552
E 14
Mavvc 12,144,88
C 10,12,13
Mavvc 28,148,88
E 15
Mavvc 24,148,80
Rv 144,198,24576
Mvvvc 176,192,64
Mavvc 20,148,72
E 16
Mavvc 16,148,64
W 5
Mavvc 32,152,64
E 17
Mavvc 36,152,72
Rv 148,198,25600
C 11,14,15
Mavvc 40,152,80
E 18
Mavvc 44,152,88
C 16,16,17
Mavvc 60,156,88
E 19
Mavvc 56,156,80
Rv 152,198,26624
Mvvvc 180,192,72
Mavvc 52,156,72
E 20
Ds 196,24,56,2048
Mavvc 48,156,64
W 5
Mavvc 64,160,64
E 21
Mavvc 68,160,72
Rv 156,198,27648
Mvvvc 184,192,80
C 17,18,19
Mavvc 72,160,80
E 22
Mavvc 76,160,88
C 18,20,21
Mavvc 92,164,88
E 23
Mavvc 88,164,80
Rv 160,198,28672
Mavvc 84,164,72
E 24
Mavvc 80,164,64
Mvvvc 188,192,88
W 5
Mavvc 96,168,64
E 25
Mavvc 100,168,72
C 19,22,23
Mavvc 104,168,80
Rv 164,198,29696
E 26
Mavvc 108,168,88
C 24,24,25
Mavvc 124,172,88
E 27
Ds 196,24,56,3072
Mavvc 120,172,80
Mavvc 116,172,72
E 28
AS 56,56,16384
Mavvc 112,172,64
Rv 168,198,30720
W 5
Mavvc 0,144,96
E 29
Mavvc 4,144,104
C 25,26,27
Mavvc 8,144,112
E 30
Mavvc 12,144,120
C 26,28,29
Mavvc 28,148,120
Rv 172,198,31744
E 31
Mavvc 24,148,112
Mvvvc 176,192,96
Mavvc 20,148,104
E 32
Mavvc 16,148,96
W 4
Mavvc 32,152,96
E 33
Mavvc 36,152,104
Mvvvc 180,192,104
C 27,30,31
Mavvc 40,152,112
E 34
Mavvc 44,152,120
C 32,32,33
Mavvc 60,156,120
E 35
Mavvc 56,156,112
WV 8
B
Mavvc 52,156,104
Rv 144,197,16384
E 36
Mavvc 48,156,96
AM 59,16384
W 15
Mavvc 64,160,96
Rv 148,197,17408
E 37
Ds 196,28,57,0
Mavvc 68,160,104
Mvvvc 184,192,112
C 33,34,35
Mavvc 72,160,112
E 38
Mavvc 76,160,120
Rv 152,197,18432
C 34,36,37
Mavvc 92,164,120
E 39
Ds 196,28,57,1024
Mavvc 88,164,112
Mavvc 84,164,104
Rv 156,197,19456
E 40
Mavvc 80,164,96
Mvvvc 188,192,120
W 15
Mavvc 96,168,96
E 41
Ds 196,28,57,2048
Mavvc 100,168,104
Rv 160,197,20480
C 35,38,39
Mavvc 104,168,112
E 42
Mavvc 108,168,120
C 40,40,41
Mavvc 124,172,120
Rv 164,197,21504
E 43
Ds 196,28,57,3072
Mavvc 120,172,112
Mavvc 116,172,104
E 44
AS 57,57,16384
Mavvc 112,172,96
Rv 168,197,22528
W 5
XAT1_B12%=:
AS 60,60,1
AT1_B12%=:
Mvvan 64,144,128,128
E 45
Mvvan 72,144,144,132
C 41,42,43
Mvvan 80,144,160,136
Rv 172,197,23552
E 46
Mvvan 88,144,176,140
C 42,44,45
Mvvac 88,148,180
E 47
Mvvac 80,148,164
Rv 144,197,24576
Mvvac 72,148,148
E 48
Mvvac 64,148,132
W 5
Mvvac 64,152,136
E 49
Mvvac 72,152,152
Rv 148,197,25600
C 43,46,47
Mvvac 80,152,168
E 50
Mvvac 88,152,184
C 48,48,49
Mvvac 88,156,188
E 51
Mvvac 80,156,172
Rv 152,197,26624
Mvvac 72,156,156
E 52
Mvvac 64,156,140
W 5
Mvvan 68,160,128,128
E 53
Mvvan 76,160,144,132
Rv 156,197,27648
C 49,50,51
Mvvan 84,160,160,136
E 54
Mvvan 92,160,176,140
C 50,52,53
Mvvac 92,164,180
E 55
Mvvac 84,164,164
Rv 160,197,28672
Mvvac 76,164,148
E 56
Mvvac 68,164,132
W 5
Mvvac 68,168,136
E 57
Mvvac 76,168,152
C 51,54,55
Mvvac 84,168,168
Rv 164,197,29696
E 58
Mvvac 92,168,184
C 56,56,57
Mvvac 92,172,188
E 59
Mvvac 84,172,172
Mvvac 76,172,156
E 60
Mvvac 68,172,140
Rv 168,197,30720
W 5
Mvvan 96,144,128,128
E 61
Mvvan 104,144,144,132
C 57,58,59
Mvvan 112,144,160,136
E 62
Mvvan 120,144,176,140
C 58,60,61
Mvvac 120,148,180
Rv 172,197,31744
E 63
Mvvac 112,148,164
Mvvac 104,148,148
E 64
Mvvac 96,148,132
W 4
Mvvac 96,152,136
E 65
Mvvac 104,152,152
C 59,62,63
Mvvac 112,152,168
E 66
Mvvac 120,152,184
C 64,64,65
Mvvac 120,156,188
E 67
Mvvac 112,156,172
WV 8
B
Mvvac 104,156,156
Rv 144,198,0
E 68
Mvvac 96,156,140
W 15
Mvvan 100,160,128,128
Rv 148,198,1024
E 69
Mvvan 108,160,144,132
C 65,66,67
AM 58,16384
Mvvan 116,160,160,136
E 70
Ds 196,24,56,0
Mvvan 124,160,176,140
Rv 152,198,2048
C 66,68,69
Mvvac 124,164,180
E 71
Mvvac 116,164,164
Mvvac 108,164,148
Rv 156,198,3072
E 72
Mvvac 100,164,132
W 15
Mvvac 100,168,136
E 73
Mvvac 108,168,152
Rv 160,198,4096
C 67,70,71
Mvvac 116,168,168
E 74
Mvvac 124,168,184
C 72,72,73
Mvvac 124,172,188
Rv 164,198,5120
E 75
Mvvac 116,172,172
Mvvac 108,172,156
E 76
Mvvac 100,172,140
Rv 168,198,6144
W 5
Mavvc 0,144,0
E 77
Ds 196,24,56,1024
Mavvc 4,144,8
C 73,74,75
Mavvc 8,144,16
Rv 172,198,7168
E 78
Mavvc 12,144,24
C 74,76,77
Mavvc 28,148,24
E 79
Mavvc 24,148,16
Rv 144,198,8192
Mvvvc 176,192,0
Mavvc 20,148,8
E 80
Mavvc 16,148,0
W 5
Mavvc 32,152,0
E 81
Mavvc 36,152,8
Rv 148,198,9216
C 75,78,79
Mavvc 40,152,16
E 82
Mavvc 44,152,24
C 80,80,81
Mavvc 60,156,24
E 83
Mavvc 56,156,16
Rv 152,198,10240
Mvvvc 180,192,8
Mavvc 52,156,8
E 84
Ds 196,24,56,2048
Mavvc 48,156,0
W 5
Mavvc 64,160,0
E 85
Mavvc 68,160,8
Rv 156,198,11264
Mvvvc 184,192,16
C 81,82,83
Mavvc 72,160,16
E 86
Mavvc 76,160,24
C 82,84,85
Mavvc 92,164,24
E 87
Mavvc 88,164,16
Rv 160,198,12288
Mavvc 84,164,8
E 88
Mavvc 80,164,0
Mvvvc 188,192,24
W 5
Mavvc 96,168,0
E 89
Mavvc 100,168,8
C 83,86,87
Mavvc 104,168,16
Rv 164,198,13312
E 90
Mavvc 108,168,24
C 88,88,89
Mavvc 124,172,24
E 91
Ds 196,24,56,3072
Mavvc 120,172,16
Mavvc 116,172,8
E 92
AS 56,56,16384
Mavvc 112,172,0
Rv 168,198,14336
W 5
Mavvc 0,144,32
E 93
Mavvc 4,144,40
C 89,90,91
Mavvc 8,144,48
E 94
Mavvc 12,144,56
C 90,92,93
Mavvc 28,148,56
Rv 172,198,15360
E 95
Mavvc 24,148,48
Mvvvc 176,192,32
Mavvc 20,148,40
E 96
Mavvc 16,148,32
W 4
Mavvc 32,152,32
E 97
Mavvc 36,152,40
Mvvvc 180,192,40
C 91,94,95
Mavvc 40,152,48
E 98
Mavvc 44,152,56
C 96,96,97
Mavvc 60,156,56
E 99
Mavvc 56,156,48
WV 8
B
Mavvc 52,156,40
Rv 144,197,0
E 100
Mavvc 48,156,32
AM 59,0
W 15
Mavvc 64,160,32
Rv 148,197,1024
E 101
Ds 196,28,57,0
Mavvc 68,160,40
Mvvvc 184,192,48
C 97,98,99
Mavvc 72,160,48
E 102
Mavvc 76,160,56
Rv 152,197,2048
C 98,100,101
Mavvc 92,164,56
E 103
Ds 196,28,57,1024
Mavvc 88,164,48
Mavvc 84,164,40
Rv 156,197,3072
E 104
Mavvc 80,164,32
Mvvvc 188,192,56
W 15
Mavvc 96,168,32
E 105
Ds 196,28,57,2048
Mavvc 100,168,40
Rv 160,197,4096
C 99,102,103
Mavvc 104,168,48
E 106
Mavvc 108,168,56
C 104,104,105
Mavvc 124,172,56
Rv 164,197,5120
E 107
Ds 196,28,57,3072
Mavvc 120,172,48
Mavvc 116,172,40
E 108
AS 57,57,16384
Mavvc 112,172,32
Rv 168,197,6144
W 5
s_bitcmp1_b32 s76, s60
s_cbranch_scc1 XAT1_B9%=
s_branch XAT1_B11%=
XAT1_B13%=:
AS 60,60,1
AT1_B13%=:
Mvvan 64,144,128,128
E 45
Mvvan 72,144,144,132
C 41,42,43
Mvvan 80,144,160,136
Rv 172,197,23552
E 46
Mvvan 88,144,176,140
C 42,44,45
Mvvac 88,148,180
E 47
Mvvac 80,148,164
Rv 144,197,24576
Mvvac 72,148,148
E 48
Mvvac 64,148,132
W 5
Mvvac 64,152,136
E 49
Mvvac 72,152,152
Rv 148,197,25600
C 43,46,47
Mvvac 80,152,168
E 50
Mvvac 88,152,184
C 48,48,49
Mvvac 88,156,188
E 51
Mvvac 80,156,172
Rv 152,197,26624
Mvvac 72,156,156
E 52
Mvvac 64,156,140
W 5
Mvvan 68,160,128,128
E 53
Mvvan 76,160,144,132
Rv 156,197,27648
C 49,50,51
Mvvan 84,160,160,136
E 54
Mvvan 92,160,176,140
C 50,52,53
Mvvac 92,164,180
E 55
Mvvac 84,164,164
Rv 160,197,28672
Mvvac 76,164,148
E 56
Mvvac 68,164,132
W 5
Mvvac 68,168,136
E 57
Mvvac 76,168,152
C 51,54,55
Mvvac 84,168,168
Rv 164,197,29696
E 58
Mvvac 92,168,184
C 56,56,57
Mvvac 92,172,188
E 59
Mvvac 84,172,172
Mvvac 76,172,156
E 60
Mvvac 68,172,140
Rv 168,197,30720
W 5
Mvvan 96,144,128,128
E 61
Mvvan 104,144,144,132
C 57,58,59
Mvvan 112,144,160,136
E 62
Mvvan 120,144,176,140
C 58,60,61
Mvvac 120,148,180
Rv 172,197,31744
E 63
Mvvac 112,148,164
Mvvac 104,148,148
E 64
Mvvac 96,148,132
W 4
Mvvac 96,152,136
E 65
Mvvac 104,152,152
C 59,62,63
Mvvac 112,152,168
E 66
Mvvac 120,152,184
C 64,64,65
Mvvac 120,156,188
E 67
Mvvac 112,156,172
WV 14
B
Mvvac 104,156,156
Rv 144,198,0
E 68
s_mul_i32 s21, s10, 68
Mvvac 96,156,140
W 15
AS 21,21,0
Mvvan 100,160,128,128
Rv 148,198,1024
E 69
v_min_u32 v249, 63, v254
Mvvan 108,160,144,132
C 65,66,67
v_add_u32 v249, s21, v249
Mvvan 116,160,160,136
E 70
v_min_u32 v249, 17279, v249
Mvvan 124,160,176,140
Rv 152,198,2048
C 66,68,69
v_lshlrev_b32 v249, 7, v249
Mvvac 124,164,180
E 71
global_load_dword v248, v249, s[38:39]
Mvvac 116,164,164
s_mul_i32 s21, s10, 68
Mvvac 108,164,148
Rv 156,198,3072
E 72
AS 21,21,64
Mvvac 100,164,132
W 15
v_min_u32 v249, 3, v254
Mvvac 100,168,136
E 73
v_add_u32 v249, s21, v249
Mvvac 108,168,152
Rv 160,198,4096
C 67,70,71
v_min_u32 v249, 17279, v249
Mvvac 116,168,168
E 74
v_lshlrev_b32 v249, 7, v249
Mvvac 124,168,184
C 72,72,73
global_load_dword v248, v249, s[38:39]
Mvvac 124,172,188
Rv 164,198,5120
E 75
Mvvac 116,172,172
Mvvac 108,172,156
E 76
Mvvac 100,172,140
Rv 168,198,6144
W 5
Mavvc 0,144,0
E 77
Mavvc 4,144,8
C 73,74,75
Mavvc 8,144,16
Rv 172,198,7168
E 78
Mavvc 12,144,24
C 74,76,77
Mavvc 28,148,24
E 79
Mavvc 24,148,16
Rv 144,198,8192
Mvvvc 176,192,0
Mavvc 20,148,8
E 80
Mavvc 16,148,0
W 5
Mavvc 32,152,0
E 81
Mavvc 36,152,8
Rv 148,198,9216
C 75,78,79
Mavvc 40,152,16
E 82
Mavvc 44,152,24
C 80,80,81
Mavvc 60,156,24
E 83
Mavvc 56,156,16
Rv 152,198,10240
Mvvvc 180,192,8
Mavvc 52,156,8
E 84
Mavvc 48,156,0
W 5
Mavvc 64,160,0
E 85
Mavvc 68,160,8
Rv 156,198,11264
Mvvvc 184,192,16
C 81,82,83
Mavvc 72,160,16
E 86
Mavvc 76,160,24
C 82,84,85
Mavvc 92,164,24
E 87
Mavvc 88,164,16
Rv 160,198,12288
Mavvc 84,164,8
E 88
Mavvc 80,164,0
Mvvvc 188,192,24
W 5
Mavvc 96,168,0
E 89
Mavvc 100,168,8
C 83,86,87
Mavvc 104,168,16
Rv 164,198,13312
E 90
Mavvc 108,168,24
C 88,88,89
Mavvc 124,172,24
E 91
Mavvc 120,172,16
Mavvc 116,172,8
E 92
Mavvc 112,172,0
Rv 168,198,14336
W 5
Mavvc 0,144,32
E 93
Mavvc 4,144,40
C 89,90,91
Mavvc 8,144,48
E 94
Mavvc 12,144,56
C 90,92,93
Mavvc 28,148,56
Rv 172,198,15360
E 95
Mavvc 24,148,48
Mvvvc 176,192,32
Mavvc 20,148,40
E 96
Mavvc 16,148,32
W 4
Mavvc 32,152,32
E 97
Mavvc 36,152,40
Mvvvc 180,192,40
C 91,94,95
Mavvc 40,152,48
E 98
Mavvc 44,152,56
C 96,96,97
Mavvc 60,156,56
E 99
Mavvc 56,156,48
WV 6
B
AS 57,57,4096
SM 59,73728
Mavvc 52,156,40
Rv 144,197,0
E 100
Mavvc 48,156,32
AM 59,0
W 15
Mavvc 64,160,32
Rv 148,197,1024
E 101
Ds 196,28,57,0
Mavvc 68,160,40
Mvvvc 184,192,48
C 97,98,99
Mavvc 72,160,48
E 102
Mavvc 76,160,56
Rv 152,197,2048
C 98,100,101
Mavvc 92,164,56
E 103
Ds 196,28,57,1024
Mavvc 88,164,48
Mavvc 84,164,40
Rv 156,197,3072
E 104
Mavvc 80,164,32
Mvvvc 188,192,56
W 15
Mavvc 96,168,32
E 105
Ds 196,28,57,2048
Mavvc 100,168,40
Rv 160,197,4096
C 99,102,103
Mavvc 104,168,48
E 106
Mavvc 108,168,56
C 104,104,105
Mavvc 124,172,56
Rv 164,197,5120
E 107
Ds 196,28,57,3072
Mavvc 120,172,48
Mavvc 116,172,40
E 108
Mavvc 112,172,32
Rv 168,197,6144
W 5
s_branch XAT1_B5%=
XAT1_FIN%=:
AS 60,60,1
AT1_FIN%=:
N 7
WV 0
B
VA 200,16384,196
DWa 200,64,0
DWa 200,68,1024
DWa 200,72,2048
DWa 200,76,3072
DWa 200,80,4096
DWa 200,84,5120
DWa 200,88,6144
DWa 200,92,7168
DWa 200,96,8192
DWa 200,100,9216
)A"
R"A(DWa 200,104,10240
DWa 200,108,11264
DWa 200,112,12288
DWa 200,116,13312
DWa 200,120,14336
DWa 200,124,15360
v_mov_b32 v231, 0x5d800000
v_cmp_nle_f32 vcc, v176, v231
N 3
s_or_b64 s[70:71], s[70:71], vcc
v_cmp_nle_f32 vcc, v180, v231
N 3
s_or_b64 s[70:71], s[70:71], vcc
v_cmp_nle_f32 vcc, v184, v231
N 3
s_or_b64 s[70:71], s[70:71], vcc
v_cmp_nle_f32 vcc, v188, v231
N 3
s_or_b64 s[70:71], s[70:71], vcc
s_cmp_lg_u64 s[70:71], 0
s_cselect_b32 s16, 1, 0
v_mov_b32 v202, s16
v_mov_b32 v203, 149508
ds_write_b32 v203, v202
MV 204,128
MV 208,176
MV 205,132
MV 209,180
MV 206,136
MV 210,184
MV 207,140
MV 211,188
VA 201,157696,196
ds_write_b128 v201, v[204:207]
DWv 201,208,1024
s_sub_u32 s16, 15, s13
s_lshr_b32 s17, s16, 1
s_lshl_b32 s17, s17, 21
s_and_b32 s16, s16, 1
s_lshl_b32 s16, s16, 13
s_add_u32 s17, s17, s16
s_lshl_b32 s16, s12, 16
s_add_u32 s17, s17, s16
AS 72,17,4096
W 0
B
VA 201,161792,196
ds_read_b128 v[212:215], v201
Rv 216,201,1024
v_mov_b32 v203, 149504
ds_read_b128 v[64:67], v203
ds_read_b64 v[68:69], v203 offset:16
VA 200,49152,196
Rv 0,200,0
Rv 4,200,1024
Rv 8,200,2048
Rv 12,200,3072
Rv 16,200,4096
Rv 20,200,5120
Rv 24,200,6144
Rv 28,200,7168
Rv 32,200,8192
Rv 36,200,9216
Rv 40,200,10240
Rv 44,200,11264
Rv 48,200,12288
Rv 52,200,13312
Rv 56,200,14336
Rv 60,200,15360
v_mov_b32 v231, 0x5d800000
W 15
v_min_f32 v201, v204, v212
Fsu 204,201,204
Fsu 212,201,212
E 204
E 212
Fmu 202,208,204
W 15
Ffm 202,216,212
v_rcp_f32 v202, v202
N 0
Fmu 220,204,202
Fmu 222,212,202
v_min_f32 v201, v205, v213
Fsu 205,201,205
Fsu 213,201,213
E 205
E 213
Fmu 202,209,205
Ffm 202,217,213
v_rcp_f32 v202, v202
N 0
Fmu 230,205,202
Fmu 224,213,202
v_min_f32 v201, v206, v214
Fsu 206,201,206
Fsu 214,201,214
E 206
E 214
Fmu 202,210,206
Ffm 202,218,214
v_rcp_f32 v202, v202
N 0
Fmu 128,206,202
Fmu 226,214,202
v_min_f32 v201, v207, v215
Fsu 207,201,207
Fsu 215,201,215
E 207
E 215
Fmu 202,211,207
Ffm 202,219,215
v_rcp_f32 v202, v202
N 0
Fmu 130,207,202
Fmu 228,215,202
W 15
v_or_b32 v64, v64, v65
v_or_b32 v66, v66, v67
v_or_b32 v64, v64, v66
W 15
v_or_b32 v68, v68, v69
v_or_b32 v65, v64, v68
N 0
v_readfirstlane_b32 s16, v65
v_readfirstlane_b32 s17, v64
N 3
s_cmp_lg_u32 s16, 0
s_cbranch_scc1 GOSLOW1%=
FASTM1%=:
G 64,0
G 68,16
G 65,1
G 69,17
G 66,2
G 70,18
G 67,3
G 71,19
PM 64,64,220
PM 66,66,220
PM 68,68,220
PM 70,70,220
W 15
v_pk_fma_f32 v[64:65], v[0:1], v[222:223], v[64:65] op_sel_hi:[1,0,1]
W 11
v_pk_fma_f32 v[68:69], v[16:17], v[222:223], v[68:69] op_sel_hi:[1,0,1]
v_pk_fma_f32 v[66:67], v[2:3], v[222:223], v[66:67] op_sel_hi:[1,0,1]
v_pk_fma_f32 v[70:71], v[18:19], v[222:223], v[70:71] op_sel_hi:[1,0,1]
C 72,64,65
C 73,66,67
C 74,68,69
C 75,70,71
Svs 72,196,32,72,0
G 76,4
G 80,20
G 77,5
G 81,21
G 78,6
G 82,22
G 79,7
G 83,23
PM 76,76,230
PM 78,78,230
PM 80,80,230
PM 82,82,230
v_pk_fma_f32 v[76:77], v[4:5], v[224:225], v[76:77] op_sel_hi:[1,0,1]
W 10
v_pk_fma_f32 v[80:81], v[20:21], v[224:225], v[80:81] op_sel_hi:[1,0,1]
v_pk_fma_f32 v[78:79], v[6:7], v[224:225], v[78:79] op_sel_hi:[1,0,1]
v_pk_fma_f32 v[82:83], v[22:23], v[224:225], v[82:83] op_sel_hi:[1,0,1]
C 84,76,77
C 85,78,79
C 86,80,81
C 87,82,83
Svs 84,196,32,72,1024
G 88,8
G 92,24
G 89,9
G 93,25
G 90,10
G 94,26
G 91,11
G 95,27
PM 88,88,128
PM 90,90,128
PM 92,92,128
PM 94,94,128
v_pk_fma_f32 v[88:89], v[8:9], v[226:227], v[88:89] op_sel_hi:[1,0,1]
W 9
v_pk_fma_f32 v[92:93], v[24:25], v[226:227], v[92:93] op_sel_hi:[1,0,1]
v_pk_fma_f32 v[90:91], v[10:11], v[226:227], v[90:91] op_sel_hi:[1,0,1]
v_pk_fma_f32 v[94:95], v[26:27], v[226:227], v[94:95] op_sel_hi:[1,0,1]
C 96,88,89
C 97,90,91
C 98,92,93
C 99,94,95
Svs 96,196,32,72,2048
G 100,12
G 104,28
G 101,13
G 105,29
G 102,14
G 106,30
G 103,15
G 107,31
PM 100,100,130
PM 102,102,130
PM 104,104,130
PM 106,106,130
v_pk_fma_f32 v[100:101], v[12:13], v[228:229], v[100:101] op_sel_hi:[1,0,1]
W 8
v_pk_fma_f32 v[104:105], v[28:29], v[228:229], v[104:105] op_sel_hi:[1,0,1]
v_pk_fma_f32 v[102:103], v[14:15], v[228:229], v[102:103] op_sel_hi:[1,0,1]
v_pk_fma_f32 v[106:107], v[30:31], v[228:229], v[106:107] op_sel_hi:[1,0,1]
C 108,100,101
C 109,102,103
C 110,104,105
C 111,106,107
Svs 108,196,32,72,3072
AS 72,72,16384
G 64,32
G 68,48
G 65,33
G 69,49
G 66,34
G 70,50
G 67,35
G 71,51
PM 64,64,220
PM 66,66,220
PM 68,68,220
PM 70,70,220
W 7
v_pk_fma_f32 v[64:65], v[32:33], v[222:223], v[64:65] op_sel_hi:[1,0,1]
W 3
v_pk_fma_f32 v[68:69], v[48:49], v[222:223], v[68:69] op_sel_hi:[1,0,1]
v_pk_fma_f32 v[66:67], v[34:35], v[222:223], v[66:67] op_sel_hi:[1,0,1]
v_pk_fma_f32 v[70:71], v[50:51], v[222:223], v[70:71] op_sel_hi:[1,0,1]
C 72,64,65
C 73,66,67
C 74,68,69
C 75,70,71
Svs 72,196,32,72,0
G 76,36
G 80,52
G 77,37
G 81,53
G 78,38
G 82,54
G 79,39
G 83,55
PM 76,76,230
PM 78,78,230
PM 80,80,230
PM 82,82,230
v_pk_fma_f32 v[76:77], v[36:37], v[224:225], v[76:77] op_sel_hi:[1,0,1]
W 2
v_pk_fma_f32 v[80:81], v[52:53], v[224:225], v[80:81] op_sel_hi:[1,0,1]
v_pk_fma_f32 v[78:79], v[38:39], v[224:225], v[78:79] op_sel_hi:[1,0,1]
v_pk_fma_f32 v[82:83], v[54:55], v[224:225], v[82:83] op_sel_hi:[1,0,1]
C 84,76,77
C 85,78,79
C 86,80,81
C 87,82,83
Svs 84,196,32,72,1024
G 88,40
G 92,56
G 89,41
G 93,57
G 90,42
G 94,58
G 91,43
G 95,59
PM 88,88,128
PM 90,90,128
PM 92,92,128
PM 94,94,128
v_pk_fma_f32 v[88:89], v[40:41], v[226:227], v[88:89] op_sel_hi:[1,0,1]
W 1
v_pk_fma_f32 v[92:93], v[56:57], v[226:227], v[92:93] op_sel_hi:[1,0,1]
v_pk_fma_f32 v[90:91], v[42:43], v[226:227], v[90:91] op_sel_hi:[1,0,1]
v_pk_fma_f32 v[94:95], v[58:59], v[226:227], v[94:95] op_sel_hi:[1,0,1]
C 96,88,89
C 97,90,91
C 98,92,93
C 99,94,95
Svs 96,196,32,72,2048
G 100,44
G 104,60
G 101,45
G 105,61
G 102,46
G 106,62
G 103,47
G 107,63
PM 100,100,130
PM 102,102,130
PM 104,104,130
PM 106,106,130
v_pk_fma_f32 v[100:101], v[44:45], v[228:229], v[100:101] op_sel_hi:[1,0,1]
W 0
v_pk_fma_f32 v[104:105], v[60:61], v[228:229], v[104:105] op_sel_hi:[1,0,1]
v_pk_fma_f32 v[102:103], v[46:47], v[228:229], v[102:103] op_sel_hi:[1,0,1]
v_pk_fma_f32 v[106:107], v[62:63], v[228:229], v[106:107] op_sel_hi:[1,0,1]
C 108,100,101
C 109,102,103
C 110,104,105
C 111,106,107
Svs 108,196,32,72,3072
s_getpc_b64 s[16:17]
1:
s_add_u32 s16, s16, AE1%=-1b
s_addc_u32 s17, s17, 0
s_lshr_b32 s21, s10, 3
v_lshlrev_b32 v249, 5, v254
v_add_u32 v249, s21, v249
v_min_u32 v249, ((OE1%=-AE1%=)>>7), v249
v_lshlrev_b32 v249, 7, v249
global_load_dword v248, v249, s[16:17]
s_and_b32 s16, s10, 7
s_lshr_b32 s41, s16, 1
s_and_b32 s16, s16, 1
s_lshl_b32 s16, s16, 3
s_lshr_b32 s17, s10, 3
s_lshr_b32 s18, s17, 2
s_add_u32 s39, s16, s18
s_and_b32 s40, s17, 3
s_lshl_b32 s16, s41, 2
s_add_u32 s43, s16, s40
s_mov_b64 s[24:25], s[50:51]
SM 26,0x500000
SM 27,0x20000
s_lshl_b32 s16, s41, 10
s_lshl_b32 s17, s11, 5
s_add_u32 s16, s16, s17
s_mul_i32 s16, s16, 1280
s_mul_i32 s17, s40, 320
s_add_u32 s36, s16, s17
AE1%=:
s_waitcnt vmcnt(0)
MDONE1%=:
s_sub_u32 s23, 15, s13
s_lshl_b32 s23, s23, 2
s_lshr_b32 s22, s12, 3
s_add_u32 s23, s23, s22
s_and_b32 s19, s12, 7
s_lshl_b32 s19, s19, 2
AS 19,19,1
s_lshl_b32 s21, s23, 8
AS 21,21,32768
s_lshl_b32 s22, s19, 2
s_add_u32 s21, s21, s22
v_mov_b32 v247, s21
v_mov_b32 v246, s79
s_mov_b64 exec, 1
global_store_dword v247, v246, s[14:15] sc1
s_mov_b64 exec, -1
N 3
AS 42,36,0
M0 75776
N 0
D 232,24,42,0
D 233,24,42,1024
D 234,24,42,2048
D 235,24,42,3072
M0 79872
N 0
D 236,24,42,0
AS 42,36,20480
M0 80896
N 0
D 232,24,42,0
D 233,24,42,1024
D 234,24,42,2048
D 235,24,42,3072
M0 84992
N 0
D 236,24,42,0
s_add_u32 s28, s14, 0x2400000
s_addc_u32 s29, s15, 0
SM 30,0x1000000
SM 31,0x20000
s_add_u32 s32, s14, 0x3400000
s_addc_u32 s33, s15, 0
SM 34,0xa00000
SM 35,0x20000
v_lshrrev_b32 v117, 4, v254
v_and_b32 v118, 15, v254
v_lshrrev_b32 v118, 2, v118
v_lshl_add_u32 v118, v117, 2, v118
v_mul_u32_u24 v118, 320, v118
v_and_b32 v119, 3, v254
v_lshl_add_u32 v118, v119, 3, v118
v_and_b32 v117, 1, v117
v_lshlrev_b32 v117, 5, v117
v_add_u32 v119, v118, v117
v_sub_u32 v120, v118, v117
VA 122,65536,119
VA 123,65536,120
VA 124,106496,119
VA 125,106496,120
VA 126,0,119
VA 127,0,120
s_lshr_b32 s16, s39, 1
s_lshl_b32 s16, s16, 21
s_lshl_b32 s17, s41, 19
s_add_u32 s16, s16, s17
s_and_b32 s17, s39, 1
s_lshl_b32 s17, s17, 13
s_add_u32 s16, s16, s17
s_lshl_b32 s17, s11, 11
s_add_u32 s37, s16, s17
s_lshl_b32 s16, s41, 4
s_add_u32 s16, s16, s39
s_lshl_b32 s16, s16, 3
s_lshl_b32 s17, s11, 1
s_add_u32 s16, s16, s17
s_mul_i32 s16, s16, 20480
s_mul_i32 s17, s40, 5120
s_add_u32 s38, s16, s17
s_load_dwordx2 s[44:45], s[8:9], 0
SM 46,0x280000
SM 47,0x20000
s_load_dwordx2 s[48:49], s[8:9], 48
SM 50,0x500
SM 51,0x20000
s_load_dwordx2 s[52:53], s[8:9], 80
SM 54,0x280000
SM 55,0x20000
v_lshrrev_b32 v104, 5, v254
v_lshlrev_b32 v104, 5, v104
v_lshrrev_b32 v105, 3, v254
v_and_b32 v105, 3, v105
v_lshl_add_u32 v101, v105, 3, v104
v_and_b32 v106, 7, v254
v_mul_u32_u24 v106, 1280, v106
v_add_u32 v100, v106, v101
s_lshl_b32 s16, s39, 7
s_lshl_b32 s17, s43, 3
s_add_u32 s16, s16, s17
s_mov_b32 s66, s16
s_mul_i32 s16, s16, 1280
s_mul_i32 s17, s11, 320
s_add_u32 s65, s16, s17
s_mov_b32 s67, s17
v_lshrrev_b32 v104, 5, v254
v_mul_u32_u24 v104, 81920, v104
v_lshlrev_b32 v104, 6, v104
v_and_b32 v106, 7, v254
v_lshl_add_u32 v106, v105, 4, v106
s_and_b32 s16, s43, 1
s_lshl_b32 s16, s16, 3
v_add_u32 v106, s16, v106
v_lshl_add_u32 v102, v106, 4, v104
s_lshl_b32 s16, s39, 3
s_lshr_b32 s17, s43, 1
s_add_u32 s16, s16, s17
s_mul_i32 s16, s16, 20480
s_mul_i32 s17, s11, 5120
s_add_u32 s64, s16, s17
W 0
s_load_dwordx2 s[70:71], s[8:9], 56
s_load_dwordx2 s[76:77], s[8:9], 64
s_load_dwordx2 s[72:73], s[8:9], 72
SM 74,0x21c000
SM 75,0x20000
W 0
B
s_lshr_b32 s78, s10, 1
s_and_b32 s16, s10, 1
s_cselect_b32 s16, 54, s78
s_cmp_lt_u32 s16, 54
s_cbranch_scc0 OMN1%=
s_mov_b64 s[56:57], s[70:71]
SM 58,0x21c000
SM 59,0x20000
s_mov_b64 s[60:61], s[76:77]
SM 62,0x21c000
SM 63,0x20000
v_lshrrev_b32 v99, 2, v254
v_mul_u32_u24 v99, 3456, v99
v_and_b32 v98, 3, v254
v_lshl_add_u32 v99, v98, 4, v99
s_lshl_b32 s22, s78, 6
AS 22,22,552960
AS 21,37,0
Las 80,196,28,21,0
Las 84,196,28,21,1024
AS 21,37,16384
Las 88,196,28,21,0
Las 92,196,28,21,1024
AS 21,37,32768
Las 96,196,28,21,0
Las 100,196,28,21,1024
AS 21,37,49152
Las 104,196,28,21,0
Las 108,196,28,21,1024
AS 21,37,65536
Las 112,196,28,21,0
Las 116,196,28,21,1024
AS 21,37,81920
Las 120,196,28,21,0
Las 124,196,28,21,1024
AS 42,36,163840
M0 116736
N 0
D 232,24,42,0
D 233,24,42,1024
D 234,24,42,2048
D 235,24,42,3072
M0 120832
N 0
D 236,24,42,0
AS 42,36,184320
M0 121856
N 0
D 232,24,42,0
D 233,24,42,1024
D 234,24,42,2048
D 235,24,42,3072
M0 125952
N 0
D 236,24,42,0
WV 22
B
T 0,122,0
T 2,122,5120
T 4,123,32
T 6,123,5152
T 8,122,64
T 10,122,5184
T 12,123,96
T 14,123,5216
T 16,122,128
T 18,122,5248
T 20,123,160
T 22,123,5280
T 24,122,192
T 26,122,5312
T 28,123,224
T 30,123,5344
T 32,122,256
T 34,122,5376
T 36,123,288
T 38,123,5408
WV 20
Mavaz 0,0,80
AS 42,36,327680
M0 10240
Mavaz 4,0,84
T 40,122,10240
D 232,24,42,0
Mavaz 12,4,84
T 42,122,15360
Mavaz 8,4,80
T 44,123,10272
T 46,123,15392
Mavaz 16,8,80
T 48,122,10304
D 233,24,42,1024
Mavaz 20,8,84
T 50,122,15424
T 52,123,10336
Mavaz 28,12,84
T 54,123,15456
D 234,24,42,2048
Mavaz 24,12,80
T 56,122,10368
Mavaz 32,16,80
T 58,122,15488
T 60,123,10400
Mavaz 36,16,84
T 62,123,15520
D 235,24,42,3072
Mavaz 44,20,84
T 64,122,10432
M0 14336
Mavaz 40,20,80
T 66,122,15552
D 236,24,42,0
Mavaz 48,24,80
T 68,123,10464
T 70,123,15584
Mavaz 52,24,84
T 72,122,10496
AS 21,37,98304
Mavaz 60,28,84
T 74,122,15616
Las 128,196,28,21,0
Mavaz 56,28,80
T 76,123,10528
Mavaz 64,32,80
T 78,123,15648
Mavaz 68,32,84
Las 132,196,28,21,1024
Mavaz 76,36,84
Mavaz 72,36,80
WV 25
Mavac 0,40,88
AS 42,36,348160
M0 15360
Mavac 4,40,92
T 0,122,20480
D 232,24,42,0
Mavac 12,44,92
T 2,122,25600
Mavac 8,44,88
T 4,123,20512
T 6,123,25632
Mavac 16,48,88
T 8,122,20544
D 233,24,42,1024
Mavac 20,48,92
T 10,122,25664
T 12,123,20576
Mavac 28,52,92
T 14,123,25696
D 234,24,42,2048
Mavac 24,52,88
T 16,122,20608
Mavac 32,56,88
T 18,122,25728
T 20,123,20640
Mavac 36,56,92
T 22,123,25760
D 235,24,42,3072
Mavac 44,60,92
T 24,122,20672
M0 19456
Mavac 40,60,88
)A"
R"A(T 26,122,25792
D 236,24,42,0
Mavac 48,64,88
T 28,123,20704
T 30,123,25824
Mavac 52,64,92
T 32,122,20736
AS 21,37,114688
Mavac 60,68,92
T 34,122,25856
Las 136,196,28,21,0
Mavac 56,68,88
T 36,123,20768
Mavac 64,72,88
T 38,123,25888
Mavac 68,72,92
Las 140,196,28,21,1024
Mavac 76,76,92
Mavac 72,76,88
WV 30
Mavac 0,0,96
T 40,122,30720
T 42,122,35840
Mavac 4,0,100
T 44,123,30752
AS 21,37,131072
Mavac 12,4,100
Las 80,196,28,21,0
Mavac 8,4,96
T 46,123,35872
Mavac 16,8,96
T 48,122,30784
Mavac 20,8,100
T 50,122,35904
Mavac 28,12,100
T 52,123,30816
Mavac 24,12,96
T 54,123,35936
T 56,122,30848
Mavac 32,16,96
T 58,122,35968
Las 84,196,28,21,1024
Mavac 36,16,100
T 60,123,30880
Mavac 44,20,100
T 62,123,36000
Mavac 40,20,96
T 64,122,30912
Mavac 48,24,96
T 66,122,36032
Mavac 52,24,100
T 68,123,30944
T 70,123,36064
Mavac 60,28,100
T 72,122,30976
La 144,99,56,22,0
Mavac 56,28,96
T 74,122,36096
Mavac 64,32,96
T 76,123,31008
Mavac 68,32,100
T 78,123,36128
Mavac 76,36,100
Mavac 72,36,96
WV 17
B
Mavac 0,40,104
AS 42,36,491520
M0 75776
Mavac 4,40,108
T 0,124,0
D 232,24,42,0
Mavac 12,44,108
T 2,124,5120
T 4,125,32
Mavac 8,44,104
T 6,125,5152
D 233,24,42,1024
Mavac 16,48,104
T 8,124,64
Mavac 20,48,108
T 10,124,5184
T 12,125,96
Mavac 28,52,108
T 14,125,5216
D 234,24,42,2048
Mavac 24,52,104
T 16,124,128
T 18,124,5248
Mavac 32,56,104
T 20,125,160
D 235,24,42,3072
Mavac 36,56,108
T 22,125,5280
M0 79872
Mavac 44,60,108
T 24,124,192
D 236,24,42,0
Mavac 40,60,104
T 26,124,5312
AS 21,37,147456
Mavac 48,64,104
T 28,125,224
Las 88,196,28,21,0
Mavac 52,64,108
T 30,125,5344
Mavac 60,68,108
T 32,124,256
T 34,124,5376
Mavac 56,68,104
T 36,125,288
Las 92,196,28,21,1024
Mavac 64,72,104
T 38,125,5408
Mavac 68,72,108
La 148,99,60,22,0
Mavac 76,76,108
Mavac 72,76,104
W 0
Mavac 0,0,112
T 40,124,10240
T 42,124,15360
Mavac 4,0,116
T 44,125,10272
AS 21,37,163840
Mavac 12,4,116
Las 96,196,28,21,0
Mavac 8,4,112
T 46,125,15392
Mavac 16,8,112
T 48,124,10304
Mavac 20,8,116
T 50,124,15424
Mavac 28,12,116
T 52,125,10336
Mavac 24,12,112
T 54,125,15456
T 56,124,10368
Mavac 32,16,112
T 58,124,15488
Las 100,196,28,21,1024
Mavac 36,16,116
T 60,125,10400
Mavac 44,20,116
T 62,125,15520
Mavac 40,20,112
T 64,124,10432
Mavac 48,24,112
T 66,124,15552
T 68,125,10464
Mavac 52,24,116
T 70,125,15584
AS 22,22,55296
Mavac 60,28,116
T 72,124,10496
La 152,99,56,22,0
Mavac 56,28,112
T 74,124,15616
Mavac 64,32,112
T 76,125,10528
Mavac 68,32,116
T 78,125,15648
Mavac 76,36,116
Mavac 72,36,112
W 0
Mavac 0,40,120
AS 42,36,512000
M0 80896
Mavac 4,40,124
T 0,124,20480
D 232,24,42,0
Mavac 12,44,124
T 2,124,25600
T 4,125,20512
Mavac 8,44,120
T 6,125,25632
D 233,24,42,1024
Mavac 16,48,120
T 8,124,20544
Mavac 20,48,124
T 10,124,25664
T 12,125,20576
Mavac 28,52,124
T 14,125,25696
D 234,24,42,2048
Mavac 24,52,120
T 16,124,20608
T 18,124,25728
Mavac 32,56,120
T 20,125,20640
D 235,24,42,3072
Mavac 36,56,124
T 22,125,25760
M0 84992
Mavac 44,60,124
T 24,124,20672
D 236,24,42,0
Mavac 40,60,120
T 26,124,25792
AS 21,37,180224
Mavac 48,64,120
T 28,125,20704
Las 104,196,28,21,0
Mavac 52,64,124
T 30,125,25824
Mavac 60,68,124
T 32,124,20736
T 34,124,25856
Mavac 56,68,120
T 36,125,20768
Las 108,196,28,21,1024
Mavac 64,72,120
T 38,125,25888
Mavac 68,72,124
La 156,99,60,22,0
Mavac 76,76,124
Mavac 72,76,120
WV 29
Mavac 0,0,128
T 40,124,30720
Mavac 4,0,132
T 42,124,35840
T 44,125,30752
Mavac 12,4,132
T 46,125,35872
AS 21,37,196608
Mavac 8,4,128
Las 112,196,28,21,0
Mavac 16,8,128
T 48,124,30784
Mavac 20,8,132
T 50,124,35904
Mavac 28,12,132
T 52,125,30816
Mavac 24,12,128
T 54,125,35936
Mavac 32,16,128
T 56,124,30848
Mavac 36,16,132
T 58,124,35968
Mavac 44,20,132
T 60,125,30880
Mavac 40,20,128
T 62,125,36000
T 64,124,30912
Mavac 48,24,128
T 66,124,36032
Las 116,196,28,21,1024
Mavac 52,24,132
T 68,125,30944
Mavac 60,28,132
T 70,125,36064
Mavac 56,28,128
T 72,124,30976
Mavac 64,32,128
T 74,124,36096
Mavac 68,32,132
T 76,125,31008
Mavac 76,36,132
T 78,125,36128
Mavac 72,36,128
WV 24
B
Mavac 0,40,136
AS 42,36,655360
M0 116736
Mavac 4,40,140
T 0,126,0
D 232,24,42,0
Mavac 12,44,140
T 2,126,5120
T 4,127,32
Mavac 8,44,136
T 6,127,5152
D 233,24,42,1024
Mavac 16,48,136
T 8,126,64
Mavac 20,48,140
T 10,126,5184
T 12,127,96
Mavac 28,52,140
T 14,127,5216
D 234,24,42,2048
Mavac 24,52,136
T 16,126,128
T 18,126,5248
Mavac 32,56,136
T 20,127,160
D 235,24,42,3072
Mavac 36,56,140
T 22,127,5280
M0 120832
Mavac 44,60,140
T 24,126,192
D 236,24,42,0
Mavac 40,60,136
T 26,126,5312
AS 21,37,212992
Mavac 48,64,136
T 28,127,224
Las 120,196,28,21,0
Mavac 52,64,140
T 30,127,5344
Mavac 60,68,140
T 32,126,256
T 34,126,5376
Mavac 56,68,136
T 36,127,288
Las 124,196,28,21,1024
Mavac 64,72,136
T 38,127,5408
AS 22,22,55296
Mavac 68,72,140
La 160,99,56,22,0
Mavac 76,76,140
Mavac 72,76,136
WV 30
Mavac 0,0,80
T 40,126,10240
T 42,126,15360
Mavac 4,0,84
T 44,127,10272
AS 21,37,229376
Mavac 12,4,84
Las 128,196,28,21,0
Mavac 8,4,80
T 46,127,15392
Mavac 16,8,80
T 48,126,10304
Mavac 20,8,84
T 50,126,15424
Mavac 28,12,84
T 52,127,10336
Mavac 24,12,80
T 54,127,15456
T 56,126,10368
Mavac 32,16,80
T 58,126,15488
Las 132,196,28,21,1024
Mavac 36,16,84
T 60,127,10400
Mavac 44,20,84
T 62,127,15520
Mavac 40,20,80
T 64,126,10432
Mavac 48,24,80
T 66,126,15552
Mavac 52,24,84
T 68,127,10464
T 70,127,15584
Mavac 60,28,84
T 72,126,10496
La 164,99,60,22,0
Mavac 56,28,80
T 74,126,15616
Mavac 64,32,80
T 76,127,10528
Mavac 68,32,84
T 78,127,15648
Mavac 76,36,84
Mavac 72,36,80
WV 25
Mavac 0,40,88
AS 42,36,675840
M0 121856
Mavac 4,40,92
T 0,126,20480
D 232,24,42,0
Mavac 12,44,92
T 2,126,25600
T 4,127,20512
Mavac 8,44,88
T 6,127,25632
D 233,24,42,1024
Mavac 16,48,88
T 8,126,20544
Mavac 20,48,92
T 10,126,25664
T 12,127,20576
Mavac 28,52,92
T 14,127,25696
D 234,24,42,2048
Mavac 24,52,88
T 16,126,20608
T 18,126,25728
Mavac 32,56,88
T 20,127,20640
D 235,24,42,3072
Mavac 36,56,92
T 22,127,25760
M0 125952
Mavac 44,60,92
T 24,126,20672
D 236,24,42,0
Mavac 40,60,88
T 26,126,25792
AS 21,37,245760
Mavac 48,64,88
T 28,127,20704
Las 136,196,28,21,0
Mavac 52,64,92
T 30,127,25824
Mavac 60,68,92
T 32,126,20736
T 34,126,25856
Mavac 56,68,88
T 36,127,20768
Las 140,196,28,21,1024
Mavac 64,72,88
T 38,127,25888
AS 22,22,55296
Mavac 68,72,92
La 168,99,56,22,0
Mavac 76,76,92
Mavac 72,76,88
WV 30
Mavac 0,0,96
T 40,126,30720
Mavac 4,0,100
T 42,126,35840
T 44,127,30752
Mavac 12,4,100
T 46,127,35872
AS 21,37,262144
Mavac 8,4,96
Las 80,196,28,21,0
Mavac 16,8,96
T 48,126,30784
Mavac 20,8,100
T 50,126,35904
Mavac 28,12,100
T 52,127,30816
Mavac 24,12,96
T 54,127,35936
Mavac 32,16,96
T 56,126,30848
Mavac 36,16,100
T 58,126,35968
Mavac 44,20,100
T 60,127,30880
Mavac 40,20,96
T 62,127,36000
T 64,126,30912
Mavac 48,24,96
T 66,126,36032
Las 84,196,28,21,1024
Mavac 52,24,100
T 68,127,30944
Mavac 60,28,100
T 70,127,36064
Mavac 56,28,96
T 72,126,30976
Mavac 64,32,96
T 74,126,36096
Mavac 68,32,100
T 76,127,31008
Mavac 76,36,100
T 78,127,36128
Mavac 72,36,96
WV 24
B
Mavac 0,40,104
AS 42,36,819200
M0 10240
Mavac 4,40,108
T 0,122,0
D 232,24,42,0
Mavac 12,44,108
T 2,122,5120
T 4,123,32
Mavac 8,44,104
T 6,123,5152
D 233,24,42,1024
Mavac 16,48,104
T 8,122,64
Mavac 20,48,108
T 10,122,5184
T 12,123,96
Mavac 28,52,108
T 14,123,5216
D 234,24,42,2048
Mavac 24,52,104
T 16,122,128
T 18,122,5248
Mavac 32,56,104
T 20,123,160
D 235,24,42,3072
Mavac 36,56,108
T 22,123,5280
M0 14336
Mavac 44,60,108
T 24,122,192
D 236,24,42,0
Mavac 40,60,104
T 26,122,5312
AS 21,37,278528
Mavac 48,64,104
T 28,123,224
Las 88,196,28,21,0
Mavac 52,64,108
T 30,123,5344
Mavac 60,68,108
T 32,122,256
T 34,122,5376
Mavac 56,68,104
T 36,123,288
Las 92,196,28,21,1024
Mavac 64,72,104
T 38,123,5408
Mavac 68,72,108
La 172,99,60,22,0
Mavac 76,76,108
Mavac 72,76,104
WV 29
Mavac 0,0,112
T 40,122,10240
T 42,122,15360
Mavac 4,0,116
T 44,123,10272
AS 21,37,294912
Mavac 12,4,116
Las 96,196,28,21,0
Mavac 8,4,112
T 46,123,15392
Mavac 16,8,112
T 48,122,10304
Mavac 20,8,116
T 50,122,15424
Mavac 28,12,116
T 52,123,10336
Mavac 24,12,112
T 54,123,15456
T 56,122,10368
Mavac 32,16,112
T 58,122,15488
Las 100,196,28,21,1024
Mavac 36,16,116
T 60,123,10400
Mavac 44,20,116
T 62,123,15520
Mavac 40,20,112
T 64,122,10432
Mavac 48,24,112
T 66,122,15552
T 68,123,10464
Mavac 52,24,116
T 70,123,15584
AS 22,22,55296
Mavac 60,28,116
T 72,122,10496
La 176,99,56,22,0
Mavac 56,28,112
T 74,122,15616
Mavac 64,32,112
T 76,123,10528
Mavac 68,32,116
T 78,123,15648
Mavac 76,36,116
Mavac 72,36,112
WV 25
Mavac 0,40,120
AS 42,36,839680
M0 15360
Mavac 4,40,124
T 0,122,20480
D 232,24,42,0
Mavac 12,44,124
T 2,122,25600
T 4,123,20512
Mavac 8,44,120
T 6,123,25632
D 233,24,42,1024
Mavac 16,48,120
T 8,122,20544
Mavac 20,48,124
T 10,122,25664
T 12,123,20576
Mavac 28,52,124
T 14,123,25696
D 234,24,42,2048
Mavac 24,52,120
T 16,122,20608
T 18,122,25728
Mavac 32,56,120
T 20,123,20640
D 235,24,42,3072
Mavac 36,56,124
T 22,123,25760
M0 19456
Mavac 44,60,124
T 24,122,20672
D 236,24,42,0
Mavac 40,60,120
T 26,122,25792
AS 21,37,311296
Mavac 48,64,120
T 28,123,20704
Las 104,196,28,21,0
Mavac 52,64,124
T 30,123,25824
Mavac 60,68,124
T 32,122,20736
T 34,122,25856
Mavac 56,68,120
T 36,123,20768
Las 108,196,28,21,1024
Mavac 64,72,120
T 38,123,25888
Mavac 68,72,124
La 180,99,60,22,0
Mavac 76,76,124
Mavac 72,76,120
WV 30
Mavac 0,0,128
T 40,122,30720
Mavac 4,0,132
T 42,122,35840
T 44,123,30752
Mavac 12,4,132
T 46,123,35872
AS 21,37,327680
Mavac 8,4,128
Las 112,196,28,21,0
Mavac 16,8,128
T 48,122,30784
Mavac 20,8,132
T 50,122,35904
Mavac 28,12,132
T 52,123,30816
Mavac 24,12,128
T 54,123,35936
Mavac 32,16,128
T 56,122,30848
Mavac 36,16,132
T 58,122,35968
Mavac 44,20,132
T 60,123,30880
Mavac 40,20,128
T 62,123,36000
T 64,122,30912
Mavac 48,24,128
T 66,122,36032
Las 116,196,28,21,1024
Mavac 52,24,132
T 68,123,30944
Mavac 60,28,132
T 70,123,36064
Mavac 56,28,128
T 72,122,30976
Mavac 64,32,128
T 74,122,36096
Mavac 68,32,132
T 76,123,31008
Mavac 76,36,132
T 78,123,36128
Mavac 72,36,128
WV 24
B
Mavac 0,40,136
AS 42,36,983040
M0 75776
Mavac 4,40,140
T 0,124,0
D 232,24,42,0
Mavac 12,44,140
T 2,124,5120
T 4,125,32
Mavac 8,44,136
T 6,125,5152
D 233,24,42,1024
Mavac 16,48,136
T 8,124,64
Mavac 20,48,140
T 10,124,5184
T 12,125,96
Mavac 28,52,140
T 14,125,5216
D 234,24,42,2048
Mavac 24,52,136
T 16,124,128
T 18,124,5248
Mavac 32,56,136
T 20,125,160
D 235,24,42,3072
Mavac 36,56,140
T 22,125,5280
M0 79872
Mavac 44,60,140
T 24,124,192
D 236,24,42,0
Mavac 40,60,136
T 26,124,5312
AS 21,37,344064
Mavac 48,64,136
T 28,125,224
Las 120,196,28,21,0
Mavac 52,64,140
T 30,125,5344
Mavac 60,68,140
T 32,124,256
T 34,124,5376
Mavac 56,68,136
T 36,125,288
Las 124,196,28,21,1024
Mavac 64,72,136
T 38,125,5408
AS 22,22,55296
Mavac 68,72,140
La 184,99,56,22,0
Mavac 76,76,140
Mavac 72,76,136
WV 29
Mavac 0,0,80
T 40,124,10240
T 42,124,15360
Mavac 4,0,84
T 44,125,10272
AS 21,37,360448
Mavac 12,4,84
Las 128,196,28,21,0
Mavac 8,4,80
T 46,125,15392
Mavac 16,8,80
T 48,124,10304
Mavac 20,8,84
T 50,124,15424
Mavac 28,12,84
T 52,125,10336
Mavac 24,12,80
T 54,125,15456
T 56,124,10368
Mavac 32,16,80
T 58,124,15488
Las 132,196,28,21,1024
Mavac 36,16,84
T 60,125,10400
Mavac 44,20,84
T 62,125,15520
Mavac 40,20,80
T 64,124,10432
Mavac 48,24,80
T 66,124,15552
Mavac 52,24,84
T 68,125,10464
T 70,125,15584
Mavac 60,28,84
T 72,124,10496
La 188,99,60,22,0
Mavac 56,28,80
T 74,124,15616
Mavac 64,32,80
T 76,125,10528
Mavac 68,32,84
T 78,125,15648
Mavac 76,36,84
Mavac 72,36,80
WV 25
Mavac 0,40,88
AS 42,36,1003520
M0 80896
Mavac 4,40,92
T 0,124,20480
D 232,24,42,0
Mavac 12,44,92
T 2,124,25600
T 4,125,20512
Mavac 8,44,88
T 6,125,25632
D 233,24,42,1024
Mavac 16,48,88
T 8,124,20544
Mavac 20,48,92
T 10,124,25664
T 12,125,20576
Mavac 28,52,92
T 14,125,25696
D 234,24,42,2048
Mavac 24,52,88
T 16,124,20608
T 18,124,25728
Mavac 32,56,88
T 20,125,20640
D 235,24,42,3072
Mavac 36,56,92
T 22,125,25760
M0 84992
Mavac 44,60,92
T 24,124,20672
D 236,24,42,0
Mavac 40,60,88
T 26,124,25792
AS 21,37,376832
Mavac 48,64,88
T 28,125,20704
Las 136,196,28,21,0
Mavac 52,64,92
T 30,125,25824
Mavac 60,68,92
T 32,124,20736
T 34,124,25856
Mavac 56,68,88
T 36,125,20768
Las 140,196,28,21,1024
)A"
R"A(Mavac 64,72,88
T 38,125,25888
AS 22,22,55296
Mavac 68,72,92
La 192,99,56,22,0
Mavac 76,76,92
Mavac 72,76,88
WV 30
Mavac 0,0,96
T 40,124,30720
T 42,124,35840
Mavac 4,0,100
T 44,125,30752
AS 21,37,393216
Mavac 12,4,100
Las 80,196,28,21,0
Mavac 8,4,96
T 46,125,35872
Mavac 16,8,96
T 48,124,30784
Mavac 20,8,100
T 50,124,35904
Mavac 28,12,100
T 52,125,30816
Mavac 24,12,96
T 54,125,35936
T 56,124,30848
Mavac 32,16,96
T 58,124,35968
Las 84,196,28,21,1024
Mavac 36,16,100
T 60,125,30880
Mavac 44,20,100
T 62,125,36000
Mavac 40,20,96
T 64,124,30912
Mavac 48,24,96
T 66,124,36032
Mavac 52,24,100
T 68,125,30944
T 70,125,36064
Mavac 60,28,100
T 72,124,30976
La 196,99,60,22,0
Mavac 56,28,96
T 74,124,36096
Mavac 64,32,96
T 76,125,31008
Mavac 68,32,100
T 78,125,36128
Mavac 76,36,100
Mavac 72,36,96
WV 25
B
Mavac 0,40,104
AS 42,36,1146880
M0 116736
Mavac 4,40,108
T 0,126,0
D 232,24,42,0
Mavac 12,44,108
T 2,126,5120
Mavac 8,44,104
T 4,127,32
T 6,127,5152
Mavac 16,48,104
T 8,126,64
D 233,24,42,1024
Mavac 20,48,108
T 10,126,5184
T 12,127,96
Mavac 28,52,108
T 14,127,5216
D 234,24,42,2048
Mavac 24,52,104
T 16,126,128
Mavac 32,56,104
T 18,126,5248
T 20,127,160
Mavac 36,56,108
T 22,127,5280
D 235,24,42,3072
Mavac 44,60,108
T 24,126,192
M0 120832
Mavac 40,60,104
T 26,126,5312
D 236,24,42,0
Mavac 48,64,104
T 28,127,224
T 30,127,5344
Mavac 52,64,108
T 32,126,256
AS 21,37,409600
Mavac 60,68,108
T 34,126,5376
Las 88,196,28,21,0
Mavac 56,68,104
T 36,127,288
Mavac 64,72,104
T 38,127,5408
Mavac 68,72,108
Las 92,196,28,21,1024
Mavac 76,76,108
Mavac 72,76,104
WV 29
Mavac 0,0,112
T 40,126,10240
T 42,126,15360
Mavac 4,0,116
T 44,127,10272
AS 21,37,425984
Mavac 12,4,116
Las 96,196,28,21,0
Mavac 8,4,112
T 46,127,15392
Mavac 16,8,112
T 48,126,10304
Mavac 20,8,116
T 50,126,15424
Mavac 28,12,116
T 52,127,10336
Mavac 24,12,112
T 54,127,15456
T 56,126,10368
Mavac 32,16,112
T 58,126,15488
Las 100,196,28,21,1024
Mavac 36,16,116
T 60,127,10400
Mavac 44,20,116
T 62,127,15520
Mavac 40,20,112
T 64,126,10432
Mavac 48,24,112
T 66,126,15552
T 68,127,10464
Mavac 52,24,116
T 70,127,15584
AS 22,22,55296
Mavac 60,28,116
T 72,126,10496
La 200,99,56,22,0
Mavac 56,28,112
T 74,126,15616
Mavac 64,32,112
T 76,127,10528
Mavac 68,32,116
T 78,127,15648
Mavac 76,36,116
Mavac 72,36,112
WV 25
Mavac 0,40,120
AS 42,36,1167360
M0 121856
Mavac 4,40,124
T 0,126,20480
D 232,24,42,0
Mavac 12,44,124
T 2,126,25600
T 4,127,20512
Mavac 8,44,120
T 6,127,25632
D 233,24,42,1024
Mavac 16,48,120
T 8,126,20544
Mavac 20,48,124
T 10,126,25664
T 12,127,20576
Mavac 28,52,124
T 14,127,25696
D 234,24,42,2048
Mavac 24,52,120
T 16,126,20608
T 18,126,25728
Mavac 32,56,120
T 20,127,20640
D 235,24,42,3072
Mavac 36,56,124
T 22,127,25760
M0 125952
Mavac 44,60,124
T 24,126,20672
D 236,24,42,0
Mavac 40,60,120
T 26,126,25792
AS 21,37,442368
Mavac 48,64,120
T 28,127,20704
Las 104,196,28,21,0
Mavac 52,64,124
T 30,127,25824
Mavac 60,68,124
T 32,126,20736
T 34,126,25856
Mavac 56,68,120
T 36,127,20768
Las 108,196,28,21,1024
Mavac 64,72,120
T 38,127,25888
Mavac 68,72,124
La 204,99,60,22,0
Mavac 76,76,124
Mavac 72,76,120
WV 30
Mavac 0,0,128
T 40,126,30720
T 42,126,35840
Mavac 4,0,132
T 44,127,30752
AS 21,37,458752
Mavac 12,4,132
Las 112,196,28,21,0
Mavac 8,4,128
T 46,127,35872
Mavac 16,8,128
T 48,126,30784
Mavac 20,8,132
T 50,126,35904
Mavac 28,12,132
T 52,127,30816
Mavac 24,12,128
T 54,127,35936
T 56,126,30848
Mavac 32,16,128
T 58,126,35968
Las 116,196,28,21,1024
Mavac 36,16,132
T 60,127,30880
Mavac 44,20,132
T 62,127,36000
Mavac 40,20,128
T 64,126,30912
Mavac 48,24,128
T 66,126,36032
T 68,127,30944
Mavac 52,24,132
T 70,127,36064
AS 22,22,55296
Mavac 60,28,132
T 72,126,30976
La 208,99,56,22,0
Mavac 56,28,128
T 74,126,36096
Mavac 64,32,128
T 76,127,31008
Mavac 68,32,132
T 78,127,36128
Mavac 76,36,132
Mavac 72,36,128
WV 25
B
Mavac 0,40,136
T 0,122,0
Mavac 4,40,140
T 2,122,5120
T 4,123,32
Mavac 12,44,140
T 6,123,5152
AS 21,37,475136
Mavac 8,44,136
Las 120,196,28,21,0
Mavac 16,48,136
T 8,122,64
Mavac 20,48,140
T 10,122,5184
Mavac 28,52,140
T 12,123,96
Mavac 24,52,136
T 14,123,5216
Mavac 32,56,136
T 16,122,128
Mavac 36,56,140
T 18,122,5248
Mavac 44,60,140
T 20,123,160
Mavac 40,60,136
T 22,123,5280
T 24,122,192
Mavac 48,64,136
T 26,122,5312
Las 124,196,28,21,1024
Mavac 52,64,140
T 28,123,224
Mavac 60,68,140
T 30,123,5344
Mavac 56,68,136
T 32,122,256
Mavac 64,72,136
T 34,122,5376
Mavac 68,72,140
T 36,123,288
Mavac 76,76,140
T 38,123,5408
Mavac 72,76,136
WV 24
Mavac 0,0,80
T 40,122,10240
T 42,122,15360
Mavac 4,0,84
T 44,123,10272
AS 21,37,491520
Mavac 12,4,84
Las 128,196,28,21,0
Mavac 8,4,80
T 46,123,15392
Mavac 16,8,80
T 48,122,10304
Mavac 20,8,84
T 50,122,15424
Mavac 28,12,84
T 52,123,10336
Mavac 24,12,80
T 54,123,15456
T 56,122,10368
Mavac 32,16,80
T 58,122,15488
Las 132,196,28,21,1024
Mavac 36,16,84
T 60,123,10400
Mavac 44,20,84
T 62,123,15520
Mavac 40,20,80
T 64,122,10432
Mavac 48,24,80
T 66,122,15552
Mavac 52,24,84
T 68,123,10464
T 70,123,15584
Mavac 60,28,84
T 72,122,10496
La 212,99,60,22,0
Mavac 56,28,80
T 74,122,15616
Mavac 64,32,80
T 76,123,10528
Mavac 68,32,84
T 78,123,15648
Mavac 76,36,84
Mavac 72,36,80
WV 19
Mavac 0,40,88
T 0,122,20480
T 2,122,25600
Mavac 4,40,92
T 4,123,20512
AS 21,37,507904
Mavac 12,44,92
Las 136,196,28,21,0
Mavac 8,44,88
T 6,123,25632
Mavac 16,48,88
T 8,122,20544
Mavac 20,48,92
T 10,122,25664
Mavac 28,52,92
T 12,123,20576
Mavac 24,52,88
T 14,123,25696
T 16,122,20608
Mavac 32,56,88
T 18,122,25728
Las 140,196,28,21,1024
Mavac 36,56,92
T 20,123,20640
Mavac 44,60,92
T 22,123,25760
Mavac 40,60,88
T 24,122,20672
Mavac 48,64,88
T 26,122,25792
T 28,123,20704
Mavac 52,64,92
T 30,123,25824
AS 22,22,55296
Mavac 60,68,92
T 32,122,20736
La 216,99,56,22,0
Mavac 56,68,88
T 34,122,25856
Mavac 64,72,88
T 36,123,20768
Mavac 68,72,92
T 38,123,25888
Mavac 76,76,92
Mavac 72,76,88
WV 20
Mavac 0,0,96
T 40,122,30720
Mavac 4,0,100
T 42,122,35840
Mavac 12,4,100
T 44,123,30752
Mavac 8,4,96
T 46,123,35872
Mavac 16,8,96
T 48,122,30784
T 50,122,35904
Mavac 20,8,100
T 52,123,30816
La 220,99,60,22,0
Mavac 28,12,100
T 54,123,35936
Mavac 24,12,96
T 56,122,30848
Mavac 32,16,96
T 58,122,35968
Mavac 36,16,100
T 60,123,30880
Mavac 44,20,100
T 62,123,36000
Mavac 40,20,96
T 64,122,30912
Mavac 48,24,96
T 66,122,36032
Mavac 52,24,100
T 68,123,30944
Mavac 60,28,100
T 70,123,36064
Mavac 56,28,96
T 72,122,30976
Mavac 64,32,96
T 74,122,36096
Mavac 68,32,100
T 76,123,31008
Mavac 76,36,100
T 78,123,36128
Mavac 72,36,96
WV 13
B
Mavac 0,40,104
T 0,124,0
Mavac 4,40,108
T 2,124,5120
Mavac 12,44,108
T 4,125,32
Mavac 8,44,104
T 6,125,5152
Mavac 16,48,104
T 8,124,64
Mavac 20,48,108
T 10,124,5184
Mavac 28,52,108
T 12,125,96
Mavac 24,52,104
T 14,125,5216
Mavac 32,56,104
T 16,124,128
Mavac 36,56,108
T 18,124,5248
Mavac 44,60,108
T 20,125,160
Mavac 40,60,104
T 22,125,5280
Mavac 48,64,104
T 24,124,192
Mavac 52,64,108
T 26,124,5312
Mavac 60,68,108
T 28,125,224
Mavac 56,68,104
T 30,125,5344
Mavac 64,72,104
T 32,124,256
Mavac 68,72,108
T 34,124,5376
Mavac 76,76,108
T 36,125,288
Mavac 72,76,104
T 38,125,5408
WV 10
Mavac 0,0,112
T 40,124,10240
Mavac 4,0,116
T 42,124,15360
Mavac 12,4,116
T 44,125,10272
Mavac 8,4,112
T 46,125,15392
Mavac 16,8,112
T 48,124,10304
Mavac 20,8,116
T 50,124,15424
Mavac 28,12,116
T 52,125,10336
Mavac 24,12,112
T 54,125,15456
Mavac 32,16,112
T 56,124,10368
Mavac 36,16,116
T 58,124,15488
Mavac 44,20,116
T 60,125,10400
Mavac 40,20,112
T 62,125,15520
Mavac 48,24,112
T 64,124,10432
Mavac 52,24,116
T 66,124,15552
Mavac 60,28,116
T 68,125,10464
Mavac 56,28,112
T 70,125,15584
Mavac 64,32,112
T 72,124,10496
Mavac 68,32,116
T 74,124,15616
Mavac 76,36,116
T 76,125,10528
Mavac 72,36,112
T 78,125,15648
WV 7
Mavac 0,40,120
T 0,124,20480
Mavac 4,40,124
T 2,124,25600
Mavac 12,44,124
T 4,125,20512
Mavac 8,44,120
T 6,125,25632
Mavac 16,48,120
T 8,124,20544
Mavac 20,48,124
T 10,124,25664
Mavac 28,52,124
T 12,125,20576
Mavac 24,52,120
T 14,125,25696
Mavac 32,56,120
T 16,124,20608
Mavac 36,56,124
T 18,124,25728
Mavac 44,60,124
T 20,125,20640
Mavac 40,60,120
T 22,125,25760
Mavac 48,64,120
T 24,124,20672
Mavac 52,64,124
T 26,124,25792
Mavac 60,68,124
T 28,125,20704
Mavac 56,68,120
T 30,125,25824
Mavac 64,72,120
T 32,124,20736
Mavac 68,72,124
T 34,124,25856
Mavac 76,76,124
T 36,125,20768
Mavac 72,76,120
T 38,125,25888
WV 5
Mavac 0,0,128
T 40,124,30720
Mavac 4,0,132
T 42,124,35840
Mavac 12,4,132
T 44,125,30752
Mavac 8,4,128
T 46,125,35872
Mavac 16,8,128
T 48,124,30784
Mavac 20,8,132
T 50,124,35904
Mavac 28,12,132
T 52,125,30816
Mavac 24,12,128
T 54,125,35936
Mavac 32,16,128
T 56,124,30848
Mavac 36,16,132
T 58,124,35968
Mavac 44,20,132
T 60,125,30880
Mavac 40,20,128
T 62,125,36000
Mavac 48,24,128
T 64,124,30912
Mavac 52,24,132
T 66,124,36032
Mavac 60,28,132
T 68,125,30944
Mavac 56,28,128
T 70,125,36064
Mavac 64,32,128
T 72,124,30976
Mavac 68,32,132
T 74,124,36096
Mavac 76,36,132
T 76,125,31008
Mavac 72,36,128
T 78,125,36128
WV 2
B
Mavac 0,40,136
Mavac 4,40,140
Mavac 12,44,140
Mavac 8,44,136
Mavac 16,48,136
Mavac 20,48,140
Mavac 28,52,140
Mavac 24,52,136
Mavac 32,56,136
Mavac 36,56,140
Mavac 44,60,140
Mavac 40,60,136
Mavac 48,64,136
Mavac 52,64,140
Mavac 60,68,140
Mavac 56,68,136
Mavac 64,72,136
Mavac 68,72,140
Mavac 76,76,140
Mavac 72,76,136
s_branch OMD1%=
OMN1%=:
AS 21,37,0
Las 80,196,28,21,0
Las 84,196,28,21,1024
AS 21,37,16384
Las 88,196,28,21,0
Las 92,196,28,21,1024
AS 21,37,32768
Las 96,196,28,21,0
Las 100,196,28,21,1024
AS 21,37,49152
Las 104,196,28,21,0
Las 108,196,28,21,1024
AS 21,37,65536
Las 112,196,28,21,0
Las 116,196,28,21,1024
AS 21,37,81920
Las 120,196,28,21,0
Las 124,196,28,21,1024
AS 42,36,163840
M0 116736
N 0
D 232,24,42,0
D 233,24,42,1024
D 234,24,42,2048
D 235,24,42,3072
M0 120832
N 0
D 236,24,42,0
AS 42,36,184320
M0 121856
N 0
D 232,24,42,0
D 233,24,42,1024
D 234,24,42,2048
D 235,24,42,3072
M0 125952
N 0
D 236,24,42,0
WV 22
B
T 0,122,0
T 2,122,5120
T 4,123,32
T 6,123,5152
T 8,122,64
T 10,122,5184
T 12,123,96
T 14,123,5216
T 16,122,128
T 18,122,5248
T 20,123,160
T 22,123,5280
T 24,122,192
T 26,122,5312
T 28,123,224
T 30,123,5344
T 32,122,256
T 34,122,5376
T 36,123,288
T 38,123,5408
WV 20
Mavaz 0,0,80
AS 42,36,327680
M0 10240
Mavaz 4,0,84
T 40,122,10240
D 232,24,42,0
Mavaz 12,4,84
T 42,122,15360
Mavaz 8,4,80
T 44,123,10272
T 46,123,15392
Mavaz 16,8,80
T 48,122,10304
D 233,24,42,1024
Mavaz 20,8,84
T 50,122,15424
T 52,123,10336
Mavaz 28,12,84
T 54,123,15456
D 234,24,42,2048
Mavaz 24,12,80
T 56,122,10368
Mavaz 32,16,80
T 58,122,15488
T 60,123,10400
Mavaz 36,16,84
T 62,123,15520
D 235,24,42,3072
Mavaz 44,20,84
T 64,122,10432
M0 14336
Mavaz 40,20,80
T 66,122,15552
D 236,24,42,0
Mavaz 48,24,80
T 68,123,10464
T 70,123,15584
Mavaz 52,24,84
T 72,122,10496
AS 21,37,98304
Mavaz 60,28,84
T 74,122,15616
Las 128,196,28,21,0
Mavaz 56,28,80
T 76,123,10528
Mavaz 64,32,80
T 78,123,15648
Mavaz 68,32,84
Las 132,196,28,21,1024
Mavaz 76,36,84
Mavaz 72,36,80
WV 25
Mavac 0,40,88
AS 42,36,348160
M0 15360
Mavac 4,40,92
T 0,122,20480
D 232,24,42,0
Mavac 12,44,92
T 2,122,25600
Mavac 8,44,88
T 4,123,20512
T 6,123,25632
Mavac 16,48,88
T 8,122,20544
D 233,24,42,1024
Mavac 20,48,92
T 10,122,25664
T 12,123,20576
Mavac 28,52,92
T 14,123,25696
D 234,24,42,2048
Mavac 24,52,88
T 16,122,20608
Mavac 32,56,88
T 18,122,25728
T 20,123,20640
Mavac 36,56,92
T 22,123,25760
D 235,24,42,3072
Mavac 44,60,92
T 24,122,20672
M0 19456
Mavac 40,60,88
T 26,122,25792
D 236,24,42,0
Mavac 48,64,88
T 28,123,20704
T 30,123,25824
Mavac 52,64,92
T 32,122,20736
AS 21,37,114688
Mavac 60,68,92
T 34,122,25856
Las 136,196,28,21,0
Mavac 56,68,88
T 36,123,20768
Mavac 64,72,88
T 38,123,25888
Mavac 68,72,92
Las 140,196,28,21,1024
Mavac 76,76,92
Mavac 72,76,88
WV 30
Mavac 0,0,96
T 40,122,30720
Mavac 4,0,100
T 42,122,35840
T 44,123,30752
Mavac 12,4,100
T 46,123,35872
AS 21,37,131072
Mavac 8,4,96
Las 80,196,28,21,0
Mavac 16,8,96
T 48,122,30784
Mavac 20,8,100
T 50,122,35904
Mavac 28,12,100
T 52,123,30816
Mavac 24,12,96
T 54,123,35936
Mavac 32,16,96
T 56,122,30848
Mavac 36,16,100
T 58,122,35968
Mavac 44,20,100
T 60,123,30880
Mavac 40,20,96
T 62,123,36000
T 64,122,30912
Mavac 48,24,96
)A"
R"A(T 66,122,36032
Las 84,196,28,21,1024
Mavac 52,24,100
T 68,123,30944
Mavac 60,28,100
T 70,123,36064
Mavac 56,28,96
T 72,122,30976
Mavac 64,32,96
T 74,122,36096
Mavac 68,32,100
T 76,123,31008
Mavac 76,36,100
T 78,123,36128
Mavac 72,36,96
WV 16
B
Mavac 0,40,104
AS 42,36,491520
M0 75776
Mavac 4,40,108
T 0,124,0
D 232,24,42,0
Mavac 12,44,108
T 2,124,5120
Mavac 8,44,104
T 4,125,32
T 6,125,5152
Mavac 16,48,104
T 8,124,64
D 233,24,42,1024
Mavac 20,48,108
T 10,124,5184
T 12,125,96
Mavac 28,52,108
T 14,125,5216
D 234,24,42,2048
Mavac 24,52,104
T 16,124,128
Mavac 32,56,104
T 18,124,5248
T 20,125,160
Mavac 36,56,108
T 22,125,5280
D 235,24,42,3072
Mavac 44,60,108
T 24,124,192
M0 79872
Mavac 40,60,104
T 26,124,5312
D 236,24,42,0
Mavac 48,64,104
T 28,125,224
T 30,125,5344
Mavac 52,64,108
T 32,124,256
AS 21,37,147456
Mavac 60,68,108
T 34,124,5376
Las 88,196,28,21,0
Mavac 56,68,104
T 36,125,288
Mavac 64,72,104
T 38,125,5408
Mavac 68,72,108
Las 92,196,28,21,1024
Mavac 76,76,108
Mavac 72,76,104
W 0
Mavac 0,0,112
T 40,124,10240
Mavac 4,0,116
T 42,124,15360
T 44,125,10272
Mavac 12,4,116
T 46,125,15392
AS 21,37,163840
Mavac 8,4,112
Las 96,196,28,21,0
Mavac 16,8,112
T 48,124,10304
Mavac 20,8,116
T 50,124,15424
Mavac 28,12,116
T 52,125,10336
Mavac 24,12,112
T 54,125,15456
Mavac 32,16,112
T 56,124,10368
Mavac 36,16,116
T 58,124,15488
Mavac 44,20,116
T 60,125,10400
Mavac 40,20,112
T 62,125,15520
T 64,124,10432
Mavac 48,24,112
T 66,124,15552
Las 100,196,28,21,1024
Mavac 52,24,116
T 68,125,10464
Mavac 60,28,116
T 70,125,15584
Mavac 56,28,112
T 72,124,10496
Mavac 64,32,112
T 74,124,15616
Mavac 68,32,116
T 76,125,10528
Mavac 76,36,116
T 78,125,15648
Mavac 72,36,112
W 0
Mavac 0,40,120
AS 42,36,512000
M0 80896
Mavac 4,40,124
T 0,124,20480
D 232,24,42,0
Mavac 12,44,124
T 2,124,25600
Mavac 8,44,120
T 4,125,20512
T 6,125,25632
Mavac 16,48,120
T 8,124,20544
D 233,24,42,1024
Mavac 20,48,124
T 10,124,25664
T 12,125,20576
Mavac 28,52,124
T 14,125,25696
D 234,24,42,2048
Mavac 24,52,120
T 16,124,20608
Mavac 32,56,120
T 18,124,25728
T 20,125,20640
Mavac 36,56,124
T 22,125,25760
D 235,24,42,3072
Mavac 44,60,124
T 24,124,20672
M0 84992
Mavac 40,60,120
T 26,124,25792
D 236,24,42,0
Mavac 48,64,120
T 28,125,20704
T 30,125,25824
Mavac 52,64,124
T 32,124,20736
AS 21,37,180224
Mavac 60,68,124
T 34,124,25856
Las 104,196,28,21,0
Mavac 56,68,120
T 36,125,20768
Mavac 64,72,120
T 38,125,25888
Mavac 68,72,124
Las 108,196,28,21,1024
Mavac 76,76,124
Mavac 72,76,120
WV 25
Mavac 0,0,128
T 40,124,30720
Mavac 4,0,132
T 42,124,35840
T 44,125,30752
Mavac 12,4,132
T 46,125,35872
AS 21,37,196608
Mavac 8,4,128
Las 112,196,28,21,0
Mavac 16,8,128
T 48,124,30784
Mavac 20,8,132
T 50,124,35904
Mavac 28,12,132
T 52,125,30816
Mavac 24,12,128
T 54,125,35936
Mavac 32,16,128
T 56,124,30848
Mavac 36,16,132
T 58,124,35968
Mavac 44,20,132
T 60,125,30880
Mavac 40,20,128
T 62,125,36000
T 64,124,30912
Mavac 48,24,128
T 66,124,36032
Las 116,196,28,21,1024
Mavac 52,24,132
T 68,125,30944
Mavac 60,28,132
T 70,125,36064
Mavac 56,28,128
T 72,124,30976
Mavac 64,32,128
T 74,124,36096
Mavac 68,32,132
T 76,125,31008
Mavac 76,36,132
T 78,125,36128
Mavac 72,36,128
WV 20
B
Mavac 0,40,136
AS 42,36,655360
M0 116736
Mavac 4,40,140
T 0,126,0
D 232,24,42,0
Mavac 12,44,140
T 2,126,5120
Mavac 8,44,136
T 4,127,32
T 6,127,5152
Mavac 16,48,136
T 8,126,64
D 233,24,42,1024
Mavac 20,48,140
T 10,126,5184
T 12,127,96
Mavac 28,52,140
T 14,127,5216
D 234,24,42,2048
Mavac 24,52,136
T 16,126,128
Mavac 32,56,136
T 18,126,5248
T 20,127,160
Mavac 36,56,140
T 22,127,5280
D 235,24,42,3072
Mavac 44,60,140
T 24,126,192
M0 120832
Mavac 40,60,136
T 26,126,5312
D 236,24,42,0
Mavac 48,64,136
T 28,127,224
T 30,127,5344
Mavac 52,64,140
T 32,126,256
AS 21,37,212992
Mavac 60,68,140
T 34,126,5376
Las 120,196,28,21,0
Mavac 56,68,136
T 36,127,288
Mavac 64,72,136
T 38,127,5408
Mavac 68,72,140
Las 124,196,28,21,1024
Mavac 76,76,140
Mavac 72,76,136
WV 25
Mavac 0,0,80
T 40,126,10240
Mavac 4,0,84
T 42,126,15360
T 44,127,10272
Mavac 12,4,84
T 46,127,15392
AS 21,37,229376
Mavac 8,4,80
Las 128,196,28,21,0
Mavac 16,8,80
T 48,126,10304
Mavac 20,8,84
T 50,126,15424
Mavac 28,12,84
T 52,127,10336
Mavac 24,12,80
T 54,127,15456
Mavac 32,16,80
T 56,126,10368
Mavac 36,16,84
T 58,126,15488
Mavac 44,20,84
T 60,127,10400
Mavac 40,20,80
T 62,127,15520
T 64,126,10432
Mavac 48,24,80
T 66,126,15552
Las 132,196,28,21,1024
Mavac 52,24,84
T 68,127,10464
Mavac 60,28,84
T 70,127,15584
Mavac 56,28,80
T 72,126,10496
Mavac 64,32,80
T 74,126,15616
Mavac 68,32,84
T 76,127,10528
Mavac 76,36,84
T 78,127,15648
Mavac 72,36,80
WV 20
Mavac 0,40,88
AS 42,36,675840
M0 121856
Mavac 4,40,92
T 0,126,20480
D 232,24,42,0
Mavac 12,44,92
T 2,126,25600
Mavac 8,44,88
T 4,127,20512
T 6,127,25632
Mavac 16,48,88
T 8,126,20544
D 233,24,42,1024
Mavac 20,48,92
T 10,126,25664
T 12,127,20576
Mavac 28,52,92
T 14,127,25696
D 234,24,42,2048
Mavac 24,52,88
T 16,126,20608
Mavac 32,56,88
T 18,126,25728
T 20,127,20640
Mavac 36,56,92
T 22,127,25760
D 235,24,42,3072
Mavac 44,60,92
T 24,126,20672
M0 125952
Mavac 40,60,88
T 26,126,25792
D 236,24,42,0
Mavac 48,64,88
T 28,127,20704
T 30,127,25824
Mavac 52,64,92
T 32,126,20736
AS 21,37,245760
Mavac 60,68,92
T 34,126,25856
Las 136,196,28,21,0
Mavac 56,68,88
T 36,127,20768
Mavac 64,72,88
T 38,127,25888
Mavac 68,72,92
Las 140,196,28,21,1024
Mavac 76,76,92
Mavac 72,76,88
WV 25
Mavac 0,0,96
T 40,126,30720
Mavac 4,0,100
T 42,126,35840
T 44,127,30752
Mavac 12,4,100
T 46,127,35872
AS 21,37,262144
Mavac 8,4,96
Las 80,196,28,21,0
Mavac 16,8,96
T 48,126,30784
Mavac 20,8,100
T 50,126,35904
Mavac 28,12,100
T 52,127,30816
Mavac 24,12,96
T 54,127,35936
Mavac 32,16,96
T 56,126,30848
Mavac 36,16,100
T 58,126,35968
Mavac 44,20,100
T 60,127,30880
Mavac 40,20,96
T 62,127,36000
T 64,126,30912
Mavac 48,24,96
T 66,126,36032
Las 84,196,28,21,1024
Mavac 52,24,100
T 68,127,30944
Mavac 60,28,100
T 70,127,36064
Mavac 56,28,96
T 72,126,30976
Mavac 64,32,96
T 74,126,36096
Mavac 68,32,100
T 76,127,31008
Mavac 76,36,100
T 78,127,36128
Mavac 72,36,96
WV 20
B
Mavac 0,40,104
AS 42,36,819200
M0 10240
Mavac 4,40,108
T 0,122,0
D 232,24,42,0
Mavac 12,44,108
T 2,122,5120
Mavac 8,44,104
T 4,123,32
T 6,123,5152
Mavac 16,48,104
T 8,122,64
D 233,24,42,1024
Mavac 20,48,108
T 10,122,5184
T 12,123,96
Mavac 28,52,108
T 14,123,5216
D 234,24,42,2048
Mavac 24,52,104
T 16,122,128
Mavac 32,56,104
T 18,122,5248
T 20,123,160
Mavac 36,56,108
T 22,123,5280
D 235,24,42,3072
Mavac 44,60,108
T 24,122,192
M0 14336
Mavac 40,60,104
T 26,122,5312
D 236,24,42,0
Mavac 48,64,104
T 28,123,224
T 30,123,5344
Mavac 52,64,108
T 32,122,256
AS 21,37,278528
Mavac 60,68,108
T 34,122,5376
Las 88,196,28,21,0
Mavac 56,68,104
T 36,123,288
Mavac 64,72,104
T 38,123,5408
Mavac 68,72,108
Las 92,196,28,21,1024
Mavac 76,76,108
Mavac 72,76,104
WV 25
Mavac 0,0,112
T 40,122,10240
Mavac 4,0,116
T 42,122,15360
T 44,123,10272
Mavac 12,4,116
T 46,123,15392
AS 21,37,294912
Mavac 8,4,112
Las 96,196,28,21,0
Mavac 16,8,112
T 48,122,10304
Mavac 20,8,116
T 50,122,15424
Mavac 28,12,116
T 52,123,10336
Mavac 24,12,112
T 54,123,15456
Mavac 32,16,112
T 56,122,10368
Mavac 36,16,116
T 58,122,15488
Mavac 44,20,116
T 60,123,10400
Mavac 40,20,112
T 62,123,15520
T 64,122,10432
Mavac 48,24,112
T 66,122,15552
Las 100,196,28,21,1024
Mavac 52,24,116
T 68,123,10464
Mavac 60,28,116
T 70,123,15584
Mavac 56,28,112
T 72,122,10496
Mavac 64,32,112
T 74,122,15616
Mavac 68,32,116
T 76,123,10528
Mavac 76,36,116
T 78,123,15648
Mavac 72,36,112
WV 20
Mavac 0,40,120
AS 42,36,839680
M0 15360
Mavac 4,40,124
T 0,122,20480
D 232,24,42,0
Mavac 12,44,124
T 2,122,25600
Mavac 8,44,120
T 4,123,20512
T 6,123,25632
Mavac 16,48,120
T 8,122,20544
D 233,24,42,1024
Mavac 20,48,124
T 10,122,25664
T 12,123,20576
Mavac 28,52,124
T 14,123,25696
D 234,24,42,2048
Mavac 24,52,120
T 16,122,20608
Mavac 32,56,120
T 18,122,25728
T 20,123,20640
Mavac 36,56,124
T 22,123,25760
D 235,24,42,3072
Mavac 44,60,124
T 24,122,20672
M0 19456
Mavac 40,60,120
T 26,122,25792
D 236,24,42,0
Mavac 48,64,120
T 28,123,20704
T 30,123,25824
Mavac 52,64,124
T 32,122,20736
AS 21,37,311296
Mavac 60,68,124
T 34,122,25856
Las 104,196,28,21,0
Mavac 56,68,120
T 36,123,20768
Mavac 64,72,120
T 38,123,25888
Mavac 68,72,124
Las 108,196,28,21,1024
Mavac 76,76,124
Mavac 72,76,120
WV 25
Mavac 0,0,128
T 40,122,30720
Mavac 4,0,132
T 42,122,35840
T 44,123,30752
Mavac 12,4,132
T 46,123,35872
AS 21,37,327680
Mavac 8,4,128
Las 112,196,28,21,0
Mavac 16,8,128
T 48,122,30784
Mavac 20,8,132
T 50,122,35904
Mavac 28,12,132
T 52,123,30816
Mavac 24,12,128
T 54,123,35936
Mavac 32,16,128
T 56,122,30848
Mavac 36,16,132
T 58,122,35968
Mavac 44,20,132
T 60,123,30880
Mavac 40,20,128
T 62,123,36000
T 64,122,30912
Mavac 48,24,128
T 66,122,36032
Las 116,196,28,21,1024
Mavac 52,24,132
T 68,123,30944
Mavac 60,28,132
T 70,123,36064
Mavac 56,28,128
T 72,122,30976
Mavac 64,32,128
T 74,122,36096
Mavac 68,32,132
T 76,123,31008
Mavac 76,36,132
T 78,123,36128
Mavac 72,36,128
WV 20
B
Mavac 0,40,136
AS 42,36,983040
M0 75776
Mavac 4,40,140
T 0,124,0
D 232,24,42,0
Mavac 12,44,140
T 2,124,5120
Mavac 8,44,136
T 4,125,32
T 6,125,5152
Mavac 16,48,136
T 8,124,64
D 233,24,42,1024
Mavac 20,48,140
T 10,124,5184
T 12,125,96
Mavac 28,52,140
T 14,125,5216
D 234,24,42,2048
Mavac 24,52,136
T 16,124,128
Mavac 32,56,136
T 18,124,5248
T 20,125,160
Mavac 36,56,140
T 22,125,5280
D 235,24,42,3072
Mavac 44,60,140
T 24,124,192
M0 79872
Mavac 40,60,136
T 26,124,5312
D 236,24,42,0
Mavac 48,64,136
T 28,125,224
T 30,125,5344
Mavac 52,64,140
T 32,124,256
AS 21,37,344064
Mavac 60,68,140
T 34,124,5376
Las 120,196,28,21,0
Mavac 56,68,136
T 36,125,288
Mavac 64,72,136
T 38,125,5408
Mavac 68,72,140
Las 124,196,28,21,1024
Mavac 76,76,140
Mavac 72,76,136
WV 25
Mavac 0,0,80
T 40,124,10240
Mavac 4,0,84
T 42,124,15360
T 44,125,10272
Mavac 12,4,84
T 46,125,15392
AS 21,37,360448
Mavac 8,4,80
Las 128,196,28,21,0
Mavac 16,8,80
T 48,124,10304
Mavac 20,8,84
T 50,124,15424
Mavac 28,12,84
T 52,125,10336
Mavac 24,12,80
T 54,125,15456
Mavac 32,16,80
T 56,124,10368
Mavac 36,16,84
T 58,124,15488
Mavac 44,20,84
T 60,125,10400
Mavac 40,20,80
T 62,125,15520
T 64,124,10432
Mavac 48,24,80
T 66,124,15552
Las 132,196,28,21,1024
Mavac 52,24,84
T 68,125,10464
Mavac 60,28,84
T 70,125,15584
Mavac 56,28,80
T 72,124,10496
Mavac 64,32,80
T 74,124,15616
Mavac 68,32,84
T 76,125,10528
Mavac 76,36,84
T 78,125,15648
Mavac 72,36,80
WV 20
Mavac 0,40,88
AS 42,36,1003520
M0 80896
Mavac 4,40,92
T 0,124,20480
D 232,24,42,0
Mavac 12,44,92
T 2,124,25600
Mavac 8,44,88
T 4,125,20512
T 6,125,25632
Mavac 16,48,88
T 8,124,20544
D 233,24,42,1024
Mavac 20,48,92
T 10,124,25664
T 12,125,20576
Mavac 28,52,92
T 14,125,25696
D 234,24,42,2048
Mavac 24,52,88
T 16,124,20608
Mavac 32,56,88
T 18,124,25728
T 20,125,20640
Mavac 36,56,92
T 22,125,25760
D 235,24,42,3072
Mavac 44,60,92
T 24,124,20672
M0 84992
Mavac 40,60,88
T 26,124,25792
D 236,24,42,0
Mavac 48,64,88
T 28,125,20704
T 30,125,25824
Mavac 52,64,92
T 32,124,20736
AS 21,37,376832
Mavac 60,68,92
T 34,124,25856
Las 136,196,28,21,0
Mavac 56,68,88
T 36,125,20768
Mavac 64,72,88
T 38,125,25888
Mavac 68,72,92
Las 140,196,28,21,1024
Mavac 76,76,92
Mavac 72,76,88
WV 25
Mavac 0,0,96
T 40,124,30720
Mavac 4,0,100
T 42,124,35840
T 44,125,30752
Mavac 12,4,100
T 46,125,35872
AS 21,37,393216
Mavac 8,4,96
Las 80,196,28,21,0
Mavac 16,8,96
T 48,124,30784
Mavac 20,8,100
T 50,124,35904
Mavac 28,12,100
T 52,125,30816
Mavac 24,12,96
T 54,125,35936
Mavac 32,16,96
T 56,124,30848
Mavac 36,16,100
T 58,124,35968
Mavac 44,20,100
T 60,125,30880
Mavac 40,20,96
T 62,125,36000
T 64,124,30912
Mavac 48,24,96
T 66,124,36032
Las 84,196,28,21,1024
Mavac 52,24,100
T 68,125,30944
Mavac 60,28,100
T 70,125,36064
Mavac 56,28,96
T 72,124,30976
Mavac 64,32,96
T 74,124,36096
Mavac 68,32,100
T 76,125,31008
Mavac 76,36,100
T 78,125,36128
Mavac 72,36,96
WV 20
B
Mavac 0,40,104
AS 42,36,1146880
M0 116736
Mavac 4,40,108
T 0,126,0
D 232,24,42,0
Mavac 12,44,108
T 2,126,5120
Mavac 8,44,104
T 4,127,32
T 6,127,5152
Mavac 16,48,104
T 8,126,64
D 233,24,42,1024
Mavac 20,48,108
T 10,126,5184
T 12,127,96
Mavac 28,52,108
T 14,127,5216
D 234,24,42,2048
)A"
R"A(Mavac 24,52,104
T 16,126,128
Mavac 32,56,104
T 18,126,5248
T 20,127,160
Mavac 36,56,108
T 22,127,5280
D 235,24,42,3072
Mavac 44,60,108
T 24,126,192
M0 120832
Mavac 40,60,104
T 26,126,5312
D 236,24,42,0
Mavac 48,64,104
T 28,127,224
T 30,127,5344
Mavac 52,64,108
T 32,126,256
AS 21,37,409600
Mavac 60,68,108
T 34,126,5376
Las 88,196,28,21,0
Mavac 56,68,104
T 36,127,288
Mavac 64,72,104
T 38,127,5408
Mavac 68,72,108
Las 92,196,28,21,1024
Mavac 76,76,108
Mavac 72,76,104
WV 25
Mavac 0,0,112
T 40,126,10240
Mavac 4,0,116
T 42,126,15360
T 44,127,10272
Mavac 12,4,116
T 46,127,15392
AS 21,37,425984
Mavac 8,4,112
Las 96,196,28,21,0
Mavac 16,8,112
T 48,126,10304
Mavac 20,8,116
T 50,126,15424
Mavac 28,12,116
T 52,127,10336
Mavac 24,12,112
T 54,127,15456
Mavac 32,16,112
T 56,126,10368
Mavac 36,16,116
T 58,126,15488
Mavac 44,20,116
T 60,127,10400
Mavac 40,20,112
T 62,127,15520
T 64,126,10432
Mavac 48,24,112
T 66,126,15552
Las 100,196,28,21,1024
Mavac 52,24,116
T 68,127,10464
Mavac 60,28,116
T 70,127,15584
Mavac 56,28,112
T 72,126,10496
Mavac 64,32,112
T 74,126,15616
Mavac 68,32,116
T 76,127,10528
Mavac 76,36,116
T 78,127,15648
Mavac 72,36,112
WV 20
Mavac 0,40,120
AS 42,36,1167360
M0 121856
Mavac 4,40,124
T 0,126,20480
D 232,24,42,0
Mavac 12,44,124
T 2,126,25600
Mavac 8,44,120
T 4,127,20512
T 6,127,25632
Mavac 16,48,120
T 8,126,20544
D 233,24,42,1024
Mavac 20,48,124
T 10,126,25664
T 12,127,20576
Mavac 28,52,124
T 14,127,25696
D 234,24,42,2048
Mavac 24,52,120
T 16,126,20608
Mavac 32,56,120
T 18,126,25728
T 20,127,20640
Mavac 36,56,124
T 22,127,25760
D 235,24,42,3072
Mavac 44,60,124
T 24,126,20672
M0 125952
Mavac 40,60,120
T 26,126,25792
D 236,24,42,0
Mavac 48,64,120
T 28,127,20704
T 30,127,25824
Mavac 52,64,124
T 32,126,20736
AS 21,37,442368
Mavac 60,68,124
T 34,126,25856
Las 104,196,28,21,0
Mavac 56,68,120
T 36,127,20768
Mavac 64,72,120
T 38,127,25888
Mavac 68,72,124
Las 108,196,28,21,1024
Mavac 76,76,124
Mavac 72,76,120
WV 25
Mavac 0,0,128
T 40,126,30720
Mavac 4,0,132
T 42,126,35840
T 44,127,30752
Mavac 12,4,132
T 46,127,35872
AS 21,37,458752
Mavac 8,4,128
Las 112,196,28,21,0
Mavac 16,8,128
T 48,126,30784
Mavac 20,8,132
T 50,126,35904
Mavac 28,12,132
T 52,127,30816
Mavac 24,12,128
T 54,127,35936
Mavac 32,16,128
T 56,126,30848
Mavac 36,16,132
T 58,126,35968
Mavac 44,20,132
T 60,127,30880
Mavac 40,20,128
T 62,127,36000
T 64,126,30912
Mavac 48,24,128
T 66,126,36032
Las 116,196,28,21,1024
Mavac 52,24,132
T 68,127,30944
Mavac 60,28,132
T 70,127,36064
Mavac 56,28,128
T 72,126,30976
Mavac 64,32,128
T 74,126,36096
Mavac 68,32,132
T 76,127,31008
Mavac 76,36,132
T 78,127,36128
Mavac 72,36,128
WV 20
B
Mavac 0,40,136
T 0,122,0
Mavac 4,40,140
T 2,122,5120
T 4,123,32
Mavac 12,44,140
T 6,123,5152
AS 21,37,475136
Mavac 8,44,136
Las 120,196,28,21,0
Mavac 16,48,136
T 8,122,64
Mavac 20,48,140
T 10,122,5184
Mavac 28,52,140
T 12,123,96
Mavac 24,52,136
T 14,123,5216
Mavac 32,56,136
T 16,122,128
Mavac 36,56,140
T 18,122,5248
Mavac 44,60,140
T 20,123,160
Mavac 40,60,136
T 22,123,5280
T 24,122,192
Mavac 48,64,136
T 26,122,5312
Las 124,196,28,21,1024
Mavac 52,64,140
T 28,123,224
Mavac 60,68,140
T 30,123,5344
Mavac 56,68,136
T 32,122,256
Mavac 64,72,136
T 34,122,5376
Mavac 68,72,140
T 36,123,288
Mavac 76,76,140
T 38,123,5408
Mavac 72,76,136
WV 20
Mavac 0,0,80
T 40,122,10240
Mavac 4,0,84
T 42,122,15360
T 44,123,10272
Mavac 12,4,84
T 46,123,15392
AS 21,37,491520
Mavac 8,4,80
Las 128,196,28,21,0
Mavac 16,8,80
T 48,122,10304
Mavac 20,8,84
T 50,122,15424
Mavac 28,12,84
T 52,123,10336
Mavac 24,12,80
T 54,123,15456
Mavac 32,16,80
T 56,122,10368
Mavac 36,16,84
T 58,122,15488
Mavac 44,20,84
T 60,123,10400
Mavac 40,20,80
T 62,123,15520
T 64,122,10432
Mavac 48,24,80
T 66,122,15552
Las 132,196,28,21,1024
Mavac 52,24,84
T 68,123,10464
Mavac 60,28,84
T 70,123,15584
Mavac 56,28,80
T 72,122,10496
Mavac 64,32,80
T 74,122,15616
Mavac 68,32,84
T 76,123,10528
Mavac 76,36,84
T 78,123,15648
Mavac 72,36,80
WV 15
Mavac 0,40,88
T 0,122,20480
Mavac 4,40,92
T 2,122,25600
T 4,123,20512
Mavac 12,44,92
T 6,123,25632
AS 21,37,507904
Mavac 8,44,88
Las 136,196,28,21,0
Mavac 16,48,88
T 8,122,20544
Mavac 20,48,92
T 10,122,25664
Mavac 28,52,92
T 12,123,20576
Mavac 24,52,88
T 14,123,25696
Mavac 32,56,88
T 16,122,20608
Mavac 36,56,92
T 18,122,25728
Mavac 44,60,92
T 20,123,20640
Mavac 40,60,88
T 22,123,25760
T 24,122,20672
Mavac 48,64,88
T 26,122,25792
Las 140,196,28,21,1024
Mavac 52,64,92
T 28,123,20704
Mavac 60,68,92
T 30,123,25824
Mavac 56,68,88
T 32,122,20736
Mavac 64,72,88
T 34,122,25856
Mavac 68,72,92
T 36,123,20768
Mavac 76,76,92
T 38,123,25888
Mavac 72,76,88
WV 15
Mavac 0,0,96
T 40,122,30720
Mavac 4,0,100
T 42,122,35840
Mavac 12,4,100
T 44,123,30752
Mavac 8,4,96
T 46,123,35872
Mavac 16,8,96
T 48,122,30784
Mavac 20,8,100
T 50,122,35904
Mavac 28,12,100
T 52,123,30816
Mavac 24,12,96
T 54,123,35936
Mavac 32,16,96
T 56,122,30848
Mavac 36,16,100
T 58,122,35968
Mavac 44,20,100
T 60,123,30880
Mavac 40,20,96
T 62,123,36000
Mavac 48,24,96
T 64,122,30912
Mavac 52,24,100
T 66,122,36032
Mavac 60,28,100
T 68,123,30944
Mavac 56,28,96
T 70,123,36064
Mavac 64,32,96
T 72,122,30976
Mavac 68,32,100
T 74,122,36096
Mavac 76,36,100
T 76,123,31008
Mavac 72,36,96
T 78,123,36128
WV 8
B
Mavac 0,40,104
T 0,124,0
Mavac 4,40,108
T 2,124,5120
Mavac 12,44,108
T 4,125,32
Mavac 8,44,104
T 6,125,5152
Mavac 16,48,104
T 8,124,64
Mavac 20,48,108
T 10,124,5184
Mavac 28,52,108
T 12,125,96
Mavac 24,52,104
T 14,125,5216
Mavac 32,56,104
T 16,124,128
Mavac 36,56,108
T 18,124,5248
Mavac 44,60,108
T 20,125,160
Mavac 40,60,104
T 22,125,5280
Mavac 48,64,104
T 24,124,192
Mavac 52,64,108
T 26,124,5312
Mavac 60,68,108
T 28,125,224
Mavac 56,68,104
T 30,125,5344
Mavac 64,72,104
T 32,124,256
Mavac 68,72,108
T 34,124,5376
Mavac 76,76,108
T 36,125,288
Mavac 72,76,104
T 38,125,5408
WV 6
Mavac 0,0,112
T 40,124,10240
Mavac 4,0,116
T 42,124,15360
Mavac 12,4,116
T 44,125,10272
Mavac 8,4,112
T 46,125,15392
Mavac 16,8,112
T 48,124,10304
Mavac 20,8,116
T 50,124,15424
Mavac 28,12,116
T 52,125,10336
Mavac 24,12,112
T 54,125,15456
Mavac 32,16,112
T 56,124,10368
Mavac 36,16,116
T 58,124,15488
Mavac 44,20,116
T 60,125,10400
Mavac 40,20,112
T 62,125,15520
Mavac 48,24,112
T 64,124,10432
Mavac 52,24,116
T 66,124,15552
Mavac 60,28,116
T 68,125,10464
Mavac 56,28,112
T 70,125,15584
Mavac 64,32,112
T 72,124,10496
Mavac 68,32,116
T 74,124,15616
Mavac 76,36,116
T 76,125,10528
Mavac 72,36,112
T 78,125,15648
WV 4
Mavac 0,40,120
T 0,124,20480
Mavac 4,40,124
T 2,124,25600
Mavac 12,44,124
T 4,125,20512
Mavac 8,44,120
T 6,125,25632
Mavac 16,48,120
T 8,124,20544
Mavac 20,48,124
T 10,124,25664
Mavac 28,52,124
T 12,125,20576
Mavac 24,52,120
T 14,125,25696
Mavac 32,56,120
T 16,124,20608
Mavac 36,56,124
T 18,124,25728
Mavac 44,60,124
T 20,125,20640
Mavac 40,60,120
T 22,125,25760
Mavac 48,64,120
T 24,124,20672
Mavac 52,64,124
T 26,124,25792
Mavac 60,68,124
T 28,125,20704
Mavac 56,68,120
T 30,125,25824
Mavac 64,72,120
T 32,124,20736
Mavac 68,72,124
T 34,124,25856
Mavac 76,76,124
T 36,125,20768
Mavac 72,76,120
T 38,125,25888
WV 2
Mavac 0,0,128
T 40,124,30720
Mavac 4,0,132
T 42,124,35840
Mavac 12,4,132
T 44,125,30752
Mavac 8,4,128
T 46,125,35872
Mavac 16,8,128
T 48,124,30784
Mavac 20,8,132
T 50,124,35904
Mavac 28,12,132
T 52,125,30816
Mavac 24,12,128
T 54,125,35936
Mavac 32,16,128
T 56,124,30848
Mavac 36,16,132
T 58,124,35968
Mavac 44,20,132
T 60,125,30880
Mavac 40,20,128
T 62,125,36000
Mavac 48,24,128
T 64,124,30912
Mavac 52,24,132
T 66,124,36032
Mavac 60,28,132
T 68,125,30944
Mavac 56,28,128
T 70,125,36064
Mavac 64,32,128
T 72,124,30976
Mavac 68,32,132
T 74,124,36096
Mavac 76,36,132
T 76,125,31008
Mavac 72,36,128
T 78,125,36128
WV 0
B
Mavac 0,40,136
Mavac 4,40,140
Mavac 12,44,140
Mavac 8,44,136
Mavac 16,48,136
Mavac 20,48,140
Mavac 28,52,140
Mavac 24,52,136
Mavac 32,56,136
Mavac 36,56,140
Mavac 44,60,140
Mavac 40,60,136
Mavac 48,64,136
Mavac 52,64,140
Mavac 60,68,140
Mavac 56,68,136
Mavac 64,72,136
Mavac 68,72,140
Mavac 76,76,140
Mavac 72,76,136
OMD1%=:
G 0,0
G 4,8
G 1,1
G 5,9
G 2,2
G 6,10
G 3,3
G 7,11
C 8,0,1
C 9,2,3
C 10,4,5
C 11,6,7
Svs 8,196,32,38,0
G 16,16
G 20,24
G 17,17
G 21,25
G 18,18
G 22,26
G 19,19
G 23,27
C 24,16,17
C 25,18,19
C 26,20,21
C 27,22,23
Svs 24,196,32,38,1024
G 32,32
G 36,40
G 33,33
G 37,41
G 34,34
G 38,42
G 35,35
G 39,43
C 40,32,33
C 41,34,35
C 42,36,37
C 43,38,39
Svs 40,196,32,38,2048
G 48,48
G 52,56
G 49,49
G 53,57
G 50,50
G 54,58
G 51,51
G 55,59
C 56,48,49
C 57,50,51
C 58,52,53
C 59,54,55
Svs 56,196,32,38,3072
G 0,64
G 4,72
G 1,65
G 5,73
G 2,66
G 6,74
G 3,67
G 7,75
C 8,0,1
C 9,2,3
C 10,4,5
C 11,6,7
AS 16,38,4096
Svs 8,196,32,16,0
AS 38,38,20480
G 16,4
G 20,12
G 17,5
G 21,13
G 18,6
G 22,14
G 19,7
G 23,15
C 24,16,17
C 25,18,19
C 26,20,21
C 27,22,23
Svs 24,196,32,38,0
G 32,20
G 36,28
G 33,21
G 37,29
G 34,22
G 38,30
G 35,23
G 39,31
C 40,32,33
C 41,34,35
C 42,36,37
C 43,38,39
Svs 40,196,32,38,1024
G 48,36
G 52,44
G 49,37
G 53,45
G 50,38
G 54,46
G 51,39
G 55,47
C 56,48,49
C 57,50,51
C 58,52,53
C 59,54,55
Svs 56,196,32,38,2048
G 0,52
G 4,60
G 1,53
G 5,61
G 2,54
G 6,62
G 3,55
G 7,63
C 8,0,1
C 9,2,3
C 10,4,5
C 11,6,7
Svs 8,196,32,38,3072
G 16,68
G 20,76
G 17,69
G 21,77
G 18,70
G 22,78
G 19,71
G 23,79
C 24,16,17
C 25,18,19
C 26,20,21
C 27,22,23
AS 16,38,4096
Svs 24,196,32,16,0
buffer_load_dwordx2 v[40:41], v100, s[44:47], s65 offen offset:0
buffer_load_dwordx2 v[42:43], v100, s[44:47], s65 offen offset:64
buffer_load_dwordx2 v[44:45], v100, s[44:47], s65 offen offset:128
buffer_load_dwordx2 v[46:47], v100, s[44:47], s65 offen offset:192
buffer_load_dwordx2 v[48:49], v100, s[44:47], s65 offen offset:256
buffer_load_dwordx2 v[50:51], v101, s[48:51], s67 offen offset:0
buffer_load_dwordx2 v[52:53], v101, s[48:51], s67 offen offset:64
buffer_load_dwordx2 v[54:55], v101, s[48:51], s67 offen offset:128
buffer_load_dwordx2 v[56:57], v101, s[48:51], s67 offen offset:192
buffer_load_dwordx2 v[58:59], v101, s[48:51], s67 offen offset:256
s_mov_b64 s[24:25], s[70:71]
SM 26,0x21c000
SM 27,0x20000
s_mov_b64 s[28:29], s[76:77]
SM 30,0x21c000
SM 31,0x20000
s_waitcnt vmcnt(0)
B
s_and_b32 s16, s10, 1
s_cselect_b32 s16, 54, s78
s_cmp_lt_u32 s16, 54
s_cbranch_scc0 NOST1%=
VA 98,10240,196
DWa 98,144,0
DWa 98,148,40960
DWa 98,152,1024
DWa 98,156,41984
DWa 98,160,2048
DWa 98,164,43008
DWa 98,168,3072
DWa 98,172,44032
DWa 98,176,4096
DWa 98,180,45056
DWa 98,184,5120
DWa 98,188,46080
DWa 98,192,6144
DWa 98,196,47104
DWa 98,200,7168
DWa 98,204,48128
DWa 98,208,8192
DWa 98,212,49152
DWa 98,216,9216
DWa 98,220,50176
NOST1%=:
B
s_and_b32 s16, s10, 1
s_cselect_b32 s16, 54, s78
s_cmp_lt_u32 s16, 54
s_cbranch_scc1 PF1%=
Lvs 0,102,32,64,0
Lvs 8,102,32,64,1024
Lvs 16,102,32,64,2048
Lvs 24,102,32,64,3072
AS 21,64,4096
Lvs 32,102,32,21,0
AS 64,64,2621440
Lvs 4,102,32,64,0
Lvs 12,102,32,64,1024
Lvs 20,102,32,64,2048
Lvs 28,102,32,64,3072
AS 21,64,4096
Lvs 36,102,32,21,0
v_and_b32 v108, 7, v254
v_lshlrev_b32 v108, 4, v108
VA 108,152064,108
s_lshl_b32 s16, s11, 7
v_add_u32 v103, s16, v108
LO 110,50
HI 111,50
LO 112,51
HI 113,51
LO 114,52
HI 115,52
LO 116,53
HI 117,53
LO 118,54
HI 119,54
LO 120,55
HI 121,55
LO 122,56
HI 123,56
LO 124,57
HI 125,57
LO 126,58
HI 127,58
LO 128,59
HI 129,59
PA 104,110,112
PA 104,104,114
PA 104,104,116
PA 104,104,118
PA 104,104,120
PA 104,104,122
PA 104,104,124
PA 104,104,126
PA 104,104,128
Fad 99,104,105
s_waitcnt vmcnt(9)
LO 88,0
HI 89,0
s_waitcnt vmcnt(4)
LO 104,4
HI 105,4
PA 88,88,104
LO 90,1
HI 91,1
LO 104,5
HI 105,5
PA 90,90,104
LO 92,2
HI 93,2
LO 104,6
HI 105,6
PA 92,92,104
LO 94,3
HI 95,3
LO 104,7
HI 105,7
PA 94,94,104
v_permlane32_swap_b32 v88, v92
v_permlane32_swap_b32 v89, v93
v_permlane32_swap_b32 v90, v94
v_permlane32_swap_b32 v91, v95
PA 64,88,92
PA 66,90,94
LO 104,40
HI 105,40
LO 106,41
HI 107,41
PA 64,64,104
PA 66,66,106
LO 88,8
HI 89,8
s_waitcnt vmcnt(3)
LO 104,12
HI 105,12
PA 88,88,104
LO 90,9
HI 91,9
LO 104,13
HI 105,13
PA 90,90,104
LO 92,10
HI 93,10
LO 104,14
HI 105,14
PA 92,92,104
LO 94,11
HI 95,11
LO 104,15
HI 105,15
PA 94,94,104
v_permlane32_swap_b32 v88, v92
v_permlane32_swap_b32 v89, v93
v_permlane32_swap_b32 v90, v94
)A"
R"A(v_permlane32_swap_b32 v91, v95
PA 68,88,92
PA 70,90,94
LO 104,42
HI 105,42
LO 106,43
HI 107,43
PA 68,68,104
PA 70,70,106
LO 88,16
HI 89,16
s_waitcnt vmcnt(2)
LO 104,20
HI 105,20
PA 88,88,104
LO 90,17
HI 91,17
LO 104,21
HI 105,21
PA 90,90,104
LO 92,18
HI 93,18
LO 104,22
HI 105,22
PA 92,92,104
LO 94,19
HI 95,19
LO 104,23
HI 105,23
PA 94,94,104
v_permlane32_swap_b32 v88, v92
v_permlane32_swap_b32 v89, v93
v_permlane32_swap_b32 v90, v94
v_permlane32_swap_b32 v91, v95
PA 72,88,92
PA 74,90,94
LO 104,44
HI 105,44
LO 106,45
HI 107,45
PA 72,72,104
PA 74,74,106
LO 88,24
HI 89,24
s_waitcnt vmcnt(1)
LO 104,28
HI 105,28
PA 88,88,104
LO 90,25
HI 91,25
LO 104,29
HI 105,29
PA 90,90,104
LO 92,26
HI 93,26
LO 104,30
HI 105,30
PA 92,92,104
LO 94,27
HI 95,27
LO 104,31
HI 105,31
PA 94,94,104
v_permlane32_swap_b32 v88, v92
v_permlane32_swap_b32 v89, v93
v_permlane32_swap_b32 v90, v94
v_permlane32_swap_b32 v91, v95
PA 76,88,92
PA 78,90,94
LO 104,46
HI 105,46
LO 106,47
HI 107,47
PA 76,76,104
PA 78,78,106
LO 88,32
HI 89,32
s_waitcnt vmcnt(0)
LO 104,36
HI 105,36
PA 88,88,104
LO 90,33
HI 91,33
LO 104,37
HI 105,37
PA 90,90,104
LO 92,34
HI 93,34
LO 104,38
HI 105,38
PA 92,92,104
LO 94,35
HI 95,35
LO 104,39
HI 105,39
PA 94,94,104
v_permlane32_swap_b32 v88, v92
v_permlane32_swap_b32 v89, v93
v_permlane32_swap_b32 v90, v94
v_permlane32_swap_b32 v91, v95
PA 80,88,92
PA 82,90,94
LO 104,48
HI 105,48
LO 106,49
HI 107,49
PA 80,80,104
PA 82,82,106
PA 104,64,66
v_pk_mul_f32 v[106:107], v[64:65], v[64:65]
v_pk_mul_f32 v[88:89], v[64:65], v[110:111]
v_pk_fma_f32 v[106:107], v[66:67], v[66:67], v[106:107]
v_pk_fma_f32 v[88:89], v[66:67], v[112:113], v[88:89]
PA 104,104,68
v_pk_fma_f32 v[106:107], v[68:69], v[68:69], v[106:107]
v_pk_fma_f32 v[88:89], v[68:69], v[114:115], v[88:89]
PA 104,104,70
v_pk_fma_f32 v[106:107], v[70:71], v[70:71], v[106:107]
v_pk_fma_f32 v[88:89], v[70:71], v[116:117], v[88:89]
PA 104,104,72
v_pk_fma_f32 v[106:107], v[72:73], v[72:73], v[106:107]
v_pk_fma_f32 v[88:89], v[72:73], v[118:119], v[88:89]
PA 104,104,74
v_pk_fma_f32 v[106:107], v[74:75], v[74:75], v[106:107]
v_pk_fma_f32 v[88:89], v[74:75], v[120:121], v[88:89]
PA 104,104,76
v_pk_fma_f32 v[106:107], v[76:77], v[76:77], v[106:107]
v_pk_fma_f32 v[88:89], v[76:77], v[122:123], v[88:89]
PA 104,104,78
v_pk_fma_f32 v[106:107], v[78:79], v[78:79], v[106:107]
v_pk_fma_f32 v[88:89], v[78:79], v[124:125], v[88:89]
PA 104,104,80
v_pk_fma_f32 v[106:107], v[80:81], v[80:81], v[106:107]
v_pk_fma_f32 v[88:89], v[80:81], v[126:127], v[88:89]
PA 104,104,82
v_pk_fma_f32 v[106:107], v[82:83], v[82:83], v[106:107]
v_pk_fma_f32 v[88:89], v[82:83], v[128:129], v[88:89]
Fad 96,104,105
Fad 97,106,107
Fad 98,88,89
v_add_f32_dpp v96, v96, v96 row_ror:8 row_mask:0xf bank_mask:0xf
v_add_f32_dpp v97, v97, v97 row_ror:8 row_mask:0xf bank_mask:0xf
v_add_f32_dpp v98, v98, v98 row_ror:8 row_mask:0xf bank_mask:0xf
v_add_f32_dpp v99, v99, v99 row_ror:8 row_mask:0xf bank_mask:0xf
MV 104,96
MV 105,97
MV 106,98
MV 107,99
v_permlane16_swap_b32 v96, v104
v_permlane16_swap_b32 v97, v105
v_permlane16_swap_b32 v98, v106
v_permlane16_swap_b32 v99, v107
Fad 96,96,104
Fad 97,97,105
Fad 98,98,106
Fad 99,99,107
MV 104,96
MV 105,97
MV 106,98
MV 107,99
v_permlane32_swap_b32 v96, v104
v_permlane32_swap_b32 v97, v105
v_permlane32_swap_b32 v98, v106
v_permlane32_swap_b32 v99, v107
Fad 96,96,104
Fad 97,97,105
Fad 98,98,106
Fad 99,99,107
ds_write_b128 v103, v[96:99]
W 0
B
Rv 0,108,0
Rv 4,108,128
Rv 8,108,256
Rv 12,108,384
W 2
PA 0,0,4
PA 2,2,6
W 0
PA 8,8,12
PA 10,10,14
PA 0,0,8
PA 2,2,10
v_mul_f32 v88, 0x3acccccd, v0
v_mul_f32 v90, 0x3acccccd, v1
v_fma_f32 v90, -v88, v88, v90
v_max_f32 v90, 0, v90
v_add_f32 v91, 0x3727c5ac, v90
v_rsq_f32 v91, v91
v_fma_f32 v92, -v88, v3, v2
Fmu 92,92,91
v_add_f32 v104, 1.0, v91
Fmu 105,88,91
v_xor_b32 v105, 0x80000000, v105
FM 64,64,104,105
FM 65,65,104,105
FM 66,66,104,105
FM 67,67,104,105
C 24,64,65
C 25,66,67
FM 68,68,104,105
FM 69,69,104,105
FM 70,70,104,105
FM 71,71,104,105
C 26,68,69
C 27,70,71
FM 72,72,104,105
FM 73,73,104,105
FM 74,74,104,105
FM 75,75,104,105
C 28,72,73
C 29,74,75
FM 76,76,104,105
FM 77,77,104,105
FM 78,78,104,105
FM 79,79,104,105
C 30,76,77
C 31,78,79
FM 80,80,104,105
FM 81,81,104,105
FM 82,82,104,105
FM 83,83,104,105
C 32,80,81
C 33,82,83
B
v_mov_b32 v162, 151552
ds_read_b32 v160, v162
W 0
v_readfirstlane_b32 s16, v160
N 3
s_and_b32 s68, s16, 0xffff
s_lshr_b32 s69, s16, 16
v_and_b32 v160, 7, v254
v_add_u32 v160, s66, v160
v_cmp_ne_u32 vcc, s68, v160
v_cmp_ne_u32 s[16:17], s69, v160
N 4
s_and_b64 exec, vcc, s[16:17]
buffer_store_dwordx2 v[24:25], v100, s[52:55], s65 offen offset:0 sc1
buffer_store_dwordx2 v[26:27], v100, s[52:55], s65 offen offset:64 sc1
buffer_store_dwordx2 v[28:29], v100, s[52:55], s65 offen offset:128 sc1
buffer_store_dwordx2 v[30:31], v100, s[52:55], s65 offen offset:192 sc1
buffer_store_dwordx2 v[32:33], v100, s[52:55], s65 offen offset:256 sc1
s_mov_b64 exec, -1
s_branch PE1%=
PF1%=:
Lvs 0,102,32,64,0
Lvs 8,102,32,64,1024
Lvs 16,102,32,64,2048
Lvs 24,102,32,64,3072
AS 21,64,4096
Lvs 32,102,32,21,0
AS 64,64,2621440
Lvs 4,102,32,64,0
Lvs 12,102,32,64,1024
Lvs 20,102,32,64,2048
Lvs 28,102,32,64,3072
AS 21,64,4096
Lvs 36,102,32,21,0
v_and_b32 v108, 7, v254
v_lshlrev_b32 v108, 4, v108
VA 108,152064,108
s_lshl_b32 s16, s11, 7
v_add_u32 v103, s16, v108
LO 110,50
HI 111,50
LO 112,51
HI 113,51
LO 114,52
HI 115,52
LO 116,53
HI 117,53
LO 118,54
HI 119,54
LO 120,55
HI 121,55
LO 122,56
HI 123,56
LO 124,57
HI 125,57
LO 126,58
HI 127,58
LO 128,59
HI 129,59
PA 104,110,112
PA 104,104,114
PA 104,104,116
PA 104,104,118
PA 104,104,120
PA 104,104,122
PA 104,104,124
PA 104,104,126
PA 104,104,128
Fad 99,104,105
s_waitcnt vmcnt(9)
LO 88,0
HI 89,0
s_waitcnt vmcnt(4)
LO 104,4
HI 105,4
PA 88,88,104
LO 90,1
HI 91,1
LO 104,5
HI 105,5
PA 90,90,104
LO 92,2
HI 93,2
LO 104,6
HI 105,6
PA 92,92,104
LO 94,3
HI 95,3
LO 104,7
HI 105,7
PA 94,94,104
v_permlane32_swap_b32 v88, v92
v_permlane32_swap_b32 v89, v93
v_permlane32_swap_b32 v90, v94
v_permlane32_swap_b32 v91, v95
PA 64,88,92
PA 66,90,94
LO 104,40
HI 105,40
LO 106,41
HI 107,41
PA 64,64,104
PA 66,66,106
LO 88,8
HI 89,8
s_waitcnt vmcnt(3)
LO 104,12
HI 105,12
PA 88,88,104
LO 90,9
HI 91,9
LO 104,13
HI 105,13
PA 90,90,104
LO 92,10
HI 93,10
LO 104,14
HI 105,14
PA 92,92,104
LO 94,11
HI 95,11
LO 104,15
HI 105,15
PA 94,94,104
v_permlane32_swap_b32 v88, v92
v_permlane32_swap_b32 v89, v93
v_permlane32_swap_b32 v90, v94
v_permlane32_swap_b32 v91, v95
PA 68,88,92
PA 70,90,94
LO 104,42
HI 105,42
LO 106,43
HI 107,43
PA 68,68,104
PA 70,70,106
LO 88,16
HI 89,16
s_waitcnt vmcnt(2)
LO 104,20
HI 105,20
PA 88,88,104
LO 90,17
HI 91,17
LO 104,21
HI 105,21
PA 90,90,104
LO 92,18
HI 93,18
LO 104,22
HI 105,22
PA 92,92,104
LO 94,19
HI 95,19
LO 104,23
HI 105,23
PA 94,94,104
v_permlane32_swap_b32 v88, v92
v_permlane32_swap_b32 v89, v93
v_permlane32_swap_b32 v90, v94
v_permlane32_swap_b32 v91, v95
PA 72,88,92
PA 74,90,94
LO 104,44
HI 105,44
LO 106,45
HI 107,45
PA 72,72,104
PA 74,74,106
LO 88,24
HI 89,24
s_waitcnt vmcnt(1)
LO 104,28
HI 105,28
PA 88,88,104
LO 90,25
HI 91,25
LO 104,29
HI 105,29
PA 90,90,104
LO 92,26
HI 93,26
LO 104,30
HI 105,30
PA 92,92,104
LO 94,27
HI 95,27
LO 104,31
HI 105,31
PA 94,94,104
v_permlane32_swap_b32 v88, v92
v_permlane32_swap_b32 v89, v93
v_permlane32_swap_b32 v90, v94
v_permlane32_swap_b32 v91, v95
PA 76,88,92
PA 78,90,94
LO 104,46
HI 105,46
LO 106,47
HI 107,47
PA 76,76,104
PA 78,78,106
LO 88,32
HI 89,32
s_waitcnt vmcnt(0)
LO 104,36
HI 105,36
PA 88,88,104
LO 90,33
HI 91,33
LO 104,37
HI 105,37
PA 90,90,104
LO 92,34
HI 93,34
LO 104,38
HI 105,38
PA 92,92,104
LO 94,35
HI 95,35
LO 104,39
HI 105,39
PA 94,94,104
v_permlane32_swap_b32 v88, v92
v_permlane32_swap_b32 v89, v93
v_permlane32_swap_b32 v90, v94
v_permlane32_swap_b32 v91, v95
PA 80,88,92
PA 82,90,94
LO 104,48
HI 105,48
LO 106,49
HI 107,49
PA 80,80,104
PA 82,82,106
PA 104,64,66
v_pk_mul_f32 v[106:107], v[64:65], v[64:65]
v_pk_mul_f32 v[88:89], v[64:65], v[110:111]
v_pk_fma_f32 v[106:107], v[66:67], v[66:67], v[106:107]
v_pk_fma_f32 v[88:89], v[66:67], v[112:113], v[88:89]
PA 104,104,68
v_pk_fma_f32 v[106:107], v[68:69], v[68:69], v[106:107]
v_pk_fma_f32 v[88:89], v[68:69], v[114:115], v[88:89]
PA 104,104,70
v_pk_fma_f32 v[106:107], v[70:71], v[70:71], v[106:107]
v_pk_fma_f32 v[88:89], v[70:71], v[116:117], v[88:89]
PA 104,104,72
v_pk_fma_f32 v[106:107], v[72:73], v[72:73], v[106:107]
v_pk_fma_f32 v[88:89], v[72:73], v[118:119], v[88:89]
PA 104,104,74
v_pk_fma_f32 v[106:107], v[74:75], v[74:75], v[106:107]
v_pk_fma_f32 v[88:89], v[74:75], v[120:121], v[88:89]
PA 104,104,76
v_pk_fma_f32 v[106:107], v[76:77], v[76:77], v[106:107]
v_pk_fma_f32 v[88:89], v[76:77], v[122:123], v[88:89]
PA 104,104,78
v_pk_fma_f32 v[106:107], v[78:79], v[78:79], v[106:107]
v_pk_fma_f32 v[88:89], v[78:79], v[124:125], v[88:89]
PA 104,104,80
v_pk_fma_f32 v[106:107], v[80:81], v[80:81], v[106:107]
v_pk_fma_f32 v[88:89], v[80:81], v[126:127], v[88:89]
PA 104,104,82
v_pk_fma_f32 v[106:107], v[82:83], v[82:83], v[106:107]
v_pk_fma_f32 v[88:89], v[82:83], v[128:129], v[88:89]
Fad 96,104,105
Fad 97,106,107
Fad 98,88,89
v_add_f32_dpp v96, v96, v96 row_ror:8 row_mask:0xf bank_mask:0xf
v_add_f32_dpp v97, v97, v97 row_ror:8 row_mask:0xf bank_mask:0xf
v_add_f32_dpp v98, v98, v98 row_ror:8 row_mask:0xf bank_mask:0xf
v_add_f32_dpp v99, v99, v99 row_ror:8 row_mask:0xf bank_mask:0xf
MV 104,96
MV 105,97
MV 106,98
MV 107,99
v_permlane16_swap_b32 v96, v104
v_permlane16_swap_b32 v97, v105
v_permlane16_swap_b32 v98, v106
v_permlane16_swap_b32 v99, v107
Fad 96,96,104
Fad 97,97,105
Fad 98,98,106
Fad 99,99,107
MV 104,96
MV 105,97
MV 106,98
MV 107,99
v_permlane32_swap_b32 v96, v104
v_permlane32_swap_b32 v97, v105
v_permlane32_swap_b32 v98, v106
v_permlane32_swap_b32 v99, v107
Fad 96,96,104
Fad 97,97,105
Fad 98,98,106
Fad 99,99,107
ds_write_b128 v103, v[96:99]
W 0
B
Rv 0,108,0
Rv 4,108,128
Rv 8,108,256
Rv 12,108,384
W 2
PA 0,0,4
PA 2,2,6
W 0
PA 8,8,12
PA 10,10,14
PA 0,0,8
PA 2,2,10
v_mul_f32 v88, 0x3acccccd, v0
v_mul_f32 v90, 0x3acccccd, v1
v_fma_f32 v90, -v88, v88, v90
v_max_f32 v90, 0, v90
v_add_f32 v91, 0x3727c5ac, v90
v_rsq_f32 v91, v91
v_fma_f32 v92, -v88, v3, v2
Fmu 92,92,91
v_add_f32 v104, 1.0, v91
Fmu 105,88,91
v_xor_b32 v105, 0x80000000, v105
FM 64,64,104,105
FM 65,65,104,105
FM 66,66,104,105
FM 67,67,104,105
C 24,64,65
C 25,66,67
FM 68,68,104,105
FM 69,69,104,105
FM 70,70,104,105
FM 71,71,104,105
C 26,68,69
C 27,70,71
FM 72,72,104,105
FM 73,73,104,105
FM 74,74,104,105
FM 75,75,104,105
C 28,72,73
C 29,74,75
FM 76,76,104,105
FM 77,77,104,105
FM 78,78,104,105
FM 79,79,104,105
C 30,76,77
C 31,78,79
FM 80,80,104,105
FM 81,81,104,105
FM 82,82,104,105
FM 83,83,104,105
C 32,80,81
C 33,82,83
GW 224,v24
GW 225,v25
GW 226,v26
GW 227,v27
GW 228,v28
GW 229,v29
GW 230,v30
GW 231,v31
GW 232,v32
GW 233,v33
GW 234,v100
s_add_u32 s36, s14, 0x10000
s_addc_u32 s37, s15, 0
SM 38,0x8000
SM 39,0x20000
s_lshl_b32 s17, s11, 10
SM 20,0
SW_F1%=:
Lvs 128,196,36,17,0
AS 16,17,4096
Lvs 132,196,36,16,0
AS 16,17,8192
Lvs 136,196,36,16,0
AS 16,17,12288
Lvs 140,196,36,16,0
AS 16,17,16384
Lvs 144,196,36,16,0
AS 16,17,20480
Lvs 148,196,36,16,0
AS 16,17,24576
Lvs 152,196,36,16,0
AS 16,17,28672
Lvs 156,196,36,16,0
s_waitcnt vmcnt(0)
v_xor_b32 v160, s79, v131
v_xor_b32 v161, s79, v135
v_or_b32 v160, v160, v161
v_xor_b32 v161, s79, v139
v_or_b32 v160, v160, v161
v_xor_b32 v161, s79, v143
v_or_b32 v160, v160, v161
v_xor_b32 v161, s79, v147
v_or_b32 v160, v160, v161
v_xor_b32 v161, s79, v151
v_or_b32 v160, v160, v161
v_xor_b32 v161, s79, v155
v_or_b32 v160, v160, v161
v_xor_b32 v161, s79, v159
v_or_b32 v160, v160, v161
v_cmp_ne_u32 vcc, 0, v160
N 4
s_cbranch_vccz SWOK_F1%=
AS 20,20,1
s_cmp_lt_u32 s20, 0x10000
s_cbranch_scc0 SWOK_F1%=
s_sleep 1
s_branch SW_F1%=
SWOK_F1%=:
s_lshl_b32 s16, s11, 6
v_add_u32 v161, s16, v254
v_sub_u32 v161, 2047, v161
v_ashrrev_i32 v160, 31, v128
v_or_b32 v160, 0x80000000, v160
v_xor_b32 v160, v160, v128
v_cmp_o_f32 vcc, v128, v128
v_and_b32 v160, 0xfffff800, v160
N 0
v_cndmask_b32 v160, 0, v160, vcc
)A"
R"A(v_subrev_u32 v162, 0, v161
v_or_b32 v128, v160, v162
v_ashrrev_i32 v160, 31, v132
v_or_b32 v160, 0x80000000, v160
v_xor_b32 v160, v160, v132
v_cmp_o_f32 vcc, v132, v132
v_and_b32 v160, 0xfffff800, v160
N 0
v_cndmask_b32 v160, 0, v160, vcc
v_subrev_u32 v162, 256, v161
v_or_b32 v132, v160, v162
v_ashrrev_i32 v160, 31, v136
v_or_b32 v160, 0x80000000, v160
v_xor_b32 v160, v160, v136
v_cmp_o_f32 vcc, v136, v136
v_and_b32 v160, 0xfffff800, v160
N 0
v_cndmask_b32 v160, 0, v160, vcc
v_subrev_u32 v162, 512, v161
v_or_b32 v136, v160, v162
v_ashrrev_i32 v160, 31, v140
v_or_b32 v160, 0x80000000, v160
v_xor_b32 v160, v160, v140
v_cmp_o_f32 vcc, v140, v140
v_and_b32 v160, 0xfffff800, v160
N 0
v_cndmask_b32 v160, 0, v160, vcc
v_subrev_u32 v162, 768, v161
v_or_b32 v140, v160, v162
v_ashrrev_i32 v160, 31, v144
v_or_b32 v160, 0x80000000, v160
v_xor_b32 v160, v160, v144
v_cmp_o_f32 vcc, v144, v144
v_and_b32 v160, 0xfffff800, v160
N 0
v_cndmask_b32 v160, 0, v160, vcc
v_subrev_u32 v162, 1024, v161
v_or_b32 v144, v160, v162
v_ashrrev_i32 v160, 31, v148
v_or_b32 v160, 0x80000000, v160
v_xor_b32 v160, v160, v148
v_cmp_o_f32 vcc, v148, v148
v_and_b32 v160, 0xfffff800, v160
N 0
v_cndmask_b32 v160, 0, v160, vcc
v_subrev_u32 v162, 1280, v161
v_or_b32 v148, v160, v162
v_ashrrev_i32 v160, 31, v152
v_or_b32 v160, 0x80000000, v160
v_xor_b32 v160, v160, v152
v_cmp_o_f32 vcc, v152, v152
v_and_b32 v160, 0xfffff800, v160
N 0
v_cndmask_b32 v160, 0, v160, vcc
v_subrev_u32 v162, 1536, v161
v_or_b32 v152, v160, v162
v_ashrrev_i32 v160, 31, v156
v_or_b32 v160, 0x80000000, v160
v_xor_b32 v160, v160, v156
v_cmp_o_f32 vcc, v156, v156
v_and_b32 v160, 0xfffff800, v160
N 0
v_cndmask_b32 v160, 0, v160, vcc
v_subrev_u32 v162, 1792, v161
v_or_b32 v156, v160, v162
MV 164,128
v_mov_b32 v165, 0
v_mov_b32 v166, 0
v_min_u32 v160, v164, v132
v_max_u32 v164, v164, v132
v_max_u32 v165, v165, v166
v_max_u32 v165, v165, v160
v_min_u32 v160, v164, v136
v_max_u32 v164, v164, v136
v_max_u32 v165, v165, v166
v_max_u32 v165, v165, v160
v_min_u32 v160, v164, v140
v_max_u32 v164, v164, v140
v_max_u32 v165, v165, v166
v_max_u32 v165, v165, v160
v_min_u32 v160, v164, v144
v_max_u32 v164, v164, v144
v_max_u32 v165, v165, v166
v_max_u32 v165, v165, v160
v_min_u32 v160, v164, v148
v_max_u32 v164, v164, v148
v_max_u32 v165, v165, v166
v_max_u32 v165, v165, v160
v_min_u32 v160, v164, v152
v_max_u32 v164, v164, v152
v_max_u32 v165, v165, v166
v_max_u32 v165, v165, v160
v_min_u32 v160, v164, v156
v_max_u32 v164, v164, v156
v_max_u32 v165, v165, v166
v_max_u32 v165, v165, v160
v_mov_b32_dpp v167, v164 quad_perm:[1,0,3,2] row_mask:0xf bank_mask:0xf
N 0
v_mov_b32_dpp v168, v165 quad_perm:[1,0,3,2] row_mask:0xf bank_mask:0xf
v_min_u32 v160, v164, v167
v_max_u32 v164, v164, v167
v_max_u32 v165, v165, v168
v_max_u32 v165, v165, v160
v_mov_b32_dpp v167, v164 quad_perm:[2,3,0,1] row_mask:0xf bank_mask:0xf
N 0
v_mov_b32_dpp v168, v165 quad_perm:[2,3,0,1] row_mask:0xf bank_mask:0xf
v_min_u32 v160, v164, v167
v_max_u32 v164, v164, v167
v_max_u32 v165, v165, v168
v_max_u32 v165, v165, v160
v_mov_b32_dpp v167, v164 row_half_mirror row_mask:0xf bank_mask:0xf
N 0
v_mov_b32_dpp v168, v165 row_half_mirror row_mask:0xf bank_mask:0xf
v_min_u32 v160, v164, v167
v_max_u32 v164, v164, v167
v_max_u32 v165, v165, v168
v_max_u32 v165, v165, v160
v_mov_b32_dpp v167, v164 row_mirror row_mask:0xf bank_mask:0xf
N 0
v_mov_b32_dpp v168, v165 row_mirror row_mask:0xf bank_mask:0xf
v_min_u32 v160, v164, v167
v_max_u32 v164, v164, v167
v_max_u32 v165, v165, v168
v_max_u32 v165, v165, v160
MV 167,164
MV 168,165
N 0
v_permlane16_swap_b32 v164, v167
v_permlane16_swap_b32 v165, v168
v_min_u32 v160, v164, v167
v_max_u32 v164, v164, v167
v_max_u32 v165, v165, v168
v_max_u32 v165, v165, v160
MV 167,164
MV 168,165
N 0
v_permlane32_swap_b32 v164, v167
v_permlane32_swap_b32 v165, v168
v_min_u32 v160, v164, v167
v_max_u32 v164, v164, v167
v_max_u32 v165, v165, v168
v_max_u32 v165, v165, v160
v_mov_b32 v169, 151560
ds_write_b64 v169, v[164:165]
W 0
B
v_mov_b32 v169, 151552
ds_read_b128 v[172:175], v169
Rv 176,169,16
W 1
v_min_u32 v160, v172, v174
v_max_u32 v172, v172, v174
v_max_u32 v173, v173, v175
v_max_u32 v173, v173, v160
W 0
v_min_u32 v160, v172, v176
v_max_u32 v172, v172, v176
v_max_u32 v173, v173, v177
v_max_u32 v173, v173, v160
v_min_u32 v160, v172, v178
v_max_u32 v172, v172, v178
v_max_u32 v173, v173, v179
v_max_u32 v173, v173, v160
v_and_b32 v172, 2047, v172
v_and_b32 v173, 2047, v173
v_sub_u32 v172, 2047, v172
v_sub_u32 v173, 2047, v173
v_readfirstlane_b32 s68, v172
v_readfirstlane_b32 s69, v173
v_and_b32 v208, 1, v254
v_lshrrev_b32 v209, 1, v254
v_and_b32 v209, 3, v209
v_lshrrev_b32 v226, 3, v254
VA 226,5,226
v_mov_b32 v210, s68
v_mov_b32 v211, s69
v_cmp_eq_u32 vcc, 1, v208
N 1
v_cndmask_b32 v210, v210, v211, vcc
v_lshrrev_b32 v212, 4, v210
v_mul_u32_u24 v212, 20480, v212
v_lshl_add_u32 v212, v226, 10, v212
v_and_b32 v213, 15, v210
v_lshl_add_u32 v213, v209, 4, v213
v_lshl_add_u32 v212, v213, 4, v212
v_mul_u32_u24 v213, 1280, v210
v_lshl_add_u32 v213, v209, 3, v213
v_lshl_add_u32 v213, v226, 6, v213
v_lshlrev_b32 v214, 4, v210
s_mov_b32 exec_lo, -1
s_mov_b32 exec_hi, 0xff
buffer_load_dwordx4 v[216:219], v214, s[36:39], 0 offen offset:0 sc1
SM 18,0
Lvs 64,212,32,18,0
SM 18,2621440
Lvs 68,212,32,18,0
SM 18,5242880
Lvs 72,212,32,18,0
SM 18,7864320
Lvs 76,212,32,18,0
buffer_load_dwordx2 v[0:1], v213, s[44:47], 0 offen offset:0
buffer_load_dwordx2 v[2:3], v213, s[44:47], 0 offen offset:32
s_mov_b64 exec, -1
s_mul_i32 s17, s78, 40960
AS 17,17,10240
M0 92160
N 0
D 196,72,17,0
D 196,72,17,1024
D 196,72,17,2048
D 196,72,17,3072
M0 96256
AS 17,17,4096
D 196,72,17,0
D 196,72,17,1024
D 196,72,17,2048
D 196,72,17,3072
M0 100352
AS 17,17,4096
D 196,72,17,0
D 196,72,17,1024
v_lshrrev_b32 v222, 4, v254
v_and_b32 v223, 15, v254
v_lshrrev_b32 v223, 2, v223
v_lshl_add_u32 v223, v222, 2, v223
v_and_b32 v222, 3, v254
v_lshlrev_b32 v222, 3, v222
v_lshl_add_u32 v224, v223, 6, v222
VA 224,32,224
v_mul_u32_u24 v225, 1280, v223
v_add_u32 v225, v225, v222
VA 225,81920,225
Ta 0,224,0
Ta 2,224,1024
Ta 4,224,2048
Ta 6,224,3072
Ta 8,224,4096
Ta 10,224,5120
Ta 12,224,6144
Ta 14,224,7168
Ta 16,224,8192
Ta 18,224,9216
Ta 20,224,10240
Ta 22,224,11264
Ta 24,224,12288
Ta 26,224,13312
Ta 28,224,14336
Ta 30,224,15360
Ta 32,224,16384
Ta 34,224,17408
Ta 36,224,18432
Ta 38,224,19456
Ta 40,224,20480
Ta 42,224,21504
Ta 44,224,22528
Ta 46,224,23552
Ta 48,224,24576
Ta 50,224,25600
Ta 52,224,26624
Ta 54,224,27648
Ta 56,224,28672
Ta 58,224,29696
Ta 60,224,30720
Ta 62,224,31744
Ta 64,224,32768
Ta 66,224,33792
Ta 68,224,34816
Ta 70,224,35840
Ta 72,224,36864
Ta 74,224,37888
Ta 76,224,38912
Ta 78,224,39936
v_lshlrev_b32 v221, 10, v226
v_lshl_add_u32 v220, v209, 4, v208
v_lshl_add_u32 v221, v220, 4, v221
VA 227,122880,221
v_mul_u32_u24 v221, 2560, v208
v_lshl_add_u32 v221, v209, 4, v221
v_lshl_add_u32 v221, v226, 7, v221
VA 221,143360,221
VA 220,122880,196
s_waitcnt vmcnt(0)
v_add_f32 v222, 1.0, v218
Fmu 223,217,218
v_xor_b32 v223, 0x80000000, v223
LO 40,64
HI 41,64
LO 228,68
HI 229,68
PA 40,40,228
LO 228,72
HI 229,72
PA 40,40,228
LO 228,76
HI 229,76
PA 40,40,228
LO 228,0
HI 229,0
PA 40,40,228
LO 42,65
HI 43,65
LO 228,69
HI 229,69
PA 42,42,228
LO 228,73
HI 229,73
PA 42,42,228
LO 228,77
HI 229,77
PA 42,42,228
LO 228,1
HI 229,1
PA 42,42,228
LO 44,66
HI 45,66
LO 228,70
HI 229,70
PA 44,44,228
LO 228,74
HI 229,74
PA 44,44,228
LO 228,78
HI 229,78
PA 44,44,228
LO 228,2
HI 229,2
PA 44,44,228
LO 46,67
HI 47,67
LO 228,71
HI 229,71
PA 46,46,228
LO 228,75
HI 229,75
PA 46,46,228
LO 228,79
HI 229,79
PA 46,46,228
LO 228,3
HI 229,3
PA 46,46,228
FM 48,40,218,223
FM 49,41,218,223
FM 50,42,218,223
FM 51,43,218,223
FM 52,44,218,223
FM 53,45,218,223
FM 54,46,218,223
FM 55,47,218,223
C 56,48,49
C 57,50,51
C 58,52,53
C 59,54,55
FM 40,40,222,223
FM 41,41,222,223
FM 42,42,222,223
FM 43,43,222,223
FM 44,44,222,223
FM 45,45,222,223
FM 46,46,222,223
FM 47,47,222,223
s_mov_b32 exec_lo, -1
s_mov_b32 exec_hi, 0xff
ds_write_b128 v227, v[56:59]
ds_write_b128 v221, v[40:43]
DWv 221,44,64
s_mov_b64 exec, -1
W 0
B
Rv 0,220,0
Rv 4,220,1024
Rv 8,220,2048
Rv 12,220,3072
Rv 16,220,4096
Rv 20,220,5120
Rv 24,220,6144
Rv 28,220,7168
Rv 32,220,8192
Rv 36,220,9216
Rv 40,220,10240
Rv 44,220,11264
Rv 48,220,12288
Rv 52,220,13312
Rv 56,220,14336
Rv 60,220,15360
Rv 64,220,16384
Rv 68,220,17408
Rv 72,220,18432
Rv 76,220,19456
Ta 80,225,320
Ta 82,225,20800
Ta 84,225,352
Ta 86,225,20832
Ta 88,225,384
Ta 90,225,20864
Ta 92,225,416
Ta 94,225,20896
Ta 96,225,448
Ta 98,225,20928
Ta 100,225,480
Ta 102,225,20960
Ta 104,225,512
Ta 106,225,20992
Ta 108,225,544
Ta 110,225,21024
Ta 112,225,576
Ta 114,225,21056
Ta 116,225,608
Ta 118,225,21088
W 15
Mvavz 180,0,0
W 15
Mvavz 184,4,4
W 15
Mvavc 180,8,8
W 15
Mvavc 184,12,12
W 15
Mvavc 180,16,16
W 15
Mvavc 184,20,20
W 15
Mvavc 180,24,24
W 15
Mvavc 184,28,28
W 15
Mvavc 180,32,32
W 15
Mvavc 184,36,36
W 15
Mvavc 180,40,40
W 15
Mvavc 184,44,44
W 15
Mvavc 180,48,48
W 15
Mvavc 184,52,52
W 15
Mvavc 180,56,56
W 15
Mvavc 184,60,60
W 15
Mvavc 180,64,64
W 15
Mvavc 184,68,68
W 15
Mvavc 180,72,72
W 15
Mvavc 184,76,76
VA 215,149504,196
N 6
Fad 160,180,184
Fad 161,181,185
Fad 162,182,186
Fad 163,183,187
v_mul_f32 v168, 0xbfb8aa3b, v160
E 168
N 0
v_add_f32 v168, 1.0, v168
v_rcp_f32 v168, v168
N 0
Fmu 168,168,160
v_mul_f32 v169, 0xbfb8aa3b, v161
E 169
N 0
v_add_f32 v169, 1.0, v169
v_rcp_f32 v169, v169
N 0
Fmu 169,169,161
v_mul_f32 v170, 0xbfb8aa3b, v162
E 170
N 0
v_add_f32 v170, 1.0, v170
v_rcp_f32 v170, v170
N 0
Fmu 170,170,162
v_mul_f32 v171, 0xbfb8aa3b, v163
E 171
N 0
v_add_f32 v171, 1.0, v171
v_rcp_f32 v171, v171
N 0
Fmu 171,171,163
W 0
B
ds_read_b128 v[164:167], v215
W 0
Fmu 168,168,164
Fmu 169,169,165
Fmu 170,170,166
Fmu 171,171,167
C 172,168,169
C 173,170,171
VA 215,150536,196
ds_write_b64 v215, v[172:173]
s_add_u32 s40, s14, 0x5420000
s_addc_u32 s41, s15, 0
SM 42,0x43800
SM 43,0x20000
v_and_b32 v208, 15, v254
v_mul_u32_u24 v208, 2560, v208
v_lshrrev_b32 v209, 4, v254
v_lshl_add_u32 v208, v209, 4, v208
s_mul_i32 s16, s78, 5120
AS 16,16,640
W 0
B
VA 215,150528,196
ds_read_b128 v[176:179], v215
W 0
Mvavz 48,80,176
Mvavz 52,84,176
Mvavz 56,88,176
Mvavz 60,92,176
Mvavz 64,96,176
Mvavz 68,100,176
Mvavz 72,104,176
Mvavz 76,108,176
Mvavz 80,112,176
Mvavz 84,116,176
s_mov_b32 exec_lo, 0x00030003
s_mov_b32 exec_hi, 0x00030003
Svs 48,208,40,16,0
Svs 52,208,40,16,64
Svs 56,208,40,16,128
Svs 60,208,40,16,192
Svs 64,208,40,16,256
Svs 68,208,40,16,320
Svs 72,208,40,16,384
Svs 76,208,40,16,448
Svs 80,208,40,16,512
Svs 84,208,40,16,576
s_mov_b64 exec, -1
s_waitcnt vmcnt(0)
B
G 24,224
G 25,225
G 26,226
G 27,227
G 28,228
G 29,229
G 30,230
G 31,231
G 32,232
G 33,233
G 100,234
v_and_b32 v160, 7, v254
v_add_u32 v160, s66, v160
v_cmp_ne_u32 vcc, s68, v160
v_cmp_ne_u32 s[16:17], s69, v160
N 4
s_and_b64 exec, vcc, s[16:17]
buffer_store_dwordx2 v[24:25], v100, s[52:55], s65 offen offset:0 sc1
buffer_store_dwordx2 v[26:27], v100, s[52:55], s65 offen offset:64 sc1
buffer_store_dwordx2 v[28:29], v100, s[52:55], s65 offen offset:128 sc1
buffer_store_dwordx2 v[30:31], v100, s[52:55], s65 offen offset:192 sc1
buffer_store_dwordx2 v[32:33], v100, s[52:55], s65 offen offset:256 sc1
s_mov_b64 exec, -1
s_cmp_lt_u32 s78, 20
s_cbranch_scc0 PE1%=
B
s_lshl_b32 s16, s78, 8
v_lshlrev_b32 v200, 2, v254
AS 16,16,71680
buffer_load_dword v0, v200, s[40:43], s16 offen sc1
AS 16,16,5120
buffer_load_dword v1, v200, s[40:43], s16 offen sc1
AS 16,16,5120
buffer_load_dword v2, v200, s[40:43], s16 offen sc1
AS 16,16,5120
buffer_load_dword v3, v200, s[40:43], s16 offen sc1
AS 16,16,5120
buffer_load_dword v4, v200, s[40:43], s16 offen sc1
AS 16,16,5120
buffer_load_dword v5, v200, s[40:43], s16 offen sc1
AS 16,16,5120
buffer_load_dword v6, v200, s[40:43], s16 offen sc1
AS 16,16,5120
buffer_load_dword v7, v200, s[40:43], s16 offen sc1
AS 16,16,5120
buffer_load_dword v8, v200, s[40:43], s16 offen sc1
AS 16,16,5120
buffer_load_dword v9, v200, s[40:43], s16 offen sc1
AS 16,16,5120
buffer_load_dword v10, v200, s[40:43], s16 offen sc1
)A"
R"A(AS 16,16,5120
buffer_load_dword v11, v200, s[40:43], s16 offen sc1
AS 16,16,5120
buffer_load_dword v12, v200, s[40:43], s16 offen sc1
AS 16,16,5120
buffer_load_dword v13, v200, s[40:43], s16 offen sc1
AS 16,16,5120
s_waitcnt vmcnt(12)
Fad 0,0,1
s_waitcnt vmcnt(11)
Fad 0,0,2
s_waitcnt vmcnt(10)
Fad 0,0,3
s_waitcnt vmcnt(9)
Fad 0,0,4
s_waitcnt vmcnt(8)
Fad 0,0,5
s_waitcnt vmcnt(7)
Fad 0,0,6
s_waitcnt vmcnt(6)
Fad 0,0,7
s_waitcnt vmcnt(5)
Fad 0,0,8
s_waitcnt vmcnt(4)
Fad 0,0,9
s_waitcnt vmcnt(3)
Fad 0,0,10
s_waitcnt vmcnt(2)
Fad 0,0,11
s_waitcnt vmcnt(1)
Fad 0,0,12
s_waitcnt vmcnt(0)
Fad 0,0,13
VA 201,151808,200
ds_write_b32 v201, v0 offset:256
W 0
B
PE1%=:
OE1%=:
s_branch FM1_DONE%=
XN1_SPIN%=:
SM 20,0
XN1_L%=:
s_sleep 2
global_load_dword v246, v247, s[14:15] sc1
s_waitcnt vmcnt(0)
v_cmp_ne_u32 vcc, s79, v246
N 4
s_cbranch_vccz XN1_OK%=
AS 20,20,1
s_cmp_lt_u32 s20, 65536
s_cbranch_scc1 XN1_L%=
s_branch XN1_OK%=
KP1_SPIN%=:
SM 20,0
KP1_L%=:
s_sleep 2
global_load_dword v246, v247, s[14:15] sc1
s_waitcnt vmcnt(0)
v_cmp_ne_u32 vcc, s79, v246
N 4
s_cbranch_vccz KP1_OK%=
AS 20,20,1
s_cmp_lt_u32 s20, 65536
s_cbranch_scc1 KP1_L%=
s_branch KP1_OK%=
GOSLOW1%=:
s_cmp_eq_u32 s17, 0
s_cbranch_scc1 SBACK1%=
s_getpc_b64 s[22:23]
SRET1%=:
s_add_u32 s22, s22, SBACK1%=-SRET1%=
s_addc_u32 s23, s23, 0
s_getpc_b64 s[16:17]
SJ1%=:
s_add_u32 s16, s16, SLOW%=-SJ1%=
s_addc_u32 s17, s17, 0
s_setpc_b64 s[16:17]
SBACK1%=:
s_and_b32 s16, s10, 7
s_lshr_b32 s41, s16, 1
s_and_b32 s16, s16, 1
s_lshl_b32 s16, s16, 3
s_lshr_b32 s17, s10, 3
s_lshr_b32 s18, s17, 2
s_add_u32 s39, s16, s18
s_and_b32 s40, s17, 3
s_lshl_b32 s16, s41, 2
s_add_u32 s43, s16, s40
s_mov_b64 s[24:25], s[50:51]
SM 26,0x500000
SM 27,0x20000
s_lshl_b32 s16, s41, 10
s_lshl_b32 s17, s11, 5
s_add_u32 s16, s16, s17
s_mul_i32 s16, s16, 1280
s_mul_i32 s17, s40, 320
s_add_u32 s36, s16, s17
s_waitcnt vmcnt(0)
B
s_getpc_b64 s[16:17]
SK1%=:
s_add_u32 s16, s16, MDONE1%=-SK1%=
s_addc_u32 s17, s17, -1
s_setpc_b64 s[16:17]
FM1_DONE%=:
s_getpc_b64 s[16:17]
FJ5%=:
s_add_u32 s16, s16, FM_END%=-FJ5%=
s_addc_u32 s17, s17, 0
s_setpc_b64 s[16:17]
FM_W2%=:
s_waitcnt vmcnt(20)
B
Rv 0,140,0
T 32,144,0
T 34,144,1024
Rv 4,140,2048
T 36,145,0
T 38,145,1024
Rv 8,140,4096
T 40,146,0
T 42,146,1024
Rv 12,140,6144
T 44,147,0
T 46,147,1024
Rv 16,140,8192
T 48,148,0
T 50,148,1024
Rv 20,140,10240
T 52,149,0
T 54,149,1024
Rv 24,140,12288
T 56,150,0
T 58,150,1024
Rv 28,140,14336
T 60,151,0
T 62,151,1024
WV 16
B
AS 79,79,1
Mavvz 0,0,32
Mavvz 4,0,36
Rv 64,141,0
Mavvz 8,0,40
T 96,144,16384
Mavvz 12,0,44
v_dot2c_f32_bf16 v181, v168, v180
v_dot2c_f32_bf16 v182, v168, v168
Mavvz 16,0,48
T 98,144,17408
v_dot2c_f32_bf16 v181, v169, v180
Mavvz 20,0,52
v_dot2c_f32_bf16 v182, v169, v169
AM 35,0
Mavvz 24,0,56
v_dot2c_f32_bf16 v181, v170, v180
D 236,28,33,0
Mavvz 28,0,60
Rv 68,141,2048
Mavvz 60,4,60
T 100,145,16384
Mavvz 56,4,56
v_dot2c_f32_bf16 v182, v170, v170
v_dot2c_f32_bf16 v181, v171, v180
Mavvz 52,4,52
T 102,145,17408
Mavvz 48,4,48
v_dot2c_f32_bf16 v182, v171, v171
v_dot2c_f32_bf16 v181, v172, v180
Mavvz 44,4,44
Rv 72,141,4096
v_dot2c_f32_bf16 v182, v172, v172
Mavvz 40,4,40
D 237,28,33,1024
Mavvz 36,4,36
T 104,146,16384
Mavvz 32,4,32
v_dot2c_f32_bf16 v181, v173, v180
v_dot2c_f32_bf16 v182, v173, v173
Mavvz 64,8,32
T 106,146,17408
Mavvz 68,8,36
v_dot2c_f32_bf16 v181, v174, v180
v_dot2c_f32_bf16 v182, v174, v174
Mavvz 72,8,40
Rv 76,141,6144
Mavvz 76,8,44
v_dot2c_f32_bf16 v181, v175, v180
Mavvz 80,8,48
v_dot2c_f32_bf16 v182, v175, v175
D 238,28,33,2048
Mavvz 84,8,52
T 108,147,16384
v_dot2c_f32_bf16 v181, v176, v180
Mavvz 88,8,56
T 110,147,17408
Mavvz 92,8,60
v_dot2c_f32_bf16 v182, v176, v176
v_dot2c_f32_bf16 v181, v177, v180
Mavvz 124,12,60
Rv 80,141,8192
Mavvz 120,12,56
T 112,148,16384
Mavvz 116,12,52
v_dot2c_f32_bf16 v182, v177, v177
v_dot2c_f32_bf16 v181, v178, v180
Mavvz 112,12,48
v_dot2c_f32_bf16 v182, v178, v178
D 239,28,33,3072
Mavvz 108,12,44
v_dot2c_f32_bf16 v181, v179, v180
AS 33,33,262144
Mavvz 104,12,40
T 114,148,17408
Mavvz 100,12,36
Rv 84,141,10240
v_dot2c_f32_bf16 v182, v179, v179
Mavvz 96,12,32
T 116,149,16384
Mavvz 128,16,32
N 3
Mavvz 132,16,36
v_add_f32_dpp v181, v181, v181 row_ror:8 row_mask:0xf bank_mask:0xf
AM 34,69632
Mavvz 136,16,40
v_add_f32_dpp v182, v182, v182 row_ror:8 row_mask:0xf bank_mask:0xf
D 132,24,32,0
Mavvz 140,16,44
T 118,149,17408
MV 186,181
Mavvz 144,16,48
Rv 88,141,12288
MV 187,182
Mavvz 148,16,52
T 120,150,16384
Mavvz 152,16,56
N 1
Mavvz 156,16,60
T 122,150,17408
v_permlane16_swap_b32 v181, v186
Mavvz 188,20,60
v_permlane16_swap_b32 v182, v187
N 1
Mavvz 184,20,56
D 133,24,32,1024
Mavvz 180,20,52
Rv 92,141,14336
Fad 181,181,186
Mavvz 176,20,48
T 124,151,16384
Mavvz 172,20,44
Fad 182,182,187
MV 186,181
Mavvz 168,20,40
T 126,151,17408
Mavvz 164,20,36
MV 187,182
Mavvz 160,20,32
N 1
Mavvz 192,24,32
D 134,24,32,2048
Mavvz 196,24,36
v_permlane32_swap_b32 v181, v186
Mavvz 200,24,40
v_permlane32_swap_b32 v182, v187
Mavvz 204,24,44
N 1
Mavvz 208,24,48
Fad 181,181,186
Mavvz 212,24,52
Fad 182,182,187
Mavvz 216,24,56
MV 186,181
Mavvz 220,24,60
MV 187,182
D 135,24,32,3072
Mavvz 252,28,60
AS 32,32,128
Mavvz 248,28,56
ds_write_b64 v185, v[186:187]
Mavvz 244,28,52
Mavvz 240,28,48
Mavvz 236,28,44
Mavvz 232,28,40
Mavvz 228,28,36
Mavvz 224,28,32
WV 12
B
Mavvc 0,64,96
Mavvc 4,64,100
Rv 0,140,32768
Mavvc 8,64,104
T 32,144,32768
Mavvc 12,64,108
Mavvc 16,64,112
T 34,144,33792
Mavvc 20,64,116
AM 35,16384
Mavvc 24,64,120
D 236,28,33,0
Mavvc 28,64,124
Rv 4,140,34816
Mavvc 60,68,124
T 36,145,32768
Mavvc 56,68,120
Mavvc 52,68,116
T 38,145,33792
Mavvc 48,68,112
Mavvc 44,68,108
Rv 8,140,36864
Mavvc 40,68,104
D 237,28,33,1024
Mavvc 36,68,100
T 40,146,32768
Mavvc 32,68,96
Mavvc 64,72,96
T 42,146,33792
Mavvc 68,72,100
Mavvc 72,72,104
Rv 12,140,38912
Mavvc 76,72,108
Mavvc 80,72,112
D 238,28,33,2048
Mavvc 84,72,116
T 44,147,32768
Mavvc 88,72,120
T 46,147,33792
Mavvc 92,72,124
Mavvc 124,76,124
Rv 16,140,40960
Mavvc 120,76,120
T 48,148,32768
Mavvc 116,76,116
Mavvc 112,76,112
D 239,28,33,3072
Mavvc 108,76,108
AS 33,33,262144
Mavvc 104,76,104
T 50,148,33792
Mavvc 100,76,100
Rv 20,140,43008
Mavvc 96,76,96
T 52,149,32768
Mavvc 128,80,96
Mavvc 132,80,100
AM 34,0
Mavvc 136,80,104
D 128,24,32,0
Mavvc 140,80,108
T 54,149,33792
Mavvc 144,80,112
Rv 24,140,45056
Mavvc 148,80,116
T 56,150,32768
Mavvc 152,80,120
Mavvc 156,80,124
T 58,150,33792
Mavvc 188,84,124
Mavvc 184,84,120
D 129,24,32,1024
Mavvc 180,84,116
Rv 28,140,47104
Mavvc 176,84,112
T 60,151,32768
Mavvc 172,84,108
Mavvc 168,84,104
T 62,151,33792
Mavvc 164,84,100
Mavvc 160,84,96
Mavvc 192,88,96
D 130,24,32,2048
Mavvc 196,88,100
Mavvc 200,88,104
Mavvc 204,88,108
Mavvc 208,88,112
Mavvc 212,88,116
Mavvc 216,88,120
Mavvc 220,88,124
D 131,24,32,3072
Mavvc 252,92,124
Mavvc 248,92,120
Mavvc 244,92,116
ds_read_b64 v[188:189], v184 offset:0
Mavvc 240,92,112
ds_read_b64 v[190:191], v184 offset:64
Mavvc 236,92,108
ds_read_b64 v[192:193], v184 offset:128
Mavvc 232,92,104
ds_read_b64 v[194:195], v184 offset:192
Mavvc 228,92,100
Mavvc 224,92,96
WV 12
B
Mavvc 0,0,32
Mavvc 4,0,36
Rv 64,141,32768
Fad 181,188,190
Mavvc 8,0,40
T 96,144,0
Fad 182,189,191
Mavvc 12,0,44
Fad 186,192,194
Fad 187,193,195
Mavvc 16,0,48
T 98,144,1024
Fad 181,181,186
Mavvc 20,0,52
Fad 182,182,187
AM 35,32768
Mavvc 24,0,56
v_mul_f32 v181, 0x3acccccd, v181
D 236,28,33,0
Mavvc 28,0,60
Rv 68,141,34816
v_mul_f32 v182, 0x3acccccd, v182
Mavvc 60,4,60
T 100,145,0
v_fma_f32 v182, -v181, v181, v182
Mavvc 56,4,56
v_max_f32 v182, 0, v182
v_add_f32 v182, 0x3727c5ac, v182
Mavvc 52,4,52
T 102,145,1024
v_rsq_f32 v182, v182
Mavvc 48,4,48
N 0
v_mul_f32 v182, 0x3e0293ee, v182
Mavvc 44,4,44
Rv 72,141,36864
Fmu 181,181,182
Mavvc 40,4,40
v_xor_b32 v181, 0x80000000, v181
D 237,28,33,1024
Mavvc 36,4,36
T 104,146,0
LO 196,168
Mavvc 32,4,32
HI 197,168
FM 196,196,182,181
Mavvc 64,8,32
T 106,146,1024
FM 197,197,182,181
Mavvc 68,8,36
C 168,196,197
LO 196,169
Mavvc 72,8,40
Rv 76,141,38912
HI 197,169
Mavvc 76,8,44
FM 196,196,182,181
FM 197,197,182,181
Mavvc 80,8,48
C 169,196,197
D 238,28,33,2048
Mavvc 84,8,52
T 108,147,0
LO 196,170
Mavvc 88,8,56
T 110,147,1024
HI 197,170
Mavvc 92,8,60
FM 196,196,182,181
FM 197,197,182,181
Mavvc 124,12,60
Rv 80,141,40960
C 170,196,197
Mavvc 120,12,56
T 112,148,0
LO 196,171
Mavvc 116,12,52
HI 197,171
FM 196,196,182,181
Mavvc 112,12,48
FM 197,197,182,181
D 239,28,33,3072
Mavvc 108,12,44
C 171,196,197
AS 33,33,262144
Mavvc 104,12,40
T 114,148,1024
LO 196,172
Mavvc 100,12,36
Rv 84,141,43008
HI 197,172
Mavvc 96,12,32
T 116,149,0
FM 196,196,182,181
Mavvc 128,16,32
FM 197,197,182,181
C 172,196,197
Mavvc 132,16,36
LO 196,173
AM 34,4096
Mavvc 136,16,40
HI 197,173
D 132,24,32,0
Mavvc 140,16,44
T 118,149,1024
FM 196,196,182,181
Mavvc 144,16,48
Rv 88,141,45056
FM 197,197,182,181
Mavvc 148,16,52
T 120,150,0
C 173,196,197
Mavvc 152,16,56
LO 196,174
HI 197,174
Mavvc 156,16,60
T 122,150,1024
FM 196,196,182,181
Mavvc 188,20,60
FM 197,197,182,181
C 174,196,197
Mavvc 184,20,56
LO 196,175
D 133,24,32,1024
Mavvc 180,20,52
Rv 92,141,47104
HI 197,175
Mavvc 176,20,48
T 124,151,0
FM 196,196,182,181
Mavvc 172,20,44
FM 197,197,182,181
C 175,196,197
Mavvc 168,20,40
T 126,151,1024
LO 196,176
Mavvc 164,20,36
HI 197,176
Mavvc 160,20,32
FM 196,196,182,181
FM 197,197,182,181
Mavvc 192,24,32
C 176,196,197
D 134,24,32,2048
Mavvc 196,24,36
LO 196,177
Mavvc 200,24,40
HI 197,177
Mavvc 204,24,44
FM 196,196,182,181
Mavvc 208,24,48
FM 197,197,182,181
Mavvc 212,24,52
C 177,196,197
Mavvc 216,24,56
LO 196,178
HI 197,178
Mavvc 220,24,60
FM 196,196,182,181
D 135,24,32,3072
Mavvc 252,28,60
FM 197,197,182,181
AS 32,32,128
Mavvc 248,28,56
C 178,196,197
Mavvc 244,28,52
LO 196,179
Mavvc 240,28,48
HI 197,179
Mavvc 236,28,44
FM 196,196,182,181
Mavvc 232,28,40
FM 197,197,182,181
Mavvc 228,28,36
C 179,196,197
Mavvc 224,28,32
s_lshr_b32 s18, s10, 4
s_mul_i32 s18, s18, 163840
s_lshr_b32 s19, s10, 1
s_and_b32 s19, s19, 7
s_lshl_b32 s19, s19, 10
s_add_u32 s18, s18, s19
s_and_b32 s19, s10, 1
s_lshl_b32 s19, s19, 7
s_add_u32 s18, s18, s19
AS 19,18,65536
Svs 168,205,48,18,0
Svs 172,205,48,19,0
Svs 176,206,48,18,0
WV 15
B
Mavvc 0,64,96
Mavvc 4,64,100
Rv 0,142,0
Mavvc 8,64,104
T 32,144,16384
Mavvc 12,64,108
Mavvc 16,64,112
T 34,144,17408
Mavvc 20,64,116
AM 35,0
Mavvc 24,64,120
D 236,28,33,0
Mavvc 28,64,124
Rv 4,142,2048
Mavvc 60,68,124
T 36,145,16384
s_lshr_b32 s21, s10, 3
Mavvc 56,68,120
Mavvc 52,68,116
T 38,145,17408
Mavvc 48,68,112
s_mul_i32 s21, s21, 160
Mavvc 44,68,108
Rv 8,142,4096
Mavvc 40,68,104
D 237,28,33,1024
Mavvc 36,68,100
T 40,146,16384
AS 21,21,128
Mavvc 32,68,96
Mavvc 64,72,96
T 42,146,17408
Mavvc 68,72,100
v_min_u32 v249, 31, v254
Mavvc 72,72,104
Rv 12,142,6144
Mavvc 76,72,108
Mavvc 80,72,112
v_add_u32 v249, s21, v249
D 238,28,33,2048
Mavvc 84,72,116
T 44,147,16384
Mavvc 88,72,120
T 46,147,17408
Mavvc 92,72,124
v_and_b32 v248, 7, v249
Mavvc 124,76,124
Rv 16,142,8192
Mavvc 120,76,120
T 48,148,16384
Mavvc 116,76,116
v_lshrrev_b32 v249, 3, v249
Mavvc 112,76,112
D 239,28,33,3072
Mavvc 108,76,108
AS 33,33,262144
Mavvc 104,76,104
T 50,148,17408
v_lshlrev_b32 v249, 13, v249
Mavvc 100,76,100
Rv 20,142,10240
Mavvc 96,76,96
T 52,149,16384
Mavvc 128,80,96
v_lshl_add_u32 v249, v248, 7, v249
Mavvc 132,80,100
AM 34,32768
Mavvc 136,80,104
D 128,24,32,0
Mavvc 140,80,108
T 54,149,17408
s_and_b32 s21, s10, 7
Mavvc 144,80,112
Rv 24,142,12288
Mavvc 148,80,116
T 56,150,16384
Mavvc 152,80,120
s_lshl_b32 s21, s21, 10
Mavvc 156,80,124
T 58,150,17408
Mavvc 188,84,124
Mavvc 184,84,120
buffer_load_dword v248, v249, s[44:47], s21 offen
D 129,24,32,1024
Mavvc 180,84,116
Rv 28,142,14336
Mavvc 176,84,112
T 60,151,16384
Mavvc 172,84,108
Mavvc 168,84,104
T 62,151,17408
Mavvc 164,84,100
Mavvc 160,84,96
Mavvc 192,88,96
D 130,24,32,2048
Mavvc 196,88,100
Mavvc 200,88,104
Mavvc 204,88,108
Mavvc 208,88,112
Mavvc 212,88,116
Mavvc 216,88,120
Mavvc 220,88,124
D 131,24,32,3072
Mavvc 252,92,124
Mavvc 248,92,120
Mavvc 244,92,116
Mavvc 240,92,112
Mavvc 236,92,108
Mavvc 232,92,104
Mavvc 228,92,100
Mavvc 224,92,96
WV 16
B
Mavvc 0,0,32
Mavvc 4,0,36
Rv 64,143,0
Mavvc 8,0,40
)A"
R"A(T 96,144,32768
Mavvc 12,0,44
Mavvc 16,0,48
T 98,144,33792
Mavvc 20,0,52
AM 35,16384
Mavvc 24,0,56
D 236,28,33,0
Mavvc 28,0,60
Rv 68,143,2048
Mavvc 60,4,60
T 100,145,32768
Mavvc 56,4,56
Mavvc 52,4,52
T 102,145,33792
Mavvc 48,4,48
Mavvc 44,4,44
Rv 72,143,4096
Mavvc 40,4,40
D 237,28,33,1024
Mavvc 36,4,36
T 104,146,32768
Mavvc 32,4,32
Mavvc 64,8,32
T 106,146,33792
Mavvc 68,8,36
Mavvc 72,8,40
Rv 76,143,6144
Mavvc 76,8,44
Mavvc 80,8,48
D 238,28,33,2048
Mavvc 84,8,52
T 108,147,32768
Mavvc 88,8,56
T 110,147,33792
Mavvc 92,8,60
Mavvc 124,12,60
Rv 80,143,8192
Mavvc 120,12,56
T 112,148,32768
Mavvc 116,12,52
Mavvc 112,12,48
D 239,28,33,3072
Mavvc 108,12,44
AS 33,33,262144
Mavvc 104,12,40
T 114,148,33792
Mavvc 100,12,36
Rv 84,143,10240
Mavvc 96,12,32
T 116,149,32768
Mavvc 128,16,32
Mavvc 132,16,36
AM 34,36864
Mavvc 136,16,40
D 132,24,32,0
Mavvc 140,16,44
T 118,149,33792
Mavvc 144,16,48
Rv 88,143,12288
Mavvc 148,16,52
T 120,150,32768
Mavvc 152,16,56
Mavvc 156,16,60
T 122,150,33792
Mavvc 188,20,60
Mavvc 184,20,56
D 133,24,32,1024
Mavvc 180,20,52
Rv 92,143,14336
Mavvc 176,20,48
T 124,151,32768
Mavvc 172,20,44
Mavvc 168,20,40
T 126,151,33792
Mavvc 164,20,36
Mavvc 160,20,32
Mavvc 192,24,32
D 134,24,32,2048
Mavvc 196,24,36
Mavvc 200,24,40
Mavvc 204,24,44
Mavvc 208,24,48
Mavvc 212,24,52
Mavvc 216,24,56
Mavvc 220,24,60
D 135,24,32,3072
Mavvc 252,28,60
AS 32,32,128
Mavvc 248,28,56
Mavvc 244,28,52
Mavvc 240,28,48
Mavvc 236,28,44
Mavvc 232,28,40
Mavvc 228,28,36
Mavvc 224,28,32
WV 13
B
Mavvc 0,64,96
Mavvc 4,64,100
Rv 0,140,0
Mavvc 8,64,104
T 32,144,0
Mavvc 12,64,108
Mavvc 16,64,112
T 34,144,1024
Mavvc 20,64,116
AM 35,32768
Mavvc 24,64,120
D 236,28,33,0
Mavvc 28,64,124
Rv 4,140,2048
Mavvc 60,68,124
T 36,145,0
Mavvc 56,68,120
Mavvc 52,68,116
T 38,145,1024
Mavvc 48,68,112
Mavvc 44,68,108
Rv 8,140,4096
Mavvc 40,68,104
D 237,28,33,1024
Mavvc 36,68,100
T 40,146,0
Mavvc 32,68,96
Mavvc 64,72,96
T 42,146,1024
Mavvc 68,72,100
Mavvc 72,72,104
Rv 12,140,6144
Mavvc 76,72,108
Mavvc 80,72,112
D 238,28,33,2048
Mavvc 84,72,116
T 44,147,0
Mavvc 88,72,120
T 46,147,1024
Mavvc 92,72,124
Mavvc 124,76,124
Rv 16,140,8192
Mavvc 120,76,120
T 48,148,0
Mavvc 116,76,116
Mavvc 112,76,112
D 239,28,33,3072
Mavvc 108,76,108
AS 33,33,262144
Mavvc 104,76,104
T 50,148,1024
Mavvc 100,76,100
Rv 20,140,10240
Mavvc 96,76,96
T 52,149,0
Mavvc 128,80,96
Mavvc 132,80,100
AM 34,65536
Mavvc 136,80,104
D 128,24,32,0
Mavvc 140,80,108
T 54,149,1024
Mavvc 144,80,112
Rv 24,140,12288
Mavvc 148,80,116
T 56,150,0
Mavvc 152,80,120
Mavvc 156,80,124
T 58,150,1024
Mavvc 188,84,124
Mavvc 184,84,120
D 129,24,32,1024
Mavvc 180,84,116
Rv 28,140,14336
Mavvc 176,84,112
T 60,151,0
Mavvc 172,84,108
Mavvc 168,84,104
T 62,151,1024
Mavvc 164,84,100
Mavvc 160,84,96
Mavvc 192,88,96
D 130,24,32,2048
Mavvc 196,88,100
Mavvc 200,88,104
Mavvc 204,88,108
Mavvc 208,88,112
Mavvc 212,88,116
Mavvc 216,88,120
Mavvc 220,88,124
D 131,24,32,3072
Mavvc 252,92,124
Mavvc 248,92,120
Mavvc 244,92,116
Mavvc 240,92,112
Mavvc 236,92,108
Mavvc 232,92,104
Mavvc 228,92,100
Mavvc 224,92,96
WV 12
B
Mavvc 0,0,32
Mavvc 4,0,36
Rv 64,141,0
Mavvc 8,0,40
T 96,144,16384
Mavvc 12,0,44
Mavvc 16,0,48
T 98,144,17408
Mavvc 20,0,52
AM 35,0
Mavvc 24,0,56
D 236,28,33,0
Mavvc 28,0,60
Rv 68,141,2048
Mavvc 60,4,60
T 100,145,16384
Mavvc 56,4,56
Mavvc 52,4,52
T 102,145,17408
Mavvc 48,4,48
Mavvc 44,4,44
Rv 72,141,4096
Mavvc 40,4,40
D 237,28,33,1024
Mavvc 36,4,36
T 104,146,16384
Mavvc 32,4,32
Mavvc 64,8,32
T 106,146,17408
Mavvc 68,8,36
Mavvc 72,8,40
Rv 76,141,6144
Mavvc 76,8,44
Mavvc 80,8,48
D 238,28,33,2048
Mavvc 84,8,52
T 108,147,16384
Mavvc 88,8,56
T 110,147,17408
Mavvc 92,8,60
Mavvc 124,12,60
Rv 80,141,8192
Mavvc 120,12,56
T 112,148,16384
Mavvc 116,12,52
Mavvc 112,12,48
D 239,28,33,3072
Mavvc 108,12,44
AS 33,33,262144
Mavvc 104,12,40
T 114,148,17408
Mavvc 100,12,36
Rv 84,141,10240
Mavvc 96,12,32
T 116,149,16384
Mavvc 128,16,32
Mavvc 132,16,36
AM 34,69632
Mavvc 136,16,40
D 132,24,32,0
Mavvc 140,16,44
T 118,149,17408
Mavvc 144,16,48
Rv 88,141,12288
Mavvc 148,16,52
T 120,150,16384
Mavvc 152,16,56
Mavvc 156,16,60
T 122,150,17408
Mavvc 188,20,60
Mavvc 184,20,56
D 133,24,32,1024
Mavvc 180,20,52
Rv 92,141,14336
Mavvc 176,20,48
T 124,151,16384
Mavvc 172,20,44
Mavvc 168,20,40
T 126,151,17408
Mavvc 164,20,36
Mavvc 160,20,32
Mavvc 192,24,32
D 134,24,32,2048
Mavvc 196,24,36
Mavvc 200,24,40
Mavvc 204,24,44
Mavvc 208,24,48
Mavvc 212,24,52
Mavvc 216,24,56
Mavvc 220,24,60
D 135,24,32,3072
Mavvc 252,28,60
AS 32,32,128
Mavvc 248,28,56
Mavvc 244,28,52
Mavvc 240,28,48
Mavvc 236,28,44
Mavvc 232,28,40
Mavvc 228,28,36
Mavvc 224,28,32
WV 12
B
s_getpc_b64 s[16:17]
1:
s_add_u32 s16, s16, QS2%=-1b
s_addc_u32 s17, s17, 0
s_lshr_b32 s21, s10, 3
v_lshlrev_b32 v249, 5, v254
v_add_u32 v249, s21, v249
v_min_u32 v249, ((QE2%=-QS2%=)>>7), v249
v_lshlrev_b32 v249, 7, v249
global_load_dword v248, v249, s[16:17]
Mavvc 0,64,96
Mavvc 4,64,100
Rv 0,140,32768
Mavvc 8,64,104
T 32,144,32768
Mavvc 12,64,108
Mavvc 16,64,112
T 34,144,33792
Mavvc 20,64,116
AM 35,16384
Mavvc 24,64,120
D 236,28,33,0
Mavvc 28,64,124
Rv 4,140,34816
Mavvc 60,68,124
T 36,145,32768
Mavvc 56,68,120
Mavvc 52,68,116
T 38,145,33792
Mavvc 48,68,112
Mavvc 44,68,108
Rv 8,140,36864
Mavvc 40,68,104
D 237,28,33,1024
Mavvc 36,68,100
T 40,146,32768
Mavvc 32,68,96
Mavvc 64,72,96
T 42,146,33792
Mavvc 68,72,100
Mavvc 72,72,104
Rv 12,140,38912
Mavvc 76,72,108
Mavvc 80,72,112
D 238,28,33,2048
Mavvc 84,72,116
T 44,147,32768
Mavvc 88,72,120
T 46,147,33792
Mavvc 92,72,124
Mavvc 124,76,124
Rv 16,140,40960
Mavvc 120,76,120
T 48,148,32768
Mavvc 116,76,116
Mavvc 112,76,112
D 239,28,33,3072
Mavvc 108,76,108
AS 33,33,262144
Mavvc 104,76,104
T 50,148,33792
Mavvc 100,76,100
Rv 20,140,43008
Mavvc 96,76,96
T 52,149,32768
Mavvc 128,80,96
Mavvc 132,80,100
AM 34,0
Mavvc 136,80,104
D 128,24,32,0
Mavvc 140,80,108
T 54,149,33792
Mavvc 144,80,112
Rv 24,140,45056
Mavvc 148,80,116
T 56,150,32768
Mavvc 152,80,120
Mavvc 156,80,124
T 58,150,33792
Mavvc 188,84,124
Mavvc 184,84,120
D 129,24,32,1024
Mavvc 180,84,116
Rv 28,140,47104
Mavvc 176,84,112
T 60,151,32768
Mavvc 172,84,108
Mavvc 168,84,104
T 62,151,33792
Mavvc 164,84,100
Mavvc 160,84,96
Mavvc 192,88,96
D 130,24,32,2048
Mavvc 196,88,100
Mavvc 200,88,104
Mavvc 204,88,108
Mavvc 208,88,112
Mavvc 212,88,116
Mavvc 216,88,120
Mavvc 220,88,124
D 131,24,32,3072
Mavvc 252,92,124
Mavvc 248,92,120
Mavvc 244,92,116
Mavvc 240,92,112
Mavvc 236,92,108
Mavvc 232,92,104
Mavvc 228,92,100
Mavvc 224,92,96
WV 13
B
Mavvc 0,0,32
Mavvc 4,0,36
Rv 64,141,32768
Mavvc 8,0,40
T 96,144,0
Mavvc 12,0,44
Mavvc 16,0,48
T 98,144,1024
Mavvc 20,0,52
AM 35,32768
Mavvc 24,0,56
D 236,28,33,0
Mavvc 28,0,60
Rv 68,141,34816
Mavvc 60,4,60
T 100,145,0
Mavvc 56,4,56
Mavvc 52,4,52
T 102,145,1024
Mavvc 48,4,48
Mavvc 44,4,44
Rv 72,141,36864
Mavvc 40,4,40
D 237,28,33,1024
Mavvc 36,4,36
T 104,146,0
Mavvc 32,4,32
Mavvc 64,8,32
T 106,146,1024
Mavvc 68,8,36
Mavvc 72,8,40
Rv 76,141,38912
Mavvc 76,8,44
Mavvc 80,8,48
D 238,28,33,2048
Mavvc 84,8,52
T 108,147,0
Mavvc 88,8,56
T 110,147,1024
Mavvc 92,8,60
Mavvc 124,12,60
Rv 80,141,40960
Mavvc 120,12,56
T 112,148,0
Mavvc 116,12,52
Mavvc 112,12,48
D 239,28,33,3072
Mavvc 108,12,44
AS 33,33,262144
Mavvc 104,12,40
T 114,148,1024
Mavvc 100,12,36
Rv 84,141,43008
Mavvc 96,12,32
T 116,149,0
Mavvc 128,16,32
Mavvc 132,16,36
AM 34,4096
Mavvc 136,16,40
D 132,24,32,0
Mavvc 140,16,44
T 118,149,1024
Mavvc 144,16,48
Rv 88,141,45056
Mavvc 148,16,52
T 120,150,0
Mavvc 152,16,56
Mavvc 156,16,60
T 122,150,1024
Mavvc 188,20,60
Mavvc 184,20,56
D 133,24,32,1024
Mavvc 180,20,52
Rv 92,141,47104
Mavvc 176,20,48
T 124,151,0
Mavvc 172,20,44
Mavvc 168,20,40
T 126,151,1024
Mavvc 164,20,36
Mavvc 160,20,32
Mavvc 192,24,32
D 134,24,32,2048
Mavvc 196,24,36
Mavvc 200,24,40
Mavvc 204,24,44
Mavvc 208,24,48
Mavvc 212,24,52
Mavvc 216,24,56
Mavvc 220,24,60
D 135,24,32,3072
Mavvc 252,28,60
AS 32,32,128
Mavvc 248,28,56
Mavvc 244,28,52
Mavvc 240,28,48
Mavvc 236,28,44
Mavvc 232,28,40
Mavvc 228,28,36
Mavvc 224,28,32
WV 12
B
s_sub_u32 s23, 15, s13
s_cmp_ge_u32 s11, 2
s_cselect_b32 s23, s13, s23
s_lshl_b32 s21, s23, 8
AS 21,21,0
v_mov_b32 v247, s21
v_min_u32 v246, 15, v254
v_lshl_add_u32 v247, v246, 2, v247
global_load_dword v246, v247, s[14:15] sc1
Mavvc 0,64,96
Mavvc 4,64,100
Rv 0,142,0
Mavvc 8,64,104
T 32,144,16384
Mavvc 12,64,108
Mavvc 16,64,112
T 34,144,17408
Mavvc 20,64,116
AM 35,0
Mavvc 24,64,120
D 236,28,33,0
Mavvc 28,64,124
Rv 4,142,2048
Mavvc 60,68,124
T 36,145,16384
Mavvc 56,68,120
Mavvc 52,68,116
T 38,145,17408
Mavvc 48,68,112
Mavvc 44,68,108
Rv 8,142,4096
Mavvc 40,68,104
D 237,28,33,1024
Mavvc 36,68,100
T 40,146,16384
Mavvc 32,68,96
Mavvc 64,72,96
T 42,146,17408
Mavvc 68,72,100
Mavvc 72,72,104
Rv 12,142,6144
Mavvc 76,72,108
Mavvc 80,72,112
D 238,28,33,2048
Mavvc 84,72,116
T 44,147,16384
Mavvc 88,72,120
T 46,147,17408
Mavvc 92,72,124
Mavvc 124,76,124
Rv 16,142,8192
Mavvc 120,76,120
T 48,148,16384
Mavvc 116,76,116
Mavvc 112,76,112
D 239,28,33,3072
Mavvc 108,76,108
AS 33,33,262144
Mavvc 104,76,104
T 50,148,17408
Mavvc 100,76,100
Rv 20,142,10240
Mavvc 96,76,96
T 52,149,16384
Mavvc 128,80,96
Mavvc 132,80,100
AM 34,32768
Mavvc 136,80,104
D 128,24,32,0
Mavvc 140,80,108
T 54,149,17408
Mavvc 144,80,112
Rv 24,142,12288
Mavvc 148,80,116
T 56,150,16384
Mavvc 152,80,120
Mavvc 156,80,124
T 58,150,17408
Mavvc 188,84,124
Mavvc 184,84,120
D 129,24,32,1024
Mavvc 180,84,116
Rv 28,142,14336
Mavvc 176,84,112
T 60,151,16384
Mavvc 172,84,108
Mavvc 168,84,104
T 62,151,17408
Mavvc 164,84,100
Mavvc 160,84,96
Mavvc 192,88,96
D 130,24,32,2048
Mavvc 196,88,100
Mavvc 200,88,104
Mavvc 204,88,108
Mavvc 208,88,112
Mavvc 212,88,116
Mavvc 216,88,120
Mavvc 220,88,124
D 131,24,32,3072
Mavvc 252,92,124
Mavvc 248,92,120
Mavvc 244,92,116
Mavvc 240,92,112
Mavvc 236,92,108
Mavvc 232,92,104
Mavvc 228,92,100
Mavvc 224,92,96
WV 13
B
Mavvc 0,0,32
Mavvc 4,0,36
Rv 64,143,0
Mavvc 8,0,40
T 96,144,32768
Mavvc 12,0,44
Mavvc 16,0,48
T 98,144,33792
Mavvc 20,0,52
AM 35,16384
Mavvc 24,0,56
D 236,28,33,0
Mavvc 28,0,60
Rv 68,143,2048
Mavvc 60,4,60
T 100,145,32768
Mavvc 56,4,56
Mavvc 52,4,52
T 102,145,33792
Mavvc 48,4,48
Mavvc 44,4,44
Rv 72,143,4096
Mavvc 40,4,40
D 237,28,33,1024
Mavvc 36,4,36
T 104,146,32768
Mavvc 32,4,32
Mavvc 64,8,32
T 106,146,33792
Mavvc 68,8,36
Mavvc 72,8,40
Rv 76,143,6144
Mavvc 76,8,44
Mavvc 80,8,48
D 238,28,33,2048
Mavvc 84,8,52
T 108,147,32768
Mavvc 88,8,56
T 110,147,33792
Mavvc 92,8,60
Mavvc 124,12,60
Rv 80,143,8192
Mavvc 120,12,56
T 112,148,32768
Mavvc 116,12,52
Mavvc 112,12,48
D 239,28,33,3072
Mavvc 108,12,44
AS 33,33,262144
Mavvc 104,12,40
T 114,148,33792
Mavvc 100,12,36
Rv 84,143,10240
Mavvc 96,12,32
T 116,149,32768
Mavvc 128,16,32
Mavvc 132,16,36
AM 34,36864
Mavvc 136,16,40
D 132,24,32,0
Mavvc 140,16,44
T 118,149,33792
Mavvc 144,16,48
Rv 88,143,12288
Mavvc 148,16,52
T 120,150,32768
Mavvc 152,16,56
Mavvc 156,16,60
T 122,150,33792
Mavvc 188,20,60
Mavvc 184,20,56
D 133,24,32,1024
Mavvc 180,20,52
Rv 92,143,14336
Mavvc 176,20,48
T 124,151,32768
Mavvc 172,20,44
Mavvc 168,20,40
T 126,151,33792
Mavvc 164,20,36
Mavvc 160,20,32
Mavvc 192,24,32
D 134,24,32,2048
Mavvc 196,24,36
Mavvc 200,24,40
Mavvc 204,24,44
Mavvc 208,24,48
Mavvc 212,24,52
Mavvc 216,24,56
Mavvc 220,24,60
D 135,24,32,3072
Mavvc 252,28,60
AS 32,32,128
Mavvc 248,28,56
Mavvc 244,28,52
Mavvc 240,28,48
Mavvc 236,28,44
Mavvc 232,28,40
Mavvc 228,28,36
Mavvc 224,28,32
WV 12
B
v_cmp_ne_u32 vcc, s79, v246
N 4
s_cbranch_vccnz XN2_SPIN%=
XN2_OK%=:
Mavvc 0,64,96
Mavvc 4,64,100
Rv 0,140,0
Mavvc 8,64,104
T 32,144,0
Mavvc 12,64,108
Mavvc 16,64,112
T 34,144,1024
Mavvc 20,64,116
AM 35,32768
Mavvc 24,64,120
D 236,28,33,0
Mavvc 28,64,124
Rv 4,140,2048
Mavvc 60,68,124
T 36,145,0
s_and_b32 s21, s12, 3
Mavvc 56,68,120
Mavvc 52,68,116
T 38,145,1024
s_mul_i32 s21, s21, 40960
Mavvc 48,68,112
Mavvc 44,68,108
Rv 8,140,4096
s_add_u32 s21, s21, s53
Mavvc 40,68,104
D 237,28,33,1024
Mavvc 36,68,100
T 40,146,0
AS 21,21,0
Mavvc 32,68,96
Mavvc 64,72,96
T 42,146,1024
v_min_u32 v249, 63, v254
Mavvc 68,72,100
Mavvc 72,72,104
Rv 12,140,6144
v_lshlrev_b32 v249, 7, v249
Mavvc 76,72,108
Mavvc 80,72,112
buffer_load_dword v248, v249, s[48:51], s21 offen sc1
)A"
R"A(D 238,28,33,2048
Mavvc 84,72,116
T 44,147,0
Mavvc 88,72,120
T 46,147,1024
Mavvc 92,72,124
Mavvc 124,76,124
Rv 16,140,8192
Mavvc 120,76,120
T 48,148,0
Mavvc 116,76,116
Mavvc 112,76,112
D 239,28,33,3072
Mavvc 108,76,108
AS 33,33,262144
Mavvc 104,76,104
T 50,148,1024
Mavvc 100,76,100
Rv 20,140,10240
Mavvc 96,76,96
T 52,149,0
Mavvc 128,80,96
Mavvc 132,80,100
T 54,149,1024
Mavvc 136,80,104
Mavvc 140,80,108
Rv 24,140,12288
Mavvc 144,80,112
Mavvc 148,80,116
T 56,150,0
Mavvc 152,80,120
AS 55,53,0
M0 81920
Mavvc 156,80,124
T 58,150,1024
Ds 243,48,55,0
Mavvc 188,84,124
Mavvc 184,84,120
Rv 28,140,14336
Mavvc 180,84,116
Mavvc 176,84,112
T 60,151,0
Mavvc 172,84,108
Mavvc 168,84,104
T 62,151,1024
Ds 243,48,55,1024
Mavvc 164,84,100
Mavvc 160,84,96
Mavvc 192,88,96
Mavvc 196,88,100
Mavvc 200,88,104
Mavvc 204,88,108
Ds 243,48,55,2048
Mavvc 208,88,112
Mavvc 212,88,116
Mavvc 216,88,120
Mavvc 220,88,124
Mavvc 252,92,124
Mavvc 248,92,120
Ds 243,48,55,3072
Mavvc 244,92,116
Mavvc 240,92,112
Mavvc 236,92,108
Mavvc 232,92,104
Mavvc 228,92,100
Mavvc 224,92,96
WV 13
B
Mavvc 0,0,32
Mavvc 4,0,36
Rv 64,141,0
Mavvc 8,0,40
T 96,144,16384
Mavvc 12,0,44
Mavvc 16,0,48
T 98,144,17408
Mavvc 20,0,52
AM 35,0
Mavvc 24,0,56
D 236,28,33,0
Mavvc 28,0,60
Rv 68,141,2048
Mavvc 60,4,60
T 100,145,16384
s_and_b32 s21, s12, 3
Mavvc 56,4,56
Mavvc 52,4,52
T 102,145,17408
s_mul_i32 s21, s21, 40960
Mavvc 48,4,48
Mavvc 44,4,44
Rv 72,141,4096
s_add_u32 s21, s21, s53
Mavvc 40,4,40
D 237,28,33,1024
Mavvc 36,4,36
T 104,146,16384
AS 21,21,8192
Mavvc 32,4,32
Mavvc 64,8,32
T 106,146,17408
v_min_u32 v249, 63, v254
Mavvc 68,8,36
Mavvc 72,8,40
Rv 76,141,6144
v_lshlrev_b32 v249, 7, v249
Mavvc 76,8,44
Mavvc 80,8,48
buffer_load_dword v248, v249, s[48:51], s21 offen sc1
D 238,28,33,2048
Mavvc 84,8,52
T 108,147,16384
Mavvc 88,8,56
T 110,147,17408
Mavvc 92,8,60
Mavvc 124,12,60
Rv 80,141,8192
Mavvc 120,12,56
T 112,148,16384
Mavvc 116,12,52
Mavvc 112,12,48
D 239,28,33,3072
Mavvc 108,12,44
AS 33,33,262144
Mavvc 104,12,40
T 114,148,17408
Mavvc 100,12,36
Rv 84,141,10240
Mavvc 96,12,32
T 116,149,16384
Mavvc 128,16,32
Mavvc 132,16,36
T 118,149,17408
Mavvc 136,16,40
Mavvc 140,16,44
Rv 88,141,12288
Mavvc 144,16,48
Mavvc 148,16,52
T 120,150,16384
Mavvc 152,16,56
AS 55,53,8192
M0 86016
Mavvc 156,16,60
T 122,150,17408
Ds 243,48,55,0
Mavvc 188,20,60
Mavvc 184,20,56
Rv 92,141,14336
Mavvc 180,20,52
Mavvc 176,20,48
T 124,151,16384
Mavvc 172,20,44
Mavvc 168,20,40
T 126,151,17408
Ds 243,48,55,1024
Mavvc 164,20,36
Mavvc 160,20,32
Mavvc 192,24,32
Mavvc 196,24,36
Mavvc 200,24,40
Mavvc 204,24,44
Ds 243,48,55,2048
Mavvc 208,24,48
Mavvc 212,24,52
Mavvc 216,24,56
Mavvc 220,24,60
Mavvc 252,28,60
Mavvc 248,28,56
Ds 243,48,55,3072
Mavvc 244,28,52
Mavvc 240,28,48
Mavvc 236,28,44
Mavvc 232,28,40
Mavvc 228,28,36
Mavvc 224,28,32
WV 13
B
Mavvc 0,64,96
Mavvc 4,64,100
Rv 0,140,32768
Mavvc 8,64,104
T 32,144,32768
Mavvc 12,64,108
Mavvc 16,64,112
T 34,144,33792
Mavvc 20,64,116
s_add_u32 s55, s52, 0x0
M0 122880
Mavvc 24,64,120
Rv 4,140,34816
D 236,44,55,0
Mavvc 28,64,124
Mavvc 60,68,124
T 36,145,32768
s_and_b32 s21, s12, 3
Mavvc 56,68,120
Mavvc 52,68,116
T 38,145,33792
s_mul_i32 s21, s21, 40960
Mavvc 48,68,112
Mavvc 44,68,108
Rv 8,140,36864
s_add_u32 s21, s21, s53
Mavvc 40,68,104
D 237,44,55,1024
Mavvc 36,68,100
T 40,146,32768
AS 21,21,16384
Mavvc 32,68,96
Mavvc 64,72,96
T 42,146,33792
v_min_u32 v249, 31, v254
Mavvc 68,72,100
Mavvc 72,72,104
Rv 12,140,38912
v_lshlrev_b32 v249, 7, v249
Mavvc 76,72,108
Mavvc 80,72,112
T 44,147,32768
buffer_load_dword v248, v249, s[48:51], s21 offen sc1
Mavvc 84,72,116
D 238,44,55,2048
Mavvc 88,72,120
T 46,147,33792
Mavvc 92,72,124
Mavvc 124,76,124
Rv 16,140,40960
Mavvc 120,76,120
T 48,148,32768
Mavvc 116,76,116
Mavvc 112,76,112
T 50,148,33792
Mavvc 108,76,108
D 239,44,55,3072
Mavvc 104,76,104
Rv 20,140,43008
Mavvc 100,76,100
Mavvc 96,76,96
T 52,149,32768
Mavvc 128,80,96
Mavvc 132,80,100
T 54,149,33792
Mavvc 136,80,104
AS 55,53,16384
M0 16384
Mavvc 140,80,108
Rv 24,140,45056
Ds 243,48,55,0
Mavvc 144,80,112
Mavvc 148,80,116
T 56,150,32768
Mavvc 152,80,120
Mavvc 156,80,124
T 58,150,33792
Mavvc 188,84,124
Mavvc 184,84,120
Rv 28,140,47104
Mavvc 180,84,116
Ds 243,48,55,1024
Mavvc 176,84,112
T 60,151,32768
Mavvc 172,84,108
Mavvc 168,84,104
T 62,151,33792
Mavvc 164,84,100
Mavvc 160,84,96
Mavvc 192,88,96
Mavvc 196,88,100
Mavvc 200,88,104
Ds 243,48,55,2048
Mavvc 204,88,108
Mavvc 208,88,112
Mavvc 212,88,116
Mavvc 216,88,120
Mavvc 220,88,124
Mavvc 252,92,124
Mavvc 248,92,120
Ds 243,48,55,3072
Mavvc 244,92,116
Mavvc 240,92,112
Mavvc 236,92,108
Mavvc 232,92,104
Mavvc 228,92,100
Mavvc 224,92,96
WV 13
B
Mavvc 0,0,32
Mavvc 4,0,36
Rv 64,141,32768
Mavvc 8,0,40
T 96,144,0
Mavvc 12,0,44
Mavvc 16,0,48
T 98,144,1024
Mavvc 20,0,52
s_add_u32 s55, s52, 0x80000
M0 139264
Mavvc 24,0,56
Rv 68,141,34816
D 236,44,55,0
Mavvc 28,0,60
Mavvc 60,4,60
T 100,145,0
Mavvc 56,4,56
Mavvc 52,4,52
T 102,145,1024
Mavvc 48,4,48
Mavvc 44,4,44
Rv 72,141,36864
Mavvc 40,4,40
D 237,44,55,1024
Mavvc 36,4,36
T 104,146,0
Mavvc 32,4,32
Mavvc 64,8,32
T 106,146,1024
Mavvc 68,8,36
Mavvc 72,8,40
Rv 76,141,38912
Mavvc 76,8,44
Mavvc 80,8,48
T 108,147,0
Mavvc 84,8,52
D 238,44,55,2048
Mavvc 88,8,56
T 110,147,1024
Mavvc 92,8,60
Mavvc 124,12,60
Rv 80,141,40960
Mavvc 120,12,56
T 112,148,0
Mavvc 116,12,52
Mavvc 112,12,48
T 114,148,1024
Mavvc 108,12,44
D 239,44,55,3072
Mavvc 104,12,40
Rv 84,141,43008
Mavvc 100,12,36
Mavvc 96,12,32
T 116,149,0
Mavvc 128,16,32
Mavvc 132,16,36
T 118,149,1024
Mavvc 136,16,40
AS 55,53,24576
M0 20480
Mavvc 140,16,44
Rv 88,141,45056
Ds 243,48,55,0
Mavvc 144,16,48
Mavvc 148,16,52
T 120,150,0
Mavvc 152,16,56
Mavvc 156,16,60
T 122,150,1024
Mavvc 188,20,60
Mavvc 184,20,56
Rv 92,141,47104
Mavvc 180,20,52
Ds 243,48,55,1024
Mavvc 176,20,48
T 124,151,0
Mavvc 172,20,44
Mavvc 168,20,40
T 126,151,1024
Mavvc 164,20,36
Mavvc 160,20,32
Mavvc 192,24,32
Mavvc 196,24,36
Mavvc 200,24,40
Ds 243,48,55,2048
Mavvc 204,24,44
Mavvc 208,24,48
Mavvc 212,24,52
Mavvc 216,24,56
Mavvc 220,24,60
Mavvc 252,28,60
Mavvc 248,28,56
Ds 243,48,55,3072
Mavvc 244,28,52
Mavvc 240,28,48
Mavvc 236,28,44
Mavvc 232,28,40
Mavvc 228,28,36
Mavvc 224,28,32
WV 12
B
Mavvc 0,64,96
Mavvc 4,64,100
Mavvc 8,64,104
Mavvc 12,64,108
Mavvc 16,64,112
s_add_u32 s55, s52, 0x100000
M0 106496
Mavvc 20,64,116
Rv 192,245,0
D 236,44,55,0
Mavvc 24,64,120
Mavvc 28,64,124
Rv 196,245,1024
Mavvc 60,68,124
Mavvc 56,68,120
Mavvc 52,68,116
D 237,44,55,1024
Mavvc 48,68,112
Rv 200,245,2048
Mavvc 44,68,108
Mavvc 40,68,104
Mavvc 36,68,100
Rv 204,245,3072
Mavvc 32,68,96
D 238,44,55,2048
Mavvc 64,72,96
Mavvc 68,72,100
T 128,240,16384
Mavvc 72,72,104
T 130,240,17408
Mavvc 76,72,108
D 239,44,55,3072
Mavvc 80,72,112
X 242,32,240
Mavvc 84,72,116
T 132,242,16384
Mavvc 88,72,120
T 134,242,17408
Mavvc 92,72,124
AS 55,53,32768
M0 49152
Mavvc 124,76,124
Ds 243,48,55,0
Mavvc 120,76,120
X 241,64,240
Mavvc 116,76,116
T 136,241,16384
Mavvc 112,76,112
T 138,241,17408
Mavvc 108,76,108
X 242,96,240
T 140,242,16384
Mavvc 104,76,104
T 142,242,17408
Ds 243,48,55,1024
Mavvc 100,76,100
X 241,128,240
Mavvc 96,76,96
T 144,241,16384
Mavvc 128,80,96
T 146,241,17408
Mavvc 132,80,100
Mavvc 136,80,104
Ds 243,48,55,2048
Mavvc 140,80,108
X 242,160,240
Mavvc 144,80,112
T 148,242,16384
Mavvc 148,80,116
T 150,242,17408
Mavvc 152,80,120
X 241,192,240
T 152,241,16384
Mavvc 156,80,124
T 154,241,17408
Ds 243,48,55,3072
Mavvc 188,84,124
X 242,224,240
Mavvc 184,84,120
T 156,242,16384
Mavvc 180,84,116
T 158,242,17408
Mavvc 176,84,112
AS 55,53,40960
M0 53248
Mavvc 172,84,108
Ds 243,48,55,0
Mavvc 168,84,104
Mavvc 164,84,100
Mavvc 160,84,96
Mavvc 192,88,96
Ds 243,48,55,1024
Mavvc 196,88,100
Mavvc 200,88,104
Mavvc 204,88,108
Mavvc 208,88,112
Mavvc 212,88,116
Ds 243,48,55,2048
Mavvc 216,88,120
Mavvc 220,88,124
Mavvc 252,92,124
Mavvc 248,92,120
Mavvc 244,92,116
Ds 243,48,55,3072
Mavvc 240,92,112
Mavvc 236,92,108
Mavvc 232,92,104
Mavvc 228,92,100
Mavvc 224,92,96
s_add_u32 s36, s14, 0x1400000
s_addc_u32 s37, s15, 0
SM 38,0x1000000
SM 39,0x20000
s_lshl_b32 s16, s12, 19
s_lshl_b32 s17, s13, 16
s_add_u32 s40, s16, s17
s_add_u32 s24, s14, 0x400000
s_addc_u32 s25, s15, 0
SM 26,0x2000000
SM 27,0x20000
s_add_u32 s28, s14, 0x1400000
s_addc_u32 s29, s15, 0
SM 30,0x1000000
SM 31,0x20000
s_lshl_b32 s16, s12, 19
s_lshl_b32 s17, s11, 12
s_add_u32 s56, s16, s17
s_mov_b32 s57, s56
AS 58,17,0
AS 59,17,65536
AS 56,56,32768
QS2%=:
W 0
Mvvvz 0,128,192
Rv 208,245,4096
Mvvvz 4,128,196
Rv 212,245,5120
Mvvvz 8,128,200
Mvvvz 12,128,204
Rv 216,245,6144
Mvvvz 28,132,204
Mvvvz 24,132,200
Rv 220,245,7168
Mvvvz 20,132,196
Mvvvz 16,132,192
T 160,240,24576
Mvvvz 32,136,192
T 162,240,25600
Mvvvz 36,136,196
X 242,32,240
Mvvvz 40,136,200
T 164,242,24576
Mvvvz 44,136,204
T 166,242,25600
Mvvvz 60,140,204
X 241,64,240
Mvvvz 56,140,200
T 168,241,24576
Mvvvz 52,140,196
T 170,241,25600
Mvvvz 48,140,192
X 242,96,240
Mvvvz 64,144,192
T 172,242,24576
Mvvvz 68,144,196
T 174,242,25600
Mvvvz 72,144,200
X 241,128,240
Mvvvz 76,144,204
T 176,241,24576
Mvvvz 92,148,204
T 178,241,25600
Mvvvz 88,148,200
X 242,160,240
Mvvvz 84,148,196
T 180,242,24576
Mvvvz 80,148,192
T 182,242,25600
Mvvvz 96,152,192
X 241,192,240
Mvvvz 100,152,196
T 184,241,24576
Mvvvz 104,152,200
T 186,241,25600
Mvvvz 108,152,204
X 242,224,240
Mvvvz 124,156,204
T 188,242,24576
Mvvvz 120,156,200
T 190,242,25600
Mvvvz 116,156,196
Mvvvz 112,156,192
WV 12
B
Mvvvc 0,160,208
Rv 192,244,0
Mvvvc 4,160,212
Rv 196,244,1024
Mvvvc 8,160,216
s_add_u32 s55, s52, 0x180000
M0 122880
Mvvvc 12,160,220
D 236,44,55,0
Mvvvc 28,164,220
Rv 200,244,2048
Mvvvc 24,164,216
D 237,44,55,1024
Mvvvc 20,164,212
Rv 204,244,3072
Mvvvc 16,164,208
D 238,44,55,2048
Mvvvc 32,168,208
T 128,240,32768
Mvvvc 36,168,212
T 130,240,33792
X 242,32,240
Mvvvc 40,168,216
T 132,242,32768
T 134,242,33792
D 239,44,55,3072
Mvvvc 44,168,220
s_add_u32 s55, s52, 0x140000
M0 114688
Mvvvc 60,172,220
X 241,64,240
D 236,44,55,0
Mvvvc 56,172,216
T 136,241,32768
T 138,241,33792
Mvvvc 52,172,212
X 242,96,240
D 237,44,55,1024
Mvvvc 48,172,208
T 140,242,32768
Mvvvc 64,176,208
T 142,242,33792
X 241,128,240
Mvvvc 68,176,212
T 144,241,32768
D 238,44,55,2048
Mvvvc 72,176,216
T 146,241,33792
X 242,160,240
Mvvvc 76,176,220
T 148,242,32768
T 150,242,33792
D 239,44,55,3072
Mvvvc 92,180,220
X 241,192,240
AS 55,53,49152
M0 81920
Mvvvc 88,180,216
T 152,241,32768
Ds 243,48,55,0
Mvvvc 84,180,212
T 154,241,33792
Mvvvc 80,180,208
X 242,224,240
T 156,242,32768
Mvvvc 96,184,208
T 158,242,33792
Ds 243,48,55,1024
Mvvvc 100,184,212
Mvvvc 104,184,216
Ds 243,48,55,2048
Mvvvc 108,184,220
Mvvvc 124,188,220
Ds 243,48,55,3072
Mvvvc 120,188,216
Mvvvc 116,188,212
Mvvvc 112,188,208
W 0
Mvvvc 0,128,192
Rv 208,244,4096
Mvvvc 4,128,196
Rv 212,244,5120
Mvvvc 8,128,200
AS 55,54,49152
M0 65536
Mvvvc 12,128,204
Ds 243,48,55,0
Mvvvc 28,132,204
Rv 216,244,6144
Mvvvc 24,132,200
Ds 243,48,55,1024
Mvvvc 20,132,196
Rv 220,244,7168
Mvvvc 16,132,192
Ds 243,48,55,2048
Mvvvc 32,136,192
T 160,240,40960
Mvvvc 36,136,196
T 162,240,41984
X 242,32,240
Mvvvc 40,136,200
T 164,242,40960
T 166,242,41984
Ds 243,48,55,3072
Mvvvc 44,136,204
AS 55,53,57344
M0 86016
Mvvvc 60,140,204
X 241,64,240
Ds 243,48,55,0
Mvvvc 56,140,200
T 168,241,40960
T 170,241,41984
Mvvvc 52,140,196
X 242,96,240
Ds 243,48,55,1024
Mvvvc 48,140,192
T 172,242,40960
Mvvvc 64,144,192
T 174,242,41984
X 241,128,240
Mvvvc 68,144,196
T 176,241,40960
Ds 243,48,55,2048
Mvvvc 72,144,200
T 178,241,41984
X 242,160,240
Mvvvc 76,144,204
T 180,242,40960
T 182,242,41984
Ds 243,48,55,3072
Mvvvc 92,148,204
X 241,192,240
AS 55,54,57344
M0 69632
Mvvvc 88,148,200
T 184,241,40960
Ds 243,48,55,0
Mvvvc 84,148,196
T 186,241,41984
Mvvvc 80,148,192
X 242,224,240
T 188,242,40960
Mvvvc 96,152,192
T 190,242,41984
Ds 243,48,55,1024
Mvvvc 100,152,196
Mvvvc 104,152,200
Ds 243,48,55,2048
Mvvvc 108,152,204
Mvvvc 124,156,204
Ds 243,48,55,3072
Mvvvc 120,156,200
Mvvvc 116,156,196
Mvvvc 112,156,192
WV 24
B
Mvvvc 0,160,208
Rv 192,244,32768
Mvvvc 4,160,212
Rv 196,244,33792
Mvvvc 8,160,216
s_add_u32 s55, s52, 0x200000
M0 139264
Mvvvc 12,160,220
D 236,44,55,0
Mvvvc 28,164,220
Rv 200,244,34816
)A"
R"A(Mvvvc 24,164,216
D 237,44,55,1024
Mvvvc 20,164,212
Rv 204,244,35840
Mvvvc 16,164,208
D 238,44,55,2048
Mvvvc 32,168,208
T 128,240,0
Mvvvc 36,168,212
T 130,240,1024
X 242,32,240
Mvvvc 40,168,216
T 132,242,0
T 134,242,1024
D 239,44,55,3072
Mvvvc 44,168,220
s_add_u32 s55, s52, 0x1c0000
M0 131072
Mvvvc 60,172,220
X 241,64,240
D 236,44,55,0
Mvvvc 56,172,216
T 136,241,0
T 138,241,1024
Mvvvc 52,172,212
X 242,96,240
D 237,44,55,1024
Mvvvc 48,172,208
T 140,242,0
Mvvvc 64,176,208
T 142,242,1024
X 241,128,240
Mvvvc 68,176,212
T 144,241,0
D 238,44,55,2048
Mvvvc 72,176,216
T 146,241,1024
X 242,160,240
Mvvvc 76,176,220
T 148,242,0
T 150,242,1024
D 239,44,55,3072
Mvvvc 92,180,220
X 241,192,240
AS 55,53,65536
M0 16384
Mvvvc 88,180,216
T 152,241,0
Ds 243,48,55,0
Mvvvc 84,180,212
T 154,241,1024
Mvvvc 80,180,208
X 242,224,240
T 156,242,0
Mvvvc 96,184,208
T 158,242,1024
Ds 243,48,55,1024
Mvvvc 100,184,212
Mvvvc 104,184,216
Ds 243,48,55,2048
Mvvvc 108,184,220
Mvvvc 124,188,220
Ds 243,48,55,3072
Mvvvc 120,188,216
Mvvvc 116,188,212
Mvvvc 112,188,208
W 0
Mvvvc 0,128,192
Rv 208,244,36864
Mvvvc 4,128,196
Rv 212,244,37888
Mvvvc 8,128,200
AS 55,54,65536
M0 0
Mvvvc 12,128,204
Ds 243,48,55,0
Mvvvc 28,132,204
Rv 216,244,38912
Mvvvc 24,132,200
Ds 243,48,55,1024
Mvvvc 20,132,196
Rv 220,244,39936
Mvvvc 16,132,192
Ds 243,48,55,2048
Mvvvc 32,136,192
T 160,240,8192
Mvvvc 36,136,196
T 162,240,9216
X 242,32,240
Mvvvc 40,136,200
T 164,242,8192
T 166,242,9216
Ds 243,48,55,3072
Mvvvc 44,136,204
AS 55,53,73728
M0 20480
Mvvvc 60,140,204
X 241,64,240
Ds 243,48,55,0
Mvvvc 56,140,200
T 168,241,8192
T 170,241,9216
Mvvvc 52,140,196
X 242,96,240
Ds 243,48,55,1024
Mvvvc 48,140,192
T 172,242,8192
Mvvvc 64,144,192
T 174,242,9216
X 241,128,240
Mvvvc 68,144,196
T 176,241,8192
Ds 243,48,55,2048
Mvvvc 72,144,200
T 178,241,9216
X 242,160,240
Mvvvc 76,144,204
T 180,242,8192
T 182,242,9216
Ds 243,48,55,3072
Mvvvc 92,148,204
X 241,192,240
AS 55,54,73728
M0 4096
Mvvvc 88,148,200
T 184,241,8192
Ds 243,48,55,0
Mvvvc 84,148,196
T 186,241,9216
Mvvvc 80,148,192
X 242,224,240
T 188,242,8192
Mvvvc 96,152,192
T 190,242,9216
Ds 243,48,55,1024
Mvvvc 100,152,196
Mvvvc 104,152,200
Ds 243,48,55,2048
Mvvvc 108,152,204
Mvvvc 124,156,204
Ds 243,48,55,3072
Mvvvc 120,156,200
Mvvvc 116,156,196
Mvvvc 112,156,192
WV 24
B
Mvvvc 0,160,208
Rv 192,245,0
Mvvvc 4,160,212
Rv 196,245,1024
Mvvvc 8,160,216
s_add_u32 s55, s52, 0x280000
M0 106496
Mvvvc 12,160,220
D 236,44,55,0
Mvvvc 28,164,220
Rv 200,245,2048
Mvvvc 24,164,216
D 237,44,55,1024
Mvvvc 20,164,212
Rv 204,245,3072
Mvvvc 16,164,208
D 238,44,55,2048
Mvvvc 32,168,208
T 128,240,16384
Mvvvc 36,168,212
T 130,240,17408
X 242,32,240
Mvvvc 40,168,216
T 132,242,16384
T 134,242,17408
D 239,44,55,3072
Mvvvc 44,168,220
s_add_u32 s55, s52, 0x240000
M0 98304
Mvvvc 60,172,220
X 241,64,240
D 236,44,55,0
Mvvvc 56,172,216
T 136,241,16384
T 138,241,17408
Mvvvc 52,172,212
X 242,96,240
D 237,44,55,1024
Mvvvc 48,172,208
T 140,242,16384
Mvvvc 64,176,208
T 142,242,17408
X 241,128,240
Mvvvc 68,176,212
T 144,241,16384
D 238,44,55,2048
Mvvvc 72,176,216
T 146,241,17408
X 242,160,240
Mvvvc 76,176,220
T 148,242,16384
T 150,242,17408
D 239,44,55,3072
Mvvvc 92,180,220
X 241,192,240
AS 55,53,81920
M0 49152
Mvvvc 88,180,216
T 152,241,16384
Ds 243,48,55,0
Mvvvc 84,180,212
T 154,241,17408
Mvvvc 80,180,208
X 242,224,240
T 156,242,16384
Mvvvc 96,184,208
T 158,242,17408
Ds 243,48,55,1024
Mvvvc 100,184,212
Mvvvc 104,184,216
Ds 243,48,55,2048
Mvvvc 108,184,220
Mvvvc 124,188,220
Ds 243,48,55,3072
Mvvvc 120,188,216
Mvvvc 116,188,212
Mvvvc 112,188,208
W 0
Mvvvc 0,128,192
Rv 208,245,4096
Mvvvc 4,128,196
Rv 212,245,5120
Mvvvc 8,128,200
AS 55,54,81920
M0 32768
Mvvvc 12,128,204
Ds 243,48,55,0
Mvvvc 28,132,204
Rv 216,245,6144
Mvvvc 24,132,200
Ds 243,48,55,1024
Mvvvc 20,132,196
Rv 220,245,7168
Mvvvc 16,132,192
Ds 243,48,55,2048
Mvvvc 32,136,192
T 160,240,24576
Mvvvc 36,136,196
T 162,240,25600
X 242,32,240
Mvvvc 40,136,200
T 164,242,24576
T 166,242,25600
Ds 243,48,55,3072
Mvvvc 44,136,204
AS 55,53,90112
M0 53248
Mvvvc 60,140,204
X 241,64,240
Ds 243,48,55,0
Mvvvc 56,140,200
T 168,241,24576
T 170,241,25600
Mvvvc 52,140,196
X 242,96,240
Ds 243,48,55,1024
Mvvvc 48,140,192
T 172,242,24576
Mvvvc 64,144,192
T 174,242,25600
X 241,128,240
Mvvvc 68,144,196
T 176,241,24576
Ds 243,48,55,2048
Mvvvc 72,144,200
T 178,241,25600
X 242,160,240
Mvvvc 76,144,204
T 180,242,24576
T 182,242,25600
Ds 243,48,55,3072
Mvvvc 92,148,204
X 241,192,240
AS 55,54,90112
M0 36864
Mvvvc 88,148,200
T 184,241,24576
Ds 243,48,55,0
Mvvvc 84,148,196
T 186,241,25600
Mvvvc 80,148,192
X 242,224,240
T 188,242,24576
Mvvvc 96,152,192
T 190,242,25600
Ds 243,48,55,1024
Mvvvc 100,152,196
Mvvvc 104,152,200
Ds 243,48,55,2048
Mvvvc 108,152,204
Mvvvc 124,156,204
Ds 243,48,55,3072
Mvvvc 120,156,200
Mvvvc 116,156,196
Mvvvc 112,156,192
WV 24
B
Mvvvc 0,160,208
Rv 192,244,0
Mvvvc 4,160,212
Rv 196,244,1024
Mvvvc 8,160,216
s_add_u32 s55, s52, 0x300000
M0 122880
Mvvvc 12,160,220
D 236,44,55,0
Mvvvc 28,164,220
Rv 200,244,2048
Mvvvc 24,164,216
D 237,44,55,1024
Mvvvc 20,164,212
Rv 204,244,3072
Mvvvc 16,164,208
D 238,44,55,2048
Mvvvc 32,168,208
T 128,240,32768
Mvvvc 36,168,212
T 130,240,33792
X 242,32,240
Mvvvc 40,168,216
T 132,242,32768
T 134,242,33792
D 239,44,55,3072
Mvvvc 44,168,220
s_add_u32 s55, s52, 0x2c0000
M0 114688
Mvvvc 60,172,220
X 241,64,240
D 236,44,55,0
Mvvvc 56,172,216
T 136,241,32768
T 138,241,33792
Mvvvc 52,172,212
X 242,96,240
D 237,44,55,1024
Mvvvc 48,172,208
T 140,242,32768
Mvvvc 64,176,208
T 142,242,33792
X 241,128,240
Mvvvc 68,176,212
T 144,241,32768
D 238,44,55,2048
Mvvvc 72,176,216
T 146,241,33792
X 242,160,240
Mvvvc 76,176,220
T 148,242,32768
T 150,242,33792
D 239,44,55,3072
Mvvvc 92,180,220
X 241,192,240
AS 55,53,98304
M0 81920
Mvvvc 88,180,216
T 152,241,32768
Ds 243,48,55,0
Mvvvc 84,180,212
T 154,241,33792
Mvvvc 80,180,208
X 242,224,240
T 156,242,32768
Mvvvc 96,184,208
T 158,242,33792
Ds 243,48,55,1024
Mvvvc 100,184,212
Mvvvc 104,184,216
Ds 243,48,55,2048
Mvvvc 108,184,220
Mvvvc 124,188,220
Ds 243,48,55,3072
Mvvvc 120,188,216
Mvvvc 116,188,212
Mvvvc 112,188,208
W 0
Mvvvc 0,128,192
Rv 208,244,4096
Mvvvc 4,128,196
Rv 212,244,5120
Mvvvc 8,128,200
AS 55,54,98304
M0 65536
Mvvvc 12,128,204
Ds 243,48,55,0
Mvvvc 28,132,204
Rv 216,244,6144
Mvvvc 24,132,200
Ds 243,48,55,1024
Mvvvc 20,132,196
Rv 220,244,7168
Mvvvc 16,132,192
Ds 243,48,55,2048
Mvvvc 32,136,192
T 160,240,40960
Mvvvc 36,136,196
T 162,240,41984
X 242,32,240
Mvvvc 40,136,200
T 164,242,40960
T 166,242,41984
Ds 243,48,55,3072
Mvvvc 44,136,204
AS 55,53,106496
M0 86016
Mvvvc 60,140,204
X 241,64,240
Ds 243,48,55,0
Mvvvc 56,140,200
T 168,241,40960
T 170,241,41984
Mvvvc 52,140,196
X 242,96,240
Ds 243,48,55,1024
Mvvvc 48,140,192
T 172,242,40960
Mvvvc 64,144,192
T 174,242,41984
X 241,128,240
Mvvvc 68,144,196
T 176,241,40960
Ds 243,48,55,2048
Mvvvc 72,144,200
T 178,241,41984
X 242,160,240
Mvvvc 76,144,204
T 180,242,40960
T 182,242,41984
Ds 243,48,55,3072
Mvvvc 92,148,204
X 241,192,240
AS 55,54,106496
M0 69632
Mvvvc 88,148,200
T 184,241,40960
Ds 243,48,55,0
Mvvvc 84,148,196
T 186,241,41984
Mvvvc 80,148,192
X 242,224,240
T 188,242,40960
Mvvvc 96,152,192
T 190,242,41984
Ds 243,48,55,1024
Mvvvc 100,152,196
Mvvvc 104,152,200
Ds 243,48,55,2048
Mvvvc 108,152,204
Mvvvc 124,156,204
Ds 243,48,55,3072
Mvvvc 120,156,200
Mvvvc 116,156,196
Mvvvc 112,156,192
WV 24
B
Mvvvc 0,160,208
Rv 192,244,32768
G 224,0
Mvvvc 4,160,212
Rv 196,244,33792
G 225,1
Mvvvc 8,160,216
G 226,2
G 227,3
Mvvvc 12,160,220
Rv 200,244,34816
G 228,32
Mvvvc 28,164,220
G 229,33
G 230,34
Mvvvc 24,164,216
Rv 204,244,35840
G 231,35
Mvvvc 20,164,212
C 232,224,225
C 233,226,227
Mvvvc 16,164,208
T 128,240,0
C 234,228,229
Mvvvc 32,168,208
T 130,240,1024
C 235,230,231
Mvvvc 36,168,212
X 242,32,240
Svs 232,243,36,40,0
Mvvvc 40,168,216
T 132,242,0
G 224,4
Mvvvc 44,168,220
T 134,242,1024
G 225,5
Mvvvc 60,172,220
X 241,64,240
G 226,6
Mvvvc 56,172,216
T 136,241,0
G 227,7
Mvvvc 52,172,212
T 138,241,1024
G 228,36
Mvvvc 48,172,208
X 242,96,240
G 229,37
Mvvvc 64,176,208
T 140,242,0
G 230,38
Mvvvc 68,176,212
T 142,242,1024
G 231,39
Mvvvc 72,176,216
X 241,128,240
C 232,224,225
Mvvvc 76,176,220
T 144,241,0
C 233,226,227
Mvvvc 92,180,220
T 146,241,1024
C 234,228,229
Mvvvc 88,180,216
X 242,160,240
C 235,230,231
Mvvvc 84,180,212
T 148,242,0
Svs 232,243,36,40,1024
Mvvvc 80,180,208
T 150,242,1024
G 224,8
Mvvvc 96,184,208
X 241,192,240
G 225,9
Mvvvc 100,184,212
T 152,241,0
G 226,10
Mvvvc 104,184,216
T 154,241,1024
G 227,11
Mvvvc 108,184,220
X 242,224,240
G 228,40
Mvvvc 124,188,220
T 156,242,0
G 229,41
Mvvvc 120,188,216
T 158,242,1024
G 230,42
Mvvvc 116,188,212
G 231,43
Mvvvc 112,188,208
C 232,224,225
C 233,226,227
C 234,228,229
C 235,230,231
Svs 232,243,36,40,2048
G 224,12
G 225,13
G 226,14
G 227,15
G 228,44
G 229,45
G 230,46
G 231,47
C 232,224,225
C 233,226,227
C 234,228,229
C 235,230,231
Svs 232,243,36,40,3072
W 0
Mvvvc 0,128,192
Rv 208,244,36864
AS 40,40,4096
Mvvvc 4,128,196
Rv 212,244,37888
G 224,16
Mvvvc 8,128,200
G 225,17
G 226,18
Mvvvc 12,128,204
Rv 216,244,38912
G 227,19
Mvvvc 28,132,204
G 228,48
G 229,49
Mvvvc 24,132,200
Rv 220,244,39936
G 230,50
Mvvvc 20,132,196
G 231,51
C 232,224,225
Mvvvc 16,132,192
T 160,240,8192
C 233,226,227
Mvvvc 32,136,192
T 162,240,9216
C 234,228,229
Mvvvc 36,136,196
X 242,32,240
C 235,230,231
Mvvvc 40,136,200
T 164,242,8192
Svs 232,243,36,40,0
Mvvvc 44,136,204
T 166,242,9216
G 224,20
Mvvvc 60,140,204
X 241,64,240
G 225,21
Mvvvc 56,140,200
T 168,241,8192
G 226,22
Mvvvc 52,140,196
T 170,241,9216
G 227,23
Mvvvc 48,140,192
X 242,96,240
G 228,52
Mvvvc 64,144,192
T 172,242,8192
G 229,53
Mvvvc 68,144,196
T 174,242,9216
G 230,54
Mvvvc 72,144,200
X 241,128,240
G 231,55
Mvvvc 76,144,204
T 176,241,8192
C 232,224,225
Mvvvc 92,148,204
T 178,241,9216
C 233,226,227
Mvvvc 88,148,200
X 242,160,240
C 234,228,229
Mvvvc 84,148,196
T 180,242,8192
C 235,230,231
Mvvvc 80,148,192
T 182,242,9216
Svs 232,243,36,40,1024
Mvvvc 96,152,192
X 241,192,240
G 224,24
Mvvvc 100,152,196
T 184,241,8192
G 225,25
Mvvvc 104,152,200
T 186,241,9216
G 226,26
Mvvvc 108,152,204
X 242,224,240
G 227,27
Mvvvc 124,156,204
T 188,242,8192
G 228,56
Mvvvc 120,156,200
T 190,242,9216
G 229,57
Mvvvc 116,156,196
G 230,58
Mvvvc 112,156,192
G 231,59
C 232,224,225
C 233,226,227
C 234,228,229
C 235,230,231
Svs 232,243,36,40,2048
G 224,28
G 225,29
G 226,30
G 227,31
G 228,60
G 229,61
G 230,62
G 231,63
C 232,224,225
C 233,226,227
C 234,228,229
C 235,230,231
Svs 232,243,36,40,3072
WV 8
B
s_getpc_b64 s[16:17]
1:
s_add_u32 s16, s16, QE2%=-1b
s_addc_u32 s17, s17, 0
s_lshr_b32 s21, s10, 3
v_lshlrev_b32 v249, 5, v254
v_add_u32 v249, s21, v249
v_min_u32 v249, ((AE2%=-QE2%=)>>7), v249
v_lshlrev_b32 v249, 7, v249
global_load_dword v248, v249, s[16:17]
Mvvvc 0,160,208
Rv 192,245,0
AS 40,40,4096
Mvvvc 4,160,212
Rv 196,245,1024
G 224,64
Mvvvc 8,160,216
G 225,65
G 226,66
Mvvvc 12,160,220
Rv 200,245,2048
Mvvvc 28,164,220
G 227,67
G 228,96
Mvvvc 24,164,216
Rv 204,245,3072
G 229,97
Mvvvc 20,164,212
G 230,98
G 231,99
Mvvvc 16,164,208
T 128,240,16384
C 232,224,225
Mvvvc 32,168,208
T 130,240,17408
C 233,226,227
Mvvvc 36,168,212
X 242,32,240
C 234,228,229
Mvvvc 40,168,216
T 132,242,16384
C 235,230,231
Mvvvc 44,168,220
T 134,242,17408
Svs 232,243,36,40,0
Mvvvc 60,172,220
X 241,64,240
G 224,68
Mvvvc 56,172,216
T 136,241,16384
G 225,69
Mvvvc 52,172,212
T 138,241,17408
G 226,70
Mvvvc 48,172,208
X 242,96,240
G 227,71
Mvvvc 64,176,208
T 140,242,16384
G 228,100
Mvvvc 68,176,212
T 142,242,17408
G 229,101
Mvvvc 72,176,216
X 241,128,240
G 230,102
Mvvvc 76,176,220
T 144,241,16384
G 231,103
Mvvvc 92,180,220
T 146,241,17408
C 232,224,225
Mvvvc 88,180,216
X 242,160,240
C 233,226,227
Mvvvc 84,180,212
T 148,242,16384
C 234,228,229
Mvvvc 80,180,208
T 150,242,17408
C 235,230,231
Mvvvc 96,184,208
X 241,192,240
Svs 232,243,36,40,1024
Mvvvc 100,184,212
T 152,241,16384
G 224,72
Mvvvc 104,184,216
T 154,241,17408
G 225,73
Mvvvc 108,184,220
X 242,224,240
G 226,74
Mvvvc 124,188,220
T 156,242,16384
G 227,75
Mvvvc 120,188,216
T 158,242,17408
G 228,104
Mvvvc 116,188,212
G 229,105
Mvvvc 112,188,208
G 230,106
)A"
R"A(G 231,107
C 232,224,225
C 233,226,227
C 234,228,229
C 235,230,231
Svs 232,243,36,40,2048
W 0
s_load_dwordx2 s[42:43], s[8:9], 40
Mvvvc 0,128,192
Rv 208,245,4096
G 224,76
Mvvvc 4,128,196
Rv 212,245,5120
G 225,77
Mvvvc 8,128,200
G 226,78
G 227,79
Mvvvc 12,128,204
Rv 216,245,6144
G 228,108
Mvvvc 28,132,204
G 229,109
G 230,110
Mvvvc 24,132,200
Rv 220,245,7168
G 231,111
Mvvvc 20,132,196
C 232,224,225
C 233,226,227
Mvvvc 16,132,192
T 160,240,24576
C 234,228,229
Mvvvc 32,136,192
T 162,240,25600
C 235,230,231
Mvvvc 36,136,196
X 242,32,240
Svs 232,243,36,40,3072
Mvvvc 40,136,200
T 164,242,24576
AS 40,40,4096
Mvvvc 44,136,204
T 166,242,25600
G 224,80
Mvvvc 60,140,204
X 241,64,240
G 225,81
Mvvvc 56,140,200
T 168,241,24576
G 226,82
Mvvvc 52,140,196
T 170,241,25600
G 227,83
Mvvvc 48,140,192
X 242,96,240
G 228,112
Mvvvc 64,144,192
T 172,242,24576
G 229,113
Mvvvc 68,144,196
T 174,242,25600
G 230,114
Mvvvc 72,144,200
X 241,128,240
G 231,115
Mvvvc 76,144,204
T 176,241,24576
C 232,224,225
Mvvvc 92,148,204
T 178,241,25600
C 233,226,227
Mvvvc 88,148,200
X 242,160,240
C 234,228,229
Mvvvc 84,148,196
T 180,242,24576
C 235,230,231
Mvvvc 80,148,192
T 182,242,25600
Svs 232,243,36,40,0
Mvvvc 96,152,192
X 241,192,240
G 224,84
Mvvvc 100,152,196
T 184,241,24576
G 225,85
Mvvvc 104,152,200
T 186,241,25600
G 226,86
Mvvvc 108,152,204
X 242,224,240
G 227,87
Mvvvc 124,156,204
T 188,242,24576
G 228,116
Mvvvc 120,156,200
T 190,242,25600
G 229,117
Mvvvc 116,156,196
G 230,118
Mvvvc 112,156,192
G 231,119
C 232,224,225
C 233,226,227
C 234,228,229
C 235,230,231
Svs 232,243,36,40,1024
G 224,88
G 225,89
G 226,90
G 227,91
G 228,120
G 229,121
G 230,122
G 231,123
C 232,224,225
C 233,226,227
C 234,228,229
C 235,230,231
Svs 232,243,36,40,2048
W 0
B
Mvvvc 0,160,208
Rv 192,244,0
G 224,92
Mvvvc 4,160,212
Rv 196,244,1024
G 225,93
Mvvvc 8,160,216
G 226,94
G 227,95
Mvvvc 12,160,220
Rv 200,244,2048
Mvvvc 28,164,220
G 228,124
G 229,125
Mvvvc 24,164,216
Rv 204,244,3072
G 230,126
Mvvvc 20,164,212
G 231,127
C 232,224,225
Mvvvc 16,164,208
T 128,240,32768
C 233,226,227
Mvvvc 32,168,208
T 130,240,33792
C 234,228,229
Mvvvc 36,168,212
X 242,32,240
C 235,230,231
Mvvvc 40,168,216
T 132,242,32768
Svs 232,243,36,40,3072
Mvvvc 44,168,220
T 134,242,33792
AS 40,40,4096
Mvvvc 60,172,220
X 241,64,240
G 224,128
Mvvvc 56,172,216
T 136,241,32768
G 225,129
Mvvvc 52,172,212
T 138,241,33792
G 226,130
Mvvvc 48,172,208
X 242,96,240
G 227,131
Mvvvc 64,176,208
T 140,242,32768
G 228,160
Mvvvc 68,176,212
T 142,242,33792
G 229,161
Mvvvc 72,176,216
X 241,128,240
G 230,162
Mvvvc 76,176,220
T 144,241,32768
G 231,163
Mvvvc 92,180,220
T 146,241,33792
C 232,224,225
Mvvvc 88,180,216
X 242,160,240
C 233,226,227
Mvvvc 84,180,212
T 148,242,32768
C 234,228,229
Mvvvc 80,180,208
T 150,242,33792
C 235,230,231
Mvvvc 96,184,208
X 241,192,240
Svs 232,243,36,40,0
Mvvvc 100,184,212
T 152,241,32768
G 224,132
Mvvvc 104,184,216
T 154,241,33792
G 225,133
Mvvvc 108,184,220
X 242,224,240
G 226,134
Mvvvc 124,188,220
T 156,242,32768
G 227,135
Mvvvc 120,188,216
T 158,242,33792
G 228,164
Mvvvc 116,188,212
G 229,165
Mvvvc 112,188,208
G 230,166
G 231,167
C 232,224,225
C 233,226,227
C 234,228,229
C 235,230,231
Svs 232,243,36,40,1024
W 0
Mvvvc 0,128,192
Rv 208,244,4096
G 224,136
Mvvvc 4,128,196
Rv 212,244,5120
G 225,137
Mvvvc 8,128,200
G 226,138
G 227,139
Mvvvc 12,128,204
Rv 216,244,6144
G 228,168
Mvvvc 28,132,204
G 229,169
G 230,170
Mvvvc 24,132,200
Rv 220,244,7168
G 231,171
Mvvvc 20,132,196
C 232,224,225
C 233,226,227
Mvvvc 16,132,192
T 160,240,40960
C 234,228,229
Mvvvc 32,136,192
T 162,240,41984
C 235,230,231
Mvvvc 36,136,196
X 242,32,240
Svs 232,243,36,40,2048
Mvvvc 40,136,200
T 164,242,40960
G 224,140
Mvvvc 44,136,204
T 166,242,41984
G 225,141
Mvvvc 60,140,204
X 241,64,240
G 226,142
Mvvvc 56,140,200
T 168,241,40960
G 227,143
Mvvvc 52,140,196
T 170,241,41984
G 228,172
Mvvvc 48,140,192
X 242,96,240
G 229,173
Mvvvc 64,144,192
T 172,242,40960
G 230,174
Mvvvc 68,144,196
T 174,242,41984
G 231,175
Mvvvc 72,144,200
X 241,128,240
C 232,224,225
Mvvvc 76,144,204
T 176,241,40960
C 233,226,227
Mvvvc 92,148,204
T 178,241,41984
C 234,228,229
Mvvvc 88,148,200
X 242,160,240
C 235,230,231
Mvvvc 84,148,196
T 180,242,40960
Svs 232,243,36,40,3072
Mvvvc 80,148,192
T 182,242,41984
AS 40,40,4096
Mvvvc 96,152,192
X 241,192,240
G 224,144
Mvvvc 100,152,196
T 184,241,40960
G 225,145
Mvvvc 104,152,200
T 186,241,41984
G 226,146
Mvvvc 108,152,204
X 242,224,240
G 227,147
Mvvvc 124,156,204
T 188,242,40960
G 228,176
Mvvvc 120,156,200
T 190,242,41984
G 229,177
Mvvvc 116,156,196
G 230,178
Mvvvc 112,156,192
G 231,179
C 232,224,225
C 233,226,227
C 234,228,229
C 235,230,231
Svs 232,243,36,40,0
G 224,148
G 225,149
G 226,150
G 227,151
G 228,180
G 229,181
G 230,182
G 231,183
C 232,224,225
C 233,226,227
C 234,228,229
C 235,230,231
Svs 232,243,36,40,1024
W 0
B
Mvvvc 0,160,208
Rv 192,244,32768
G 224,152
Mvvvc 4,160,212
Rv 196,244,33792
G 225,153
Mvvvc 8,160,216
G 226,154
G 227,155
Mvvvc 12,160,220
Rv 200,244,34816
Mvvvc 28,164,220
G 228,184
G 229,185
Mvvvc 24,164,216
Rv 204,244,35840
G 230,186
Mvvvc 20,164,212
G 231,187
C 232,224,225
Mvvvc 16,164,208
T 128,240,0
C 233,226,227
Mvvvc 32,168,208
T 130,240,1024
C 234,228,229
Mvvvc 36,168,212
X 242,32,240
C 235,230,231
Mvvvc 40,168,216
T 132,242,0
Svs 232,243,36,40,2048
Mvvvc 44,168,220
T 134,242,1024
G 224,156
Mvvvc 60,172,220
X 241,64,240
G 225,157
Mvvvc 56,172,216
T 136,241,0
G 226,158
Mvvvc 52,172,212
T 138,241,1024
G 227,159
Mvvvc 48,172,208
X 242,96,240
G 228,188
Mvvvc 64,176,208
T 140,242,0
G 229,189
Mvvvc 68,176,212
T 142,242,1024
G 230,190
Mvvvc 72,176,216
X 241,128,240
G 231,191
Mvvvc 76,176,220
T 144,241,0
C 232,224,225
Mvvvc 92,180,220
T 146,241,1024
C 233,226,227
Mvvvc 88,180,216
X 242,160,240
C 234,228,229
Mvvvc 84,180,212
T 148,242,0
C 235,230,231
Mvvvc 80,180,208
T 150,242,1024
Svs 232,243,36,40,3072
Mvvvc 96,184,208
X 241,192,240
AS 40,40,4096
Mvvvc 100,184,212
T 152,241,0
G 224,192
Mvvvc 104,184,216
T 154,241,1024
G 225,193
Mvvvc 108,184,220
X 242,224,240
G 226,194
Mvvvc 124,188,220
T 156,242,0
G 227,195
Mvvvc 120,188,216
T 158,242,1024
G 228,224
Mvvvc 116,188,212
G 229,225
Mvvvc 112,188,208
G 230,226
G 231,227
C 232,224,225
C 233,226,227
C 234,228,229
C 235,230,231
Svs 232,243,36,40,0
W 0
Mvvvc 0,128,192
Rv 208,244,36864
G 224,196
Mvvvc 4,128,196
Rv 212,244,37888
G 225,197
Mvvvc 8,128,200
G 226,198
G 227,199
Mvvvc 12,128,204
Rv 216,244,38912
G 228,228
Mvvvc 28,132,204
G 229,229
G 230,230
Mvvvc 24,132,200
Rv 220,244,39936
G 231,231
Mvvvc 20,132,196
C 232,224,225
C 233,226,227
Mvvvc 16,132,192
T 160,240,8192
C 234,228,229
Mvvvc 32,136,192
T 162,240,9216
C 235,230,231
Mvvvc 36,136,196
X 242,32,240
Svs 232,243,36,40,1024
Mvvvc 40,136,200
T 164,242,8192
G 224,200
Mvvvc 44,136,204
T 166,242,9216
G 225,201
Mvvvc 60,140,204
X 241,64,240
G 226,202
Mvvvc 56,140,200
T 168,241,8192
G 227,203
Mvvvc 52,140,196
T 170,241,9216
G 228,232
Mvvvc 48,140,192
X 242,96,240
G 229,233
Mvvvc 64,144,192
T 172,242,8192
G 230,234
Mvvvc 68,144,196
T 174,242,9216
G 231,235
Mvvvc 72,144,200
X 241,128,240
C 232,224,225
Mvvvc 76,144,204
T 176,241,8192
C 233,226,227
Mvvvc 92,148,204
T 178,241,9216
C 234,228,229
Mvvvc 88,148,200
X 242,160,240
C 235,230,231
Mvvvc 84,148,196
T 180,242,8192
Svs 232,243,36,40,2048
Mvvvc 80,148,192
T 182,242,9216
G 224,204
Mvvvc 96,152,192
X 241,192,240
G 225,205
Mvvvc 100,152,196
T 184,241,8192
G 226,206
Mvvvc 104,152,200
T 186,241,9216
G 227,207
Mvvvc 108,152,204
X 242,224,240
G 228,236
Mvvvc 124,156,204
T 188,242,8192
G 229,237
Mvvvc 120,156,200
T 190,242,9216
G 230,238
Mvvvc 116,156,196
G 231,239
Mvvvc 112,156,192
C 232,224,225
C 233,226,227
C 234,228,229
C 235,230,231
Svs 232,243,36,40,3072
AS 40,40,4096
G 224,208
G 225,209
G 226,210
G 227,211
G 228,240
G 229,241
G 230,242
G 231,243
C 232,224,225
C 233,226,227
C 234,228,229
C 235,230,231
Svs 232,243,36,40,0
W 0
B
Mvvvc 0,160,208
Rv 192,245,0
G 224,212
Mvvvc 4,160,212
Rv 196,245,1024
G 225,213
Mvvvc 8,160,216
G 226,214
G 227,215
Mvvvc 12,160,220
Rv 200,245,2048
Mvvvc 28,164,220
G 228,244
G 229,245
Mvvvc 24,164,216
Rv 204,245,3072
G 230,246
Mvvvc 20,164,212
G 231,247
C 232,224,225
Mvvvc 16,164,208
T 128,240,16384
C 233,226,227
Mvvvc 32,168,208
T 130,240,17408
C 234,228,229
Mvvvc 36,168,212
X 242,32,240
C 235,230,231
Mvvvc 40,168,216
T 132,242,16384
Svs 232,243,36,40,1024
Mvvvc 44,168,220
T 134,242,17408
G 224,216
Mvvvc 60,172,220
X 241,64,240
G 225,217
Mvvvc 56,172,216
T 136,241,16384
G 226,218
Mvvvc 52,172,212
T 138,241,17408
G 227,219
Mvvvc 48,172,208
X 242,96,240
G 228,248
Mvvvc 64,176,208
T 140,242,16384
G 229,249
Mvvvc 68,176,212
T 142,242,17408
G 230,250
Mvvvc 72,176,216
X 241,128,240
G 231,251
Mvvvc 76,176,220
T 144,241,16384
C 232,224,225
Mvvvc 92,180,220
T 146,241,17408
C 233,226,227
Mvvvc 88,180,216
X 242,160,240
C 234,228,229
Mvvvc 84,180,212
T 148,242,16384
C 235,230,231
Mvvvc 80,180,208
T 150,242,17408
Svs 232,243,36,40,2048
Mvvvc 96,184,208
X 241,192,240
G 224,220
Mvvvc 100,184,212
T 152,241,16384
G 225,221
Mvvvc 104,184,216
T 154,241,17408
G 226,222
Mvvvc 108,184,220
X 242,224,240
G 227,223
Mvvvc 124,188,220
T 156,242,16384
G 228,252
Mvvvc 120,188,216
T 158,242,17408
G 229,253
Mvvvc 116,188,212
G 230,254
Mvvvc 112,188,208
G 231,255
C 232,224,225
C 233,226,227
C 234,228,229
C 235,230,231
Svs 232,243,36,40,3072
W 0
Mvvvc 0,128,192
Rv 208,245,4096
Mvvvc 4,128,196
Rv 212,245,5120
Mvvvc 8,128,200
Mvvvc 12,128,204
Rv 216,245,6144
Mvvvc 28,132,204
Mvvvc 24,132,200
Rv 220,245,7168
Mvvvc 20,132,196
Mvvvc 16,132,192
T 160,240,24576
Mvvvc 32,136,192
T 162,240,25600
Mvvvc 36,136,196
X 242,32,240
Mvvvc 40,136,200
T 164,242,24576
Mvvvc 44,136,204
T 166,242,25600
Mvvvc 60,140,204
X 241,64,240
Mvvvc 56,140,200
T 168,241,24576
Mvvvc 52,140,196
T 170,241,25600
Mvvvc 48,140,192
X 242,96,240
Mvvvc 64,144,192
T 172,242,24576
Mvvvc 68,144,196
T 174,242,25600
Mvvvc 72,144,200
X 241,128,240
Mvvvc 76,144,204
T 176,241,24576
Mvvvc 92,148,204
T 178,241,25600
Mvvvc 88,148,200
X 242,160,240
Mvvvc 84,148,196
T 180,242,24576
Mvvvc 80,148,192
T 182,242,25600
Mvvvc 96,152,192
X 241,192,240
Mvvvc 100,152,196
T 184,241,24576
Mvvvc 104,152,200
T 186,241,25600
Mvvvc 108,152,204
X 242,224,240
Mvvvc 124,156,204
T 188,242,24576
Mvvvc 120,156,200
T 190,242,25600
Mvvvc 116,156,196
Mvvvc 112,156,192
W 0
B
Mvvvc 0,160,208
Mvvvc 4,160,212
Mvvvc 8,160,216
Mvvvc 12,160,220
Mvvvc 28,164,220
Mvvvc 24,164,216
Mvvvc 20,164,212
Mvvvc 16,164,208
Mvvvc 32,168,208
Mvvvc 36,168,212
Mvvvc 40,168,216
Mvvvc 44,168,220
Mvvvc 60,172,220
Mvvvc 56,172,216
Mvvvc 52,172,212
Mvvvc 48,172,208
Mvvvc 64,176,208
Mvvvc 68,176,212
Mvvvc 72,176,216
Mvvvc 76,176,220
Mvvvc 92,180,220
Mvvvc 88,180,216
Mvvvc 84,180,212
Mvvvc 80,180,208
Mvvvc 96,184,208
Mvvvc 100,184,212
Mvvvc 104,184,216
Mvvvc 108,184,220
Mvvvc 124,188,220
Mvvvc 120,188,216
Mvvvc 116,188,212
Mvvvc 112,188,208
QE2%=:
WV 0
B
s_lshl_b32 s21, s12, 8
AS 21,21,20480
s_lshl_b32 s22, s13, 2
s_add_u32 s21, s21, s22
v_mov_b32 v247, s21
v_mov_b32 v246, s79
s_mov_b64 exec, 1
global_store_dword v247, v246, s[14:15] sc1
s_mov_b64 exec, -1
N 3
VA 224,131072,243
C 128,0,1
C 129,2,3
C 130,16,17
C 131,18,19
DWv 224,128,0
C 132,32,33
C 133,34,35
C 134,48,49
C 135,50,51
DWv 224,132,1024
C 136,64,65
C 137,66,67
C 138,80,81
C 139,82,83
DWv 224,136,2048
C 140,96,97
C 141,98,99
C 142,112,113
C 143,114,115
DWv 224,140,3072
C 144,4,5
C 145,6,7
C 146,20,21
C 147,22,23
DWv 224,144,4096
C 148,36,37
C 149,38,39
C 150,52,53
C 151,54,55
DWv 224,148,5120
C 152,68,69
C 153,70,71
C 154,84,85
C 155,86,87
DWv 224,152,6144
C 156,100,101
C 157,102,103
C 158,116,117
C 159,118,119
DWv 224,156,7168
C 128,8,9
C 129,10,11
C 130,24,25
C 131,26,27
DWv 224,128,8192
C 132,40,41
C 133,42,43
C 134,56,57
C 135,58,59
DWv 224,132,9216
C 136,72,73
C 137,74,75
C 138,88,89
C 139,90,91
DWv 224,136,10240
C 140,104,105
C 141,106,107
C 142,120,121
C 143,122,123
DWv 224,140,11264
C 144,12,13
C 145,14,15
C 146,28,29
C 147,30,31
DWv 224,144,12288
C 148,44,45
C 149,46,47
C 150,60,61
C 151,62,63
DWv 224,148,13312
C 152,76,77
C 153,78,79
C 154,92,93
C 155,94,95
DWv 224,152,14336
)A"
R"A(C 156,108,109
C 157,110,111
C 158,124,125
C 159,126,127
DWv 224,156,15360
W 0
B
Ra 128,224,0
Ra 132,224,1024
Ra 136,224,2048
Ra 140,224,3072
Ra 144,224,4096
Ra 148,224,5120
Ra 152,224,6144
Ra 156,224,7168
Ra 160,224,8192
Ra 164,224,9216
Ra 168,224,10240
Ra 172,224,11264
Ra 176,224,12288
Ra 180,224,13312
Ra 184,224,14336
Ra 188,224,15360
VA 225,98304,243
Ra 192,225,0
Ra 196,225,1024
Ra 200,225,2048
Ra 204,225,3072
Ra 208,225,4096
Ra 212,225,5120
Ra 216,225,6144
Ra 220,225,7168
Ra 224,225,8192
Ra 228,225,9216
Ra 232,225,10240
Ra 236,225,11264
Ra 240,225,12288
Ra 244,225,13312
Ra 248,225,14336
Ra 252,225,15360
s_add_u32 s32, s14, 0x2400000
s_addc_u32 s33, s15, 0
SM 34,0x1000000
SM 35,0x20000
v_lshlrev_b32 v196, 4, v254
VA 197,0,196
VA 198,65536,196
v_mov_b32 v199, 0xff800000
v_mov_b32 v192, 0x3f803f80
v_mov_b32 v193, 0x3f803f80
v_mov_b32 v194, 0x3f803f80
v_mov_b32 v195, 0x3f803f80
v_and_b32 v200, 15, v254
v_lshrrev_b32 v201, 4, v254
v_lshlrev_b32 v201, 2, v201
VA 202,0,201
v_cmp_le_u32 s[62:63], v202, v200
VA 202,1,201
v_cmp_le_u32 s[64:65], v202, v200
VA 202,2,201
v_cmp_le_u32 s[66:67], v202, v200
VA 202,3,201
v_cmp_le_u32 s[68:69], v202, v200
s_lshl_b32 s61, s13, 1
AS 61,61,2
SM 60,0
s_mov_b64 s[70:71], 0
s_mov_b64 s[50:51], s[42:43]
VA 237,0,254
v_mul_u32_u24 v238, 3277, v237
v_lshrrev_b32 v238, 16, v238
v_mul_u32_u24 v239, 20, v238
v_sub_u32 v239, v237, v239
v_lshrrev_b32 v240, 1, v239
v_lshrrev_b32 v241, 2, v238
v_and_b32 v241, 1, v241
v_xor_b32 v240, v240, v241
v_and_b32 v239, 1, v239
v_lshlrev_b32 v239, 4, v239
v_lshl_add_u32 v239, v240, 5, v239
v_mul_u32_u24 v238, 1280, v238
v_add_u32 v238, v238, v239
MV 232,238
VA 237,64,254
v_mul_u32_u24 v238, 3277, v237
v_lshrrev_b32 v238, 16, v238
v_mul_u32_u24 v239, 20, v238
v_sub_u32 v239, v237, v239
v_lshrrev_b32 v240, 1, v239
v_lshrrev_b32 v241, 2, v238
v_and_b32 v241, 1, v241
v_xor_b32 v240, v240, v241
v_and_b32 v239, 1, v239
v_lshlrev_b32 v239, 4, v239
v_lshl_add_u32 v239, v240, 5, v239
v_mul_u32_u24 v238, 1280, v238
v_add_u32 v238, v238, v239
v_subrev_u32 v233, 1024, v238
VA 237,128,254
v_mul_u32_u24 v238, 3277, v237
v_lshrrev_b32 v238, 16, v238
v_mul_u32_u24 v239, 20, v238
v_sub_u32 v239, v237, v239
v_lshrrev_b32 v240, 1, v239
v_lshrrev_b32 v241, 2, v238
v_and_b32 v241, 1, v241
v_xor_b32 v240, v240, v241
v_and_b32 v239, 1, v239
v_lshlrev_b32 v239, 4, v239
v_lshl_add_u32 v239, v240, 5, v239
v_mul_u32_u24 v238, 1280, v238
v_add_u32 v238, v238, v239
v_subrev_u32 v234, 2048, v238
VA 237,192,254
v_mul_u32_u24 v238, 3277, v237
v_lshrrev_b32 v238, 16, v238
v_mul_u32_u24 v239, 20, v238
v_sub_u32 v239, v237, v239
v_lshrrev_b32 v240, 1, v239
v_lshrrev_b32 v241, 2, v238
v_and_b32 v241, 1, v241
v_xor_b32 v240, v240, v241
v_and_b32 v239, 1, v239
v_lshlrev_b32 v239, 4, v239
v_lshl_add_u32 v239, v240, 5, v239
v_mul_u32_u24 v238, 1280, v238
v_add_u32 v238, v238, v239
v_subrev_u32 v235, 3072, v238
VA 237,256,254
v_mul_u32_u24 v238, 3277, v237
v_lshrrev_b32 v238, 16, v238
v_mul_u32_u24 v239, 20, v238
v_sub_u32 v239, v237, v239
v_lshrrev_b32 v240, 1, v239
v_lshrrev_b32 v241, 2, v238
v_and_b32 v241, 1, v241
v_xor_b32 v240, v240, v241
v_and_b32 v239, 1, v239
v_lshlrev_b32 v239, 4, v239
v_lshl_add_u32 v239, v240, 5, v239
v_mul_u32_u24 v238, 1280, v238
v_add_u32 v238, v238, v239
MV 236,238
B
AM 59,0
N 0
Ds 196,28,57,0
Ds 196,28,57,1024
Ds 196,28,57,2048
Ds 196,28,57,3072
AS 57,57,16384
s_cmp_eq_u32 s13, 0
s_cbranch_scc0 AT2_M1%=
SM 73,0x0
SM 74,0x0
SM 75,0x8000
SM 76,0x0
SM 77,0x0
SM 78,0x0
s_branch AT2_MD%=
AT2_M1%=:
s_cmp_eq_u32 s13, 1
s_cbranch_scc0 AT2_M2%=
SM 73,0x0
SM 74,0x0
SM 75,0x8000
SM 76,0x2
SM 77,0x0
SM 78,0x0
s_branch AT2_MD%=
AT2_M2%=:
s_cmp_eq_u32 s13, 2
s_cbranch_scc0 AT2_M3%=
SM 73,0x0
SM 74,0x0
SM 75,0x8000
SM 76,0x0
SM 77,0x0
SM 78,0x8
s_branch AT2_MD%=
AT2_M3%=:
s_cmp_eq_u32 s13, 3
s_cbranch_scc0 AT2_M4%=
SM 73,0x0
SM 74,0x0
SM 75,0x8000
SM 76,0x0
SM 77,0x0
SM 78,0x20
s_branch AT2_MD%=
AT2_M4%=:
s_cmp_eq_u32 s13, 4
s_cbranch_scc0 AT2_M5%=
SM 73,0x0
SM 74,0x0
SM 75,0x8000
SM 76,0x0
SM 77,0x0
SM 78,0x80
s_branch AT2_MD%=
AT2_M5%=:
s_cmp_eq_u32 s13, 5
s_cbranch_scc0 AT2_M6%=
SM 73,0x0
SM 74,0x0
SM 75,0x8000
SM 76,0x0
SM 77,0x0
SM 78,0x200
s_branch AT2_MD%=
AT2_M6%=:
s_cmp_eq_u32 s13, 6
s_cbranch_scc0 AT2_M7%=
SM 73,0x8000
SM 74,0x0
SM 75,0x0
SM 76,0x0
SM 77,0x0
SM 78,0x800
s_branch AT2_MD%=
AT2_M7%=:
SM 73,0x0
SM 74,0x20000
SM 75,0x0
SM 76,0x0
SM 77,0x4000
SM 78,0x2000
AT2_MD%=:
WV 4
B
Rv 144,197,0
Rv 148,197,1024
Rv 152,197,2048
Rv 156,197,3072
Rv 160,197,4096
Rv 164,197,5120
Rv 168,197,6144
W 5
s_cmp_eq_u32 s13, 0
s_cbranch_scc1 AT2_B0%=
s_branch AT2_B10%=
XAT2_B0%=:
AS 60,60,1
AT2_B0%=:
Mvvaz 0,144,128
Mvvaz 8,144,144
Mvvaz 16,144,160
Rv 172,197,7168
Mvvaz 24,144,176
Mvvac 24,148,180
Mvvac 16,148,164
Rv 144,197,8192
Mvvac 8,148,148
Mvvac 0,148,132
W 5
Mvvac 0,152,136
Mvvac 8,152,152
Rv 148,197,9216
Mvvac 16,152,168
Mvvac 24,152,184
Mvvac 24,156,188
Mvvac 16,156,172
Rv 152,197,10240
Mvvac 8,156,156
Mvvac 0,156,140
W 5
Mvvaz 12,160,144
Rv 156,197,11264
Mvvaz 20,160,160
N 3
K 0,199,0,62
K 1,199,1,64
K 2,199,2,66
K 3,199,3,68
Mvvaz 28,160,176
v_max3_f32 v200, v0, v1, v2
Fma 200,200,3
v_max3_f32 v202, v8, v9, v10
Fma 202,202,11
Mvvac 28,164,180
v_max3_f32 v204, v16, v17, v18
Fma 204,204,19
v_max3_f32 v206, v24, v25, v26
Fma 206,206,27
Mvvac 20,164,164
Rv 160,197,12288
MV 201,200
MV 203,202
MV 205,204
MV 207,206
Mvvac 12,164,148
v_permlane16_swap_b32 v200, v201
v_permlane16_swap_b32 v202, v203
v_permlane16_swap_b32 v204, v205
v_permlane16_swap_b32 v206, v207
Fma 200,200,201
Fma 202,202,203
Fma 204,204,205
Fma 206,206,207
W 5
MV 201,200
MV 203,202
MV 205,204
MV 207,206
Mvvac 12,168,152
v_permlane32_swap_b32 v200, v201
v_permlane32_swap_b32 v202, v203
v_permlane32_swap_b32 v204, v205
v_permlane32_swap_b32 v206, v207
Mvvac 20,168,168
Rv 164,197,13312
Fma 200,200,201
Fma 202,202,203
Fma 204,204,205
Fma 206,206,207
Mvvac 28,168,184
v_sub_f32 v128, 0, v200
v_sub_f32 v129, 0, v200
v_sub_f32 v130, 0, v200
v_sub_f32 v131, 0, v200
Mvvac 28,172,188
v_sub_f32 v132, 0, v202
v_sub_f32 v133, 0, v202
v_sub_f32 v134, 0, v202
v_sub_f32 v135, 0, v202
Mvvac 20,172,172
v_sub_f32 v136, 0, v204
v_sub_f32 v137, 0, v204
v_sub_f32 v138, 0, v204
v_sub_f32 v139, 0, v204
Mvvac 12,172,156
v_sub_f32 v140, 0, v206
v_sub_f32 v141, 0, v206
v_sub_f32 v142, 0, v206
v_sub_f32 v143, 0, v206
Rv 168,197,14336
W 5
Mvvan 48,144,160,136
Mvvan 56,144,176,140
Mvvac 56,148,180
Rv 172,197,15360
Mvvac 48,148,164
Fad 0,0,128
E 0
Fad 1,1,128
W 4
E 1
Fad 2,2,128
Mvvac 48,152,168
E 2
Mvvac 56,152,184
Fad 3,3,128
Mvvac 56,156,188
E 3
AS 56,56,8192
SM 58,32768
Mvvac 48,156,172
W 0
B
C 0,0,1
W 15
C 1,2,3
AM 58,0
v_mov_b32 v2, 0
Ds 196,24,56,0
Mvvan 60,160,176,140
Mvvac 60,164,180
Fad 8,8,132
E 8
Fad 9,9,132
W 15
E 9
v_mov_b32 v3, 0
Fad 10,10,132
Ds 196,24,56,1024
E 10
C 8,8,9
Mvvac 60,168,184
Fad 11,11,132
Mvvac 60,172,188
E 11
Fad 12,12,132
K 12,199,12,62
E 12
C 9,10,11
Fad 13,13,132
K 13,199,13,64
E 13
Ds 196,24,56,2048
Fad 14,14,132
K 14,199,14,66
E 14
Fad 15,15,132
K 15,199,15,68
E 15
C 10,12,13
Fad 16,16,136
E 16
Fad 17,17,136
Ds 196,24,56,3072
E 17
C 11,14,15
Fad 18,18,136
AS 56,56,4096
E 18
C 16,16,17
Fad 19,19,136
E 19
Fad 20,20,136
E 20
Fad 21,21,136
E 21
C 17,18,19
Fad 22,22,136
AM 58,4096
E 22
Ds 196,24,56,0
C 18,20,21
Fad 23,23,136
E 23
Fad 24,24,140
E 24
Fad 25,25,140
E 25
C 19,22,23
Fad 26,26,140
Ds 196,24,56,1024
E 26
C 24,24,25
Fad 27,27,140
E 27
Fad 28,28,140
E 28
Fad 29,29,140
E 29
Ds 196,24,56,2048
C 25,26,27
Fad 30,30,140
E 30
Fad 31,31,140
E 31
C 26,28,29
C 27,30,31
Ds 196,24,56,3072
AS 56,56,28672
W 0
B
AM 59,16384
N 0
Ds 196,28,57,0
Ds 196,28,57,1024
Ds 196,28,57,2048
Ds 196,28,57,3072
AS 57,57,16384
XAT2_B1%=:
AS 60,60,1
AT2_B1%=:
K 48,199,48,62
E 48
K 49,199,49,64
E 49
K 50,199,50,66
E 50
K 51,199,51,68
E 51
C 48,48,49
C 49,50,51
v_mov_b32 v50, 0
E 56
E 57
v_mov_b32 v51, 0
E 58
C 56,56,57
E 59
K 60,199,60,62
E 60
K 61,199,61,64
E 61
C 57,58,59
K 62,199,62,66
E 62
K 63,199,63,68
E 63
C 58,60,61
C 59,62,63
WV 12
B
Rv 144,198,0
Rv 148,198,1024
AM 58,16384
N 0
Ds 196,24,56,0
Rv 152,198,2048
Rv 156,198,3072
Rv 160,198,4096
Ds 196,24,56,1024
Rv 164,198,5120
Rv 168,198,6144
W 5
Mavvz 0,144,0
Ds 196,24,56,2048
Mavvz 4,144,8
Mavvz 8,144,16
Rv 172,198,7168
Mvvvz 176,192,0
Mavvz 12,144,24
Mavvz 28,148,24
Mavvz 24,148,16
Rv 144,198,8192
Mavvz 20,148,8
Mavvz 16,148,0
Ds 196,24,56,3072
W 5
Mavvz 32,152,0
Mavvz 36,152,8
Rv 148,198,9216
Mvvvz 180,192,8
AS 56,56,4096
Mavvz 40,152,16
Mavvz 44,152,24
Mavvz 60,156,24
Mavvz 56,156,16
Rv 152,198,10240
Mavvz 52,156,8
Mavvz 48,156,0
W 5
Mavvz 64,160,0
Mvvvz 184,192,16
Mavvz 68,160,8
Rv 156,198,11264
AM 58,20480
Mavvz 72,160,16
Ds 196,24,56,0
Mavvz 76,160,24
Mavvz 92,164,24
Mavvz 88,164,16
Rv 160,198,12288
Mavvz 84,164,8
Mavvz 80,164,0
W 5
Mavvz 96,168,0
Mvvvz 188,192,24
Mavvz 100,168,8
Ds 196,24,56,1024
Mavvz 104,168,16
Rv 164,198,13312
Mavvz 108,168,24
Mavvz 124,172,24
Mavvz 120,172,16
Mavvz 116,172,8
Mavvz 112,172,0
Rv 168,198,14336
W 5
Ds 196,24,56,2048
Mavvc 8,144,48
Mavvc 12,144,56
Mavvc 28,148,56
Rv 172,198,15360
Mavvc 24,148,48
Ds 196,24,56,3072
W 4
AS 56,56,28672
Mavvc 40,152,48
Mavvc 44,152,56
Mavvc 60,156,56
Mavvc 56,156,48
WV 12
B
AS 57,57,8192
SM 59,98304
VA 197,32768,197
Rv 144,197,0
W 15
Rv 148,197,1024
Mvvvc 184,192,48
AM 59,0
N 0
Ds 196,28,57,0
Mavvc 72,160,48
Mavvc 76,160,56
Rv 152,197,2048
Mavvc 92,164,56
Mavvc 88,164,48
Ds 196,28,57,1024
Rv 156,197,3072
W 15
Mvvvc 188,192,56
Rv 160,197,4096
Ds 196,28,57,2048
Mavvc 104,168,48
Mavvc 108,168,56
Mavvc 124,172,56
Rv 164,197,5120
Mavvc 120,172,48
Ds 196,28,57,3072
Rv 168,197,6144
W 5
XAT2_B2%=:
AS 60,60,1
AT2_B2%=:
Mvvaz 0,144,192
AS 57,57,4096
Mvvaz 8,144,208
Mvvaz 16,144,224
Rv 172,197,7168
Mvvaz 24,144,240
Mvvac 24,148,244
Mvvac 16,148,228
Rv 144,197,8192
AM 59,4096
Mvvac 8,148,212
Ds 196,28,57,0
Mvvac 0,148,196
W 5
Mvvac 0,152,200
Mvvac 8,152,216
Rv 148,197,9216
Mvvac 16,152,232
Mvvac 24,152,248
Mvvac 24,156,252
Mvvac 16,156,236
Rv 152,197,10240
Mvvac 8,156,220
Mvvac 0,156,204
Ds 196,28,57,1024
W 5
Mvvaz 4,160,192
Mvvaz 12,160,208
Rv 156,197,11264
Mvvaz 20,160,224
N 1
v_max3_f32 v200, v0, v1, v2
Fma 200,200,3
v_max3_f32 v202, v8, v9, v10
Fma 202,202,11
Mvvaz 28,160,240
v_max3_f32 v204, v16, v17, v18
Fma 204,204,19
v_max3_f32 v206, v24, v25, v26
Fma 206,206,27
Mvvac 28,164,244
MV 201,200
MV 203,202
MV 205,204
MV 207,206
Mvvac 20,164,228
Rv 160,197,12288
v_permlane16_swap_b32 v200, v201
v_permlane16_swap_b32 v202, v203
v_permlane16_swap_b32 v204, v205
Mvvac 12,164,212
v_permlane16_swap_b32 v206, v207
Fma 200,200,201
Fma 202,202,203
Fma 204,204,205
Mvvac 4,164,196
Fma 206,206,207
MV 201,200
MV 203,202
MV 205,204
W 5
Mvvac 4,168,200
MV 207,206
v_permlane32_swap_b32 v200, v201
v_permlane32_swap_b32 v202, v203
Ds 196,28,57,2048
Mvvac 12,168,216
v_permlane32_swap_b32 v204, v205
v_permlane32_swap_b32 v206, v207
Fma 200,200,201
Fma 202,202,203
Mvvac 20,168,232
Rv 164,197,13312
Fma 204,204,205
Fma 206,206,207
v_sub_f32 v128, 0, v200
v_sub_f32 v129, 0, v200
Mvvac 28,168,248
v_sub_f32 v130, 0, v200
v_sub_f32 v131, 0, v200
v_sub_f32 v132, 0, v202
Mvvac 28,172,252
v_sub_f32 v133, 0, v202
v_sub_f32 v134, 0, v202
v_sub_f32 v135, 0, v202
v_sub_f32 v136, 0, v204
Mvvac 20,172,236
v_sub_f32 v137, 0, v204
v_sub_f32 v138, 0, v204
v_sub_f32 v139, 0, v204
v_sub_f32 v140, 0, v206
Mvvac 12,172,220
v_sub_f32 v141, 0, v206
v_sub_f32 v142, 0, v206
v_sub_f32 v143, 0, v206
Mvvac 4,172,204
Rv 168,197,14336
W 5
Mvvan 32,144,192,128
Mvvan 40,144,208,132
Ds 196,28,57,3072
Mvvan 48,144,224,136
Mvvan 56,144,240,140
AS 57,57,28672
Mvvac 56,148,244
Rv 172,197,15360
Mvvac 48,148,228
Fad 0,0,128
Mvvac 40,148,212
E 0
Mvvac 32,148,196
Fad 1,1,128
W 4
Mvvac 32,152,200
E 1
Mvvac 40,152,216
Fad 2,2,128
Mvvac 48,152,232
E 2
Mvvac 56,152,248
C 0,0,1
Fad 3,3,128
Mvvac 56,156,252
E 3
Mvvac 48,156,236
Fad 4,4,128
WV 16
B
Mvvac 40,156,220
E 4
Mvvac 32,156,204
Fad 5,5,128
W 15
Mvvan 36,160,192,128
E 5
Mvvan 44,160,208,132
C 1,2,3
Fad 6,6,128
AM 58,0
Mvvan 52,160,224,136
E 6
Ds 196,24,56,0
Mvvan 60,160,240,140
C 2,4,5
)A"
R"A(Fad 7,7,128
Mvvac 60,164,244
E 7
Mvvac 52,164,228
Fad 8,8,132
Mvvac 44,164,212
E 8
Mvvac 36,164,196
Fad 9,9,132
W 15
Mvvac 36,168,200
E 9
Mvvac 44,168,216
C 3,6,7
Fad 10,10,132
Ds 196,24,56,1024
Mvvac 52,168,232
E 10
Mvvac 60,168,248
C 8,8,9
Fad 11,11,132
Mvvac 60,172,252
E 11
Mvvac 52,172,236
Fad 12,12,132
Mvvac 44,172,220
E 12
Mvvac 36,172,204
Fad 13,13,132
E 13
Ds 196,24,56,2048
C 9,10,11
Fad 14,14,132
E 14
C 10,12,13
Fad 15,15,132
E 15
Fad 16,16,136
E 16
Fad 17,17,136
Ds 196,24,56,3072
E 17
C 11,14,15
Fad 18,18,136
AS 56,56,4096
E 18
C 16,16,17
Fad 19,19,136
E 19
Fad 20,20,136
E 20
Fad 21,21,136
E 21
C 17,18,19
Fad 22,22,136
AM 58,4096
E 22
Ds 196,24,56,0
C 18,20,21
Fad 23,23,136
E 23
Fad 24,24,140
E 24
Fad 25,25,140
E 25
C 19,22,23
Fad 26,26,140
Ds 196,24,56,1024
E 26
C 24,24,25
Fad 27,27,140
E 27
Fad 28,28,140
E 28
Fad 29,29,140
E 29
Ds 196,24,56,2048
C 25,26,27
Fad 30,30,140
E 30
Fad 31,31,140
E 31
C 26,28,29
E 32
Ds 196,24,56,3072
E 33
C 27,30,31
AS 56,56,28672
E 34
C 32,32,33
E 35
WV 16
B
Rv 144,197,16384
E 36
Rv 148,197,17408
E 37
AM 59,16384
C 33,34,35
Ds 196,28,57,0
E 38
Rv 152,197,18432
C 34,36,37
E 39
Ds 196,28,57,1024
Rv 156,197,19456
E 40
E 41
Rv 160,197,20480
C 35,38,39
Ds 196,28,57,2048
E 42
C 40,40,41
Rv 164,197,21504
E 43
Ds 196,28,57,3072
E 44
Rv 168,197,22528
W 5
XAT2_B3%=:
AS 60,60,1
AT2_B3%=:
s_mov_b32 s16, s13
s_lshr_b32 s17, s16, 1
s_lshl_b32 s17, s17, 21
s_and_b32 s16, s16, 1
s_lshl_b32 s16, s16, 13
s_add_u32 s17, s17, s16
s_lshl_b32 s16, s12, 16
s_add_u32 s17, s17, s16
AS 72,17,0
N 7
v_mov_b32 v231, 0x5d800000
v_cmp_nle_f32 vcc, v176, v231
N 3
s_or_b64 s[70:71], s[70:71], vcc
v_cmp_nle_f32 vcc, v180, v231
N 3
s_or_b64 s[70:71], s[70:71], vcc
v_cmp_nle_f32 vcc, v184, v231
N 3
s_or_b64 s[70:71], s[70:71], vcc
v_cmp_nle_f32 vcc, v188, v231
N 3
s_or_b64 s[70:71], s[70:71], vcc
s_cmp_lg_u64 s[70:71], 0
s_cselect_b32 s16, 1, 0
s_mov_b64 s[70:71], 0
v_mov_b32 v200, s16
v_mov_b32 v201, 149520
ds_write_b32 v201, v200
v_rcp_f32 v224, v176
v_rcp_f32 v226, v180
v_rcp_f32 v228, v184
v_rcp_f32 v176, v188
G 200,0
G 204,16
G 201,1
G 205,17
G 202,2
G 206,18
G 203,3
G 207,19
PM 200,200,224
PM 202,202,224
PM 204,204,224
PM 206,206,224
C 208,200,201
C 209,202,203
C 210,204,205
C 211,206,207
Svs 208,196,32,72,0
G 212,4
G 216,20
G 213,5
G 217,21
G 214,6
G 218,22
G 215,7
G 219,23
PM 212,212,226
PM 214,214,226
PM 216,216,226
PM 218,218,226
C 220,212,213
C 221,214,215
C 222,216,217
C 223,218,219
Svs 220,196,32,72,1024
G 200,8
G 204,24
G 201,9
G 205,25
G 202,10
G 206,26
G 203,11
G 207,27
PM 200,200,228
PM 202,202,228
PM 204,204,228
PM 206,206,228
C 208,200,201
C 209,202,203
C 210,204,205
C 211,206,207
Svs 208,196,32,72,2048
G 212,12
G 216,28
G 213,13
G 217,29
G 214,14
G 218,30
G 215,15
G 219,31
PM 212,212,176
PM 214,214,176
PM 216,216,176
PM 218,218,176
C 220,212,213
C 221,214,215
C 222,216,217
C 223,218,219
Svs 220,196,32,72,3072
AS 72,72,16384
G 200,32
G 204,48
G 201,33
G 205,49
G 202,34
G 206,50
G 203,35
G 207,51
PM 200,200,224
PM 202,202,224
PM 204,204,224
PM 206,206,224
C 208,200,201
C 209,202,203
C 210,204,205
C 211,206,207
Svs 208,196,32,72,0
G 212,36
G 216,52
G 213,37
G 217,53
G 214,38
G 218,54
G 215,39
G 219,55
PM 212,212,226
PM 214,214,226
PM 216,216,226
PM 218,218,226
C 220,212,213
C 221,214,215
C 222,216,217
C 223,218,219
Svs 220,196,32,72,1024
G 200,40
G 204,56
G 201,41
G 205,57
G 202,42
G 206,58
G 203,43
G 207,59
PM 200,200,228
PM 202,202,228
PM 204,204,228
PM 206,206,228
C 208,200,201
C 209,202,203
C 210,204,205
C 211,206,207
Svs 208,196,32,72,2048
G 212,44
G 216,60
G 213,45
G 217,61
G 214,46
G 218,62
G 215,47
G 219,63
PM 212,212,176
PM 214,214,176
PM 216,216,176
PM 218,218,176
C 220,212,213
C 221,214,215
C 222,216,217
C 223,218,219
Svs 220,196,32,72,3072
AS 72,72,16384
G 200,64
G 204,80
G 201,65
G 205,81
G 202,66
G 206,82
G 203,67
G 207,83
PM 200,200,224
PM 202,202,224
PM 204,204,224
PM 206,206,224
C 208,200,201
C 209,202,203
C 210,204,205
C 211,206,207
Svs 208,196,32,72,0
G 212,68
G 216,84
G 213,69
G 217,85
G 214,70
G 218,86
G 215,71
G 219,87
PM 212,212,226
PM 214,214,226
PM 216,216,226
PM 218,218,226
C 220,212,213
C 221,214,215
C 222,216,217
C 223,218,219
Svs 220,196,32,72,1024
G 200,72
G 204,88
G 201,73
G 205,89
G 202,74
G 206,90
G 203,75
G 207,91
PM 200,200,228
PM 202,202,228
PM 204,204,228
PM 206,206,228
C 208,200,201
C 209,202,203
C 210,204,205
C 211,206,207
Svs 208,196,32,72,2048
G 212,76
G 216,92
G 213,77
G 217,93
G 214,78
G 218,94
G 215,79
G 219,95
PM 212,212,176
PM 214,214,176
PM 216,216,176
PM 218,218,176
C 220,212,213
C 221,214,215
C 222,216,217
C 223,218,219
Svs 220,196,32,72,3072
AS 72,72,16384
G 200,96
G 204,112
G 201,97
G 205,113
G 202,98
G 206,114
G 203,99
G 207,115
PM 200,200,224
PM 202,202,224
PM 204,204,224
PM 206,206,224
C 208,200,201
C 209,202,203
C 210,204,205
C 211,206,207
Svs 208,196,32,72,0
G 212,100
G 216,116
G 213,101
G 217,117
G 214,102
G 218,118
G 215,103
G 219,119
PM 212,212,226
PM 214,214,226
PM 216,216,226
PM 218,218,226
C 220,212,213
C 221,214,215
C 222,216,217
C 223,218,219
Svs 220,196,32,72,1024
G 200,104
G 204,120
G 201,105
G 205,121
G 202,106
G 206,122
G 203,107
G 207,123
PM 200,200,228
PM 202,202,228
PM 204,204,228
PM 206,206,228
C 208,200,201
C 209,202,203
C 210,204,205
C 211,206,207
Svs 208,196,32,72,2048
G 212,108
G 216,124
G 213,109
G 217,125
G 214,110
G 218,126
G 215,111
G 219,127
PM 212,212,176
PM 214,214,176
PM 216,216,176
PM 218,218,176
C 220,212,213
C 221,214,215
C 222,216,217
C 223,218,219
Svs 220,196,32,72,3072
s_bitcmp1_b32 s73, s60
s_cbranch_scc1 XAT2_B16%=
s_bitcmp1_b32 s74, s60
s_cbranch_scc1 XAT2_B19%=
XAT2_B4%=:
AS 60,60,1
AT2_B4%=:
Mvvan 64,144,192,128
AS 57,57,4096
E 45
Mvvan 72,144,208,132
C 41,42,43
Mvvan 80,144,224,136
Rv 172,197,23552
E 46
Mvvan 88,144,240,140
C 42,44,45
Mvvac 88,148,244
E 47
Mvvac 80,148,228
Rv 144,197,24576
AM 59,20480
Mvvac 72,148,212
E 48
Ds 196,28,57,0
Mvvac 64,148,196
W 6
Mvvac 64,152,200
E 49
Mvvac 72,152,216
Rv 148,197,25600
C 43,46,47
Mvvac 80,152,232
E 50
Mvvac 88,152,248
C 48,48,49
Mvvac 88,156,252
E 51
Mvvac 80,156,236
Rv 152,197,26624
Mvvac 72,156,220
E 52
Mvvac 64,156,204
Ds 196,28,57,1024
W 6
Mvvan 68,160,192,128
E 53
Mvvan 76,160,208,132
Rv 156,197,27648
C 49,50,51
Mvvan 84,160,224,136
E 54
Mvvan 92,160,240,140
C 50,52,53
Mvvac 92,164,244
E 55
Mvvac 84,164,228
Rv 160,197,28672
Mvvac 76,164,212
E 56
Mvvac 68,164,196
W 5
Mvvac 68,168,200
E 57
Ds 196,28,57,2048
Mvvac 76,168,216
C 51,54,55
Mvvac 84,168,232
Rv 164,197,29696
E 58
Mvvac 92,168,248
C 56,56,57
Mvvac 92,172,252
E 59
Mvvac 84,172,236
Mvvac 76,172,220
E 60
Mvvac 68,172,204
Rv 168,197,30720
W 5
Mvvan 96,144,192,128
E 61
Mvvan 104,144,208,132
C 57,58,59
Ds 196,28,57,3072
Mvvan 112,144,224,136
E 62
Mvvan 120,144,240,140
C 58,60,61
AS 57,57,28672
Mvvac 120,148,244
Rv 172,197,31744
E 63
Mvvac 112,148,228
Mvvac 104,148,212
E 64
Mvvac 96,148,196
W 4
Mvvac 96,152,200
E 65
Mvvac 104,152,216
C 59,62,63
Mvvac 112,152,232
E 66
Mvvac 120,152,248
C 64,64,65
Mvvac 120,156,252
E 67
Mvvac 112,156,236
WV 32
B
VA 198,32768,198
Mvvac 104,156,220
Rv 144,198,0
E 68
Mvvac 96,156,204
W 15
Mvvan 100,160,192,128
Rv 148,198,1024
E 69
Mvvan 108,160,208,132
C 65,66,67
AM 58,16384
Mvvan 116,160,224,136
E 70
Ds 196,24,56,0
Mvvan 124,160,240,140
Rv 152,198,2048
C 66,68,69
Mvvac 124,164,244
E 71
Mvvac 116,164,228
Mvvac 108,164,212
Rv 156,198,3072
E 72
Mvvac 100,164,196
W 15
Mvvac 100,168,200
E 73
Mvvac 108,168,216
Rv 160,198,4096
C 67,70,71
Ds 196,24,56,1024
Mvvac 116,168,232
E 74
Mvvac 124,168,248
C 72,72,73
Mvvac 124,172,252
Rv 164,198,5120
E 75
Mvvac 116,172,236
Mvvac 108,172,220
E 76
Mvvac 100,172,204
Rv 168,198,6144
W 5
Mavvz 0,144,0
E 77
Ds 196,24,56,2048
Mavvz 4,144,8
C 73,74,75
Mavvz 8,144,16
Rv 172,198,7168
E 78
Mavvz 12,144,24
C 74,76,77
Mavvz 28,148,24
E 79
Mavvz 24,148,16
Rv 144,198,8192
Mvvvz 176,192,0
Mavvz 20,148,8
E 80
Mavvz 16,148,0
Ds 196,24,56,3072
W 5
Mavvz 32,152,0
E 81
Mavvz 36,152,8
Rv 148,198,9216
C 75,78,79
AS 56,56,4096
Mavvz 40,152,16
E 82
Mavvz 44,152,24
C 80,80,81
Mavvz 60,156,24
E 83
Mavvz 56,156,16
Rv 152,198,10240
Mvvvz 180,192,8
Mavvz 52,156,8
E 84
Mavvz 48,156,0
W 5
Mavvz 64,160,0
E 85
Mavvz 68,160,8
Rv 156,198,11264
Mvvvz 184,192,16
C 81,82,83
AM 58,20480
Mavvz 72,160,16
E 86
Ds 196,24,56,0
Mavvz 76,160,24
C 82,84,85
Mavvz 92,164,24
E 87
Mavvz 88,164,16
Rv 160,198,12288
Mavvz 84,164,8
E 88
Mavvz 80,164,0
Mvvvz 188,192,24
W 5
Mavvz 96,168,0
E 89
Mavvz 100,168,8
C 83,86,87
Ds 196,24,56,1024
Mavvz 104,168,16
Rv 164,198,13312
E 90
Mavvz 108,168,24
C 88,88,89
Mavvz 124,172,24
E 91
Mavvz 120,172,16
Mavvz 116,172,8
E 92
Mavvz 112,172,0
Rv 168,198,14336
W 5
Mavvc 0,144,32
E 93
Ds 196,24,56,2048
Mavvc 4,144,40
C 89,90,91
Mavvc 8,144,48
E 94
Mavvc 12,144,56
C 90,92,93
Mavvc 28,148,56
Rv 172,198,15360
E 95
Mavvc 24,148,48
Mvvvc 176,192,32
Mavvc 20,148,40
E 96
Mavvc 16,148,32
Ds 196,24,56,3072
W 4
Mavvc 32,152,32
E 97
Mavvc 36,152,40
Mvvvc 180,192,40
C 91,94,95
AS 56,56,28672
Mavvc 40,152,48
E 98
Mavvc 44,152,56
C 96,96,97
Mavvc 60,156,56
E 99
Mavvc 56,156,48
WV 32
B
Mavvc 52,156,40
Rv 144,197,0
E 100
Mavvc 48,156,32
W 15
Mavvc 64,160,32
Rv 148,197,1024
E 101
AM 59,0
Mavvc 68,160,40
Mvvvc 184,192,48
C 97,98,99
Ds 196,28,57,0
Mavvc 72,160,48
E 102
Mavvc 76,160,56
Rv 152,197,2048
C 98,100,101
Mavvc 92,164,56
E 103
Mavvc 88,164,48
Ds 196,28,57,1024
Mavvc 84,164,40
Rv 156,197,3072
E 104
Mavvc 80,164,32
Mvvvc 188,192,56
W 15
Mavvc 96,168,32
E 105
Mavvc 100,168,40
Rv 160,197,4096
C 99,102,103
Ds 196,28,57,2048
Mavvc 104,168,48
E 106
Mavvc 108,168,56
C 104,104,105
Mavvc 124,172,56
Rv 164,197,5120
E 107
Mavvc 120,172,48
Ds 196,28,57,3072
Mavvc 116,172,40
E 108
Mavvc 112,172,32
Rv 168,197,6144
W 5
XAT2_B5%=:
AS 60,60,1
AT2_B5%=:
Mvvan 0,144,192,128
AS 57,57,4096
E 109
Mvvan 8,144,208,132
C 105,106,107
Mvvan 16,144,224,136
Rv 172,197,7168
E 110
Mvvan 24,144,240,140
C 106,108,109
Mvvac 24,148,244
E 111
Mvvac 16,148,228
Rv 144,197,8192
AM 59,4096
Mvvac 8,148,212
E 112
Ds 196,28,57,0
Mvvac 0,148,196
W 5
Mvvac 0,152,200
E 113
Mvvac 8,152,216
Rv 148,197,9216
C 107,110,111
Mvvac 16,152,232
E 114
Mvvac 24,152,248
C 112,112,113
Mvvac 24,156,252
E 115
Mvvac 16,156,236
Rv 152,197,10240
Mvvac 8,156,220
E 116
Mvvac 0,156,204
Ds 196,28,57,1024
W 5
Mvvan 4,160,192,128
E 117
Mvvan 12,160,208,132
Rv 156,197,11264
C 113,114,115
Mvvan 20,160,224,136
E 118
Mvvan 28,160,240,140
C 114,116,117
Mvvac 28,164,244
E 119
Mvvac 20,164,228
Rv 160,197,12288
Mvvac 12,164,212
E 120
Mvvac 4,164,196
W 5
Mvvac 4,168,200
E 121
Ds 196,28,57,2048
Mvvac 12,168,216
C 115,118,119
Mvvac 20,168,232
Rv 164,197,13312
E 122
Mvvac 28,168,248
C 120,120,121
Mvvac 28,172,252
E 123
Mvvac 20,172,236
Mvvac 12,172,220
E 124
Mvvac 4,172,204
Rv 168,197,14336
W 5
Mvvan 32,144,192,128
E 125
Mvvan 40,144,208,132
C 121,122,123
Ds 196,28,57,3072
Mvvan 48,144,224,136
E 126
Mvvan 56,144,240,140
C 122,124,125
AS 57,57,28672
Mvvac 56,148,244
Rv 172,197,15360
E 127
Mvvac 48,148,228
Mvvac 40,148,212
E 0
Mvvac 32,148,196
W 4
Mvvac 32,152,200
E 1
Mvvac 40,152,216
C 123,126,127
Mvvac 48,152,232
E 2
Mvvac 56,152,248
C 0,0,1
Mvvac 56,156,252
E 3
Mvvac 48,156,236
WV 16
B
Mvvac 40,156,220
Rv 144,198,16384
E 4
Mvvac 32,156,204
W 15
Mvvan 36,160,192,128
Rv 148,198,17408
E 5
Mvvan 44,160,208,132
C 1,2,3
AM 58,0
Mvvan 52,160,224,136
E 6
Ds 196,24,56,0
Mvvan 60,160,240,140
Rv 152,198,18432
C 2,4,5
Mvvac 60,164,244
E 7
Mvvac 52,164,228
Mvvac 44,164,212
Rv 156,198,19456
E 8
Mvvac 36,164,196
W 15
Mvvac 36,168,200
E 9
Mvvac 44,168,216
Rv 160,198,20480
C 3,6,7
Ds 196,24,56,1024
Mvvac 52,168,232
E 10
Mvvac 60,168,248
C 8,8,9
Mvvac 60,172,252
Rv 164,198,21504
E 11
Mvvac 52,172,236
Mvvac 44,172,220
E 12
Mvvac 36,172,204
Rv 168,198,22528
W 5
Mavvc 0,144,64
E 13
Ds 196,24,56,2048
Mavvc 4,144,72
C 9,10,11
Mavvc 8,144,80
Rv 172,198,23552
E 14
Mavvc 12,144,88
C 10,12,13
Mavvc 28,148,88
E 15
Mavvc 24,148,80
Rv 144,198,24576
Mvvvc 176,192,64
Mavvc 20,148,72
E 16
Mavvc 16,148,64
Ds 196,24,56,3072
W 5
Mavvc 32,152,64
E 17
Mavvc 36,152,72
Rv 148,198,25600
C 11,14,15
AS 56,56,4096
Mavvc 40,152,80
E 18
Mavvc 44,152,88
C 16,16,17
)A"
R"A(Mavvc 60,156,88
E 19
Mavvc 56,156,80
Rv 152,198,26624
Mvvvc 180,192,72
Mavvc 52,156,72
E 20
Mavvc 48,156,64
W 5
Mavvc 64,160,64
E 21
Mavvc 68,160,72
Rv 156,198,27648
Mvvvc 184,192,80
C 17,18,19
AM 58,4096
Mavvc 72,160,80
E 22
Ds 196,24,56,0
Mavvc 76,160,88
C 18,20,21
Mavvc 92,164,88
E 23
Mavvc 88,164,80
Rv 160,198,28672
Mavvc 84,164,72
E 24
Mavvc 80,164,64
Mvvvc 188,192,88
W 5
Mavvc 96,168,64
E 25
Mavvc 100,168,72
C 19,22,23
Ds 196,24,56,1024
Mavvc 104,168,80
Rv 164,198,29696
E 26
Mavvc 108,168,88
C 24,24,25
Mavvc 124,172,88
E 27
Mavvc 120,172,80
Mavvc 116,172,72
E 28
Mavvc 112,172,64
Rv 168,198,30720
W 5
Mavvc 0,144,96
E 29
Ds 196,24,56,2048
Mavvc 4,144,104
C 25,26,27
Mavvc 8,144,112
E 30
Mavvc 12,144,120
C 26,28,29
Mavvc 28,148,120
Rv 172,198,31744
E 31
Mavvc 24,148,112
Mvvvc 176,192,96
Mavvc 20,148,104
E 32
Mavvc 16,148,96
Ds 196,24,56,3072
W 4
Mavvc 32,152,96
E 33
Mavvc 36,152,104
Mvvvc 180,192,104
C 27,30,31
AS 56,56,28672
Mavvc 40,152,112
E 34
Mavvc 44,152,120
C 32,32,33
Mavvc 60,156,120
E 35
Mavvc 56,156,112
WV 16
B
Mavvc 52,156,104
Rv 144,197,16384
E 36
Mavvc 48,156,96
W 15
Mavvc 64,160,96
Rv 148,197,17408
E 37
AM 59,16384
Mavvc 68,160,104
Mvvvc 184,192,112
C 33,34,35
Ds 196,28,57,0
Mavvc 72,160,112
E 38
Mavvc 76,160,120
Rv 152,197,18432
C 34,36,37
Mavvc 92,164,120
E 39
Mavvc 88,164,112
Ds 196,28,57,1024
Mavvc 84,164,104
Rv 156,197,19456
E 40
Mavvc 80,164,96
Mvvvc 188,192,120
W 15
Mavvc 96,168,96
E 41
Mavvc 100,168,104
Rv 160,197,20480
C 35,38,39
Ds 196,28,57,2048
Mavvc 104,168,112
E 42
Mavvc 108,168,120
C 40,40,41
Mavvc 124,172,120
Rv 164,197,21504
E 43
Mavvc 120,172,112
Ds 196,28,57,3072
Mavvc 116,172,104
E 44
Mavvc 112,172,96
Rv 168,197,22528
W 5
s_bitcmp1_b32 s75, s60
s_cbranch_scc1 XAT2_B7%=
XAT2_B6%=:
AS 60,60,1
AT2_B6%=:
Mvvan 64,144,192,128
AS 57,57,4096
E 45
Mvvan 72,144,208,132
C 41,42,43
Mvvan 80,144,224,136
Rv 172,197,23552
E 46
Mvvan 88,144,240,140
C 42,44,45
Mvvac 88,148,244
E 47
Mvvac 80,148,228
Rv 144,197,24576
AM 59,20480
Mvvac 72,148,212
E 48
Ds 196,28,57,0
Mvvac 64,148,196
W 5
Mvvac 64,152,200
E 49
Mvvac 72,152,216
Rv 148,197,25600
C 43,46,47
Mvvac 80,152,232
E 50
Mvvac 88,152,248
C 48,48,49
Mvvac 88,156,252
E 51
Mvvac 80,156,236
Rv 152,197,26624
Mvvac 72,156,220
E 52
Mvvac 64,156,204
Ds 196,28,57,1024
W 5
Mvvan 68,160,192,128
E 53
Mvvan 76,160,208,132
Rv 156,197,27648
C 49,50,51
Mvvan 84,160,224,136
E 54
Mvvan 92,160,240,140
C 50,52,53
Mvvac 92,164,244
E 55
Mvvac 84,164,228
Rv 160,197,28672
Mvvac 76,164,212
E 56
Mvvac 68,164,196
W 5
Mvvac 68,168,200
E 57
Ds 196,28,57,2048
Mvvac 76,168,216
C 51,54,55
Mvvac 84,168,232
Rv 164,197,29696
E 58
Mvvac 92,168,248
C 56,56,57
Mvvac 92,172,252
E 59
Mvvac 84,172,236
Mvvac 76,172,220
E 60
Mvvac 68,172,204
Rv 168,197,30720
W 5
Mvvan 96,144,192,128
E 61
Mvvan 104,144,208,132
C 57,58,59
Ds 196,28,57,3072
Mvvan 112,144,224,136
E 62
Mvvan 120,144,240,140
C 58,60,61
AS 57,57,28672
Mvvac 120,148,244
Rv 172,197,31744
E 63
Mvvac 112,148,228
Mvvac 104,148,212
E 64
Mvvac 96,148,196
W 4
Mvvac 96,152,200
E 65
Mvvac 104,152,216
C 59,62,63
Mvvac 112,152,232
E 66
Mvvac 120,152,248
C 64,64,65
Mvvac 120,156,252
E 67
Mvvac 112,156,236
WV 16
B
Mvvac 104,156,220
Rv 144,198,0
E 68
Mvvac 96,156,204
W 15
Mvvan 100,160,192,128
Rv 148,198,1024
E 69
Mvvan 108,160,208,132
C 65,66,67
AM 58,16384
Mvvan 116,160,224,136
E 70
Ds 196,24,56,0
Mvvan 124,160,240,140
Rv 152,198,2048
C 66,68,69
Mvvac 124,164,244
E 71
Mvvac 116,164,228
Mvvac 108,164,212
Rv 156,198,3072
E 72
Mvvac 100,164,196
W 15
Mvvac 100,168,200
E 73
Mvvac 108,168,216
Rv 160,198,4096
C 67,70,71
Ds 196,24,56,1024
Mvvac 116,168,232
E 74
Mvvac 124,168,248
C 72,72,73
Mvvac 124,172,252
Rv 164,198,5120
E 75
Mvvac 116,172,236
Mvvac 108,172,220
E 76
Mvvac 100,172,204
Rv 168,198,6144
W 5
Mavvc 0,144,0
E 77
Ds 196,24,56,2048
Mavvc 4,144,8
C 73,74,75
Mavvc 8,144,16
Rv 172,198,7168
E 78
Mavvc 12,144,24
C 74,76,77
Mavvc 28,148,24
E 79
Mavvc 24,148,16
Rv 144,198,8192
Mvvvc 176,192,0
Mavvc 20,148,8
E 80
Mavvc 16,148,0
Ds 196,24,56,3072
W 5
Mavvc 32,152,0
E 81
Mavvc 36,152,8
Rv 148,198,9216
C 75,78,79
AS 56,56,4096
Mavvc 40,152,16
E 82
Mavvc 44,152,24
C 80,80,81
Mavvc 60,156,24
E 83
Mavvc 56,156,16
Rv 152,198,10240
Mvvvc 180,192,8
Mavvc 52,156,8
E 84
Mavvc 48,156,0
W 5
Mavvc 64,160,0
E 85
Mavvc 68,160,8
Rv 156,198,11264
Mvvvc 184,192,16
C 81,82,83
AM 58,20480
Mavvc 72,160,16
E 86
Ds 196,24,56,0
Mavvc 76,160,24
C 82,84,85
Mavvc 92,164,24
E 87
Mavvc 88,164,16
Rv 160,198,12288
Mavvc 84,164,8
E 88
Mavvc 80,164,0
Mvvvc 188,192,24
W 5
Mavvc 96,168,0
E 89
Mavvc 100,168,8
C 83,86,87
Ds 196,24,56,1024
Mavvc 104,168,16
Rv 164,198,13312
E 90
Mavvc 108,168,24
C 88,88,89
Mavvc 124,172,24
E 91
Mavvc 120,172,16
Mavvc 116,172,8
E 92
Mavvc 112,172,0
Rv 168,198,14336
W 5
Mavvc 0,144,32
E 93
Ds 196,24,56,2048
Mavvc 4,144,40
C 89,90,91
Mavvc 8,144,48
E 94
Mavvc 12,144,56
C 90,92,93
Mavvc 28,148,56
Rv 172,198,15360
E 95
Mavvc 24,148,48
Mvvvc 176,192,32
Mavvc 20,148,40
E 96
Mavvc 16,148,32
Ds 196,24,56,3072
W 4
Mavvc 32,152,32
E 97
Mavvc 36,152,40
Mvvvc 180,192,40
C 91,94,95
AS 56,56,28672
Mavvc 40,152,48
E 98
Mavvc 44,152,56
C 96,96,97
Mavvc 60,156,56
E 99
Mavvc 56,156,48
WV 16
B
Mavvc 52,156,40
Rv 144,197,0
E 100
Mavvc 48,156,32
W 15
Mavvc 64,160,32
Rv 148,197,1024
E 101
AM 59,0
Mavvc 68,160,40
Mvvvc 184,192,48
C 97,98,99
Ds 196,28,57,0
Mavvc 72,160,48
E 102
Mavvc 76,160,56
Rv 152,197,2048
C 98,100,101
Mavvc 92,164,56
E 103
Mavvc 88,164,48
Ds 196,28,57,1024
Mavvc 84,164,40
Rv 156,197,3072
E 104
Mavvc 80,164,32
Mvvvc 188,192,56
W 15
Mavvc 96,168,32
E 105
Mavvc 100,168,40
Rv 160,197,4096
C 99,102,103
Ds 196,28,57,2048
Mavvc 104,168,48
E 106
Mavvc 108,168,56
C 104,104,105
Mavvc 124,172,56
Rv 164,197,5120
E 107
Mavvc 120,172,48
Ds 196,28,57,3072
Mavvc 116,172,40
E 108
Mavvc 112,172,32
Rv 168,197,6144
W 5
s_branch XAT2_B5%=
XAT2_B7%=:
AS 60,60,1
AT2_B7%=:
Mvvan 64,144,192,128
AS 57,57,4096
E 45
Mvvan 72,144,208,132
C 41,42,43
Mvvan 80,144,224,136
Rv 172,197,23552
E 46
Mvvan 88,144,240,140
C 42,44,45
Mvvac 88,148,244
E 47
Mvvac 80,148,228
Rv 144,197,24576
AM 59,20480
Mvvac 72,148,212
E 48
Ds 196,28,57,0
Mvvac 64,148,196
W 5
Mvvac 64,152,200
E 49
Mvvac 72,152,216
Rv 148,197,25600
C 43,46,47
Mvvac 80,152,232
E 50
Mvvac 88,152,248
C 48,48,49
Mvvac 88,156,252
E 51
Mvvac 80,156,236
Rv 152,197,26624
Mvvac 72,156,220
E 52
Mvvac 64,156,204
Ds 196,28,57,1024
W 5
Mvvan 68,160,192,128
E 53
Mvvan 76,160,208,132
Rv 156,197,27648
C 49,50,51
Mvvan 84,160,224,136
E 54
Mvvan 92,160,240,140
C 50,52,53
Mvvac 92,164,244
E 55
Mvvac 84,164,228
Rv 160,197,28672
Mvvac 76,164,212
E 56
Mvvac 68,164,196
W 5
Mvvac 68,168,200
E 57
Ds 196,28,57,2048
Mvvac 76,168,216
C 51,54,55
Mvvac 84,168,232
Rv 164,197,29696
E 58
Mvvac 92,168,248
C 56,56,57
Mvvac 92,172,252
E 59
Mvvac 84,172,236
Mvvac 76,172,220
E 60
Mvvac 68,172,204
Rv 168,197,30720
W 5
Mvvan 96,144,192,128
E 61
Mvvan 104,144,208,132
C 57,58,59
Ds 196,28,57,3072
Mvvan 112,144,224,136
E 62
Mvvan 120,144,240,140
C 58,60,61
AS 57,57,28672
Mvvac 120,148,244
Rv 172,197,31744
E 63
Mvvac 112,148,228
Mvvac 104,148,212
E 64
Mvvac 96,148,196
W 4
Mvvac 96,152,200
E 65
Mvvac 104,152,216
C 59,62,63
Mvvac 112,152,232
E 66
Mvvac 120,152,248
C 64,64,65
Mvvac 120,156,252
E 67
Mvvac 112,156,236
WV 16
B
Mvvac 104,156,220
Rv 144,198,0
E 68
Mvvac 96,156,204
W 15
Mvvan 100,160,192,128
Rv 148,198,1024
E 69
Mvvan 108,160,208,132
C 65,66,67
Mvvan 116,160,224,136
E 70
Mvvan 124,160,240,140
Rv 152,198,2048
C 66,68,69
Mvvac 124,164,244
E 71
Mvvac 116,164,228
Mvvac 108,164,212
Rv 156,198,3072
E 72
Mvvac 100,164,196
W 15
Mvvac 100,168,200
E 73
Mvvac 108,168,216
Rv 160,198,4096
C 67,70,71
Mvvac 116,168,232
E 74
Mvvac 124,168,248
C 72,72,73
Mvvac 124,172,252
Rv 164,198,5120
E 75
Mvvac 116,172,236
Mvvac 108,172,220
E 76
Mvvac 100,172,204
Rv 168,198,6144
W 5
Mavvc 0,144,0
E 77
Mavvc 4,144,8
C 73,74,75
Mavvc 8,144,16
Rv 172,198,7168
E 78
Mavvc 12,144,24
C 74,76,77
Mavvc 28,148,24
E 79
Mavvc 24,148,16
Rv 144,198,8192
Mvvvc 176,192,0
Mavvc 20,148,8
E 80
Mavvc 16,148,0
W 5
Mavvc 32,152,0
E 81
Mavvc 36,152,8
Rv 148,198,9216
C 75,78,79
Mavvc 40,152,16
E 82
Mavvc 44,152,24
C 80,80,81
Mavvc 60,156,24
E 83
Mavvc 56,156,16
Rv 152,198,10240
Mvvvc 180,192,8
Mavvc 52,156,8
E 84
Mavvc 48,156,0
W 5
Mavvc 64,160,0
E 85
Mavvc 68,160,8
Rv 156,198,11264
Mvvvc 184,192,16
C 81,82,83
Mavvc 72,160,16
E 86
Mavvc 76,160,24
C 82,84,85
Mavvc 92,164,24
E 87
Mavvc 88,164,16
Rv 160,198,12288
Mavvc 84,164,8
E 88
Mavvc 80,164,0
Mvvvc 188,192,24
W 5
Mavvc 96,168,0
E 89
Mavvc 100,168,8
C 83,86,87
Mavvc 104,168,16
Rv 164,198,13312
E 90
Mavvc 108,168,24
C 88,88,89
Mavvc 124,172,24
E 91
Mavvc 120,172,16
Mavvc 116,172,8
E 92
Mavvc 112,172,0
Rv 168,198,14336
W 5
Mavvc 0,144,32
E 93
Mavvc 4,144,40
C 89,90,91
Mavvc 8,144,48
E 94
Mavvc 12,144,56
C 90,92,93
Mavvc 28,148,56
Rv 172,198,15360
E 95
Mavvc 24,148,48
Mvvvc 176,192,32
Mavvc 20,148,40
E 96
Mavvc 16,148,32
W 4
Mavvc 32,152,32
E 97
Mavvc 36,152,40
Mvvvc 180,192,40
C 91,94,95
Mavvc 40,152,48
E 98
Mavvc 44,152,56
C 96,96,97
Mavvc 60,156,56
E 99
Mavvc 56,156,48
WV 8
B
Mavvc 52,156,40
E 100
Mavvc 48,156,32
W 15
Mavvc 64,160,32
E 101
AM 59,0
Mavvc 68,160,40
Mvvvc 184,192,48
C 97,98,99
Ds 196,28,57,0
Mavvc 72,160,48
E 102
Mavvc 76,160,56
C 98,100,101
Mavvc 92,164,56
E 103
Mavvc 88,164,48
Ds 196,28,57,1024
Mavvc 84,164,40
E 104
Mavvc 80,164,32
Mvvvc 188,192,56
W 15
Mavvc 96,168,32
E 105
Mavvc 100,168,40
C 99,102,103
Ds 196,28,57,2048
Mavvc 104,168,48
E 106
Mavvc 108,168,56
C 104,104,105
Mavvc 124,172,56
E 107
Mavvc 120,172,48
Ds 196,28,57,3072
Mavvc 116,172,40
E 108
Mavvc 112,172,32
XAT2_B8%=:
AS 60,60,1
AT2_B8%=:
AS 57,57,4096
E 109
C 105,106,107
E 110
C 106,108,109
E 111
AM 59,4096
E 112
Ds 196,28,57,0
E 113
C 107,110,111
E 114
C 112,112,113
E 115
E 116
Ds 196,28,57,1024
E 117
C 113,114,115
E 118
C 114,116,117
E 119
E 120
E 121
Ds 196,28,57,2048
C 115,118,119
E 122
C 120,120,121
E 123
E 124
E 125
C 121,122,123
Ds 196,28,57,3072
E 126
C 122,124,125
AS 57,57,28672
E 127
N 0
C 123,126,127
WV 8
B
Rv 144,198,16384
Rv 148,198,17408
Rv 152,198,18432
Rv 156,198,19456
Rv 160,198,20480
Rv 164,198,21504
Rv 168,198,22528
W 5
Mavvc 0,144,64
Mavvc 4,144,72
Mavvc 8,144,80
Rv 172,198,23552
Mvvvc 176,192,64
Mavvc 12,144,88
Mavvc 28,148,88
Mavvc 24,148,80
Rv 144,198,24576
Mavvc 20,148,72
Mavvc 16,148,64
W 5
Mavvc 32,152,64
Mavvc 36,152,72
Rv 148,198,25600
Mvvvc 180,192,72
Mavvc 40,152,80
Mavvc 44,152,88
Mavvc 60,156,88
Mavvc 56,156,80
Rv 152,198,26624
Mavvc 52,156,72
Mavvc 48,156,64
W 5
Mavvc 64,160,64
Mvvvc 184,192,80
Mavvc 68,160,72
Rv 156,198,27648
Mavvc 72,160,80
Mavvc 76,160,88
Mavvc 92,164,88
Mavvc 88,164,80
Rv 160,198,28672
Mavvc 84,164,72
Mavvc 80,164,64
W 5
Mavvc 96,168,64
Mvvvc 188,192,88
Mavvc 100,168,72
Mavvc 104,168,80
Rv 164,198,29696
Mavvc 108,168,88
Mavvc 124,172,88
Mavvc 120,172,80
Mavvc 116,172,72
Mavvc 112,172,64
Rv 168,198,30720
W 5
Mavvc 0,144,96
Mavvc 4,144,104
Mavvc 8,144,112
Mvvvc 176,192,96
Mavvc 12,144,120
Mavvc 28,148,120
Rv 172,198,31744
Mavvc 24,148,112
Mavvc 20,148,104
Mavvc 16,148,96
W 4
Mavvc 32,152,96
Mavvc 36,152,104
Mvvvc 180,192,104
Mavvc 40,152,112
Mavvc 44,152,120
Mavvc 60,156,120
Mavvc 56,156,112
W 0
B
Mavvc 52,156,104
Mavvc 48,156,96
W 15
Mavvc 64,160,96
Mvvvc 184,192,112
Mavvc 68,160,104
Mavvc 72,160,112
Mavvc 76,160,120
Mavvc 92,164,120
Mavvc 88,164,112
Mavvc 84,164,104
Mavvc 80,164,96
W 15
Mavvc 96,168,96
Mvvvc 188,192,120
Mavvc 100,168,104
Mavvc 104,168,112
Mavvc 108,168,120
Mavvc 124,172,120
Mavvc 120,172,112
Mavvc 116,172,104
Mavvc 112,172,96
XAT2_B9%=:
AS 60,60,1
AT2_B9%=:
WV 0
B
W 0
B
s_branch XAT2_FIN%=
XAT2_B10%=:
AS 60,60,1
AT2_B10%=:
Mvvaz 0,144,128
Mvvaz 8,144,144
Mvvaz 16,144,160
Rv 172,197,7168
Mvvaz 24,144,176
Mvvac 24,148,180
Mvvac 16,148,164
Rv 144,197,8192
Mvvac 8,148,148
Mvvac 0,148,132
W 5
Mvvac 0,152,136
Mvvac 8,152,152
Rv 148,197,9216
Mvvac 16,152,168
Mvvac 24,152,184
Mvvac 24,156,188
Mvvac 16,156,172
Rv 152,197,10240
Mvvac 8,156,156
Mvvac 0,156,140
W 5
Mvvaz 4,160,128
Mvvaz 12,160,144
Rv 156,197,11264
Mvvaz 20,160,160
N 2
v_max3_f32 v200, v0, v1, v2
Fma 200,200,3
v_max3_f32 v202, v8, v9, v10
)A"
R"A(Fma 202,202,11
Mvvaz 28,160,176
v_max3_f32 v204, v16, v17, v18
Fma 204,204,19
v_max3_f32 v206, v24, v25, v26
Fma 206,206,27
Mvvac 28,164,180
MV 201,200
MV 203,202
MV 205,204
MV 207,206
Mvvac 20,164,164
Rv 160,197,12288
v_permlane16_swap_b32 v200, v201
v_permlane16_swap_b32 v202, v203
v_permlane16_swap_b32 v204, v205
Mvvac 12,164,148
v_permlane16_swap_b32 v206, v207
Fma 200,200,201
Fma 202,202,203
Fma 204,204,205
Mvvac 4,164,132
Fma 206,206,207
MV 201,200
MV 203,202
MV 205,204
W 5
Mvvac 4,168,136
MV 207,206
v_permlane32_swap_b32 v200, v201
v_permlane32_swap_b32 v202, v203
Mvvac 12,168,152
v_permlane32_swap_b32 v204, v205
v_permlane32_swap_b32 v206, v207
Fma 200,200,201
Fma 202,202,203
Mvvac 20,168,168
Rv 164,197,13312
Fma 204,204,205
Fma 206,206,207
v_sub_f32 v128, 0, v200
v_sub_f32 v129, 0, v200
Mvvac 28,168,184
v_sub_f32 v130, 0, v200
v_sub_f32 v131, 0, v200
v_sub_f32 v132, 0, v202
Mvvac 28,172,188
v_sub_f32 v133, 0, v202
v_sub_f32 v134, 0, v202
v_sub_f32 v135, 0, v202
v_sub_f32 v136, 0, v204
Mvvac 20,172,172
v_sub_f32 v137, 0, v204
v_sub_f32 v138, 0, v204
v_sub_f32 v139, 0, v204
v_sub_f32 v140, 0, v206
Mvvac 12,172,156
v_sub_f32 v141, 0, v206
v_sub_f32 v142, 0, v206
v_sub_f32 v143, 0, v206
Mvvac 4,172,140
Rv 168,197,14336
W 5
Mvvan 32,144,128,128
Mvvan 40,144,144,132
Mvvan 48,144,160,136
Mvvan 56,144,176,140
Mvvac 56,148,180
Rv 172,197,15360
Mvvac 48,148,164
Fad 0,0,128
Mvvac 40,148,148
E 0
Mvvac 32,148,132
Fad 1,1,128
W 4
Mvvac 32,152,136
E 1
Mvvac 40,152,152
Fad 2,2,128
Mvvac 48,152,168
E 2
Mvvac 56,152,184
C 0,0,1
Fad 3,3,128
Mvvac 56,156,188
E 3
Mvvac 48,156,172
Fad 4,4,128
W 0
B
Mvvac 40,156,156
E 4
Mvvac 32,156,140
Fad 5,5,128
W 15
Mvvan 36,160,128,128
E 5
Mvvan 44,160,144,132
C 1,2,3
Fad 6,6,128
Mvvan 52,160,160,136
E 6
Mvvan 60,160,176,140
C 2,4,5
Fad 7,7,128
Mvvac 60,164,180
E 7
Mvvac 52,164,164
Fad 8,8,132
AM 58,0
Mvvac 44,164,148
E 8
Ds 196,24,56,0
Mvvac 36,164,132
Fad 9,9,132
W 15
Mvvac 36,168,136
E 9
Mvvac 44,168,152
C 3,6,7
Fad 10,10,132
Mvvac 52,168,168
E 10
Mvvac 60,168,184
C 8,8,9
Fad 11,11,132
Mvvac 60,172,188
E 11
Mvvac 52,172,172
Fad 12,12,132
Mvvac 44,172,156
E 12
Mvvac 36,172,140
Fad 13,13,132
E 13
C 9,10,11
Fad 14,14,132
E 14
C 10,12,13
Fad 15,15,132
E 15
Ds 196,24,56,1024
Fad 16,16,136
E 16
Fad 17,17,136
E 17
C 11,14,15
Fad 18,18,136
E 18
C 16,16,17
Fad 19,19,136
E 19
Fad 20,20,136
E 20
Fad 21,21,136
E 21
C 17,18,19
Fad 22,22,136
E 22
Ds 196,24,56,2048
C 18,20,21
Fad 23,23,136
E 23
Fad 24,24,140
E 24
Fad 25,25,140
E 25
C 19,22,23
Fad 26,26,140
E 26
C 24,24,25
Fad 27,27,140
E 27
Fad 28,28,140
E 28
Fad 29,29,140
E 29
Ds 196,24,56,3072
C 25,26,27
Fad 30,30,140
E 30
AS 56,56,16384
Fad 31,31,140
E 31
C 26,28,29
E 32
E 33
C 27,30,31
E 34
C 32,32,33
E 35
W 0
B
Rv 144,197,16384
E 36
Rv 148,197,17408
E 37
AM 59,16384
C 33,34,35
Ds 196,28,57,0
E 38
Rv 152,197,18432
C 34,36,37
E 39
Ds 196,28,57,1024
Rv 156,197,19456
E 40
E 41
Rv 160,197,20480
C 35,38,39
Ds 196,28,57,2048
E 42
C 40,40,41
Rv 164,197,21504
E 43
Ds 196,28,57,3072
E 44
Rv 168,197,22528
AS 57,57,16384
W 5
XAT2_B11%=:
AS 60,60,1
AT2_B11%=:
Mvvan 64,144,128,128
E 45
Mvvan 72,144,144,132
C 41,42,43
Mvvan 80,144,160,136
Rv 172,197,23552
E 46
Mvvan 88,144,176,140
C 42,44,45
Mvvac 88,148,180
E 47
Mvvac 80,148,164
Rv 144,197,24576
Mvvac 72,148,148
E 48
Mvvac 64,148,132
W 5
Mvvac 64,152,136
E 49
Mvvac 72,152,152
Rv 148,197,25600
C 43,46,47
Mvvac 80,152,168
E 50
Mvvac 88,152,184
C 48,48,49
Mvvac 88,156,188
E 51
Mvvac 80,156,172
Rv 152,197,26624
Mvvac 72,156,156
E 52
Mvvac 64,156,140
W 5
Mvvan 68,160,128,128
E 53
Mvvan 76,160,144,132
Rv 156,197,27648
C 49,50,51
Mvvan 84,160,160,136
E 54
Mvvan 92,160,176,140
C 50,52,53
Mvvac 92,164,180
E 55
Mvvac 84,164,164
Rv 160,197,28672
Mvvac 76,164,148
E 56
Mvvac 68,164,132
W 5
Mvvac 68,168,136
E 57
Mvvac 76,168,152
C 51,54,55
Mvvac 84,168,168
Rv 164,197,29696
E 58
Mvvac 92,168,184
C 56,56,57
Mvvac 92,172,188
E 59
Mvvac 84,172,172
Mvvac 76,172,156
E 60
Mvvac 68,172,140
Rv 168,197,30720
W 5
Mvvan 96,144,128,128
E 61
Mvvan 104,144,144,132
C 57,58,59
Mvvan 112,144,160,136
E 62
Mvvan 120,144,176,140
C 58,60,61
Mvvac 120,148,180
Rv 172,197,31744
E 63
Mvvac 112,148,164
Mvvac 104,148,148
E 64
Mvvac 96,148,132
W 4
Mvvac 96,152,136
E 65
Mvvac 104,152,152
C 59,62,63
Mvvac 112,152,168
E 66
Mvvac 120,152,184
C 64,64,65
Mvvac 120,156,188
E 67
Mvvac 112,156,172
WV 8
B
Mvvac 104,156,156
Rv 144,198,0
E 68
Mvvac 96,156,140
W 15
Mvvan 100,160,128,128
Rv 148,198,1024
E 69
Mvvan 108,160,144,132
C 65,66,67
Mvvan 116,160,160,136
E 70
Mvvan 124,160,176,140
Rv 152,198,2048
C 66,68,69
Mvvac 124,164,180
E 71
Mvvac 116,164,164
AM 58,16384
Mvvac 108,164,148
Rv 156,198,3072
E 72
Ds 196,24,56,0
Mvvac 100,164,132
W 15
Mvvac 100,168,136
E 73
Mvvac 108,168,152
Rv 160,198,4096
C 67,70,71
Mvvac 116,168,168
E 74
Mvvac 124,168,184
C 72,72,73
Mvvac 124,172,188
Rv 164,198,5120
E 75
Mvvac 116,172,172
Mvvac 108,172,156
E 76
Mvvac 100,172,140
Rv 168,198,6144
W 5
Mavvz 0,144,0
E 77
Mavvz 4,144,8
C 73,74,75
Mavvz 8,144,16
Rv 172,198,7168
E 78
Mavvz 12,144,24
C 74,76,77
Mavvz 28,148,24
E 79
Ds 196,24,56,1024
Mavvz 24,148,16
Rv 144,198,8192
Mvvvz 176,192,0
Mavvz 20,148,8
E 80
Mavvz 16,148,0
W 5
Mavvz 32,152,0
E 81
Mavvz 36,152,8
Rv 148,198,9216
C 75,78,79
Mavvz 40,152,16
E 82
Mavvz 44,152,24
C 80,80,81
Mavvz 60,156,24
E 83
Mavvz 56,156,16
Rv 152,198,10240
Mvvvz 180,192,8
Mavvz 52,156,8
E 84
Mavvz 48,156,0
W 5
Mavvz 64,160,0
E 85
Mavvz 68,160,8
Rv 156,198,11264
Mvvvz 184,192,16
C 81,82,83
Mavvz 72,160,16
E 86
Ds 196,24,56,2048
Mavvz 76,160,24
C 82,84,85
Mavvz 92,164,24
E 87
Mavvz 88,164,16
Rv 160,198,12288
Mavvz 84,164,8
E 88
Mavvz 80,164,0
Mvvvz 188,192,24
W 5
Mavvz 96,168,0
E 89
Mavvz 100,168,8
C 83,86,87
Mavvz 104,168,16
Rv 164,198,13312
E 90
Mavvz 108,168,24
C 88,88,89
Mavvz 124,172,24
E 91
Mavvz 120,172,16
Mavvz 116,172,8
E 92
Mavvz 112,172,0
Rv 168,198,14336
W 5
Mavvc 0,144,32
E 93
Ds 196,24,56,3072
Mavvc 4,144,40
C 89,90,91
Mavvc 8,144,48
E 94
AS 56,56,16384
Mavvc 12,144,56
C 90,92,93
Mavvc 28,148,56
Rv 172,198,15360
E 95
Mavvc 24,148,48
Mvvvc 176,192,32
Mavvc 20,148,40
E 96
Mavvc 16,148,32
W 4
Mavvc 32,152,32
E 97
Mavvc 36,152,40
Mvvvc 180,192,40
C 91,94,95
Mavvc 40,152,48
E 98
Mavvc 44,152,56
C 96,96,97
Mavvc 60,156,56
E 99
Mavvc 56,156,48
WV 8
B
Mavvc 52,156,40
Rv 144,197,0
E 100
Mavvc 48,156,32
W 15
Mavvc 64,160,32
Rv 148,197,1024
E 101
AM 59,0
Mavvc 68,160,40
Mvvvc 184,192,48
C 97,98,99
Ds 196,28,57,0
Mavvc 72,160,48
E 102
Mavvc 76,160,56
Rv 152,197,2048
C 98,100,101
Mavvc 92,164,56
E 103
Mavvc 88,164,48
Ds 196,28,57,1024
Mavvc 84,164,40
Rv 156,197,3072
E 104
Mavvc 80,164,32
Mvvvc 188,192,56
W 15
Mavvc 96,168,32
E 105
Mavvc 100,168,40
Rv 160,197,4096
C 99,102,103
Ds 196,28,57,2048
Mavvc 104,168,48
E 106
Mavvc 108,168,56
C 104,104,105
Mavvc 124,172,56
Rv 164,197,5120
E 107
Mavvc 120,172,48
Ds 196,28,57,3072
Mavvc 116,172,40
E 108
Mavvc 112,172,32
Rv 168,197,6144
AS 57,57,16384
W 5
s_bitcmp1_b32 s76, s60
s_cbranch_scc1 XAT2_B12%=
s_branch XAT2_B14%=
XAT2_B12%=:
AS 60,60,1
AT2_B12%=:
Mvvan 0,144,128,128
E 109
Mvvan 8,144,144,132
C 105,106,107
Mvvan 16,144,160,136
Rv 172,197,7168
E 110
Mvvan 24,144,176,140
C 106,108,109
Mvvac 24,148,180
E 111
Mvvac 16,148,164
Rv 144,197,8192
Mvvac 8,148,148
E 112
Mvvac 0,148,132
W 5
Mvvac 0,152,136
E 113
Mvvac 8,152,152
Rv 148,197,9216
C 107,110,111
Mvvac 16,152,168
E 114
Mvvac 24,152,184
C 112,112,113
Mvvac 24,156,188
E 115
Mvvac 16,156,172
Rv 152,197,10240
Mvvac 8,156,156
E 116
Mvvac 0,156,140
W 5
E 117
C 113,114,115
Mvvan 12,160,144,132
Rv 156,197,11264
Mvvan 20,160,160,136
E 118
Mvvan 28,160,176,140
C 114,116,117
Mvvac 28,164,180
E 119
Mvvac 20,164,164
Rv 160,197,12288
Mvvac 12,164,148
E 120
C 115,118,119
W 5
E 121
Mvvac 12,168,152
Mvvac 20,168,168
Rv 164,197,13312
E 122
Mvvac 28,168,184
C 120,120,121
Mvvac 28,172,188
E 123
Mvvac 20,172,172
Mvvac 12,172,156
E 124
Rv 168,197,14336
W 5
E 125
C 121,122,123
Mvvan 48,144,160,136
E 126
Mvvan 56,144,176,140
C 122,124,125
Mvvac 56,148,180
Rv 172,197,15360
E 127
Mvvac 48,148,164
K 0,199,0,62
E 0
K 1,199,1,64
W 4
E 1
C 123,126,127
K 2,199,2,66
Mvvac 48,152,168
E 2
Mvvac 56,152,184
K 3,199,3,68
Mvvac 56,156,188
E 3
AS 56,56,8192
SM 58,32768
Mvvac 48,156,172
WV 8
B
Rv 144,198,16384
C 0,0,1
W 15
Rv 148,198,17408
C 1,2,3
AM 58,0
v_mov_b32 v2, 0
Ds 196,24,56,0
Mvvan 60,160,176,140
Rv 152,198,18432
Mvvac 60,164,180
Rv 156,198,19456
E 8
W 15
E 9
Rv 160,198,20480
v_mov_b32 v3, 0
Ds 196,24,56,1024
E 10
Mvvac 60,168,184
Mvvac 60,172,188
Rv 164,198,21504
E 11
C 8,8,9
K 12,199,12,62
E 12
C 9,10,11
Rv 168,198,22528
K 13,199,13,64
W 5
Mavvc 0,144,64
E 13
Ds 196,24,56,2048
Mavvc 4,144,72
K 14,199,14,66
Mavvc 8,144,80
Rv 172,198,23552
E 14
Mavvc 12,144,88
Mvvvc 176,192,64
K 15,199,15,68
Mavvc 28,148,88
E 15
Mavvc 24,148,80
Rv 144,198,24576
C 10,12,13
Mavvc 20,148,72
E 16
Mavvc 16,148,64
Ds 196,24,56,3072
W 5
Mavvc 32,152,64
E 17
Mavvc 36,152,72
Rv 148,198,25600
C 11,14,15
AS 56,56,4096
Mavvc 40,152,80
E 18
Mavvc 44,152,88
C 16,16,17
Mavvc 60,156,88
E 19
Mavvc 56,156,80
Rv 152,198,26624
Mvvvc 180,192,72
Mavvc 52,156,72
E 20
Mavvc 48,156,64
W 5
Mavvc 64,160,64
E 21
Mavvc 68,160,72
Rv 156,198,27648
Mvvvc 184,192,80
C 17,18,19
AM 58,4096
Mavvc 72,160,80
E 22
Ds 196,24,56,0
Mavvc 76,160,88
C 18,20,21
Mavvc 92,164,88
E 23
Mavvc 88,164,80
Rv 160,198,28672
Mavvc 84,164,72
E 24
Mavvc 80,164,64
Mvvvc 188,192,88
W 5
Mavvc 96,168,64
E 25
Mavvc 100,168,72
C 19,22,23
Ds 196,24,56,1024
Mavvc 104,168,80
Rv 164,198,29696
E 26
Mavvc 108,168,88
C 24,24,25
Mavvc 124,172,88
E 27
Mavvc 120,172,80
Mavvc 116,172,72
E 28
Mavvc 112,172,64
Rv 168,198,30720
W 5
Mavvc 0,144,96
E 29
Ds 196,24,56,2048
Mavvc 4,144,104
C 25,26,27
Mavvc 8,144,112
E 30
Mavvc 12,144,120
C 26,28,29
Mavvc 28,148,120
Rv 172,198,31744
E 31
Mavvc 24,148,112
Mvvvc 176,192,96
Mavvc 20,148,104
C 27,30,31
Mavvc 16,148,96
Ds 196,24,56,3072
W 4
Mavvc 32,152,96
Mavvc 36,152,104
Mvvvc 180,192,104
AS 56,56,28672
Mavvc 40,152,112
Mavvc 44,152,120
Mavvc 60,156,120
Mavvc 56,156,112
WV 12
B
Mavvc 52,156,104
Mavvc 48,156,96
W 15
Mavvc 64,160,96
Mvvvc 184,192,112
AM 59,16384
Mavvc 68,160,104
Ds 196,28,57,0
Mavvc 72,160,112
Mavvc 76,160,120
Mavvc 92,164,120
Mavvc 88,164,112
Ds 196,28,57,1024
Mavvc 84,164,104
Mavvc 80,164,96
W 15
Mavvc 96,168,96
Mvvvc 188,192,120
Mavvc 100,168,104
Ds 196,28,57,2048
Mavvc 104,168,112
Mavvc 108,168,120
Mavvc 124,172,120
Mavvc 120,172,112
Ds 196,28,57,3072
Mavvc 116,172,104
Mavvc 112,172,96
AS 57,57,16384
s_bitcmp1_b32 s77, s60
s_cbranch_scc1 XAT2_B17%=
XAT2_B13%=:
AS 60,60,1
AT2_B13%=:
K 48,199,48,62
E 48
K 49,199,49,64
E 49
K 50,199,50,66
E 50
K 51,199,51,68
E 51
C 48,48,49
C 49,50,51
v_mov_b32 v50, 0
E 56
E 57
v_mov_b32 v51, 0
E 58
C 56,56,57
E 59
K 60,199,60,62
E 60
K 61,199,61,64
E 61
C 57,58,59
K 62,199,62,66
E 62
K 63,199,63,68
E 63
C 58,60,61
C 59,62,63
WV 12
B
Rv 144,198,0
Rv 148,198,1024
AM 58,16384
N 0
Ds 196,24,56,0
Rv 152,198,2048
Rv 156,198,3072
Rv 160,198,4096
Ds 196,24,56,1024
Rv 164,198,5120
Rv 168,198,6144
W 5
Mavvc 0,144,0
Ds 196,24,56,2048
Mavvc 4,144,8
Mavvc 8,144,16
Rv 172,198,7168
Mvvvc 176,192,0
Mavvc 12,144,24
Mavvc 28,148,24
Mavvc 24,148,16
Rv 144,198,8192
Mavvc 20,148,8
Mavvc 16,148,0
Ds 196,24,56,3072
W 5
Mavvc 32,152,0
Mavvc 36,152,8
Rv 148,198,9216
Mvvvc 180,192,8
AS 56,56,4096
Mavvc 40,152,16
Mavvc 44,152,24
Mavvc 60,156,24
Mavvc 56,156,16
Rv 152,198,10240
Mavvc 52,156,8
Mavvc 48,156,0
W 5
Mavvc 64,160,0
Mvvvc 184,192,16
Mavvc 68,160,8
Rv 156,198,11264
AM 58,20480
Mavvc 72,160,16
Ds 196,24,56,0
Mavvc 76,160,24
Mavvc 92,164,24
Mavvc 88,164,16
Rv 160,198,12288
Mavvc 84,164,8
Mavvc 80,164,0
W 5
Mavvc 96,168,0
Mvvvc 188,192,24
Mavvc 100,168,8
Ds 196,24,56,1024
Mavvc 104,168,16
Rv 164,198,13312
Mavvc 108,168,24
Mavvc 124,172,24
Mavvc 120,172,16
Mavvc 116,172,8
Mavvc 112,172,0
Rv 168,198,14336
W 5
Ds 196,24,56,2048
Mavvc 8,144,48
Mavvc 12,144,56
Mavvc 28,148,56
Rv 172,198,15360
Mavvc 24,148,48
Ds 196,24,56,3072
W 4
AS 56,56,28672
Mavvc 40,152,48
Mavvc 44,152,56
Mavvc 60,156,56
Mavvc 56,156,48
WV 12
B
AS 57,57,8192
SM 59,98304
VA 197,32768,197
Rv 144,197,0
W 15
Rv 148,197,1024
)A"
R"A(Mvvvc 184,192,48
AM 59,0
N 0
Ds 196,28,57,0
Mavvc 72,160,48
Mavvc 76,160,56
Rv 152,197,2048
Mavvc 92,164,56
Mavvc 88,164,48
Ds 196,28,57,1024
Rv 156,197,3072
W 15
Mvvvc 188,192,56
Rv 160,197,4096
Ds 196,28,57,2048
Mavvc 104,168,48
Mavvc 108,168,56
Mavvc 124,172,56
Rv 164,197,5120
Mavvc 120,172,48
Ds 196,28,57,3072
Rv 168,197,6144
W 5
s_branch XAT2_B2%=
XAT2_B14%=:
AS 60,60,1
AT2_B14%=:
Mvvan 0,144,128,128
E 109
Mvvan 8,144,144,132
C 105,106,107
Mvvan 16,144,160,136
Rv 172,197,7168
E 110
Mvvan 24,144,176,140
C 106,108,109
Mvvac 24,148,180
E 111
Mvvac 16,148,164
Rv 144,197,8192
Mvvac 8,148,148
E 112
Mvvac 0,148,132
W 5
Mvvac 0,152,136
E 113
Mvvac 8,152,152
Rv 148,197,9216
C 107,110,111
Mvvac 16,152,168
E 114
Mvvac 24,152,184
C 112,112,113
Mvvac 24,156,188
E 115
Mvvac 16,156,172
Rv 152,197,10240
Mvvac 8,156,156
E 116
Mvvac 0,156,140
W 5
Mvvan 4,160,128,128
E 117
Mvvan 12,160,144,132
Rv 156,197,11264
C 113,114,115
Mvvan 20,160,160,136
E 118
Mvvan 28,160,176,140
C 114,116,117
Mvvac 28,164,180
E 119
Mvvac 20,164,164
Rv 160,197,12288
Mvvac 12,164,148
E 120
Mvvac 4,164,132
W 5
Mvvac 4,168,136
E 121
Mvvac 12,168,152
C 115,118,119
Mvvac 20,168,168
Rv 164,197,13312
E 122
Mvvac 28,168,184
C 120,120,121
Mvvac 28,172,188
E 123
Mvvac 20,172,172
Mvvac 12,172,156
E 124
Mvvac 4,172,140
Rv 168,197,14336
W 5
Mvvan 32,144,128,128
E 125
Mvvan 40,144,144,132
C 121,122,123
Mvvan 48,144,160,136
E 126
Mvvan 56,144,176,140
C 122,124,125
Mvvac 56,148,180
Rv 172,197,15360
E 127
Mvvac 48,148,164
Mvvac 40,148,148
E 0
Mvvac 32,148,132
W 4
Mvvac 32,152,136
E 1
Mvvac 40,152,152
C 123,126,127
Mvvac 48,152,168
E 2
Mvvac 56,152,184
C 0,0,1
Mvvac 56,156,188
E 3
Mvvac 48,156,172
WV 8
B
Mvvac 40,156,156
Rv 144,198,16384
E 4
Mvvac 32,156,140
W 15
Mvvan 36,160,128,128
Rv 148,198,17408
E 5
Mvvan 44,160,144,132
C 1,2,3
Mvvan 52,160,160,136
E 6
Mvvan 60,160,176,140
Rv 152,198,18432
C 2,4,5
Mvvac 60,164,180
E 7
Mvvac 52,164,164
AM 58,0
Mvvac 44,164,148
Rv 156,198,19456
E 8
Ds 196,24,56,0
Mvvac 36,164,132
W 15
Mvvac 36,168,136
E 9
Mvvac 44,168,152
Rv 160,198,20480
C 3,6,7
Mvvac 52,168,168
E 10
Mvvac 60,168,184
C 8,8,9
Mvvac 60,172,188
Rv 164,198,21504
E 11
Mvvac 52,172,172
Mvvac 44,172,156
E 12
Mvvac 36,172,140
Rv 168,198,22528
W 5
Mavvc 0,144,64
E 13
Mavvc 4,144,72
C 9,10,11
Mavvc 8,144,80
Rv 172,198,23552
E 14
Mavvc 12,144,88
C 10,12,13
Mavvc 28,148,88
E 15
Ds 196,24,56,1024
Mavvc 24,148,80
Rv 144,198,24576
Mvvvc 176,192,64
Mavvc 20,148,72
E 16
Mavvc 16,148,64
W 5
Mavvc 32,152,64
E 17
Mavvc 36,152,72
Rv 148,198,25600
C 11,14,15
Mavvc 40,152,80
E 18
Mavvc 44,152,88
C 16,16,17
Mavvc 60,156,88
E 19
Mavvc 56,156,80
Rv 152,198,26624
Mvvvc 180,192,72
Mavvc 52,156,72
E 20
Mavvc 48,156,64
W 5
Mavvc 64,160,64
E 21
Mavvc 68,160,72
Rv 156,198,27648
Mvvvc 184,192,80
C 17,18,19
Mavvc 72,160,80
E 22
Ds 196,24,56,2048
Mavvc 76,160,88
C 18,20,21
Mavvc 92,164,88
E 23
Mavvc 88,164,80
Rv 160,198,28672
Mavvc 84,164,72
E 24
Mavvc 80,164,64
Mvvvc 188,192,88
W 5
Mavvc 96,168,64
E 25
Mavvc 100,168,72
C 19,22,23
Mavvc 104,168,80
Rv 164,198,29696
E 26
Mavvc 108,168,88
C 24,24,25
Mavvc 124,172,88
E 27
Mavvc 120,172,80
Mavvc 116,172,72
E 28
Mavvc 112,172,64
Rv 168,198,30720
W 5
Mavvc 0,144,96
E 29
Ds 196,24,56,3072
Mavvc 4,144,104
C 25,26,27
Mavvc 8,144,112
E 30
AS 56,56,16384
Mavvc 12,144,120
C 26,28,29
Mavvc 28,148,120
Rv 172,198,31744
E 31
Mavvc 24,148,112
Mvvvc 176,192,96
Mavvc 20,148,104
E 32
Mavvc 16,148,96
W 4
Mavvc 32,152,96
E 33
Mavvc 36,152,104
Mvvvc 180,192,104
C 27,30,31
Mavvc 40,152,112
E 34
Mavvc 44,152,120
C 32,32,33
Mavvc 60,156,120
E 35
Mavvc 56,156,112
WV 8
B
Mavvc 52,156,104
Rv 144,197,16384
E 36
Mavvc 48,156,96
W 15
Mavvc 64,160,96
Rv 148,197,17408
E 37
AM 59,16384
Mavvc 68,160,104
Mvvvc 184,192,112
C 33,34,35
Ds 196,28,57,0
Mavvc 72,160,112
E 38
Mavvc 76,160,120
Rv 152,197,18432
C 34,36,37
Mavvc 92,164,120
E 39
Mavvc 88,164,112
Ds 196,28,57,1024
Mavvc 84,164,104
Rv 156,197,19456
E 40
Mavvc 80,164,96
Mvvvc 188,192,120
W 15
Mavvc 96,168,96
E 41
Mavvc 100,168,104
Rv 160,197,20480
C 35,38,39
Ds 196,28,57,2048
Mavvc 104,168,112
E 42
Mavvc 108,168,120
C 40,40,41
Mavvc 124,172,120
Rv 164,197,21504
E 43
Mavvc 120,172,112
Ds 196,28,57,3072
Mavvc 116,172,104
E 44
Mavvc 112,172,96
Rv 168,197,22528
AS 57,57,16384
W 5
XAT2_B15%=:
AS 60,60,1
AT2_B15%=:
Mvvan 64,144,128,128
E 45
Mvvan 72,144,144,132
C 41,42,43
Mvvan 80,144,160,136
Rv 172,197,23552
E 46
Mvvan 88,144,176,140
C 42,44,45
Mvvac 88,148,180
E 47
Mvvac 80,148,164
Rv 144,197,24576
Mvvac 72,148,148
E 48
Mvvac 64,148,132
W 5
Mvvac 64,152,136
E 49
Mvvac 72,152,152
Rv 148,197,25600
C 43,46,47
Mvvac 80,152,168
E 50
Mvvac 88,152,184
C 48,48,49
Mvvac 88,156,188
E 51
Mvvac 80,156,172
Rv 152,197,26624
Mvvac 72,156,156
E 52
Mvvac 64,156,140
W 5
Mvvan 68,160,128,128
E 53
Mvvan 76,160,144,132
Rv 156,197,27648
C 49,50,51
Mvvan 84,160,160,136
E 54
Mvvan 92,160,176,140
C 50,52,53
Mvvac 92,164,180
E 55
Mvvac 84,164,164
Rv 160,197,28672
Mvvac 76,164,148
E 56
Mvvac 68,164,132
W 5
Mvvac 68,168,136
E 57
Mvvac 76,168,152
C 51,54,55
Mvvac 84,168,168
Rv 164,197,29696
E 58
Mvvac 92,168,184
C 56,56,57
Mvvac 92,172,188
E 59
Mvvac 84,172,172
Mvvac 76,172,156
E 60
Mvvac 68,172,140
Rv 168,197,30720
W 5
Mvvan 96,144,128,128
E 61
Mvvan 104,144,144,132
C 57,58,59
Mvvan 112,144,160,136
E 62
Mvvan 120,144,176,140
C 58,60,61
Mvvac 120,148,180
Rv 172,197,31744
E 63
Mvvac 112,148,164
Mvvac 104,148,148
E 64
Mvvac 96,148,132
W 4
Mvvac 96,152,136
E 65
Mvvac 104,152,152
C 59,62,63
Mvvac 112,152,168
E 66
Mvvac 120,152,184
C 64,64,65
Mvvac 120,156,188
E 67
Mvvac 112,156,172
WV 8
B
Mvvac 104,156,156
Rv 144,198,0
E 68
Mvvac 96,156,140
W 15
Mvvan 100,160,128,128
Rv 148,198,1024
E 69
Mvvan 108,160,144,132
C 65,66,67
Mvvan 116,160,160,136
E 70
Mvvan 124,160,176,140
Rv 152,198,2048
C 66,68,69
Mvvac 124,164,180
E 71
Mvvac 116,164,164
AM 58,16384
Mvvac 108,164,148
Rv 156,198,3072
E 72
Ds 196,24,56,0
Mvvac 100,164,132
W 15
Mvvac 100,168,136
E 73
Mvvac 108,168,152
Rv 160,198,4096
C 67,70,71
Mvvac 116,168,168
E 74
Mvvac 124,168,184
C 72,72,73
Mvvac 124,172,188
Rv 164,198,5120
E 75
Mvvac 116,172,172
Mvvac 108,172,156
E 76
Mvvac 100,172,140
Rv 168,198,6144
W 5
Mavvc 0,144,0
E 77
Mavvc 4,144,8
C 73,74,75
Mavvc 8,144,16
Rv 172,198,7168
E 78
Mavvc 12,144,24
C 74,76,77
Mavvc 28,148,24
E 79
Ds 196,24,56,1024
Mavvc 24,148,16
Rv 144,198,8192
Mvvvc 176,192,0
Mavvc 20,148,8
E 80
Mavvc 16,148,0
W 5
Mavvc 32,152,0
E 81
Mavvc 36,152,8
Rv 148,198,9216
C 75,78,79
Mavvc 40,152,16
E 82
Mavvc 44,152,24
C 80,80,81
Mavvc 60,156,24
E 83
Mavvc 56,156,16
Rv 152,198,10240
Mvvvc 180,192,8
Mavvc 52,156,8
E 84
Mavvc 48,156,0
W 5
Mavvc 64,160,0
E 85
Mavvc 68,160,8
Rv 156,198,11264
Mvvvc 184,192,16
C 81,82,83
Mavvc 72,160,16
E 86
Ds 196,24,56,2048
Mavvc 76,160,24
C 82,84,85
Mavvc 92,164,24
E 87
Mavvc 88,164,16
Rv 160,198,12288
Mavvc 84,164,8
E 88
Mavvc 80,164,0
Mvvvc 188,192,24
W 5
Mavvc 96,168,0
E 89
Mavvc 100,168,8
C 83,86,87
Mavvc 104,168,16
Rv 164,198,13312
E 90
Mavvc 108,168,24
C 88,88,89
Mavvc 124,172,24
E 91
Mavvc 120,172,16
Mavvc 116,172,8
E 92
Mavvc 112,172,0
Rv 168,198,14336
W 5
Mavvc 0,144,32
E 93
Ds 196,24,56,3072
Mavvc 4,144,40
C 89,90,91
Mavvc 8,144,48
E 94
AS 56,56,16384
Mavvc 12,144,56
C 90,92,93
Mavvc 28,148,56
Rv 172,198,15360
E 95
Mavvc 24,148,48
Mvvvc 176,192,32
Mavvc 20,148,40
E 96
Mavvc 16,148,32
W 4
Mavvc 32,152,32
E 97
Mavvc 36,152,40
Mvvvc 180,192,40
C 91,94,95
Mavvc 40,152,48
E 98
Mavvc 44,152,56
C 96,96,97
Mavvc 60,156,56
E 99
Mavvc 56,156,48
WV 8
B
Mavvc 52,156,40
Rv 144,197,0
E 100
Mavvc 48,156,32
W 15
Mavvc 64,160,32
Rv 148,197,1024
E 101
AM 59,0
Mavvc 68,160,40
Mvvvc 184,192,48
C 97,98,99
Ds 196,28,57,0
Mavvc 72,160,48
E 102
Mavvc 76,160,56
Rv 152,197,2048
C 98,100,101
Mavvc 92,164,56
E 103
Mavvc 88,164,48
Ds 196,28,57,1024
Mavvc 84,164,40
Rv 156,197,3072
E 104
Mavvc 80,164,32
Mvvvc 188,192,56
W 15
Mavvc 96,168,32
E 105
Mavvc 100,168,40
Rv 160,197,4096
C 99,102,103
Ds 196,28,57,2048
Mavvc 104,168,48
E 106
Mavvc 108,168,56
C 104,104,105
Mavvc 124,172,56
Rv 164,197,5120
E 107
Mavvc 120,172,48
Ds 196,28,57,3072
Mavvc 116,172,40
E 108
Mavvc 112,172,32
Rv 168,197,6144
AS 57,57,16384
W 5
s_bitcmp1_b32 s78, s60
s_cbranch_scc1 XAT2_B12%=
s_branch XAT2_B14%=
XAT2_B16%=:
AS 60,60,1
AT2_B16%=:
Mvvan 64,144,192,128
AS 57,57,4096
E 45
Mvvan 72,144,208,132
C 41,42,43
Mvvan 80,144,224,136
Rv 172,197,23552
E 46
Mvvan 88,144,240,140
C 42,44,45
Mvvac 88,148,244
E 47
Mvvac 80,148,228
Rv 144,197,24576
AM 59,20480
Mvvac 72,148,212
E 48
Ds 196,28,57,0
Mvvac 64,148,196
W 6
Mvvac 64,152,200
E 49
Mvvac 72,152,216
Rv 148,197,25600
C 43,46,47
Mvvac 80,152,232
E 50
Mvvac 88,152,248
C 48,48,49
Mvvac 88,156,252
E 51
Mvvac 80,156,236
Rv 152,197,26624
Mvvac 72,156,220
E 52
Mvvac 64,156,204
Ds 196,28,57,1024
W 6
Mvvan 68,160,192,128
E 53
Mvvan 76,160,208,132
Rv 156,197,27648
C 49,50,51
Mvvan 84,160,224,136
E 54
Mvvan 92,160,240,140
C 50,52,53
Mvvac 92,164,244
E 55
Mvvac 84,164,228
Rv 160,197,28672
Mvvac 76,164,212
E 56
Mvvac 68,164,196
W 5
Mvvac 68,168,200
E 57
Ds 196,28,57,2048
Mvvac 76,168,216
C 51,54,55
Mvvac 84,168,232
Rv 164,197,29696
E 58
Mvvac 92,168,248
C 56,56,57
Mvvac 92,172,252
E 59
Mvvac 84,172,236
Mvvac 76,172,220
E 60
Mvvac 68,172,204
Rv 168,197,30720
W 5
Mvvan 96,144,192,128
E 61
Mvvan 104,144,208,132
C 57,58,59
Ds 196,28,57,3072
Mvvan 112,144,224,136
E 62
Mvvan 120,144,240,140
C 58,60,61
AS 57,57,28672
Mvvac 120,148,244
Rv 172,197,31744
E 63
Mvvac 112,148,228
Mvvac 104,148,212
E 64
Mvvac 96,148,196
W 4
Mvvac 96,152,200
E 65
Mvvac 104,152,216
C 59,62,63
Mvvac 112,152,232
E 66
Mvvac 120,152,248
C 64,64,65
Mvvac 120,156,252
E 67
Mvvac 112,156,236
WV 32
B
VA 198,32768,198
Mvvac 104,156,220
Rv 144,198,0
E 68
Mvvac 96,156,204
W 15
Mvvan 100,160,192,128
Rv 148,198,1024
E 69
Mvvan 108,160,208,132
C 65,66,67
Mvvan 116,160,224,136
E 70
Mvvan 124,160,240,140
Rv 152,198,2048
C 66,68,69
Mvvac 124,164,244
E 71
Mvvac 116,164,228
Mvvac 108,164,212
Rv 156,198,3072
E 72
Mvvac 100,164,196
W 15
Mvvac 100,168,200
E 73
Mvvac 108,168,216
Rv 160,198,4096
C 67,70,71
Mvvac 116,168,232
E 74
Mvvac 124,168,248
C 72,72,73
Mvvac 124,172,252
Rv 164,198,5120
E 75
Mvvac 116,172,236
Mvvac 108,172,220
E 76
Mvvac 100,172,204
Rv 168,198,6144
W 5
Mavvz 0,144,0
E 77
Mavvz 4,144,8
C 73,74,75
Mavvz 8,144,16
Rv 172,198,7168
E 78
Mavvz 12,144,24
C 74,76,77
Mavvz 28,148,24
E 79
Mavvz 24,148,16
Rv 144,198,8192
Mvvvz 176,192,0
Mavvz 20,148,8
E 80
Mavvz 16,148,0
W 5
Mavvz 32,152,0
E 81
Mavvz 36,152,8
Rv 148,198,9216
C 75,78,79
Mavvz 40,152,16
E 82
Mavvz 44,152,24
C 80,80,81
Mavvz 60,156,24
E 83
Mavvz 56,156,16
Rv 152,198,10240
Mvvvz 180,192,8
Mavvz 52,156,8
E 84
Mavvz 48,156,0
W 5
Mavvz 64,160,0
E 85
Mavvz 68,160,8
Rv 156,198,11264
Mvvvz 184,192,16
C 81,82,83
Mavvz 72,160,16
E 86
Mavvz 76,160,24
C 82,84,85
Mavvz 92,164,24
E 87
Mavvz 88,164,16
Rv 160,198,12288
Mavvz 84,164,8
E 88
Mavvz 80,164,0
Mvvvz 188,192,24
W 5
Mavvz 96,168,0
E 89
Mavvz 100,168,8
C 83,86,87
Mavvz 104,168,16
Rv 164,198,13312
E 90
Mavvz 108,168,24
C 88,88,89
Mavvz 124,172,24
E 91
Mavvz 120,172,16
Mavvz 116,172,8
E 92
Mavvz 112,172,0
Rv 168,198,14336
W 5
Mavvc 0,144,32
E 93
Mavvc 4,144,40
C 89,90,91
Mavvc 8,144,48
E 94
Mavvc 12,144,56
C 90,92,93
Mavvc 28,148,56
Rv 172,198,15360
E 95
Mavvc 24,148,48
Mvvvc 176,192,32
Mavvc 20,148,40
E 96
Mavvc 16,148,32
W 4
Mavvc 32,152,32
E 97
Mavvc 36,152,40
Mvvvc 180,192,40
C 91,94,95
Mavvc 40,152,48
E 98
Mavvc 44,152,56
C 96,96,97
Mavvc 60,156,56
E 99
Mavvc 56,156,48
WV 24
B
Mavvc 52,156,40
E 100
Mavvc 48,156,32
W 15
Mavvc 64,160,32
E 101
AM 59,0
Mavvc 68,160,40
Mvvvc 184,192,48
C 97,98,99
Ds 196,28,57,0
Mavvc 72,160,48
E 102
Mavvc 76,160,56
C 98,100,101
Mavvc 92,164,56
E 103
Mavvc 88,164,48
Ds 196,28,57,1024
Mavvc 84,164,40
E 104
Mavvc 80,164,32
Mvvvc 188,192,56
W 15
Mavvc 96,168,32
E 105
Mavvc 100,168,40
C 99,102,103
Ds 196,28,57,2048
Mavvc 104,168,48
E 106
Mavvc 108,168,56
C 104,104,105
Mavvc 124,172,56
E 107
Mavvc 120,172,48
Ds 196,28,57,3072
Mavvc 116,172,40
E 108
Mavvc 112,172,32
s_branch XAT2_B8%=
XAT2_B17%=:
AS 60,60,1
AT2_B17%=:
K 48,199,48,62
E 48
K 49,199,49,64
E 49
K 50,199,50,66
)A"
R"A(E 50
K 51,199,51,68
E 51
C 48,48,49
C 49,50,51
v_mov_b32 v50, 0
E 56
E 57
v_mov_b32 v51, 0
E 58
C 56,56,57
E 59
K 60,199,60,62
E 60
K 61,199,61,64
E 61
C 57,58,59
K 62,199,62,66
E 62
K 63,199,63,68
E 63
C 58,60,61
C 59,62,63
WV 12
B
Rv 144,198,0
Rv 148,198,1024
Rv 152,198,2048
Rv 156,198,3072
Rv 160,198,4096
Rv 164,198,5120
Rv 168,198,6144
W 5
Mavvc 0,144,0
Mavvc 4,144,8
Mavvc 8,144,16
Rv 172,198,7168
Mvvvc 176,192,0
Mavvc 12,144,24
Mavvc 28,148,24
Mavvc 24,148,16
Rv 144,198,8192
Mavvc 20,148,8
Mavvc 16,148,0
W 5
Mavvc 32,152,0
Mavvc 36,152,8
Rv 148,198,9216
Mvvvc 180,192,8
Mavvc 40,152,16
Mavvc 44,152,24
Mavvc 60,156,24
Mavvc 56,156,16
Rv 152,198,10240
Mavvc 52,156,8
Mavvc 48,156,0
W 5
Mavvc 64,160,0
Mvvvc 184,192,16
Mavvc 68,160,8
Rv 156,198,11264
Mavvc 72,160,16
Mavvc 76,160,24
Mavvc 92,164,24
Mavvc 88,164,16
Rv 160,198,12288
Mavvc 84,164,8
Mavvc 80,164,0
W 5
Mavvc 96,168,0
Mvvvc 188,192,24
Mavvc 100,168,8
Mavvc 104,168,16
Rv 164,198,13312
Mavvc 108,168,24
Mavvc 124,172,24
Mavvc 120,172,16
Mavvc 116,172,8
Mavvc 112,172,0
Rv 168,198,14336
W 5
Mavvc 8,144,48
Mavvc 12,144,56
Mavvc 28,148,56
Rv 172,198,15360
Mavvc 24,148,48
W 4
Mavvc 40,152,48
Mavvc 44,152,56
Mavvc 60,156,56
Mavvc 56,156,48
WV 4
B
AS 57,57,8192
SM 59,98304
VA 197,32768,197
W 15
Mvvvc 184,192,48
AM 59,0
N 0
Ds 196,28,57,0
Mavvc 72,160,48
Mavvc 76,160,56
Mavvc 92,164,56
Mavvc 88,164,48
Ds 196,28,57,1024
W 15
Mvvvc 188,192,56
Ds 196,28,57,2048
Mavvc 104,168,48
Mavvc 108,168,56
Mavvc 124,172,56
Mavvc 120,172,48
Ds 196,28,57,3072
XAT2_B18%=:
AS 60,60,1
AT2_B18%=:
AS 57,57,4096
AM 59,4096
N 0
Ds 196,28,57,0
Ds 196,28,57,1024
Ds 196,28,57,2048
Ds 196,28,57,3072
AS 57,57,28672
WV 8
B
W 0
B
s_branch XAT2_B3%=
XAT2_B19%=:
AS 60,60,1
AT2_B19%=:
GW 0,0
GW 1,0
GW 2,0
GW 3,0
GW 4,0
GW 5,0
GW 6,0
GW 7,0
GW 8,0
GW 9,0
GW 10,0
GW 11,0
GW 12,0
GW 13,0
GW 14,0
GW 15,0
GW 16,0
GW 17,0
GW 18,0
GW 19,0
GW 20,0
GW 21,0
GW 22,0
GW 23,0
GW 24,0
GW 25,0
GW 26,0
GW 27,0
GW 28,0
GW 29,0
GW 30,0
GW 31,0
GW 32,0
GW 33,0
GW 34,0
GW 35,0
GW 36,0
GW 37,0
GW 38,0
GW 39,0
GW 40,0
GW 41,0
GW 42,0
GW 43,0
GW 44,0
GW 45,0
GW 46,0
GW 47,0
GW 48,0
GW 49,0
GW 50,0
GW 51,0
GW 52,0
GW 53,0
GW 54,0
GW 55,0
GW 56,0
GW 57,0
GW 58,0
GW 59,0
GW 60,0
GW 61,0
GW 62,0
GW 63,0
GW 64,0
GW 65,0
GW 66,0
GW 67,0
GW 68,0
GW 69,0
GW 70,0
GW 71,0
GW 72,0
GW 73,0
GW 74,0
GW 75,0
GW 76,0
GW 77,0
GW 78,0
GW 79,0
GW 80,0
GW 81,0
GW 82,0
GW 83,0
GW 84,0
GW 85,0
GW 86,0
GW 87,0
GW 88,0
GW 89,0
GW 90,0
GW 91,0
GW 92,0
GW 93,0
GW 94,0
GW 95,0
GW 96,0
GW 97,0
GW 98,0
GW 99,0
GW 100,0
GW 101,0
GW 102,0
GW 103,0
GW 104,0
GW 105,0
GW 106,0
GW 107,0
GW 108,0
GW 109,0
GW 110,0
GW 111,0
GW 112,0
GW 113,0
GW 114,0
GW 115,0
GW 116,0
GW 117,0
GW 118,0
GW 119,0
GW 120,0
GW 121,0
GW 122,0
GW 123,0
GW 124,0
GW 125,0
GW 126,0
GW 127,0
v_mov_b32 v176, 0
v_mov_b32 v128, 0x7f800000
v_mov_b32 v177, 0
v_mov_b32 v129, 0x7f800000
v_mov_b32 v178, 0
v_mov_b32 v130, 0x7f800000
v_mov_b32 v179, 0
v_mov_b32 v131, 0x7f800000
v_mov_b32 v180, 0
v_mov_b32 v132, 0x7f800000
v_mov_b32 v181, 0
v_mov_b32 v133, 0x7f800000
v_mov_b32 v182, 0
v_mov_b32 v134, 0x7f800000
v_mov_b32 v183, 0
v_mov_b32 v135, 0x7f800000
v_mov_b32 v184, 0
v_mov_b32 v136, 0x7f800000
v_mov_b32 v185, 0
v_mov_b32 v137, 0x7f800000
v_mov_b32 v186, 0
v_mov_b32 v138, 0x7f800000
v_mov_b32 v187, 0
v_mov_b32 v139, 0x7f800000
v_mov_b32 v188, 0
v_mov_b32 v140, 0x7f800000
v_mov_b32 v189, 0
v_mov_b32 v141, 0x7f800000
v_mov_b32 v190, 0
v_mov_b32 v142, 0x7f800000
v_mov_b32 v191, 0
v_mov_b32 v143, 0x7f800000
XAT2_B20%=:
AS 60,60,1
AT2_B20%=:
WV 0
B
VA 198,32768,198
W 0
B
XAT2_FIN%=:
AS 60,60,1
AT2_FIN%=:
N 7
WV 0
B
VA 200,32768,196
DWa 200,0,0
DWa 200,4,1024
DWa 200,8,2048
DWa 200,12,3072
DWa 200,16,4096
DWa 200,20,5120
DWa 200,24,6144
DWa 200,28,7168
DWa 200,32,8192
DWa 200,36,9216
DWa 200,40,10240
DWa 200,44,11264
DWa 200,48,12288
DWa 200,52,13312
DWa 200,56,14336
DWa 200,60,15360
v_mov_b32 v231, 0x5d800000
v_cmp_nle_f32 vcc, v176, v231
N 3
s_or_b64 s[70:71], s[70:71], vcc
v_cmp_nle_f32 vcc, v180, v231
N 3
s_or_b64 s[70:71], s[70:71], vcc
v_cmp_nle_f32 vcc, v184, v231
N 3
s_or_b64 s[70:71], s[70:71], vcc
v_cmp_nle_f32 vcc, v188, v231
N 3
s_or_b64 s[70:71], s[70:71], vcc
s_cmp_lg_u64 s[70:71], 0
s_cselect_b32 s16, 1, 0
v_mov_b32 v202, s16
v_mov_b32 v203, 149512
ds_write_b32 v203, v202
MV 204,128
MV 208,176
MV 205,132
MV 209,180
MV 206,136
MV 210,184
MV 207,140
MV 211,188
VA 201,159744,196
ds_write_b128 v201, v[204:207]
DWv 201,208,1024
s_sub_u32 s16, 15, s13
s_lshr_b32 s17, s16, 1
s_lshl_b32 s17, s17, 21
s_and_b32 s16, s16, 1
s_lshl_b32 s16, s16, 13
s_add_u32 s17, s17, s16
s_lshl_b32 s16, s12, 16
s_add_u32 s17, s17, s16
AS 72,17,0
AS 72,72,32768
W 0
B
VA 201,155648,196
ds_read_b128 v[212:215], v201
Rv 216,201,1024
v_mov_b32 v203, 149504
ds_read_b128 v[64:67], v203
ds_read_b64 v[68:69], v203 offset:16
VA 200,0,196
Rv 0,200,0
Rv 4,200,1024
Rv 8,200,2048
Rv 12,200,3072
Rv 16,200,4096
Rv 20,200,5120
Rv 24,200,6144
Rv 28,200,7168
Rv 32,200,8192
Rv 36,200,9216
Rv 40,200,10240
Rv 44,200,11264
Rv 48,200,12288
Rv 52,200,13312
Rv 56,200,14336
Rv 60,200,15360
v_mov_b32 v231, 0x5d800000
W 15
v_min_f32 v201, v204, v212
Fsu 204,201,204
Fsu 212,201,212
E 204
E 212
Fmu 202,208,204
W 15
Ffm 202,216,212
v_rcp_f32 v202, v202
N 0
Fmu 220,204,202
Fmu 222,212,202
v_min_f32 v201, v205, v213
Fsu 205,201,205
Fsu 213,201,213
E 205
E 213
Fmu 202,209,205
Ffm 202,217,213
v_rcp_f32 v202, v202
N 0
Fmu 230,205,202
Fmu 224,213,202
v_min_f32 v201, v206, v214
Fsu 206,201,206
Fsu 214,201,214
E 206
E 214
Fmu 202,210,206
Ffm 202,218,214
v_rcp_f32 v202, v202
N 0
Fmu 128,206,202
Fmu 226,214,202
v_min_f32 v201, v207, v215
Fsu 207,201,207
Fsu 215,201,215
E 207
E 215
Fmu 202,211,207
Ffm 202,219,215
v_rcp_f32 v202, v202
N 0
Fmu 130,207,202
Fmu 228,215,202
W 15
v_or_b32 v64, v64, v65
v_or_b32 v66, v66, v67
v_or_b32 v64, v64, v66
W 15
v_or_b32 v68, v68, v69
v_or_b32 v65, v64, v68
N 0
v_readfirstlane_b32 s16, v65
v_readfirstlane_b32 s17, v68
N 3
s_cmp_lg_u32 s16, 0
s_cbranch_scc1 GOSLOW2%=
FASTM2%=:
G 64,64
G 68,80
G 65,65
G 69,81
G 66,66
G 70,82
G 67,67
G 71,83
PM 64,64,220
PM 66,66,220
PM 68,68,220
PM 70,70,220
W 15
v_pk_fma_f32 v[64:65], v[0:1], v[222:223], v[64:65] op_sel_hi:[1,0,1]
W 11
v_pk_fma_f32 v[68:69], v[16:17], v[222:223], v[68:69] op_sel_hi:[1,0,1]
v_pk_fma_f32 v[66:67], v[2:3], v[222:223], v[66:67] op_sel_hi:[1,0,1]
v_pk_fma_f32 v[70:71], v[18:19], v[222:223], v[70:71] op_sel_hi:[1,0,1]
C 72,64,65
C 73,66,67
C 74,68,69
C 75,70,71
Svs 72,196,32,72,0
G 76,68
G 80,84
G 77,69
G 81,85
G 78,70
G 82,86
G 79,71
G 83,87
PM 76,76,230
PM 78,78,230
PM 80,80,230
PM 82,82,230
v_pk_fma_f32 v[76:77], v[4:5], v[224:225], v[76:77] op_sel_hi:[1,0,1]
W 10
v_pk_fma_f32 v[80:81], v[20:21], v[224:225], v[80:81] op_sel_hi:[1,0,1]
v_pk_fma_f32 v[78:79], v[6:7], v[224:225], v[78:79] op_sel_hi:[1,0,1]
v_pk_fma_f32 v[82:83], v[22:23], v[224:225], v[82:83] op_sel_hi:[1,0,1]
C 84,76,77
C 85,78,79
C 86,80,81
C 87,82,83
Svs 84,196,32,72,1024
G 88,72
G 92,88
G 89,73
G 93,89
G 90,74
G 94,90
G 91,75
G 95,91
PM 88,88,128
PM 90,90,128
PM 92,92,128
PM 94,94,128
v_pk_fma_f32 v[88:89], v[8:9], v[226:227], v[88:89] op_sel_hi:[1,0,1]
W 9
v_pk_fma_f32 v[92:93], v[24:25], v[226:227], v[92:93] op_sel_hi:[1,0,1]
v_pk_fma_f32 v[90:91], v[10:11], v[226:227], v[90:91] op_sel_hi:[1,0,1]
v_pk_fma_f32 v[94:95], v[26:27], v[226:227], v[94:95] op_sel_hi:[1,0,1]
C 96,88,89
C 97,90,91
C 98,92,93
C 99,94,95
Svs 96,196,32,72,2048
G 100,76
G 104,92
G 101,77
G 105,93
G 102,78
G 106,94
G 103,79
G 107,95
PM 100,100,130
PM 102,102,130
PM 104,104,130
PM 106,106,130
v_pk_fma_f32 v[100:101], v[12:13], v[228:229], v[100:101] op_sel_hi:[1,0,1]
W 8
v_pk_fma_f32 v[104:105], v[28:29], v[228:229], v[104:105] op_sel_hi:[1,0,1]
v_pk_fma_f32 v[102:103], v[14:15], v[228:229], v[102:103] op_sel_hi:[1,0,1]
v_pk_fma_f32 v[106:107], v[30:31], v[228:229], v[106:107] op_sel_hi:[1,0,1]
C 108,100,101
C 109,102,103
C 110,104,105
C 111,106,107
Svs 108,196,32,72,3072
AS 72,72,16384
G 64,96
G 68,112
G 65,97
G 69,113
G 66,98
G 70,114
G 67,99
G 71,115
PM 64,64,220
PM 66,66,220
PM 68,68,220
PM 70,70,220
W 7
v_pk_fma_f32 v[64:65], v[32:33], v[222:223], v[64:65] op_sel_hi:[1,0,1]
W 3
v_pk_fma_f32 v[68:69], v[48:49], v[222:223], v[68:69] op_sel_hi:[1,0,1]
v_pk_fma_f32 v[66:67], v[34:35], v[222:223], v[66:67] op_sel_hi:[1,0,1]
v_pk_fma_f32 v[70:71], v[50:51], v[222:223], v[70:71] op_sel_hi:[1,0,1]
C 72,64,65
C 73,66,67
C 74,68,69
C 75,70,71
Svs 72,196,32,72,0
G 76,100
G 80,116
G 77,101
G 81,117
G 78,102
G 82,118
G 79,103
G 83,119
PM 76,76,230
PM 78,78,230
PM 80,80,230
PM 82,82,230
v_pk_fma_f32 v[76:77], v[36:37], v[224:225], v[76:77] op_sel_hi:[1,0,1]
W 2
v_pk_fma_f32 v[80:81], v[52:53], v[224:225], v[80:81] op_sel_hi:[1,0,1]
v_pk_fma_f32 v[78:79], v[38:39], v[224:225], v[78:79] op_sel_hi:[1,0,1]
v_pk_fma_f32 v[82:83], v[54:55], v[224:225], v[82:83] op_sel_hi:[1,0,1]
C 84,76,77
C 85,78,79
C 86,80,81
C 87,82,83
Svs 84,196,32,72,1024
G 88,104
G 92,120
G 89,105
G 93,121
G 90,106
G 94,122
G 91,107
G 95,123
PM 88,88,128
PM 90,90,128
PM 92,92,128
PM 94,94,128
v_pk_fma_f32 v[88:89], v[40:41], v[226:227], v[88:89] op_sel_hi:[1,0,1]
W 1
v_pk_fma_f32 v[92:93], v[56:57], v[226:227], v[92:93] op_sel_hi:[1,0,1]
v_pk_fma_f32 v[90:91], v[42:43], v[226:227], v[90:91] op_sel_hi:[1,0,1]
v_pk_fma_f32 v[94:95], v[58:59], v[226:227], v[94:95] op_sel_hi:[1,0,1]
C 96,88,89
C 97,90,91
C 98,92,93
C 99,94,95
Svs 96,196,32,72,2048
G 100,108
G 104,124
G 101,109
G 105,125
G 102,110
G 106,126
G 103,111
G 107,127
PM 100,100,130
PM 102,102,130
PM 104,104,130
PM 106,106,130
v_pk_fma_f32 v[100:101], v[44:45], v[228:229], v[100:101] op_sel_hi:[1,0,1]
W 0
v_pk_fma_f32 v[104:105], v[60:61], v[228:229], v[104:105] op_sel_hi:[1,0,1]
v_pk_fma_f32 v[102:103], v[46:47], v[228:229], v[102:103] op_sel_hi:[1,0,1]
v_pk_fma_f32 v[106:107], v[62:63], v[228:229], v[106:107] op_sel_hi:[1,0,1]
C 108,100,101
C 109,102,103
C 110,104,105
C 111,106,107
Svs 108,196,32,72,3072
s_getpc_b64 s[16:17]
1:
s_add_u32 s16, s16, AE2%=-1b
s_addc_u32 s17, s17, 0
s_lshr_b32 s21, s10, 3
v_lshlrev_b32 v249, 5, v254
v_add_u32 v249, s21, v249
v_min_u32 v249, ((OE2%=-AE2%=)>>7), v249
v_lshlrev_b32 v249, 7, v249
global_load_dword v248, v249, s[16:17]
s_and_b32 s16, s10, 7
s_lshr_b32 s41, s16, 1
s_and_b32 s16, s16, 1
s_lshl_b32 s16, s16, 3
s_lshr_b32 s17, s10, 3
s_lshr_b32 s18, s17, 2
s_add_u32 s39, s16, s18
s_and_b32 s40, s17, 3
s_lshl_b32 s16, s41, 2
s_add_u32 s43, s16, s40
s_mov_b64 s[24:25], s[50:51]
SM 26,0x500000
SM 27,0x20000
s_lshl_b32 s16, s41, 10
s_lshl_b32 s17, s11, 5
s_add_u32 s16, s16, s17
s_mul_i32 s16, s16, 1280
s_mul_i32 s17, s40, 320
s_add_u32 s36, s16, s17
AE2%=:
s_waitcnt vmcnt(0)
MDONE2%=:
s_sub_u32 s23, 15, s13
s_lshl_b32 s23, s23, 2
s_lshr_b32 s22, s12, 3
s_add_u32 s23, s23, s22
s_and_b32 s19, s12, 7
s_lshl_b32 s19, s19, 2
AS 19,19,2
s_lshl_b32 s21, s23, 8
AS 21,21,32768
s_lshl_b32 s22, s19, 2
s_add_u32 s21, s21, s22
v_mov_b32 v247, s21
v_mov_b32 v246, s79
s_mov_b64 exec, 1
global_store_dword v247, v246, s[14:15] sc1
s_mov_b64 exec, -1
N 3
AS 42,36,0
M0 86016
N 0
D 232,24,42,0
D 233,24,42,1024
D 234,24,42,2048
D 235,24,42,3072
M0 90112
N 0
D 236,24,42,0
AS 42,36,20480
M0 91136
N 0
D 232,24,42,0
D 233,24,42,1024
D 234,24,42,2048
D 235,24,42,3072
M0 95232
N 0
D 236,24,42,0
s_add_u32 s28, s14, 0x2400000
s_addc_u32 s29, s15, 0
SM 30,0x1000000
SM 31,0x20000
s_add_u32 s32, s14, 0x3400000
s_addc_u32 s33, s15, 0
SM 34,0xa00000
SM 35,0x20000
v_lshrrev_b32 v117, 4, v254
v_and_b32 v118, 15, v254
v_lshrrev_b32 v118, 2, v118
v_lshl_add_u32 v118, v117, 2, v118
v_mul_u32_u24 v118, 320, v118
v_and_b32 v119, 3, v254
v_lshl_add_u32 v118, v119, 3, v118
v_and_b32 v117, 1, v117
v_lshlrev_b32 v117, 5, v117
v_add_u32 v119, v118, v117
v_sub_u32 v120, v118, v117
VA 122,65536,119
VA 123,65536,120
VA 124,106496,119
VA 125,106496,120
VA 126,0,119
VA 127,0,120
s_lshr_b32 s16, s39, 1
s_lshl_b32 s16, s16, 21
s_lshl_b32 s17, s41, 19
s_add_u32 s16, s16, s17
s_and_b32 s17, s39, 1
s_lshl_b32 s17, s17, 13
s_add_u32 s16, s16, s17
s_lshl_b32 s17, s11, 11
s_add_u32 s37, s16, s17
)A"
R"A(s_lshl_b32 s16, s41, 4
s_add_u32 s16, s16, s39
s_lshl_b32 s16, s16, 3
s_lshl_b32 s17, s11, 1
s_add_u32 s16, s16, s17
s_mul_i32 s16, s16, 20480
s_mul_i32 s17, s40, 5120
s_add_u32 s38, s16, s17
s_load_dwordx2 s[44:45], s[8:9], 0
SM 46,0x280000
SM 47,0x20000
s_load_dwordx2 s[48:49], s[8:9], 48
SM 50,0x500
SM 51,0x20000
s_load_dwordx2 s[52:53], s[8:9], 80
SM 54,0x280000
SM 55,0x20000
v_lshrrev_b32 v104, 5, v254
v_lshlrev_b32 v104, 5, v104
v_lshrrev_b32 v105, 3, v254
v_and_b32 v105, 3, v105
v_lshl_add_u32 v101, v105, 3, v104
v_and_b32 v106, 7, v254
v_mul_u32_u24 v106, 1280, v106
v_add_u32 v100, v106, v101
s_lshl_b32 s16, s39, 7
s_lshl_b32 s17, s43, 3
s_add_u32 s16, s16, s17
s_mov_b32 s66, s16
s_mul_i32 s16, s16, 1280
s_mul_i32 s17, s11, 320
s_add_u32 s65, s16, s17
s_mov_b32 s67, s17
v_lshrrev_b32 v104, 5, v254
v_mul_u32_u24 v104, 81920, v104
v_lshlrev_b32 v104, 6, v104
v_and_b32 v106, 7, v254
v_lshl_add_u32 v106, v105, 4, v106
s_and_b32 s16, s43, 1
s_lshl_b32 s16, s16, 3
v_add_u32 v106, s16, v106
v_lshl_add_u32 v102, v106, 4, v104
s_lshl_b32 s16, s39, 3
s_lshr_b32 s17, s43, 1
s_add_u32 s16, s16, s17
s_mul_i32 s16, s16, 20480
s_mul_i32 s17, s11, 5120
s_add_u32 s64, s16, s17
W 0
s_load_dwordx2 s[70:71], s[8:9], 56
s_load_dwordx2 s[76:77], s[8:9], 64
s_load_dwordx2 s[72:73], s[8:9], 72
SM 74,0x21c000
SM 75,0x20000
W 0
B
s_lshr_b32 s78, s10, 1
s_and_b32 s16, s10, 1
s_cselect_b32 s16, 54, s78
s_cmp_lt_u32 s16, 54
s_cbranch_scc0 OMN2%=
s_mov_b64 s[56:57], s[70:71]
SM 58,0x21c000
SM 59,0x20000
s_mov_b64 s[60:61], s[76:77]
SM 62,0x21c000
SM 63,0x20000
v_lshrrev_b32 v99, 2, v254
v_mul_u32_u24 v99, 3456, v99
v_and_b32 v98, 3, v254
v_lshl_add_u32 v99, v98, 4, v99
s_lshl_b32 s22, s78, 6
AS 22,22,1105920
AS 21,37,0
Las 80,196,28,21,0
Las 84,196,28,21,1024
AS 21,37,16384
Las 88,196,28,21,0
Las 92,196,28,21,1024
AS 21,37,32768
Las 96,196,28,21,0
Las 100,196,28,21,1024
AS 21,37,49152
Las 104,196,28,21,0
Las 108,196,28,21,1024
AS 21,37,65536
Las 112,196,28,21,0
Las 116,196,28,21,1024
AS 21,37,81920
Las 120,196,28,21,0
Las 124,196,28,21,1024
AS 42,36,163840
M0 126976
N 0
D 232,24,42,0
D 233,24,42,1024
D 234,24,42,2048
D 235,24,42,3072
M0 131072
N 0
D 236,24,42,0
AS 42,36,184320
M0 132096
N 0
D 232,24,42,0
D 233,24,42,1024
D 234,24,42,2048
D 235,24,42,3072
M0 136192
N 0
D 236,24,42,0
WV 22
B
T 0,122,0
T 2,122,5120
T 4,123,32
T 6,123,5152
T 8,122,64
T 10,122,5184
T 12,123,96
T 14,123,5216
T 16,122,128
T 18,122,5248
T 20,123,160
T 22,123,5280
T 24,122,192
T 26,122,5312
T 28,123,224
T 30,123,5344
T 32,122,256
T 34,122,5376
T 36,123,288
T 38,123,5408
WV 20
Mavaz 0,0,80
T 40,122,10240
T 42,122,15360
Mavaz 4,0,84
AS 42,36,327680
M0 20480
Mavaz 12,4,84
T 44,123,10272
D 232,24,42,0
Mavaz 8,4,80
T 46,123,15392
T 48,122,10304
Mavaz 16,8,80
D 233,24,42,1024
Mavaz 20,8,84
T 50,122,15424
Mavaz 28,12,84
T 52,123,10336
T 54,123,15456
Mavaz 24,12,80
T 56,122,10368
D 234,24,42,2048
Mavaz 32,16,80
T 58,122,15488
Mavaz 36,16,84
T 60,123,10400
T 62,123,15520
Mavaz 44,20,84
T 64,122,10432
D 235,24,42,3072
Mavaz 40,20,80
T 66,122,15552
M0 24576
Mavaz 48,24,80
T 68,123,10464
D 236,24,42,0
Mavaz 52,24,84
T 70,123,15584
T 72,122,10496
Mavaz 60,28,84
T 74,122,15616
AS 21,37,98304
Mavaz 56,28,80
T 76,123,10528
Las 128,196,28,21,0
Mavaz 64,32,80
T 78,123,15648
Mavaz 68,32,84
Las 132,196,28,21,1024
Mavaz 76,36,84
Mavaz 72,36,80
WV 25
Mavac 0,40,88
T 0,122,20480
T 2,122,25600
Mavac 4,40,92
AS 42,36,348160
M0 25600
Mavac 12,44,92
T 4,123,20512
D 232,24,42,0
Mavac 8,44,88
T 6,123,25632
T 8,122,20544
Mavac 16,48,88
D 233,24,42,1024
Mavac 20,48,92
T 10,122,25664
Mavac 28,52,92
T 12,123,20576
T 14,123,25696
Mavac 24,52,88
T 16,122,20608
D 234,24,42,2048
Mavac 32,56,88
T 18,122,25728
Mavac 36,56,92
T 20,123,20640
T 22,123,25760
Mavac 44,60,92
T 24,122,20672
D 235,24,42,3072
Mavac 40,60,88
T 26,122,25792
M0 29696
Mavac 48,64,88
T 28,123,20704
D 236,24,42,0
Mavac 52,64,92
T 30,123,25824
T 32,122,20736
Mavac 60,68,92
T 34,122,25856
AS 21,37,114688
Mavac 56,68,88
T 36,123,20768
Las 136,196,28,21,0
Mavac 64,72,88
T 38,123,25888
Mavac 68,72,92
Las 140,196,28,21,1024
Mavac 76,76,92
Mavac 72,76,88
WV 30
Mavac 0,0,96
T 40,122,30720
Mavac 4,0,100
T 42,122,35840
Mavac 12,4,100
T 44,123,30752
T 46,123,35872
Mavac 8,4,96
T 48,122,30784
AS 21,37,131072
Mavac 16,8,96
Las 80,196,28,21,0
Mavac 20,8,100
T 50,122,35904
Mavac 28,12,100
T 52,123,30816
Mavac 24,12,96
T 54,123,35936
Mavac 32,16,96
T 56,122,30848
Mavac 36,16,100
T 58,122,35968
T 60,123,30880
Mavac 44,20,100
T 62,123,36000
Las 84,196,28,21,1024
Mavac 40,20,96
T 64,122,30912
Mavac 48,24,96
T 66,122,36032
Mavac 52,24,100
T 68,123,30944
Mavac 60,28,100
T 70,123,36064
Mavac 56,28,96
T 72,122,30976
T 74,122,36096
Mavac 64,32,96
T 76,123,31008
La 144,99,56,22,0
Mavac 68,32,100
T 78,123,36128
Mavac 76,36,100
Mavac 72,36,96
WV 17
B
Mavac 0,40,104
T 0,124,0
T 2,124,5120
Mavac 4,40,108
AS 42,36,491520
M0 86016
Mavac 12,44,108
T 4,125,32
D 232,24,42,0
Mavac 8,44,104
T 6,125,5152
T 8,124,64
Mavac 16,48,104
D 233,24,42,1024
Mavac 20,48,108
T 10,124,5184
T 12,125,96
Mavac 28,52,108
T 14,125,5216
D 234,24,42,2048
Mavac 24,52,104
T 16,124,128
T 18,124,5248
Mavac 32,56,104
D 235,24,42,3072
Mavac 36,56,108
T 20,125,160
T 22,125,5280
Mavac 44,60,108
T 24,124,192
M0 90112
Mavac 40,60,104
T 26,124,5312
D 236,24,42,0
Mavac 48,64,104
T 28,125,224
AS 21,37,147456
Mavac 52,64,108
T 30,125,5344
Las 88,196,28,21,0
Mavac 60,68,108
T 32,124,256
T 34,124,5376
Mavac 56,68,104
T 36,125,288
Las 92,196,28,21,1024
Mavac 64,72,104
T 38,125,5408
Mavac 68,72,108
La 148,99,60,22,0
Mavac 76,76,108
Mavac 72,76,104
W 0
Mavac 0,0,112
T 40,124,10240
Mavac 4,0,116
T 42,124,15360
Mavac 12,4,116
T 44,125,10272
T 46,125,15392
Mavac 8,4,112
T 48,124,10304
AS 21,37,163840
Mavac 16,8,112
Las 96,196,28,21,0
Mavac 20,8,116
T 50,124,15424
Mavac 28,12,116
T 52,125,10336
Mavac 24,12,112
T 54,125,15456
Mavac 32,16,112
T 56,124,10368
Mavac 36,16,116
T 58,124,15488
T 60,125,10400
Mavac 44,20,116
T 62,125,15520
Las 100,196,28,21,1024
Mavac 40,20,112
T 64,124,10432
Mavac 48,24,112
T 66,124,15552
Mavac 52,24,116
T 68,125,10464
Mavac 60,28,116
T 70,125,15584
T 72,124,10496
Mavac 56,28,112
T 74,124,15616
AS 22,22,55296
Mavac 64,32,112
T 76,125,10528
La 152,99,56,22,0
Mavac 68,32,116
T 78,125,15648
Mavac 76,36,116
Mavac 72,36,112
W 0
Mavac 0,40,120
T 0,124,20480
T 2,124,25600
Mavac 4,40,124
AS 42,36,512000
M0 91136
Mavac 12,44,124
T 4,125,20512
D 232,24,42,0
Mavac 8,44,120
T 6,125,25632
T 8,124,20544
Mavac 16,48,120
D 233,24,42,1024
Mavac 20,48,124
T 10,124,25664
T 12,125,20576
Mavac 28,52,124
T 14,125,25696
D 234,24,42,2048
Mavac 24,52,120
T 16,124,20608
T 18,124,25728
Mavac 32,56,120
D 235,24,42,3072
Mavac 36,56,124
T 20,125,20640
T 22,125,25760
Mavac 44,60,124
T 24,124,20672
M0 95232
Mavac 40,60,120
T 26,124,25792
D 236,24,42,0
Mavac 48,64,120
T 28,125,20704
AS 21,37,180224
Mavac 52,64,124
T 30,125,25824
Las 104,196,28,21,0
Mavac 60,68,124
T 32,124,20736
T 34,124,25856
Mavac 56,68,120
T 36,125,20768
Las 108,196,28,21,1024
Mavac 64,72,120
T 38,125,25888
Mavac 68,72,124
La 156,99,60,22,0
Mavac 76,76,124
Mavac 72,76,120
WV 29
Mavac 0,0,128
T 40,124,30720
Mavac 4,0,132
T 42,124,35840
Mavac 12,4,132
T 44,125,30752
Mavac 8,4,128
T 46,125,35872
T 48,124,30784
Mavac 16,8,128
T 50,124,35904
AS 21,37,196608
Mavac 20,8,132
T 52,125,30816
Las 112,196,28,21,0
Mavac 28,12,132
T 54,125,35936
Mavac 24,12,128
T 56,124,30848
Mavac 32,16,128
T 58,124,35968
Mavac 36,16,132
T 60,125,30880
Mavac 44,20,132
T 62,125,36000
Mavac 40,20,128
T 64,124,30912
Mavac 48,24,128
T 66,124,36032
Mavac 52,24,132
T 68,125,30944
T 70,125,36064
Mavac 60,28,132
T 72,124,30976
Las 116,196,28,21,1024
Mavac 56,28,128
T 74,124,36096
Mavac 64,32,128
T 76,125,31008
Mavac 68,32,132
T 78,125,36128
Mavac 76,36,132
Mavac 72,36,128
WV 24
B
Mavac 0,40,136
T 0,126,0
T 2,126,5120
Mavac 4,40,140
AS 42,36,655360
M0 126976
Mavac 12,44,140
T 4,127,32
D 232,24,42,0
Mavac 8,44,136
T 6,127,5152
T 8,126,64
Mavac 16,48,136
D 233,24,42,1024
Mavac 20,48,140
T 10,126,5184
T 12,127,96
Mavac 28,52,140
T 14,127,5216
D 234,24,42,2048
Mavac 24,52,136
T 16,126,128
T 18,126,5248
Mavac 32,56,136
D 235,24,42,3072
Mavac 36,56,140
T 20,127,160
T 22,127,5280
Mavac 44,60,140
T 24,126,192
M0 131072
Mavac 40,60,136
T 26,126,5312
D 236,24,42,0
Mavac 48,64,136
T 28,127,224
AS 21,37,212992
Mavac 52,64,140
T 30,127,5344
Las 120,196,28,21,0
Mavac 60,68,140
T 32,126,256
T 34,126,5376
Mavac 56,68,136
T 36,127,288
Las 124,196,28,21,1024
Mavac 64,72,136
T 38,127,5408
AS 22,22,55296
Mavac 68,72,140
La 160,99,56,22,0
Mavac 76,76,140
Mavac 72,76,136
WV 30
Mavac 0,0,80
T 40,126,10240
Mavac 4,0,84
T 42,126,15360
Mavac 12,4,84
T 44,127,10272
T 46,127,15392
Mavac 8,4,80
T 48,126,10304
AS 21,37,229376
Mavac 16,8,80
Las 128,196,28,21,0
Mavac 20,8,84
T 50,126,15424
Mavac 28,12,84
T 52,127,10336
Mavac 24,12,80
T 54,127,15456
Mavac 32,16,80
T 56,126,10368
Mavac 36,16,84
T 58,126,15488
T 60,127,10400
Mavac 44,20,84
T 62,127,15520
Las 132,196,28,21,1024
Mavac 40,20,80
T 64,126,10432
Mavac 48,24,80
T 66,126,15552
Mavac 52,24,84
T 68,127,10464
Mavac 60,28,84
T 70,127,15584
Mavac 56,28,80
T 72,126,10496
T 74,126,15616
Mavac 64,32,80
T 76,127,10528
La 164,99,60,22,0
Mavac 68,32,84
T 78,127,15648
Mavac 76,36,84
Mavac 72,36,80
WV 25
Mavac 0,40,88
T 0,126,20480
T 2,126,25600
Mavac 4,40,92
AS 42,36,675840
M0 132096
Mavac 12,44,92
T 4,127,20512
D 232,24,42,0
Mavac 8,44,88
T 6,127,25632
T 8,126,20544
Mavac 16,48,88
D 233,24,42,1024
Mavac 20,48,92
T 10,126,25664
T 12,127,20576
Mavac 28,52,92
T 14,127,25696
D 234,24,42,2048
Mavac 24,52,88
T 16,126,20608
T 18,126,25728
Mavac 32,56,88
D 235,24,42,3072
Mavac 36,56,92
T 20,127,20640
T 22,127,25760
Mavac 44,60,92
T 24,126,20672
M0 136192
Mavac 40,60,88
T 26,126,25792
D 236,24,42,0
Mavac 48,64,88
T 28,127,20704
AS 21,37,245760
Mavac 52,64,92
T 30,127,25824
Las 136,196,28,21,0
Mavac 60,68,92
T 32,126,20736
T 34,126,25856
Mavac 56,68,88
T 36,127,20768
Las 140,196,28,21,1024
Mavac 64,72,88
T 38,127,25888
AS 22,22,55296
Mavac 68,72,92
La 168,99,56,22,0
Mavac 76,76,92
Mavac 72,76,88
WV 30
Mavac 0,0,96
T 40,126,30720
Mavac 4,0,100
T 42,126,35840
Mavac 12,4,100
T 44,127,30752
Mavac 8,4,96
T 46,127,35872
T 48,126,30784
Mavac 16,8,96
T 50,126,35904
AS 21,37,262144
Mavac 20,8,100
T 52,127,30816
Las 80,196,28,21,0
Mavac 28,12,100
T 54,127,35936
Mavac 24,12,96
T 56,126,30848
Mavac 32,16,96
T 58,126,35968
Mavac 36,16,100
T 60,127,30880
Mavac 44,20,100
T 62,127,36000
Mavac 40,20,96
T 64,126,30912
Mavac 48,24,96
T 66,126,36032
Mavac 52,24,100
T 68,127,30944
T 70,127,36064
Mavac 60,28,100
T 72,126,30976
Las 84,196,28,21,1024
Mavac 56,28,96
T 74,126,36096
Mavac 64,32,96
T 76,127,31008
Mavac 68,32,100
T 78,127,36128
Mavac 76,36,100
Mavac 72,36,96
WV 24
B
Mavac 0,40,104
T 0,122,0
T 2,122,5120
Mavac 4,40,108
AS 42,36,819200
M0 20480
Mavac 12,44,108
T 4,123,32
D 232,24,42,0
Mavac 8,44,104
T 6,123,5152
T 8,122,64
Mavac 16,48,104
D 233,24,42,1024
Mavac 20,48,108
T 10,122,5184
T 12,123,96
Mavac 28,52,108
T 14,123,5216
D 234,24,42,2048
Mavac 24,52,104
T 16,122,128
T 18,122,5248
Mavac 32,56,104
D 235,24,42,3072
Mavac 36,56,108
T 20,123,160
T 22,123,5280
Mavac 44,60,108
T 24,122,192
M0 24576
Mavac 40,60,104
T 26,122,5312
D 236,24,42,0
Mavac 48,64,104
T 28,123,224
AS 21,37,278528
Mavac 52,64,108
T 30,123,5344
Las 88,196,28,21,0
Mavac 60,68,108
T 32,122,256
T 34,122,5376
Mavac 56,68,104
T 36,123,288
Las 92,196,28,21,1024
Mavac 64,72,104
T 38,123,5408
Mavac 68,72,108
La 172,99,60,22,0
Mavac 76,76,108
Mavac 72,76,104
WV 29
Mavac 0,0,112
T 40,122,10240
Mavac 4,0,116
T 42,122,15360
Mavac 12,4,116
T 44,123,10272
T 46,123,15392
Mavac 8,4,112
T 48,122,10304
AS 21,37,294912
Mavac 16,8,112
Las 96,196,28,21,0
Mavac 20,8,116
T 50,122,15424
Mavac 28,12,116
T 52,123,10336
Mavac 24,12,112
T 54,123,15456
Mavac 32,16,112
T 56,122,10368
Mavac 36,16,116
T 58,122,15488
T 60,123,10400
Mavac 44,20,116
T 62,123,15520
Las 100,196,28,21,1024
Mavac 40,20,112
T 64,122,10432
Mavac 48,24,112
T 66,122,15552
Mavac 52,24,116
T 68,123,10464
Mavac 60,28,116
T 70,123,15584
T 72,122,10496
)A"
R"A(Mavac 56,28,112
T 74,122,15616
AS 22,22,55296
Mavac 64,32,112
T 76,123,10528
La 176,99,56,22,0
Mavac 68,32,116
T 78,123,15648
Mavac 76,36,116
Mavac 72,36,112
WV 25
Mavac 0,40,120
T 0,122,20480
T 2,122,25600
Mavac 4,40,124
AS 42,36,839680
M0 25600
Mavac 12,44,124
T 4,123,20512
D 232,24,42,0
Mavac 8,44,120
T 6,123,25632
T 8,122,20544
Mavac 16,48,120
D 233,24,42,1024
Mavac 20,48,124
T 10,122,25664
T 12,123,20576
Mavac 28,52,124
T 14,123,25696
D 234,24,42,2048
Mavac 24,52,120
T 16,122,20608
T 18,122,25728
Mavac 32,56,120
D 235,24,42,3072
Mavac 36,56,124
T 20,123,20640
T 22,123,25760
Mavac 44,60,124
T 24,122,20672
M0 29696
Mavac 40,60,120
T 26,122,25792
D 236,24,42,0
Mavac 48,64,120
T 28,123,20704
AS 21,37,311296
Mavac 52,64,124
T 30,123,25824
Las 104,196,28,21,0
Mavac 60,68,124
T 32,122,20736
T 34,122,25856
Mavac 56,68,120
T 36,123,20768
Las 108,196,28,21,1024
Mavac 64,72,120
T 38,123,25888
Mavac 68,72,124
La 180,99,60,22,0
Mavac 76,76,124
Mavac 72,76,120
WV 30
Mavac 0,0,128
T 40,122,30720
Mavac 4,0,132
T 42,122,35840
Mavac 12,4,132
T 44,123,30752
Mavac 8,4,128
T 46,123,35872
T 48,122,30784
Mavac 16,8,128
T 50,122,35904
AS 21,37,327680
Mavac 20,8,132
T 52,123,30816
Las 112,196,28,21,0
Mavac 28,12,132
T 54,123,35936
Mavac 24,12,128
T 56,122,30848
Mavac 32,16,128
T 58,122,35968
Mavac 36,16,132
T 60,123,30880
Mavac 44,20,132
T 62,123,36000
Mavac 40,20,128
T 64,122,30912
Mavac 48,24,128
T 66,122,36032
Mavac 52,24,132
T 68,123,30944
T 70,123,36064
Mavac 60,28,132
T 72,122,30976
Las 116,196,28,21,1024
Mavac 56,28,128
T 74,122,36096
Mavac 64,32,128
T 76,123,31008
Mavac 68,32,132
T 78,123,36128
Mavac 76,36,132
Mavac 72,36,128
WV 24
B
Mavac 0,40,136
T 0,124,0
T 2,124,5120
Mavac 4,40,140
AS 42,36,983040
M0 86016
Mavac 12,44,140
T 4,125,32
D 232,24,42,0
Mavac 8,44,136
T 6,125,5152
T 8,124,64
Mavac 16,48,136
D 233,24,42,1024
Mavac 20,48,140
T 10,124,5184
T 12,125,96
Mavac 28,52,140
T 14,125,5216
D 234,24,42,2048
Mavac 24,52,136
T 16,124,128
T 18,124,5248
Mavac 32,56,136
D 235,24,42,3072
Mavac 36,56,140
T 20,125,160
T 22,125,5280
Mavac 44,60,140
T 24,124,192
M0 90112
Mavac 40,60,136
T 26,124,5312
D 236,24,42,0
Mavac 48,64,136
T 28,125,224
AS 21,37,344064
Mavac 52,64,140
T 30,125,5344
Las 120,196,28,21,0
Mavac 60,68,140
T 32,124,256
T 34,124,5376
Mavac 56,68,136
T 36,125,288
Las 124,196,28,21,1024
Mavac 64,72,136
T 38,125,5408
AS 22,22,55296
Mavac 68,72,140
La 184,99,56,22,0
Mavac 76,76,140
Mavac 72,76,136
WV 29
Mavac 0,0,80
T 40,124,10240
Mavac 4,0,84
T 42,124,15360
Mavac 12,4,84
T 44,125,10272
T 46,125,15392
Mavac 8,4,80
T 48,124,10304
AS 21,37,360448
Mavac 16,8,80
Las 128,196,28,21,0
Mavac 20,8,84
T 50,124,15424
Mavac 28,12,84
T 52,125,10336
Mavac 24,12,80
T 54,125,15456
Mavac 32,16,80
T 56,124,10368
Mavac 36,16,84
T 58,124,15488
T 60,125,10400
Mavac 44,20,84
T 62,125,15520
Las 132,196,28,21,1024
Mavac 40,20,80
T 64,124,10432
Mavac 48,24,80
T 66,124,15552
Mavac 52,24,84
T 68,125,10464
Mavac 60,28,84
T 70,125,15584
Mavac 56,28,80
T 72,124,10496
T 74,124,15616
Mavac 64,32,80
T 76,125,10528
La 188,99,60,22,0
Mavac 68,32,84
T 78,125,15648
Mavac 76,36,84
Mavac 72,36,80
WV 25
Mavac 0,40,88
T 0,124,20480
T 2,124,25600
Mavac 4,40,92
AS 42,36,1003520
M0 91136
Mavac 12,44,92
T 4,125,20512
D 232,24,42,0
Mavac 8,44,88
T 6,125,25632
T 8,124,20544
Mavac 16,48,88
D 233,24,42,1024
Mavac 20,48,92
T 10,124,25664
T 12,125,20576
Mavac 28,52,92
T 14,125,25696
D 234,24,42,2048
Mavac 24,52,88
T 16,124,20608
T 18,124,25728
Mavac 32,56,88
D 235,24,42,3072
Mavac 36,56,92
T 20,125,20640
T 22,125,25760
Mavac 44,60,92
T 24,124,20672
M0 95232
Mavac 40,60,88
T 26,124,25792
D 236,24,42,0
Mavac 48,64,88
T 28,125,20704
AS 21,37,376832
Mavac 52,64,92
T 30,125,25824
Las 136,196,28,21,0
Mavac 60,68,92
T 32,124,20736
T 34,124,25856
Mavac 56,68,88
T 36,125,20768
Las 140,196,28,21,1024
Mavac 64,72,88
T 38,125,25888
AS 22,22,55296
Mavac 68,72,92
La 192,99,56,22,0
Mavac 76,76,92
Mavac 72,76,88
WV 30
Mavac 0,0,96
T 40,124,30720
Mavac 4,0,100
T 42,124,35840
Mavac 12,4,100
T 44,125,30752
T 46,125,35872
Mavac 8,4,96
T 48,124,30784
AS 21,37,393216
Mavac 16,8,96
Las 80,196,28,21,0
Mavac 20,8,100
T 50,124,35904
Mavac 28,12,100
T 52,125,30816
Mavac 24,12,96
T 54,125,35936
Mavac 32,16,96
T 56,124,30848
Mavac 36,16,100
T 58,124,35968
T 60,125,30880
Mavac 44,20,100
T 62,125,36000
Las 84,196,28,21,1024
Mavac 40,20,96
T 64,124,30912
Mavac 48,24,96
T 66,124,36032
Mavac 52,24,100
T 68,125,30944
Mavac 60,28,100
T 70,125,36064
Mavac 56,28,96
T 72,124,30976
T 74,124,36096
Mavac 64,32,96
T 76,125,31008
La 196,99,60,22,0
Mavac 68,32,100
T 78,125,36128
Mavac 76,36,100
Mavac 72,36,96
WV 25
B
Mavac 0,40,104
T 0,126,0
T 2,126,5120
Mavac 4,40,108
AS 42,36,1146880
M0 126976
Mavac 12,44,108
T 4,127,32
D 232,24,42,0
Mavac 8,44,104
T 6,127,5152
T 8,126,64
Mavac 16,48,104
D 233,24,42,1024
Mavac 20,48,108
T 10,126,5184
Mavac 28,52,108
T 12,127,96
T 14,127,5216
Mavac 24,52,104
T 16,126,128
D 234,24,42,2048
Mavac 32,56,104
T 18,126,5248
Mavac 36,56,108
T 20,127,160
T 22,127,5280
Mavac 44,60,108
T 24,126,192
D 235,24,42,3072
Mavac 40,60,104
T 26,126,5312
M0 131072
Mavac 48,64,104
T 28,127,224
D 236,24,42,0
Mavac 52,64,108
T 30,127,5344
T 32,126,256
Mavac 60,68,108
T 34,126,5376
AS 21,37,409600
Mavac 56,68,104
T 36,127,288
Las 88,196,28,21,0
Mavac 64,72,104
T 38,127,5408
Mavac 68,72,108
Las 92,196,28,21,1024
Mavac 76,76,108
Mavac 72,76,104
WV 29
Mavac 0,0,112
T 40,126,10240
Mavac 4,0,116
T 42,126,15360
Mavac 12,4,116
T 44,127,10272
T 46,127,15392
Mavac 8,4,112
T 48,126,10304
AS 21,37,425984
Mavac 16,8,112
Las 96,196,28,21,0
Mavac 20,8,116
T 50,126,15424
Mavac 28,12,116
T 52,127,10336
Mavac 24,12,112
T 54,127,15456
Mavac 32,16,112
T 56,126,10368
Mavac 36,16,116
T 58,126,15488
T 60,127,10400
Mavac 44,20,116
T 62,127,15520
Las 100,196,28,21,1024
Mavac 40,20,112
T 64,126,10432
Mavac 48,24,112
T 66,126,15552
Mavac 52,24,116
T 68,127,10464
Mavac 60,28,116
T 70,127,15584
T 72,126,10496
Mavac 56,28,112
T 74,126,15616
AS 22,22,55296
Mavac 64,32,112
T 76,127,10528
La 200,99,56,22,0
Mavac 68,32,116
T 78,127,15648
Mavac 76,36,116
Mavac 72,36,112
WV 25
Mavac 0,40,120
T 0,126,20480
T 2,126,25600
Mavac 4,40,124
AS 42,36,1167360
M0 132096
Mavac 12,44,124
T 4,127,20512
D 232,24,42,0
Mavac 8,44,120
T 6,127,25632
T 8,126,20544
Mavac 16,48,120
D 233,24,42,1024
Mavac 20,48,124
T 10,126,25664
T 12,127,20576
Mavac 28,52,124
T 14,127,25696
D 234,24,42,2048
Mavac 24,52,120
T 16,126,20608
T 18,126,25728
Mavac 32,56,120
D 235,24,42,3072
Mavac 36,56,124
T 20,127,20640
T 22,127,25760
Mavac 44,60,124
T 24,126,20672
M0 136192
Mavac 40,60,120
T 26,126,25792
D 236,24,42,0
Mavac 48,64,120
T 28,127,20704
AS 21,37,442368
Mavac 52,64,124
T 30,127,25824
Las 104,196,28,21,0
Mavac 60,68,124
T 32,126,20736
T 34,126,25856
Mavac 56,68,120
T 36,127,20768
Las 108,196,28,21,1024
Mavac 64,72,120
T 38,127,25888
Mavac 68,72,124
La 204,99,60,22,0
Mavac 76,76,124
Mavac 72,76,120
WV 30
Mavac 0,0,128
T 40,126,30720
Mavac 4,0,132
T 42,126,35840
Mavac 12,4,132
T 44,127,30752
T 46,127,35872
Mavac 8,4,128
T 48,126,30784
AS 21,37,458752
Mavac 16,8,128
Las 112,196,28,21,0
Mavac 20,8,132
T 50,126,35904
Mavac 28,12,132
T 52,127,30816
Mavac 24,12,128
T 54,127,35936
Mavac 32,16,128
T 56,126,30848
Mavac 36,16,132
T 58,126,35968
T 60,127,30880
Mavac 44,20,132
T 62,127,36000
Las 116,196,28,21,1024
Mavac 40,20,128
T 64,126,30912
Mavac 48,24,128
T 66,126,36032
Mavac 52,24,132
T 68,127,30944
Mavac 60,28,132
T 70,127,36064
T 72,126,30976
Mavac 56,28,128
T 74,126,36096
AS 22,22,55296
Mavac 64,32,128
T 76,127,31008
La 208,99,56,22,0
Mavac 68,32,132
T 78,127,36128
Mavac 76,36,132
Mavac 72,36,128
WV 25
B
Mavac 0,40,136
T 0,122,0
Mavac 4,40,140
T 2,122,5120
Mavac 12,44,140
T 4,123,32
Mavac 8,44,136
T 6,123,5152
T 8,122,64
Mavac 16,48,136
T 10,122,5184
AS 21,37,475136
Mavac 20,48,140
T 12,123,96
Las 120,196,28,21,0
Mavac 28,52,140
T 14,123,5216
Mavac 24,52,136
T 16,122,128
Mavac 32,56,136
T 18,122,5248
Mavac 36,56,140
T 20,123,160
Mavac 44,60,140
T 22,123,5280
Mavac 40,60,136
T 24,122,192
Mavac 48,64,136
T 26,122,5312
Mavac 52,64,140
T 28,123,224
T 30,123,5344
Mavac 60,68,140
T 32,122,256
Las 124,196,28,21,1024
Mavac 56,68,136
T 34,122,5376
Mavac 64,72,136
T 36,123,288
Mavac 68,72,140
T 38,123,5408
Mavac 76,76,140
Mavac 72,76,136
WV 24
Mavac 0,0,80
T 40,122,10240
Mavac 4,0,84
T 42,122,15360
Mavac 12,4,84
T 44,123,10272
T 46,123,15392
Mavac 8,4,80
T 48,122,10304
AS 21,37,491520
Mavac 16,8,80
Las 128,196,28,21,0
Mavac 20,8,84
T 50,122,15424
Mavac 28,12,84
T 52,123,10336
Mavac 24,12,80
T 54,123,15456
Mavac 32,16,80
T 56,122,10368
Mavac 36,16,84
T 58,122,15488
T 60,123,10400
Mavac 44,20,84
T 62,123,15520
Las 132,196,28,21,1024
Mavac 40,20,80
T 64,122,10432
Mavac 48,24,80
T 66,122,15552
Mavac 52,24,84
T 68,123,10464
Mavac 60,28,84
T 70,123,15584
Mavac 56,28,80
T 72,122,10496
T 74,122,15616
Mavac 64,32,80
T 76,123,10528
La 212,99,60,22,0
Mavac 68,32,84
T 78,123,15648
Mavac 76,36,84
Mavac 72,36,80
WV 19
Mavac 0,40,88
T 0,122,20480
Mavac 4,40,92
T 2,122,25600
Mavac 12,44,92
T 4,123,20512
T 6,123,25632
Mavac 8,44,88
T 8,122,20544
AS 21,37,507904
Mavac 16,48,88
Las 136,196,28,21,0
Mavac 20,48,92
T 10,122,25664
Mavac 28,52,92
T 12,123,20576
Mavac 24,52,88
T 14,123,25696
Mavac 32,56,88
T 16,122,20608
Mavac 36,56,92
T 18,122,25728
T 20,123,20640
Mavac 44,60,92
T 22,123,25760
Las 140,196,28,21,1024
Mavac 40,60,88
T 24,122,20672
Mavac 48,64,88
T 26,122,25792
Mavac 52,64,92
T 28,123,20704
Mavac 60,68,92
T 30,123,25824
T 32,122,20736
Mavac 56,68,88
T 34,122,25856
AS 22,22,55296
Mavac 64,72,88
T 36,123,20768
La 216,99,56,22,0
Mavac 68,72,92
T 38,123,25888
Mavac 76,76,92
Mavac 72,76,88
WV 20
Mavac 0,0,96
T 40,122,30720
Mavac 4,0,100
T 42,122,35840
Mavac 12,4,100
T 44,123,30752
Mavac 8,4,96
T 46,123,35872
Mavac 16,8,96
T 48,122,30784
Mavac 20,8,100
T 50,122,35904
Mavac 28,12,100
T 52,123,30816
Mavac 24,12,96
T 54,123,35936
Mavac 32,16,96
T 56,122,30848
Mavac 36,16,100
T 58,122,35968
T 60,123,30880
Mavac 44,20,100
T 62,123,36000
La 220,99,60,22,0
Mavac 40,20,96
T 64,122,30912
Mavac 48,24,96
T 66,122,36032
Mavac 52,24,100
T 68,123,30944
Mavac 60,28,100
T 70,123,36064
Mavac 56,28,96
T 72,122,30976
Mavac 64,32,96
T 74,122,36096
Mavac 68,32,100
T 76,123,31008
Mavac 76,36,100
T 78,123,36128
Mavac 72,36,96
WV 13
B
Mavac 0,40,104
T 0,124,0
Mavac 4,40,108
T 2,124,5120
Mavac 12,44,108
T 4,125,32
Mavac 8,44,104
T 6,125,5152
Mavac 16,48,104
T 8,124,64
Mavac 20,48,108
T 10,124,5184
Mavac 28,52,108
T 12,125,96
Mavac 24,52,104
T 14,125,5216
Mavac 32,56,104
T 16,124,128
Mavac 36,56,108
T 18,124,5248
Mavac 44,60,108
T 20,125,160
Mavac 40,60,104
T 22,125,5280
Mavac 48,64,104
T 24,124,192
Mavac 52,64,108
T 26,124,5312
Mavac 60,68,108
T 28,125,224
Mavac 56,68,104
T 30,125,5344
Mavac 64,72,104
T 32,124,256
Mavac 68,72,108
T 34,124,5376
Mavac 76,76,108
T 36,125,288
Mavac 72,76,104
T 38,125,5408
WV 10
Mavac 0,0,112
T 40,124,10240
Mavac 4,0,116
T 42,124,15360
Mavac 12,4,116
T 44,125,10272
Mavac 8,4,112
T 46,125,15392
Mavac 16,8,112
T 48,124,10304
Mavac 20,8,116
T 50,124,15424
Mavac 28,12,116
T 52,125,10336
Mavac 24,12,112
T 54,125,15456
Mavac 32,16,112
T 56,124,10368
Mavac 36,16,116
T 58,124,15488
Mavac 44,20,116
T 60,125,10400
Mavac 40,20,112
T 62,125,15520
Mavac 48,24,112
T 64,124,10432
Mavac 52,24,116
T 66,124,15552
Mavac 60,28,116
T 68,125,10464
Mavac 56,28,112
T 70,125,15584
Mavac 64,32,112
T 72,124,10496
Mavac 68,32,116
T 74,124,15616
Mavac 76,36,116
T 76,125,10528
Mavac 72,36,112
T 78,125,15648
WV 7
Mavac 0,40,120
T 0,124,20480
Mavac 4,40,124
T 2,124,25600
Mavac 12,44,124
T 4,125,20512
Mavac 8,44,120
T 6,125,25632
Mavac 16,48,120
T 8,124,20544
Mavac 20,48,124
T 10,124,25664
Mavac 28,52,124
T 12,125,20576
Mavac 24,52,120
T 14,125,25696
Mavac 32,56,120
T 16,124,20608
Mavac 36,56,124
T 18,124,25728
Mavac 44,60,124
T 20,125,20640
Mavac 40,60,120
T 22,125,25760
Mavac 48,64,120
T 24,124,20672
Mavac 52,64,124
T 26,124,25792
Mavac 60,68,124
T 28,125,20704
Mavac 56,68,120
T 30,125,25824
Mavac 64,72,120
T 32,124,20736
Mavac 68,72,124
T 34,124,25856
Mavac 76,76,124
T 36,125,20768
Mavac 72,76,120
)A"
R"A(T 38,125,25888
WV 5
Mavac 0,0,128
T 40,124,30720
Mavac 4,0,132
T 42,124,35840
Mavac 12,4,132
T 44,125,30752
Mavac 8,4,128
T 46,125,35872
Mavac 16,8,128
T 48,124,30784
Mavac 20,8,132
T 50,124,35904
Mavac 28,12,132
T 52,125,30816
Mavac 24,12,128
T 54,125,35936
Mavac 32,16,128
T 56,124,30848
Mavac 36,16,132
T 58,124,35968
Mavac 44,20,132
T 60,125,30880
Mavac 40,20,128
T 62,125,36000
Mavac 48,24,128
T 64,124,30912
Mavac 52,24,132
T 66,124,36032
Mavac 60,28,132
T 68,125,30944
Mavac 56,28,128
T 70,125,36064
Mavac 64,32,128
T 72,124,30976
Mavac 68,32,132
T 74,124,36096
Mavac 76,36,132
T 76,125,31008
Mavac 72,36,128
T 78,125,36128
WV 2
B
Mavac 0,40,136
Mavac 4,40,140
Mavac 12,44,140
Mavac 8,44,136
Mavac 16,48,136
Mavac 20,48,140
Mavac 28,52,140
Mavac 24,52,136
Mavac 32,56,136
Mavac 36,56,140
Mavac 44,60,140
Mavac 40,60,136
Mavac 48,64,136
Mavac 52,64,140
Mavac 60,68,140
Mavac 56,68,136
Mavac 64,72,136
Mavac 68,72,140
Mavac 76,76,140
Mavac 72,76,136
s_branch OMD2%=
OMN2%=:
AS 21,37,0
Las 80,196,28,21,0
Las 84,196,28,21,1024
AS 21,37,16384
Las 88,196,28,21,0
Las 92,196,28,21,1024
AS 21,37,32768
Las 96,196,28,21,0
Las 100,196,28,21,1024
AS 21,37,49152
Las 104,196,28,21,0
Las 108,196,28,21,1024
AS 21,37,65536
Las 112,196,28,21,0
Las 116,196,28,21,1024
AS 21,37,81920
Las 120,196,28,21,0
Las 124,196,28,21,1024
AS 42,36,163840
M0 126976
N 0
D 232,24,42,0
D 233,24,42,1024
D 234,24,42,2048
D 235,24,42,3072
M0 131072
N 0
D 236,24,42,0
AS 42,36,184320
M0 132096
N 0
D 232,24,42,0
D 233,24,42,1024
D 234,24,42,2048
D 235,24,42,3072
M0 136192
N 0
D 236,24,42,0
WV 22
B
T 0,122,0
T 2,122,5120
T 4,123,32
T 6,123,5152
T 8,122,64
T 10,122,5184
T 12,123,96
T 14,123,5216
T 16,122,128
T 18,122,5248
T 20,123,160
T 22,123,5280
T 24,122,192
T 26,122,5312
T 28,123,224
T 30,123,5344
T 32,122,256
T 34,122,5376
T 36,123,288
T 38,123,5408
WV 20
Mavaz 0,0,80
T 40,122,10240
T 42,122,15360
Mavaz 4,0,84
AS 42,36,327680
M0 20480
Mavaz 12,4,84
T 44,123,10272
D 232,24,42,0
Mavaz 8,4,80
T 46,123,15392
T 48,122,10304
Mavaz 16,8,80
D 233,24,42,1024
Mavaz 20,8,84
T 50,122,15424
Mavaz 28,12,84
T 52,123,10336
T 54,123,15456
Mavaz 24,12,80
T 56,122,10368
D 234,24,42,2048
Mavaz 32,16,80
T 58,122,15488
Mavaz 36,16,84
T 60,123,10400
T 62,123,15520
Mavaz 44,20,84
T 64,122,10432
D 235,24,42,3072
Mavaz 40,20,80
T 66,122,15552
M0 24576
Mavaz 48,24,80
T 68,123,10464
D 236,24,42,0
Mavaz 52,24,84
T 70,123,15584
T 72,122,10496
Mavaz 60,28,84
T 74,122,15616
AS 21,37,98304
Mavaz 56,28,80
T 76,123,10528
Las 128,196,28,21,0
Mavaz 64,32,80
T 78,123,15648
Mavaz 68,32,84
Las 132,196,28,21,1024
Mavaz 76,36,84
Mavaz 72,36,80
WV 25
Mavac 0,40,88
T 0,122,20480
T 2,122,25600
Mavac 4,40,92
AS 42,36,348160
M0 25600
Mavac 12,44,92
T 4,123,20512
D 232,24,42,0
Mavac 8,44,88
T 6,123,25632
T 8,122,20544
Mavac 16,48,88
D 233,24,42,1024
Mavac 20,48,92
T 10,122,25664
Mavac 28,52,92
T 12,123,20576
T 14,123,25696
Mavac 24,52,88
T 16,122,20608
D 234,24,42,2048
Mavac 32,56,88
T 18,122,25728
Mavac 36,56,92
T 20,123,20640
T 22,123,25760
Mavac 44,60,92
T 24,122,20672
D 235,24,42,3072
Mavac 40,60,88
T 26,122,25792
M0 29696
Mavac 48,64,88
T 28,123,20704
D 236,24,42,0
Mavac 52,64,92
T 30,123,25824
T 32,122,20736
Mavac 60,68,92
T 34,122,25856
AS 21,37,114688
Mavac 56,68,88
T 36,123,20768
Las 136,196,28,21,0
Mavac 64,72,88
T 38,123,25888
Mavac 68,72,92
Las 140,196,28,21,1024
Mavac 76,76,92
Mavac 72,76,88
WV 30
Mavac 0,0,96
T 40,122,30720
Mavac 4,0,100
T 42,122,35840
Mavac 12,4,100
T 44,123,30752
Mavac 8,4,96
T 46,123,35872
T 48,122,30784
Mavac 16,8,96
T 50,122,35904
AS 21,37,131072
Mavac 20,8,100
T 52,123,30816
Las 80,196,28,21,0
Mavac 28,12,100
T 54,123,35936
Mavac 24,12,96
T 56,122,30848
Mavac 32,16,96
T 58,122,35968
Mavac 36,16,100
T 60,123,30880
Mavac 44,20,100
T 62,123,36000
Mavac 40,20,96
T 64,122,30912
Mavac 48,24,96
T 66,122,36032
Mavac 52,24,100
T 68,123,30944
T 70,123,36064
Mavac 60,28,100
T 72,122,30976
Las 84,196,28,21,1024
Mavac 56,28,96
T 74,122,36096
Mavac 64,32,96
T 76,123,31008
Mavac 68,32,100
T 78,123,36128
Mavac 76,36,100
Mavac 72,36,96
WV 16
B
Mavac 0,40,104
T 0,124,0
T 2,124,5120
Mavac 4,40,108
AS 42,36,491520
M0 86016
Mavac 12,44,108
T 4,125,32
D 232,24,42,0
Mavac 8,44,104
T 6,125,5152
T 8,124,64
Mavac 16,48,104
D 233,24,42,1024
Mavac 20,48,108
T 10,124,5184
Mavac 28,52,108
T 12,125,96
T 14,125,5216
Mavac 24,52,104
T 16,124,128
D 234,24,42,2048
Mavac 32,56,104
T 18,124,5248
Mavac 36,56,108
T 20,125,160
T 22,125,5280
Mavac 44,60,108
T 24,124,192
D 235,24,42,3072
Mavac 40,60,104
T 26,124,5312
M0 90112
Mavac 48,64,104
T 28,125,224
D 236,24,42,0
Mavac 52,64,108
T 30,125,5344
T 32,124,256
Mavac 60,68,108
T 34,124,5376
AS 21,37,147456
Mavac 56,68,104
T 36,125,288
Las 88,196,28,21,0
Mavac 64,72,104
T 38,125,5408
Mavac 68,72,108
Las 92,196,28,21,1024
Mavac 76,76,108
Mavac 72,76,104
W 0
Mavac 0,0,112
T 40,124,10240
Mavac 4,0,116
T 42,124,15360
Mavac 12,4,116
T 44,125,10272
Mavac 8,4,112
T 46,125,15392
T 48,124,10304
Mavac 16,8,112
T 50,124,15424
AS 21,37,163840
Mavac 20,8,116
T 52,125,10336
Las 96,196,28,21,0
Mavac 28,12,116
T 54,125,15456
Mavac 24,12,112
T 56,124,10368
Mavac 32,16,112
T 58,124,15488
Mavac 36,16,116
T 60,125,10400
Mavac 44,20,116
T 62,125,15520
Mavac 40,20,112
T 64,124,10432
Mavac 48,24,112
T 66,124,15552
Mavac 52,24,116
T 68,125,10464
T 70,125,15584
Mavac 60,28,116
T 72,124,10496
Las 100,196,28,21,1024
Mavac 56,28,112
T 74,124,15616
Mavac 64,32,112
T 76,125,10528
Mavac 68,32,116
T 78,125,15648
Mavac 76,36,116
Mavac 72,36,112
W 0
Mavac 0,40,120
T 0,124,20480
T 2,124,25600
Mavac 4,40,124
AS 42,36,512000
M0 91136
Mavac 12,44,124
T 4,125,20512
D 232,24,42,0
Mavac 8,44,120
T 6,125,25632
T 8,124,20544
Mavac 16,48,120
D 233,24,42,1024
Mavac 20,48,124
T 10,124,25664
Mavac 28,52,124
T 12,125,20576
T 14,125,25696
Mavac 24,52,120
T 16,124,20608
D 234,24,42,2048
Mavac 32,56,120
T 18,124,25728
Mavac 36,56,124
T 20,125,20640
T 22,125,25760
Mavac 44,60,124
T 24,124,20672
D 235,24,42,3072
Mavac 40,60,120
T 26,124,25792
M0 95232
Mavac 48,64,120
T 28,125,20704
D 236,24,42,0
Mavac 52,64,124
T 30,125,25824
T 32,124,20736
Mavac 60,68,124
T 34,124,25856
AS 21,37,180224
Mavac 56,68,120
T 36,125,20768
Las 104,196,28,21,0
Mavac 64,72,120
T 38,125,25888
Mavac 68,72,124
Las 108,196,28,21,1024
Mavac 76,76,124
Mavac 72,76,120
WV 25
Mavac 0,0,128
T 40,124,30720
Mavac 4,0,132
T 42,124,35840
Mavac 12,4,132
T 44,125,30752
Mavac 8,4,128
T 46,125,35872
T 48,124,30784
Mavac 16,8,128
T 50,124,35904
AS 21,37,196608
Mavac 20,8,132
T 52,125,30816
Las 112,196,28,21,0
Mavac 28,12,132
T 54,125,35936
Mavac 24,12,128
T 56,124,30848
Mavac 32,16,128
T 58,124,35968
Mavac 36,16,132
T 60,125,30880
Mavac 44,20,132
T 62,125,36000
Mavac 40,20,128
T 64,124,30912
Mavac 48,24,128
T 66,124,36032
Mavac 52,24,132
T 68,125,30944
T 70,125,36064
Mavac 60,28,132
T 72,124,30976
Las 116,196,28,21,1024
Mavac 56,28,128
T 74,124,36096
Mavac 64,32,128
T 76,125,31008
Mavac 68,32,132
T 78,125,36128
Mavac 76,36,132
Mavac 72,36,128
WV 20
B
Mavac 0,40,136
T 0,126,0
T 2,126,5120
Mavac 4,40,140
AS 42,36,655360
M0 126976
Mavac 12,44,140
T 4,127,32
D 232,24,42,0
Mavac 8,44,136
T 6,127,5152
T 8,126,64
Mavac 16,48,136
D 233,24,42,1024
Mavac 20,48,140
T 10,126,5184
Mavac 28,52,140
T 12,127,96
T 14,127,5216
Mavac 24,52,136
T 16,126,128
D 234,24,42,2048
Mavac 32,56,136
T 18,126,5248
Mavac 36,56,140
T 20,127,160
T 22,127,5280
Mavac 44,60,140
T 24,126,192
D 235,24,42,3072
Mavac 40,60,136
T 26,126,5312
M0 131072
Mavac 48,64,136
T 28,127,224
D 236,24,42,0
Mavac 52,64,140
T 30,127,5344
T 32,126,256
Mavac 60,68,140
T 34,126,5376
AS 21,37,212992
Mavac 56,68,136
T 36,127,288
Las 120,196,28,21,0
Mavac 64,72,136
T 38,127,5408
Mavac 68,72,140
Las 124,196,28,21,1024
Mavac 76,76,140
Mavac 72,76,136
WV 25
Mavac 0,0,80
T 40,126,10240
Mavac 4,0,84
T 42,126,15360
Mavac 12,4,84
T 44,127,10272
Mavac 8,4,80
T 46,127,15392
T 48,126,10304
Mavac 16,8,80
T 50,126,15424
AS 21,37,229376
Mavac 20,8,84
T 52,127,10336
Las 128,196,28,21,0
Mavac 28,12,84
T 54,127,15456
Mavac 24,12,80
T 56,126,10368
Mavac 32,16,80
T 58,126,15488
Mavac 36,16,84
T 60,127,10400
Mavac 44,20,84
T 62,127,15520
Mavac 40,20,80
T 64,126,10432
Mavac 48,24,80
T 66,126,15552
Mavac 52,24,84
T 68,127,10464
T 70,127,15584
Mavac 60,28,84
T 72,126,10496
Las 132,196,28,21,1024
Mavac 56,28,80
T 74,126,15616
Mavac 64,32,80
T 76,127,10528
Mavac 68,32,84
T 78,127,15648
Mavac 76,36,84
Mavac 72,36,80
WV 20
Mavac 0,40,88
T 0,126,20480
T 2,126,25600
Mavac 4,40,92
AS 42,36,675840
M0 132096
Mavac 12,44,92
T 4,127,20512
D 232,24,42,0
Mavac 8,44,88
T 6,127,25632
T 8,126,20544
Mavac 16,48,88
D 233,24,42,1024
Mavac 20,48,92
T 10,126,25664
Mavac 28,52,92
T 12,127,20576
T 14,127,25696
Mavac 24,52,88
T 16,126,20608
D 234,24,42,2048
Mavac 32,56,88
T 18,126,25728
Mavac 36,56,92
T 20,127,20640
T 22,127,25760
Mavac 44,60,92
T 24,126,20672
D 235,24,42,3072
Mavac 40,60,88
T 26,126,25792
M0 136192
Mavac 48,64,88
T 28,127,20704
D 236,24,42,0
Mavac 52,64,92
T 30,127,25824
T 32,126,20736
Mavac 60,68,92
T 34,126,25856
AS 21,37,245760
Mavac 56,68,88
T 36,127,20768
Las 136,196,28,21,0
Mavac 64,72,88
T 38,127,25888
Mavac 68,72,92
Las 140,196,28,21,1024
Mavac 76,76,92
Mavac 72,76,88
WV 25
Mavac 0,0,96
T 40,126,30720
Mavac 4,0,100
T 42,126,35840
Mavac 12,4,100
T 44,127,30752
Mavac 8,4,96
T 46,127,35872
T 48,126,30784
Mavac 16,8,96
T 50,126,35904
AS 21,37,262144
Mavac 20,8,100
T 52,127,30816
Las 80,196,28,21,0
Mavac 28,12,100
T 54,127,35936
Mavac 24,12,96
T 56,126,30848
Mavac 32,16,96
T 58,126,35968
Mavac 36,16,100
T 60,127,30880
Mavac 44,20,100
T 62,127,36000
Mavac 40,20,96
T 64,126,30912
Mavac 48,24,96
T 66,126,36032
Mavac 52,24,100
T 68,127,30944
T 70,127,36064
Mavac 60,28,100
T 72,126,30976
Las 84,196,28,21,1024
Mavac 56,28,96
T 74,126,36096
Mavac 64,32,96
T 76,127,31008
Mavac 68,32,100
T 78,127,36128
Mavac 76,36,100
Mavac 72,36,96
WV 20
B
Mavac 0,40,104
T 0,122,0
T 2,122,5120
Mavac 4,40,108
AS 42,36,819200
M0 20480
Mavac 12,44,108
T 4,123,32
D 232,24,42,0
Mavac 8,44,104
T 6,123,5152
T 8,122,64
Mavac 16,48,104
D 233,24,42,1024
Mavac 20,48,108
T 10,122,5184
Mavac 28,52,108
T 12,123,96
T 14,123,5216
Mavac 24,52,104
T 16,122,128
D 234,24,42,2048
Mavac 32,56,104
T 18,122,5248
Mavac 36,56,108
T 20,123,160
T 22,123,5280
Mavac 44,60,108
T 24,122,192
D 235,24,42,3072
Mavac 40,60,104
T 26,122,5312
M0 24576
Mavac 48,64,104
T 28,123,224
D 236,24,42,0
Mavac 52,64,108
T 30,123,5344
T 32,122,256
Mavac 60,68,108
T 34,122,5376
AS 21,37,278528
Mavac 56,68,104
T 36,123,288
Las 88,196,28,21,0
Mavac 64,72,104
T 38,123,5408
Mavac 68,72,108
Las 92,196,28,21,1024
Mavac 76,76,108
Mavac 72,76,104
WV 25
Mavac 0,0,112
T 40,122,10240
Mavac 4,0,116
T 42,122,15360
Mavac 12,4,116
T 44,123,10272
Mavac 8,4,112
T 46,123,15392
T 48,122,10304
Mavac 16,8,112
T 50,122,15424
AS 21,37,294912
Mavac 20,8,116
T 52,123,10336
Las 96,196,28,21,0
Mavac 28,12,116
T 54,123,15456
Mavac 24,12,112
T 56,122,10368
Mavac 32,16,112
T 58,122,15488
Mavac 36,16,116
T 60,123,10400
Mavac 44,20,116
T 62,123,15520
Mavac 40,20,112
T 64,122,10432
Mavac 48,24,112
T 66,122,15552
Mavac 52,24,116
T 68,123,10464
T 70,123,15584
Mavac 60,28,116
T 72,122,10496
Las 100,196,28,21,1024
Mavac 56,28,112
T 74,122,15616
Mavac 64,32,112
T 76,123,10528
Mavac 68,32,116
T 78,123,15648
Mavac 76,36,116
Mavac 72,36,112
WV 20
Mavac 0,40,120
T 0,122,20480
T 2,122,25600
Mavac 4,40,124
AS 42,36,839680
M0 25600
Mavac 12,44,124
T 4,123,20512
D 232,24,42,0
Mavac 8,44,120
T 6,123,25632
T 8,122,20544
Mavac 16,48,120
D 233,24,42,1024
Mavac 20,48,124
T 10,122,25664
Mavac 28,52,124
T 12,123,20576
T 14,123,25696
Mavac 24,52,120
T 16,122,20608
D 234,24,42,2048
Mavac 32,56,120
T 18,122,25728
Mavac 36,56,124
T 20,123,20640
T 22,123,25760
Mavac 44,60,124
T 24,122,20672
D 235,24,42,3072
Mavac 40,60,120
T 26,122,25792
M0 29696
Mavac 48,64,120
T 28,123,20704
D 236,24,42,0
Mavac 52,64,124
T 30,123,25824
T 32,122,20736
Mavac 60,68,124
T 34,122,25856
AS 21,37,311296
Mavac 56,68,120
T 36,123,20768
Las 104,196,28,21,0
Mavac 64,72,120
T 38,123,25888
Mavac 68,72,124
Las 108,196,28,21,1024
Mavac 76,76,124
Mavac 72,76,120
WV 25
)A"
R"A(Mavac 0,0,128
T 40,122,30720
Mavac 4,0,132
T 42,122,35840
Mavac 12,4,132
T 44,123,30752
Mavac 8,4,128
T 46,123,35872
T 48,122,30784
Mavac 16,8,128
T 50,122,35904
AS 21,37,327680
Mavac 20,8,132
T 52,123,30816
Las 112,196,28,21,0
Mavac 28,12,132
T 54,123,35936
Mavac 24,12,128
T 56,122,30848
Mavac 32,16,128
T 58,122,35968
Mavac 36,16,132
T 60,123,30880
Mavac 44,20,132
T 62,123,36000
Mavac 40,20,128
T 64,122,30912
Mavac 48,24,128
T 66,122,36032
Mavac 52,24,132
T 68,123,30944
T 70,123,36064
Mavac 60,28,132
T 72,122,30976
Las 116,196,28,21,1024
Mavac 56,28,128
T 74,122,36096
Mavac 64,32,128
T 76,123,31008
Mavac 68,32,132
T 78,123,36128
Mavac 76,36,132
Mavac 72,36,128
WV 20
B
Mavac 0,40,136
T 0,124,0
T 2,124,5120
Mavac 4,40,140
AS 42,36,983040
M0 86016
Mavac 12,44,140
T 4,125,32
D 232,24,42,0
Mavac 8,44,136
T 6,125,5152
T 8,124,64
Mavac 16,48,136
D 233,24,42,1024
Mavac 20,48,140
T 10,124,5184
Mavac 28,52,140
T 12,125,96
T 14,125,5216
Mavac 24,52,136
T 16,124,128
D 234,24,42,2048
Mavac 32,56,136
T 18,124,5248
Mavac 36,56,140
T 20,125,160
T 22,125,5280
Mavac 44,60,140
T 24,124,192
D 235,24,42,3072
Mavac 40,60,136
T 26,124,5312
M0 90112
Mavac 48,64,136
T 28,125,224
D 236,24,42,0
Mavac 52,64,140
T 30,125,5344
T 32,124,256
Mavac 60,68,140
T 34,124,5376
AS 21,37,344064
Mavac 56,68,136
T 36,125,288
Las 120,196,28,21,0
Mavac 64,72,136
T 38,125,5408
Mavac 68,72,140
Las 124,196,28,21,1024
Mavac 76,76,140
Mavac 72,76,136
WV 25
Mavac 0,0,80
T 40,124,10240
Mavac 4,0,84
T 42,124,15360
Mavac 12,4,84
T 44,125,10272
Mavac 8,4,80
T 46,125,15392
T 48,124,10304
Mavac 16,8,80
T 50,124,15424
AS 21,37,360448
Mavac 20,8,84
T 52,125,10336
Las 128,196,28,21,0
Mavac 28,12,84
T 54,125,15456
Mavac 24,12,80
T 56,124,10368
Mavac 32,16,80
T 58,124,15488
Mavac 36,16,84
T 60,125,10400
Mavac 44,20,84
T 62,125,15520
Mavac 40,20,80
T 64,124,10432
Mavac 48,24,80
T 66,124,15552
Mavac 52,24,84
T 68,125,10464
T 70,125,15584
Mavac 60,28,84
T 72,124,10496
Las 132,196,28,21,1024
Mavac 56,28,80
T 74,124,15616
Mavac 64,32,80
T 76,125,10528
Mavac 68,32,84
T 78,125,15648
Mavac 76,36,84
Mavac 72,36,80
WV 20
Mavac 0,40,88
T 0,124,20480
T 2,124,25600
Mavac 4,40,92
AS 42,36,1003520
M0 91136
Mavac 12,44,92
T 4,125,20512
D 232,24,42,0
Mavac 8,44,88
T 6,125,25632
T 8,124,20544
Mavac 16,48,88
D 233,24,42,1024
Mavac 20,48,92
T 10,124,25664
Mavac 28,52,92
T 12,125,20576
T 14,125,25696
Mavac 24,52,88
T 16,124,20608
D 234,24,42,2048
Mavac 32,56,88
T 18,124,25728
Mavac 36,56,92
T 20,125,20640
T 22,125,25760
Mavac 44,60,92
T 24,124,20672
D 235,24,42,3072
Mavac 40,60,88
T 26,124,25792
M0 95232
Mavac 48,64,88
T 28,125,20704
D 236,24,42,0
Mavac 52,64,92
T 30,125,25824
T 32,124,20736
Mavac 60,68,92
T 34,124,25856
AS 21,37,376832
Mavac 56,68,88
T 36,125,20768
Las 136,196,28,21,0
Mavac 64,72,88
T 38,125,25888
Mavac 68,72,92
Las 140,196,28,21,1024
Mavac 76,76,92
Mavac 72,76,88
WV 25
Mavac 0,0,96
T 40,124,30720
Mavac 4,0,100
T 42,124,35840
Mavac 12,4,100
T 44,125,30752
Mavac 8,4,96
T 46,125,35872
T 48,124,30784
Mavac 16,8,96
T 50,124,35904
AS 21,37,393216
Mavac 20,8,100
T 52,125,30816
Las 80,196,28,21,0
Mavac 28,12,100
T 54,125,35936
Mavac 24,12,96
T 56,124,30848
Mavac 32,16,96
T 58,124,35968
Mavac 36,16,100
T 60,125,30880
Mavac 44,20,100
T 62,125,36000
Mavac 40,20,96
T 64,124,30912
Mavac 48,24,96
T 66,124,36032
Mavac 52,24,100
T 68,125,30944
T 70,125,36064
Mavac 60,28,100
T 72,124,30976
Las 84,196,28,21,1024
Mavac 56,28,96
T 74,124,36096
Mavac 64,32,96
T 76,125,31008
Mavac 68,32,100
T 78,125,36128
Mavac 76,36,100
Mavac 72,36,96
WV 20
B
Mavac 0,40,104
T 0,126,0
T 2,126,5120
Mavac 4,40,108
AS 42,36,1146880
M0 126976
Mavac 12,44,108
T 4,127,32
D 232,24,42,0
Mavac 8,44,104
T 6,127,5152
T 8,126,64
Mavac 16,48,104
D 233,24,42,1024
Mavac 20,48,108
T 10,126,5184
Mavac 28,52,108
T 12,127,96
T 14,127,5216
Mavac 24,52,104
T 16,126,128
D 234,24,42,2048
Mavac 32,56,104
T 18,126,5248
Mavac 36,56,108
T 20,127,160
T 22,127,5280
Mavac 44,60,108
T 24,126,192
D 235,24,42,3072
Mavac 40,60,104
T 26,126,5312
M0 131072
Mavac 48,64,104
T 28,127,224
D 236,24,42,0
Mavac 52,64,108
T 30,127,5344
T 32,126,256
Mavac 60,68,108
T 34,126,5376
AS 21,37,409600
Mavac 56,68,104
T 36,127,288
Las 88,196,28,21,0
Mavac 64,72,104
T 38,127,5408
Mavac 68,72,108
Las 92,196,28,21,1024
Mavac 76,76,108
Mavac 72,76,104
WV 25
Mavac 0,0,112
T 40,126,10240
Mavac 4,0,116
T 42,126,15360
Mavac 12,4,116
T 44,127,10272
Mavac 8,4,112
T 46,127,15392
T 48,126,10304
Mavac 16,8,112
T 50,126,15424
AS 21,37,425984
Mavac 20,8,116
T 52,127,10336
Las 96,196,28,21,0
Mavac 28,12,116
T 54,127,15456
Mavac 24,12,112
T 56,126,10368
Mavac 32,16,112
T 58,126,15488
Mavac 36,16,116
T 60,127,10400
Mavac 44,20,116
T 62,127,15520
Mavac 40,20,112
T 64,126,10432
Mavac 48,24,112
T 66,126,15552
Mavac 52,24,116
T 68,127,10464
T 70,127,15584
Mavac 60,28,116
T 72,126,10496
Las 100,196,28,21,1024
Mavac 56,28,112
T 74,126,15616
Mavac 64,32,112
T 76,127,10528
Mavac 68,32,116
T 78,127,15648
Mavac 76,36,116
Mavac 72,36,112
WV 20
Mavac 0,40,120
T 0,126,20480
T 2,126,25600
Mavac 4,40,124
AS 42,36,1167360
M0 132096
Mavac 12,44,124
T 4,127,20512
D 232,24,42,0
Mavac 8,44,120
T 6,127,25632
T 8,126,20544
Mavac 16,48,120
D 233,24,42,1024
Mavac 20,48,124
T 10,126,25664
Mavac 28,52,124
T 12,127,20576
T 14,127,25696
Mavac 24,52,120
T 16,126,20608
D 234,24,42,2048
Mavac 32,56,120
T 18,126,25728
Mavac 36,56,124
T 20,127,20640
T 22,127,25760
Mavac 44,60,124
T 24,126,20672
D 235,24,42,3072
Mavac 40,60,120
T 26,126,25792
M0 136192
Mavac 48,64,120
T 28,127,20704
D 236,24,42,0
Mavac 52,64,124
T 30,127,25824
T 32,126,20736
Mavac 60,68,124
T 34,126,25856
AS 21,37,442368
Mavac 56,68,120
T 36,127,20768
Las 104,196,28,21,0
Mavac 64,72,120
T 38,127,25888
Mavac 68,72,124
Las 108,196,28,21,1024
Mavac 76,76,124
Mavac 72,76,120
WV 25
Mavac 0,0,128
T 40,126,30720
Mavac 4,0,132
T 42,126,35840
Mavac 12,4,132
T 44,127,30752
Mavac 8,4,128
T 46,127,35872
T 48,126,30784
Mavac 16,8,128
T 50,126,35904
AS 21,37,458752
Mavac 20,8,132
T 52,127,30816
Las 112,196,28,21,0
Mavac 28,12,132
T 54,127,35936
Mavac 24,12,128
T 56,126,30848
Mavac 32,16,128
T 58,126,35968
Mavac 36,16,132
T 60,127,30880
Mavac 44,20,132
T 62,127,36000
Mavac 40,20,128
T 64,126,30912
Mavac 48,24,128
T 66,126,36032
Mavac 52,24,132
T 68,127,30944
T 70,127,36064
Mavac 60,28,132
T 72,126,30976
Las 116,196,28,21,1024
Mavac 56,28,128
T 74,126,36096
Mavac 64,32,128
T 76,127,31008
Mavac 68,32,132
T 78,127,36128
Mavac 76,36,132
Mavac 72,36,128
WV 20
B
Mavac 0,40,136
T 0,122,0
Mavac 4,40,140
T 2,122,5120
Mavac 12,44,140
T 4,123,32
Mavac 8,44,136
T 6,123,5152
T 8,122,64
Mavac 16,48,136
T 10,122,5184
AS 21,37,475136
Mavac 20,48,140
T 12,123,96
Las 120,196,28,21,0
Mavac 28,52,140
T 14,123,5216
Mavac 24,52,136
T 16,122,128
Mavac 32,56,136
T 18,122,5248
Mavac 36,56,140
T 20,123,160
Mavac 44,60,140
T 22,123,5280
Mavac 40,60,136
T 24,122,192
Mavac 48,64,136
T 26,122,5312
Mavac 52,64,140
T 28,123,224
T 30,123,5344
Mavac 60,68,140
T 32,122,256
Las 124,196,28,21,1024
Mavac 56,68,136
T 34,122,5376
Mavac 64,72,136
T 36,123,288
Mavac 68,72,140
T 38,123,5408
Mavac 76,76,140
Mavac 72,76,136
WV 20
Mavac 0,0,80
T 40,122,10240
Mavac 4,0,84
T 42,122,15360
Mavac 12,4,84
T 44,123,10272
Mavac 8,4,80
T 46,123,15392
T 48,122,10304
Mavac 16,8,80
T 50,122,15424
AS 21,37,491520
Mavac 20,8,84
T 52,123,10336
Las 128,196,28,21,0
Mavac 28,12,84
T 54,123,15456
Mavac 24,12,80
T 56,122,10368
Mavac 32,16,80
T 58,122,15488
Mavac 36,16,84
T 60,123,10400
Mavac 44,20,84
T 62,123,15520
Mavac 40,20,80
T 64,122,10432
Mavac 48,24,80
T 66,122,15552
Mavac 52,24,84
T 68,123,10464
T 70,123,15584
Mavac 60,28,84
T 72,122,10496
Las 132,196,28,21,1024
Mavac 56,28,80
T 74,122,15616
Mavac 64,32,80
T 76,123,10528
Mavac 68,32,84
T 78,123,15648
Mavac 76,36,84
Mavac 72,36,80
WV 15
Mavac 0,40,88
T 0,122,20480
Mavac 4,40,92
T 2,122,25600
Mavac 12,44,92
T 4,123,20512
Mavac 8,44,88
T 6,123,25632
T 8,122,20544
Mavac 16,48,88
T 10,122,25664
AS 21,37,507904
Mavac 20,48,92
T 12,123,20576
Las 136,196,28,21,0
Mavac 28,52,92
T 14,123,25696
Mavac 24,52,88
T 16,122,20608
Mavac 32,56,88
T 18,122,25728
Mavac 36,56,92
T 20,123,20640
Mavac 44,60,92
T 22,123,25760
Mavac 40,60,88
T 24,122,20672
Mavac 48,64,88
T 26,122,25792
Mavac 52,64,92
T 28,123,20704
T 30,123,25824
Mavac 60,68,92
T 32,122,20736
Las 140,196,28,21,1024
Mavac 56,68,88
T 34,122,25856
Mavac 64,72,88
T 36,123,20768
Mavac 68,72,92
T 38,123,25888
Mavac 76,76,92
Mavac 72,76,88
WV 15
Mavac 0,0,96
T 40,122,30720
Mavac 4,0,100
T 42,122,35840
Mavac 12,4,100
T 44,123,30752
Mavac 8,4,96
T 46,123,35872
Mavac 16,8,96
T 48,122,30784
Mavac 20,8,100
T 50,122,35904
Mavac 28,12,100
T 52,123,30816
Mavac 24,12,96
T 54,123,35936
Mavac 32,16,96
T 56,122,30848
Mavac 36,16,100
T 58,122,35968
Mavac 44,20,100
T 60,123,30880
Mavac 40,20,96
T 62,123,36000
Mavac 48,24,96
T 64,122,30912
Mavac 52,24,100
T 66,122,36032
Mavac 60,28,100
T 68,123,30944
Mavac 56,28,96
T 70,123,36064
Mavac 64,32,96
T 72,122,30976
Mavac 68,32,100
T 74,122,36096
Mavac 76,36,100
T 76,123,31008
Mavac 72,36,96
T 78,123,36128
WV 8
B
Mavac 0,40,104
T 0,124,0
Mavac 4,40,108
T 2,124,5120
Mavac 12,44,108
T 4,125,32
Mavac 8,44,104
T 6,125,5152
Mavac 16,48,104
T 8,124,64
Mavac 20,48,108
T 10,124,5184
Mavac 28,52,108
T 12,125,96
Mavac 24,52,104
T 14,125,5216
Mavac 32,56,104
T 16,124,128
Mavac 36,56,108
T 18,124,5248
Mavac 44,60,108
T 20,125,160
Mavac 40,60,104
T 22,125,5280
Mavac 48,64,104
T 24,124,192
Mavac 52,64,108
T 26,124,5312
Mavac 60,68,108
T 28,125,224
Mavac 56,68,104
T 30,125,5344
Mavac 64,72,104
T 32,124,256
Mavac 68,72,108
T 34,124,5376
Mavac 76,76,108
T 36,125,288
Mavac 72,76,104
T 38,125,5408
WV 6
Mavac 0,0,112
T 40,124,10240
Mavac 4,0,116
T 42,124,15360
Mavac 12,4,116
T 44,125,10272
Mavac 8,4,112
T 46,125,15392
Mavac 16,8,112
T 48,124,10304
Mavac 20,8,116
T 50,124,15424
Mavac 28,12,116
T 52,125,10336
Mavac 24,12,112
T 54,125,15456
Mavac 32,16,112
T 56,124,10368
Mavac 36,16,116
T 58,124,15488
Mavac 44,20,116
T 60,125,10400
Mavac 40,20,112
T 62,125,15520
Mavac 48,24,112
T 64,124,10432
Mavac 52,24,116
T 66,124,15552
Mavac 60,28,116
T 68,125,10464
Mavac 56,28,112
T 70,125,15584
Mavac 64,32,112
T 72,124,10496
Mavac 68,32,116
T 74,124,15616
Mavac 76,36,116
T 76,125,10528
Mavac 72,36,112
T 78,125,15648
WV 4
Mavac 0,40,120
T 0,124,20480
Mavac 4,40,124
T 2,124,25600
Mavac 12,44,124
T 4,125,20512
Mavac 8,44,120
T 6,125,25632
Mavac 16,48,120
T 8,124,20544
Mavac 20,48,124
T 10,124,25664
Mavac 28,52,124
T 12,125,20576
Mavac 24,52,120
T 14,125,25696
Mavac 32,56,120
T 16,124,20608
Mavac 36,56,124
T 18,124,25728
Mavac 44,60,124
T 20,125,20640
Mavac 40,60,120
T 22,125,25760
Mavac 48,64,120
T 24,124,20672
Mavac 52,64,124
T 26,124,25792
Mavac 60,68,124
T 28,125,20704
Mavac 56,68,120
T 30,125,25824
Mavac 64,72,120
T 32,124,20736
Mavac 68,72,124
T 34,124,25856
Mavac 76,76,124
T 36,125,20768
Mavac 72,76,120
T 38,125,25888
WV 2
Mavac 0,0,128
T 40,124,30720
Mavac 4,0,132
T 42,124,35840
Mavac 12,4,132
T 44,125,30752
Mavac 8,4,128
T 46,125,35872
Mavac 16,8,128
T 48,124,30784
Mavac 20,8,132
T 50,124,35904
Mavac 28,12,132
T 52,125,30816
Mavac 24,12,128
T 54,125,35936
Mavac 32,16,128
T 56,124,30848
Mavac 36,16,132
T 58,124,35968
Mavac 44,20,132
T 60,125,30880
Mavac 40,20,128
T 62,125,36000
Mavac 48,24,128
T 64,124,30912
Mavac 52,24,132
T 66,124,36032
Mavac 60,28,132
T 68,125,30944
Mavac 56,28,128
T 70,125,36064
Mavac 64,32,128
T 72,124,30976
Mavac 68,32,132
T 74,124,36096
Mavac 76,36,132
T 76,125,31008
Mavac 72,36,128
T 78,125,36128
WV 0
B
Mavac 0,40,136
Mavac 4,40,140
Mavac 12,44,140
Mavac 8,44,136
Mavac 16,48,136
Mavac 20,48,140
Mavac 28,52,140
Mavac 24,52,136
Mavac 32,56,136
Mavac 36,56,140
Mavac 44,60,140
Mavac 40,60,136
Mavac 48,64,136
Mavac 52,64,140
Mavac 60,68,140
Mavac 56,68,136
Mavac 64,72,136
Mavac 68,72,140
Mavac 76,76,140
Mavac 72,76,136
OMD2%=:
G 0,0
G 4,8
G 1,1
G 5,9
G 2,2
G 6,10
G 3,3
G 7,11
C 8,0,1
C 9,2,3
C 10,4,5
C 11,6,7
Svs 8,196,32,38,0
G 16,16
G 20,24
G 17,17
G 21,25
G 18,18
G 22,26
G 19,19
G 23,27
C 24,16,17
C 25,18,19
C 26,20,21
C 27,22,23
Svs 24,196,32,38,1024
G 32,32
G 36,40
G 33,33
)A"
R"A(G 37,41
G 34,34
G 38,42
G 35,35
G 39,43
C 40,32,33
C 41,34,35
C 42,36,37
C 43,38,39
Svs 40,196,32,38,2048
G 48,48
G 52,56
G 49,49
G 53,57
G 50,50
G 54,58
G 51,51
G 55,59
C 56,48,49
C 57,50,51
C 58,52,53
C 59,54,55
Svs 56,196,32,38,3072
G 0,64
G 4,72
G 1,65
G 5,73
G 2,66
G 6,74
G 3,67
G 7,75
C 8,0,1
C 9,2,3
C 10,4,5
C 11,6,7
AS 16,38,4096
Svs 8,196,32,16,0
AS 38,38,20480
G 16,4
G 20,12
G 17,5
G 21,13
G 18,6
G 22,14
G 19,7
G 23,15
C 24,16,17
C 25,18,19
C 26,20,21
C 27,22,23
Svs 24,196,32,38,0
G 32,20
G 36,28
G 33,21
G 37,29
G 34,22
G 38,30
G 35,23
G 39,31
C 40,32,33
C 41,34,35
C 42,36,37
C 43,38,39
Svs 40,196,32,38,1024
G 48,36
G 52,44
G 49,37
G 53,45
G 50,38
G 54,46
G 51,39
G 55,47
C 56,48,49
C 57,50,51
C 58,52,53
C 59,54,55
Svs 56,196,32,38,2048
G 0,52
G 4,60
G 1,53
G 5,61
G 2,54
G 6,62
G 3,55
G 7,63
C 8,0,1
C 9,2,3
C 10,4,5
C 11,6,7
Svs 8,196,32,38,3072
G 16,68
G 20,76
G 17,69
G 21,77
G 18,70
G 22,78
G 19,71
G 23,79
C 24,16,17
C 25,18,19
C 26,20,21
C 27,22,23
AS 16,38,4096
Svs 24,196,32,16,0
buffer_load_dwordx2 v[40:41], v100, s[44:47], s65 offen offset:0
buffer_load_dwordx2 v[42:43], v100, s[44:47], s65 offen offset:64
buffer_load_dwordx2 v[44:45], v100, s[44:47], s65 offen offset:128
buffer_load_dwordx2 v[46:47], v100, s[44:47], s65 offen offset:192
buffer_load_dwordx2 v[48:49], v100, s[44:47], s65 offen offset:256
buffer_load_dwordx2 v[50:51], v101, s[48:51], s67 offen offset:0
buffer_load_dwordx2 v[52:53], v101, s[48:51], s67 offen offset:64
buffer_load_dwordx2 v[54:55], v101, s[48:51], s67 offen offset:128
buffer_load_dwordx2 v[56:57], v101, s[48:51], s67 offen offset:192
buffer_load_dwordx2 v[58:59], v101, s[48:51], s67 offen offset:256
s_mov_b64 s[24:25], s[70:71]
SM 26,0x21c000
SM 27,0x20000
s_mov_b64 s[28:29], s[76:77]
SM 30,0x21c000
SM 31,0x20000
s_waitcnt vmcnt(0)
B
s_and_b32 s16, s10, 1
s_cselect_b32 s16, 54, s78
s_cmp_lt_u32 s16, 54
s_cbranch_scc0 NOST2%=
VA 98,20480,196
DWa 98,144,0
DWa 98,148,40960
DWa 98,152,1024
DWa 98,156,41984
DWa 98,160,2048
DWa 98,164,43008
DWa 98,168,3072
DWa 98,172,44032
DWa 98,176,4096
DWa 98,180,45056
DWa 98,184,5120
DWa 98,188,46080
DWa 98,192,6144
DWa 98,196,47104
DWa 98,200,7168
DWa 98,204,48128
DWa 98,208,8192
DWa 98,212,49152
DWa 98,216,9216
DWa 98,220,50176
NOST2%=:
B
s_and_b32 s16, s10, 1
s_cselect_b32 s16, 54, s78
s_cmp_lt_u32 s16, 54
s_cbranch_scc1 PF2%=
Lvs 0,102,32,64,0
Lvs 8,102,32,64,1024
Lvs 16,102,32,64,2048
Lvs 24,102,32,64,3072
AS 21,64,4096
Lvs 32,102,32,21,0
AS 64,64,2621440
Lvs 4,102,32,64,0
Lvs 12,102,32,64,1024
Lvs 20,102,32,64,2048
Lvs 28,102,32,64,3072
AS 21,64,4096
Lvs 36,102,32,21,0
v_and_b32 v108, 7, v254
v_lshlrev_b32 v108, 4, v108
VA 108,152064,108
s_lshl_b32 s16, s11, 7
v_add_u32 v103, s16, v108
LO 110,50
HI 111,50
LO 112,51
HI 113,51
LO 114,52
HI 115,52
LO 116,53
HI 117,53
LO 118,54
HI 119,54
LO 120,55
HI 121,55
LO 122,56
HI 123,56
LO 124,57
HI 125,57
LO 126,58
HI 127,58
LO 128,59
HI 129,59
PA 104,110,112
PA 104,104,114
PA 104,104,116
PA 104,104,118
PA 104,104,120
PA 104,104,122
PA 104,104,124
PA 104,104,126
PA 104,104,128
Fad 99,104,105
s_waitcnt vmcnt(9)
LO 88,0
HI 89,0
s_waitcnt vmcnt(4)
LO 104,4
HI 105,4
PA 88,88,104
LO 90,1
HI 91,1
LO 104,5
HI 105,5
PA 90,90,104
LO 92,2
HI 93,2
LO 104,6
HI 105,6
PA 92,92,104
LO 94,3
HI 95,3
LO 104,7
HI 105,7
PA 94,94,104
v_permlane32_swap_b32 v88, v92
v_permlane32_swap_b32 v89, v93
v_permlane32_swap_b32 v90, v94
v_permlane32_swap_b32 v91, v95
PA 64,88,92
PA 66,90,94
LO 104,40
HI 105,40
LO 106,41
HI 107,41
PA 64,64,104
PA 66,66,106
LO 88,8
HI 89,8
s_waitcnt vmcnt(3)
LO 104,12
HI 105,12
PA 88,88,104
LO 90,9
HI 91,9
LO 104,13
HI 105,13
PA 90,90,104
LO 92,10
HI 93,10
LO 104,14
HI 105,14
PA 92,92,104
LO 94,11
HI 95,11
LO 104,15
HI 105,15
PA 94,94,104
v_permlane32_swap_b32 v88, v92
v_permlane32_swap_b32 v89, v93
v_permlane32_swap_b32 v90, v94
v_permlane32_swap_b32 v91, v95
PA 68,88,92
PA 70,90,94
LO 104,42
HI 105,42
LO 106,43
HI 107,43
PA 68,68,104
PA 70,70,106
LO 88,16
HI 89,16
s_waitcnt vmcnt(2)
LO 104,20
HI 105,20
PA 88,88,104
LO 90,17
HI 91,17
LO 104,21
HI 105,21
PA 90,90,104
LO 92,18
HI 93,18
LO 104,22
HI 105,22
PA 92,92,104
LO 94,19
HI 95,19
LO 104,23
HI 105,23
PA 94,94,104
v_permlane32_swap_b32 v88, v92
v_permlane32_swap_b32 v89, v93
v_permlane32_swap_b32 v90, v94
v_permlane32_swap_b32 v91, v95
PA 72,88,92
PA 74,90,94
LO 104,44
HI 105,44
LO 106,45
HI 107,45
PA 72,72,104
PA 74,74,106
LO 88,24
HI 89,24
s_waitcnt vmcnt(1)
LO 104,28
HI 105,28
PA 88,88,104
LO 90,25
HI 91,25
LO 104,29
HI 105,29
PA 90,90,104
LO 92,26
HI 93,26
LO 104,30
HI 105,30
PA 92,92,104
LO 94,27
HI 95,27
LO 104,31
HI 105,31
PA 94,94,104
v_permlane32_swap_b32 v88, v92
v_permlane32_swap_b32 v89, v93
v_permlane32_swap_b32 v90, v94
v_permlane32_swap_b32 v91, v95
PA 76,88,92
PA 78,90,94
LO 104,46
HI 105,46
LO 106,47
HI 107,47
PA 76,76,104
PA 78,78,106
LO 88,32
HI 89,32
s_waitcnt vmcnt(0)
LO 104,36
HI 105,36
PA 88,88,104
LO 90,33
HI 91,33
LO 104,37
HI 105,37
PA 90,90,104
LO 92,34
HI 93,34
LO 104,38
HI 105,38
PA 92,92,104
LO 94,35
HI 95,35
LO 104,39
HI 105,39
PA 94,94,104
v_permlane32_swap_b32 v88, v92
v_permlane32_swap_b32 v89, v93
v_permlane32_swap_b32 v90, v94
v_permlane32_swap_b32 v91, v95
PA 80,88,92
PA 82,90,94
LO 104,48
HI 105,48
LO 106,49
HI 107,49
PA 80,80,104
PA 82,82,106
PA 104,64,66
v_pk_mul_f32 v[106:107], v[64:65], v[64:65]
v_pk_mul_f32 v[88:89], v[64:65], v[110:111]
v_pk_fma_f32 v[106:107], v[66:67], v[66:67], v[106:107]
v_pk_fma_f32 v[88:89], v[66:67], v[112:113], v[88:89]
PA 104,104,68
v_pk_fma_f32 v[106:107], v[68:69], v[68:69], v[106:107]
v_pk_fma_f32 v[88:89], v[68:69], v[114:115], v[88:89]
PA 104,104,70
v_pk_fma_f32 v[106:107], v[70:71], v[70:71], v[106:107]
v_pk_fma_f32 v[88:89], v[70:71], v[116:117], v[88:89]
PA 104,104,72
v_pk_fma_f32 v[106:107], v[72:73], v[72:73], v[106:107]
v_pk_fma_f32 v[88:89], v[72:73], v[118:119], v[88:89]
PA 104,104,74
v_pk_fma_f32 v[106:107], v[74:75], v[74:75], v[106:107]
v_pk_fma_f32 v[88:89], v[74:75], v[120:121], v[88:89]
PA 104,104,76
v_pk_fma_f32 v[106:107], v[76:77], v[76:77], v[106:107]
v_pk_fma_f32 v[88:89], v[76:77], v[122:123], v[88:89]
PA 104,104,78
v_pk_fma_f32 v[106:107], v[78:79], v[78:79], v[106:107]
v_pk_fma_f32 v[88:89], v[78:79], v[124:125], v[88:89]
PA 104,104,80
v_pk_fma_f32 v[106:107], v[80:81], v[80:81], v[106:107]
v_pk_fma_f32 v[88:89], v[80:81], v[126:127], v[88:89]
PA 104,104,82
v_pk_fma_f32 v[106:107], v[82:83], v[82:83], v[106:107]
v_pk_fma_f32 v[88:89], v[82:83], v[128:129], v[88:89]
Fad 96,104,105
Fad 97,106,107
Fad 98,88,89
v_add_f32_dpp v96, v96, v96 row_ror:8 row_mask:0xf bank_mask:0xf
v_add_f32_dpp v97, v97, v97 row_ror:8 row_mask:0xf bank_mask:0xf
v_add_f32_dpp v98, v98, v98 row_ror:8 row_mask:0xf bank_mask:0xf
v_add_f32_dpp v99, v99, v99 row_ror:8 row_mask:0xf bank_mask:0xf
MV 104,96
MV 105,97
MV 106,98
MV 107,99
v_permlane16_swap_b32 v96, v104
v_permlane16_swap_b32 v97, v105
v_permlane16_swap_b32 v98, v106
v_permlane16_swap_b32 v99, v107
Fad 96,96,104
Fad 97,97,105
Fad 98,98,106
Fad 99,99,107
MV 104,96
MV 105,97
MV 106,98
MV 107,99
v_permlane32_swap_b32 v96, v104
v_permlane32_swap_b32 v97, v105
v_permlane32_swap_b32 v98, v106
v_permlane32_swap_b32 v99, v107
Fad 96,96,104
Fad 97,97,105
Fad 98,98,106
Fad 99,99,107
ds_write_b128 v103, v[96:99]
W 0
B
Rv 0,108,0
Rv 4,108,128
Rv 8,108,256
Rv 12,108,384
W 2
PA 0,0,4
PA 2,2,6
W 0
PA 8,8,12
PA 10,10,14
PA 0,0,8
PA 2,2,10
v_mul_f32 v88, 0x3acccccd, v0
v_mul_f32 v90, 0x3acccccd, v1
v_fma_f32 v90, -v88, v88, v90
v_max_f32 v90, 0, v90
v_add_f32 v91, 0x3727c5ac, v90
v_rsq_f32 v91, v91
v_fma_f32 v92, -v88, v3, v2
Fmu 92,92,91
v_add_f32 v104, 1.0, v91
Fmu 105,88,91
v_xor_b32 v105, 0x80000000, v105
FM 64,64,104,105
FM 65,65,104,105
FM 66,66,104,105
FM 67,67,104,105
C 24,64,65
C 25,66,67
FM 68,68,104,105
FM 69,69,104,105
FM 70,70,104,105
FM 71,71,104,105
C 26,68,69
C 27,70,71
FM 72,72,104,105
FM 73,73,104,105
FM 74,74,104,105
FM 75,75,104,105
C 28,72,73
C 29,74,75
FM 76,76,104,105
FM 77,77,104,105
FM 78,78,104,105
FM 79,79,104,105
C 30,76,77
C 31,78,79
FM 80,80,104,105
FM 81,81,104,105
FM 82,82,104,105
FM 83,83,104,105
C 32,80,81
C 33,82,83
B
v_mov_b32 v162, 151552
ds_read_b32 v160, v162
W 0
v_readfirstlane_b32 s16, v160
N 3
s_and_b32 s68, s16, 0xffff
s_lshr_b32 s69, s16, 16
v_and_b32 v160, 7, v254
v_add_u32 v160, s66, v160
v_cmp_ne_u32 vcc, s68, v160
v_cmp_ne_u32 s[16:17], s69, v160
N 4
s_and_b64 exec, vcc, s[16:17]
buffer_store_dwordx2 v[24:25], v100, s[52:55], s65 offen offset:0 sc1
buffer_store_dwordx2 v[26:27], v100, s[52:55], s65 offen offset:64 sc1
buffer_store_dwordx2 v[28:29], v100, s[52:55], s65 offen offset:128 sc1
buffer_store_dwordx2 v[30:31], v100, s[52:55], s65 offen offset:192 sc1
buffer_store_dwordx2 v[32:33], v100, s[52:55], s65 offen offset:256 sc1
s_mov_b64 exec, -1
s_branch PE2%=
PF2%=:
Lvs 0,102,32,64,0
Lvs 8,102,32,64,1024
Lvs 16,102,32,64,2048
Lvs 24,102,32,64,3072
AS 21,64,4096
Lvs 32,102,32,21,0
AS 64,64,2621440
Lvs 4,102,32,64,0
Lvs 12,102,32,64,1024
Lvs 20,102,32,64,2048
Lvs 28,102,32,64,3072
AS 21,64,4096
Lvs 36,102,32,21,0
v_and_b32 v108, 7, v254
v_lshlrev_b32 v108, 4, v108
VA 108,152064,108
s_lshl_b32 s16, s11, 7
v_add_u32 v103, s16, v108
LO 110,50
HI 111,50
LO 112,51
HI 113,51
LO 114,52
HI 115,52
LO 116,53
HI 117,53
LO 118,54
HI 119,54
LO 120,55
HI 121,55
LO 122,56
HI 123,56
LO 124,57
HI 125,57
LO 126,58
HI 127,58
LO 128,59
HI 129,59
PA 104,110,112
PA 104,104,114
PA 104,104,116
PA 104,104,118
PA 104,104,120
PA 104,104,122
PA 104,104,124
PA 104,104,126
PA 104,104,128
Fad 99,104,105
s_waitcnt vmcnt(9)
LO 88,0
HI 89,0
s_waitcnt vmcnt(4)
LO 104,4
HI 105,4
PA 88,88,104
LO 90,1
HI 91,1
LO 104,5
HI 105,5
PA 90,90,104
LO 92,2
HI 93,2
LO 104,6
HI 105,6
PA 92,92,104
LO 94,3
HI 95,3
LO 104,7
HI 105,7
PA 94,94,104
v_permlane32_swap_b32 v88, v92
v_permlane32_swap_b32 v89, v93
v_permlane32_swap_b32 v90, v94
v_permlane32_swap_b32 v91, v95
PA 64,88,92
PA 66,90,94
LO 104,40
HI 105,40
LO 106,41
HI 107,41
PA 64,64,104
PA 66,66,106
LO 88,8
HI 89,8
s_waitcnt vmcnt(3)
LO 104,12
HI 105,12
PA 88,88,104
LO 90,9
HI 91,9
LO 104,13
HI 105,13
PA 90,90,104
LO 92,10
HI 93,10
LO 104,14
HI 105,14
PA 92,92,104
LO 94,11
HI 95,11
LO 104,15
HI 105,15
PA 94,94,104
v_permlane32_swap_b32 v88, v92
v_permlane32_swap_b32 v89, v93
v_permlane32_swap_b32 v90, v94
v_permlane32_swap_b32 v91, v95
PA 68,88,92
PA 70,90,94
LO 104,42
HI 105,42
LO 106,43
HI 107,43
PA 68,68,104
PA 70,70,106
LO 88,16
HI 89,16
s_waitcnt vmcnt(2)
LO 104,20
HI 105,20
PA 88,88,104
LO 90,17
HI 91,17
LO 104,21
HI 105,21
PA 90,90,104
LO 92,18
HI 93,18
LO 104,22
HI 105,22
PA 92,92,104
LO 94,19
HI 95,19
LO 104,23
HI 105,23
PA 94,94,104
v_permlane32_swap_b32 v88, v92
v_permlane32_swap_b32 v89, v93
v_permlane32_swap_b32 v90, v94
v_permlane32_swap_b32 v91, v95
PA 72,88,92
PA 74,90,94
LO 104,44
HI 105,44
LO 106,45
HI 107,45
PA 72,72,104
PA 74,74,106
LO 88,24
HI 89,24
s_waitcnt vmcnt(1)
LO 104,28
HI 105,28
PA 88,88,104
LO 90,25
HI 91,25
LO 104,29
HI 105,29
PA 90,90,104
LO 92,26
HI 93,26
LO 104,30
HI 105,30
PA 92,92,104
LO 94,27
HI 95,27
LO 104,31
HI 105,31
PA 94,94,104
v_permlane32_swap_b32 v88, v92
v_permlane32_swap_b32 v89, v93
v_permlane32_swap_b32 v90, v94
v_permlane32_swap_b32 v91, v95
PA 76,88,92
PA 78,90,94
LO 104,46
HI 105,46
LO 106,47
HI 107,47
PA 76,76,104
PA 78,78,106
LO 88,32
HI 89,32
s_waitcnt vmcnt(0)
LO 104,36
HI 105,36
PA 88,88,104
LO 90,33
HI 91,33
LO 104,37
HI 105,37
PA 90,90,104
LO 92,34
HI 93,34
LO 104,38
HI 105,38
PA 92,92,104
LO 94,35
HI 95,35
LO 104,39
HI 105,39
PA 94,94,104
v_permlane32_swap_b32 v88, v92
v_permlane32_swap_b32 v89, v93
v_permlane32_swap_b32 v90, v94
v_permlane32_swap_b32 v91, v95
PA 80,88,92
PA 82,90,94
LO 104,48
HI 105,48
LO 106,49
HI 107,49
PA 80,80,104
PA 82,82,106
PA 104,64,66
v_pk_mul_f32 v[106:107], v[64:65], v[64:65]
v_pk_mul_f32 v[88:89], v[64:65], v[110:111]
v_pk_fma_f32 v[106:107], v[66:67], v[66:67], v[106:107]
v_pk_fma_f32 v[88:89], v[66:67], v[112:113], v[88:89]
PA 104,104,68
v_pk_fma_f32 v[106:107], v[68:69], v[68:69], v[106:107]
v_pk_fma_f32 v[88:89], v[68:69], v[114:115], v[88:89]
PA 104,104,70
)A"
R"A(v_pk_fma_f32 v[106:107], v[70:71], v[70:71], v[106:107]
v_pk_fma_f32 v[88:89], v[70:71], v[116:117], v[88:89]
PA 104,104,72
v_pk_fma_f32 v[106:107], v[72:73], v[72:73], v[106:107]
v_pk_fma_f32 v[88:89], v[72:73], v[118:119], v[88:89]
PA 104,104,74
v_pk_fma_f32 v[106:107], v[74:75], v[74:75], v[106:107]
v_pk_fma_f32 v[88:89], v[74:75], v[120:121], v[88:89]
PA 104,104,76
v_pk_fma_f32 v[106:107], v[76:77], v[76:77], v[106:107]
v_pk_fma_f32 v[88:89], v[76:77], v[122:123], v[88:89]
PA 104,104,78
v_pk_fma_f32 v[106:107], v[78:79], v[78:79], v[106:107]
v_pk_fma_f32 v[88:89], v[78:79], v[124:125], v[88:89]
PA 104,104,80
v_pk_fma_f32 v[106:107], v[80:81], v[80:81], v[106:107]
v_pk_fma_f32 v[88:89], v[80:81], v[126:127], v[88:89]
PA 104,104,82
v_pk_fma_f32 v[106:107], v[82:83], v[82:83], v[106:107]
v_pk_fma_f32 v[88:89], v[82:83], v[128:129], v[88:89]
Fad 96,104,105
Fad 97,106,107
Fad 98,88,89
v_add_f32_dpp v96, v96, v96 row_ror:8 row_mask:0xf bank_mask:0xf
v_add_f32_dpp v97, v97, v97 row_ror:8 row_mask:0xf bank_mask:0xf
v_add_f32_dpp v98, v98, v98 row_ror:8 row_mask:0xf bank_mask:0xf
v_add_f32_dpp v99, v99, v99 row_ror:8 row_mask:0xf bank_mask:0xf
MV 104,96
MV 105,97
MV 106,98
MV 107,99
v_permlane16_swap_b32 v96, v104
v_permlane16_swap_b32 v97, v105
v_permlane16_swap_b32 v98, v106
v_permlane16_swap_b32 v99, v107
Fad 96,96,104
Fad 97,97,105
Fad 98,98,106
Fad 99,99,107
MV 104,96
MV 105,97
MV 106,98
MV 107,99
v_permlane32_swap_b32 v96, v104
v_permlane32_swap_b32 v97, v105
v_permlane32_swap_b32 v98, v106
v_permlane32_swap_b32 v99, v107
Fad 96,96,104
Fad 97,97,105
Fad 98,98,106
Fad 99,99,107
ds_write_b128 v103, v[96:99]
W 0
B
Rv 0,108,0
Rv 4,108,128
Rv 8,108,256
Rv 12,108,384
W 2
PA 0,0,4
PA 2,2,6
W 0
PA 8,8,12
PA 10,10,14
PA 0,0,8
PA 2,2,10
v_mul_f32 v88, 0x3acccccd, v0
v_mul_f32 v90, 0x3acccccd, v1
v_fma_f32 v90, -v88, v88, v90
v_max_f32 v90, 0, v90
v_add_f32 v91, 0x3727c5ac, v90
v_rsq_f32 v91, v91
v_fma_f32 v92, -v88, v3, v2
Fmu 92,92,91
v_add_f32 v104, 1.0, v91
Fmu 105,88,91
v_xor_b32 v105, 0x80000000, v105
FM 64,64,104,105
FM 65,65,104,105
FM 66,66,104,105
FM 67,67,104,105
C 24,64,65
C 25,66,67
FM 68,68,104,105
FM 69,69,104,105
FM 70,70,104,105
FM 71,71,104,105
C 26,68,69
C 27,70,71
FM 72,72,104,105
FM 73,73,104,105
FM 74,74,104,105
FM 75,75,104,105
C 28,72,73
C 29,74,75
FM 76,76,104,105
FM 77,77,104,105
FM 78,78,104,105
FM 79,79,104,105
C 30,76,77
C 31,78,79
FM 80,80,104,105
FM 81,81,104,105
FM 82,82,104,105
FM 83,83,104,105
C 32,80,81
C 33,82,83
GW 224,v24
GW 225,v25
GW 226,v26
GW 227,v27
GW 228,v28
GW 229,v29
GW 230,v30
GW 231,v31
GW 232,v32
GW 233,v33
GW 234,v100
s_add_u32 s36, s14, 0x10000
s_addc_u32 s37, s15, 0
SM 38,0x8000
SM 39,0x20000
s_lshl_b32 s17, s11, 10
SM 20,0
SW_F2%=:
Lvs 128,196,36,17,0
AS 16,17,4096
Lvs 132,196,36,16,0
AS 16,17,8192
Lvs 136,196,36,16,0
AS 16,17,12288
Lvs 140,196,36,16,0
AS 16,17,16384
Lvs 144,196,36,16,0
AS 16,17,20480
Lvs 148,196,36,16,0
AS 16,17,24576
Lvs 152,196,36,16,0
AS 16,17,28672
Lvs 156,196,36,16,0
s_waitcnt vmcnt(0)
v_xor_b32 v160, s79, v131
v_xor_b32 v161, s79, v135
v_or_b32 v160, v160, v161
v_xor_b32 v161, s79, v139
v_or_b32 v160, v160, v161
v_xor_b32 v161, s79, v143
v_or_b32 v160, v160, v161
v_xor_b32 v161, s79, v147
v_or_b32 v160, v160, v161
v_xor_b32 v161, s79, v151
v_or_b32 v160, v160, v161
v_xor_b32 v161, s79, v155
v_or_b32 v160, v160, v161
v_xor_b32 v161, s79, v159
v_or_b32 v160, v160, v161
v_cmp_ne_u32 vcc, 0, v160
N 4
s_cbranch_vccz SWOK_F2%=
AS 20,20,1
s_cmp_lt_u32 s20, 0x10000
s_cbranch_scc0 SWOK_F2%=
s_sleep 1
s_branch SW_F2%=
SWOK_F2%=:
s_lshl_b32 s16, s11, 6
v_add_u32 v161, s16, v254
v_sub_u32 v161, 2047, v161
v_ashrrev_i32 v160, 31, v128
v_or_b32 v160, 0x80000000, v160
v_xor_b32 v160, v160, v128
v_cmp_o_f32 vcc, v128, v128
v_and_b32 v160, 0xfffff800, v160
N 0
v_cndmask_b32 v160, 0, v160, vcc
v_subrev_u32 v162, 0, v161
v_or_b32 v128, v160, v162
v_ashrrev_i32 v160, 31, v132
v_or_b32 v160, 0x80000000, v160
v_xor_b32 v160, v160, v132
v_cmp_o_f32 vcc, v132, v132
v_and_b32 v160, 0xfffff800, v160
N 0
v_cndmask_b32 v160, 0, v160, vcc
v_subrev_u32 v162, 256, v161
v_or_b32 v132, v160, v162
v_ashrrev_i32 v160, 31, v136
v_or_b32 v160, 0x80000000, v160
v_xor_b32 v160, v160, v136
v_cmp_o_f32 vcc, v136, v136
v_and_b32 v160, 0xfffff800, v160
N 0
v_cndmask_b32 v160, 0, v160, vcc
v_subrev_u32 v162, 512, v161
v_or_b32 v136, v160, v162
v_ashrrev_i32 v160, 31, v140
v_or_b32 v160, 0x80000000, v160
v_xor_b32 v160, v160, v140
v_cmp_o_f32 vcc, v140, v140
v_and_b32 v160, 0xfffff800, v160
N 0
v_cndmask_b32 v160, 0, v160, vcc
v_subrev_u32 v162, 768, v161
v_or_b32 v140, v160, v162
v_ashrrev_i32 v160, 31, v144
v_or_b32 v160, 0x80000000, v160
v_xor_b32 v160, v160, v144
v_cmp_o_f32 vcc, v144, v144
v_and_b32 v160, 0xfffff800, v160
N 0
v_cndmask_b32 v160, 0, v160, vcc
v_subrev_u32 v162, 1024, v161
v_or_b32 v144, v160, v162
v_ashrrev_i32 v160, 31, v148
v_or_b32 v160, 0x80000000, v160
v_xor_b32 v160, v160, v148
v_cmp_o_f32 vcc, v148, v148
v_and_b32 v160, 0xfffff800, v160
N 0
v_cndmask_b32 v160, 0, v160, vcc
v_subrev_u32 v162, 1280, v161
v_or_b32 v148, v160, v162
v_ashrrev_i32 v160, 31, v152
v_or_b32 v160, 0x80000000, v160
v_xor_b32 v160, v160, v152
v_cmp_o_f32 vcc, v152, v152
v_and_b32 v160, 0xfffff800, v160
N 0
v_cndmask_b32 v160, 0, v160, vcc
v_subrev_u32 v162, 1536, v161
v_or_b32 v152, v160, v162
v_ashrrev_i32 v160, 31, v156
v_or_b32 v160, 0x80000000, v160
v_xor_b32 v160, v160, v156
v_cmp_o_f32 vcc, v156, v156
v_and_b32 v160, 0xfffff800, v160
N 0
v_cndmask_b32 v160, 0, v160, vcc
v_subrev_u32 v162, 1792, v161
v_or_b32 v156, v160, v162
MV 164,128
v_mov_b32 v165, 0
v_mov_b32 v166, 0
v_min_u32 v160, v164, v132
v_max_u32 v164, v164, v132
v_max_u32 v165, v165, v166
v_max_u32 v165, v165, v160
v_min_u32 v160, v164, v136
v_max_u32 v164, v164, v136
v_max_u32 v165, v165, v166
v_max_u32 v165, v165, v160
v_min_u32 v160, v164, v140
v_max_u32 v164, v164, v140
v_max_u32 v165, v165, v166
v_max_u32 v165, v165, v160
v_min_u32 v160, v164, v144
v_max_u32 v164, v164, v144
v_max_u32 v165, v165, v166
v_max_u32 v165, v165, v160
v_min_u32 v160, v164, v148
v_max_u32 v164, v164, v148
v_max_u32 v165, v165, v166
v_max_u32 v165, v165, v160
v_min_u32 v160, v164, v152
v_max_u32 v164, v164, v152
v_max_u32 v165, v165, v166
v_max_u32 v165, v165, v160
v_min_u32 v160, v164, v156
v_max_u32 v164, v164, v156
v_max_u32 v165, v165, v166
v_max_u32 v165, v165, v160
v_mov_b32_dpp v167, v164 quad_perm:[1,0,3,2] row_mask:0xf bank_mask:0xf
N 0
v_mov_b32_dpp v168, v165 quad_perm:[1,0,3,2] row_mask:0xf bank_mask:0xf
v_min_u32 v160, v164, v167
v_max_u32 v164, v164, v167
v_max_u32 v165, v165, v168
v_max_u32 v165, v165, v160
v_mov_b32_dpp v167, v164 quad_perm:[2,3,0,1] row_mask:0xf bank_mask:0xf
N 0
v_mov_b32_dpp v168, v165 quad_perm:[2,3,0,1] row_mask:0xf bank_mask:0xf
v_min_u32 v160, v164, v167
v_max_u32 v164, v164, v167
v_max_u32 v165, v165, v168
v_max_u32 v165, v165, v160
v_mov_b32_dpp v167, v164 row_half_mirror row_mask:0xf bank_mask:0xf
N 0
v_mov_b32_dpp v168, v165 row_half_mirror row_mask:0xf bank_mask:0xf
v_min_u32 v160, v164, v167
v_max_u32 v164, v164, v167
v_max_u32 v165, v165, v168
v_max_u32 v165, v165, v160
v_mov_b32_dpp v167, v164 row_mirror row_mask:0xf bank_mask:0xf
N 0
v_mov_b32_dpp v168, v165 row_mirror row_mask:0xf bank_mask:0xf
v_min_u32 v160, v164, v167
v_max_u32 v164, v164, v167
v_max_u32 v165, v165, v168
v_max_u32 v165, v165, v160
MV 167,164
MV 168,165
N 0
v_permlane16_swap_b32 v164, v167
v_permlane16_swap_b32 v165, v168
v_min_u32 v160, v164, v167
v_max_u32 v164, v164, v167
v_max_u32 v165, v165, v168
v_max_u32 v165, v165, v160
MV 167,164
MV 168,165
N 0
v_permlane32_swap_b32 v164, v167
v_permlane32_swap_b32 v165, v168
v_min_u32 v160, v164, v167
v_max_u32 v164, v164, v167
v_max_u32 v165, v165, v168
v_max_u32 v165, v165, v160
v_mov_b32 v169, 151568
ds_write_b64 v169, v[164:165]
W 0
B
v_mov_b32 v169, 151552
ds_read_b128 v[172:175], v169
Rv 176,169,16
W 1
v_min_u32 v160, v172, v174
v_max_u32 v172, v172, v174
v_max_u32 v173, v173, v175
v_max_u32 v173, v173, v160
W 0
v_min_u32 v160, v172, v176
v_max_u32 v172, v172, v176
v_max_u32 v173, v173, v177
v_max_u32 v173, v173, v160
v_min_u32 v160, v172, v178
v_max_u32 v172, v172, v178
v_max_u32 v173, v173, v179
v_max_u32 v173, v173, v160
v_and_b32 v172, 2047, v172
v_and_b32 v173, 2047, v173
v_sub_u32 v172, 2047, v172
v_sub_u32 v173, 2047, v173
v_readfirstlane_b32 s68, v172
v_readfirstlane_b32 s69, v173
v_and_b32 v208, 1, v254
v_lshrrev_b32 v209, 1, v254
v_and_b32 v209, 3, v209
v_lshrrev_b32 v226, 3, v254
VA 226,10,226
v_mov_b32 v210, s68
v_mov_b32 v211, s69
v_cmp_eq_u32 vcc, 1, v208
N 1
v_cndmask_b32 v210, v210, v211, vcc
v_lshrrev_b32 v212, 4, v210
v_mul_u32_u24 v212, 20480, v212
v_lshl_add_u32 v212, v226, 10, v212
v_and_b32 v213, 15, v210
v_lshl_add_u32 v213, v209, 4, v213
v_lshl_add_u32 v212, v213, 4, v212
v_mul_u32_u24 v213, 1280, v210
v_lshl_add_u32 v213, v209, 3, v213
v_lshl_add_u32 v213, v226, 6, v213
v_lshlrev_b32 v214, 4, v210
s_mov_b32 exec_lo, -1
s_mov_b32 exec_hi, 0xff
buffer_load_dwordx4 v[216:219], v214, s[36:39], 0 offen offset:0 sc1
SM 18,0
Lvs 64,212,32,18,0
SM 18,2621440
Lvs 68,212,32,18,0
SM 18,5242880
Lvs 72,212,32,18,0
SM 18,7864320
Lvs 76,212,32,18,0
buffer_load_dwordx2 v[0:1], v213, s[44:47], 0 offen offset:0
buffer_load_dwordx2 v[2:3], v213, s[44:47], 0 offen offset:32
s_mov_b64 exec, -1
s_mul_i32 s17, s78, 40960
AS 17,17,20480
M0 102400
N 0
D 196,72,17,0
D 196,72,17,1024
D 196,72,17,2048
D 196,72,17,3072
M0 106496
AS 17,17,4096
D 196,72,17,0
D 196,72,17,1024
D 196,72,17,2048
D 196,72,17,3072
M0 110592
AS 17,17,4096
D 196,72,17,0
D 196,72,17,1024
v_lshrrev_b32 v222, 4, v254
v_and_b32 v223, 15, v254
v_lshrrev_b32 v223, 2, v223
v_lshl_add_u32 v223, v222, 2, v223
v_and_b32 v222, 3, v254
v_lshlrev_b32 v222, 3, v222
v_lshl_add_u32 v224, v223, 6, v222
VA 224,40960,224
v_mul_u32_u24 v225, 1280, v223
v_add_u32 v225, v225, v222
VA 225,81920,225
Ta 0,224,0
Ta 2,224,1024
Ta 4,224,2048
Ta 6,224,3072
Ta 8,224,4096
Ta 10,224,5120
Ta 12,224,6144
Ta 14,224,7168
Ta 16,224,8192
Ta 18,224,9216
Ta 20,224,10240
Ta 22,224,11264
Ta 24,224,12288
Ta 26,224,13312
Ta 28,224,14336
Ta 30,224,15360
Ta 32,224,16384
Ta 34,224,17408
Ta 36,224,18432
Ta 38,224,19456
Ta 40,224,20480
Ta 42,224,21504
Ta 44,224,22528
Ta 46,224,23552
Ta 48,224,24576
Ta 50,224,25600
Ta 52,224,26624
Ta 54,224,27648
Ta 56,224,28672
Ta 58,224,29696
Ta 60,224,30720
Ta 62,224,31744
Ta 64,224,32768
Ta 66,224,33792
Ta 68,224,34816
Ta 70,224,35840
Ta 72,224,36864
Ta 74,224,37888
Ta 76,224,38912
Ta 78,224,39936
v_lshlrev_b32 v221, 10, v226
v_lshl_add_u32 v220, v209, 4, v208
v_lshl_add_u32 v221, v220, 4, v221
VA 227,122880,221
v_mul_u32_u24 v221, 2560, v208
v_lshl_add_u32 v221, v209, 4, v221
v_lshl_add_u32 v221, v226, 7, v221
VA 221,143360,221
VA 220,122880,196
s_waitcnt vmcnt(0)
v_add_f32 v222, 1.0, v218
Fmu 223,217,218
v_xor_b32 v223, 0x80000000, v223
LO 40,64
HI 41,64
LO 228,68
HI 229,68
PA 40,40,228
LO 228,72
HI 229,72
PA 40,40,228
LO 228,76
HI 229,76
PA 40,40,228
LO 228,0
HI 229,0
PA 40,40,228
LO 42,65
HI 43,65
LO 228,69
HI 229,69
PA 42,42,228
LO 228,73
HI 229,73
PA 42,42,228
LO 228,77
HI 229,77
PA 42,42,228
LO 228,1
HI 229,1
PA 42,42,228
LO 44,66
HI 45,66
LO 228,70
HI 229,70
PA 44,44,228
LO 228,74
HI 229,74
PA 44,44,228
LO 228,78
HI 229,78
PA 44,44,228
LO 228,2
HI 229,2
PA 44,44,228
LO 46,67
HI 47,67
LO 228,71
HI 229,71
PA 46,46,228
LO 228,75
HI 229,75
PA 46,46,228
LO 228,79
HI 229,79
PA 46,46,228
LO 228,3
HI 229,3
PA 46,46,228
FM 48,40,218,223
FM 49,41,218,223
FM 50,42,218,223
FM 51,43,218,223
FM 52,44,218,223
FM 53,45,218,223
FM 54,46,218,223
FM 55,47,218,223
C 56,48,49
C 57,50,51
C 58,52,53
C 59,54,55
FM 40,40,222,223
FM 41,41,222,223
FM 42,42,222,223
FM 43,43,222,223
FM 44,44,222,223
FM 45,45,222,223
FM 46,46,222,223
FM 47,47,222,223
s_mov_b32 exec_lo, -1
s_mov_b32 exec_hi, 0xff
ds_write_b128 v227, v[56:59]
ds_write_b128 v221, v[40:43]
DWv 221,44,64
s_mov_b64 exec, -1
W 0
B
Rv 0,220,0
Rv 4,220,1024
Rv 8,220,2048
Rv 12,220,3072
Rv 16,220,4096
Rv 20,220,5120
Rv 24,220,6144
Rv 28,220,7168
)A"
R"A(Rv 32,220,8192
Rv 36,220,9216
Rv 40,220,10240
Rv 44,220,11264
Rv 48,220,12288
Rv 52,220,13312
Rv 56,220,14336
Rv 60,220,15360
Rv 64,220,16384
Rv 68,220,17408
Rv 72,220,18432
Rv 76,220,19456
Ta 80,225,640
Ta 82,225,21120
Ta 84,225,672
Ta 86,225,21152
Ta 88,225,704
Ta 90,225,21184
Ta 92,225,736
Ta 94,225,21216
Ta 96,225,768
Ta 98,225,21248
Ta 100,225,800
Ta 102,225,21280
Ta 104,225,832
Ta 106,225,21312
Ta 108,225,864
Ta 110,225,21344
Ta 112,225,896
Ta 114,225,21376
Ta 116,225,928
Ta 118,225,21408
W 15
Mvavz 180,0,0
W 15
Mvavz 184,4,4
W 15
Mvavc 180,8,8
W 15
Mvavc 184,12,12
W 15
Mvavc 180,16,16
W 15
Mvavc 184,20,20
W 15
Mvavc 180,24,24
W 15
Mvavc 184,28,28
W 15
Mvavc 180,32,32
W 15
Mvavc 184,36,36
W 15
Mvavc 180,40,40
W 15
Mvavc 184,44,44
W 15
Mvavc 180,48,48
W 15
Mvavc 184,52,52
W 15
Mvavc 180,56,56
W 15
Mvavc 184,60,60
W 15
Mvavc 180,64,64
W 15
Mvavc 184,68,68
W 15
Mvavc 180,72,72
W 15
Mvavc 184,76,76
VA 215,148480,196
N 6
Fad 160,180,184
Fad 161,181,185
Fad 162,182,186
Fad 163,183,187
ds_write_b128 v215, v[160:163]
W 0
B
s_add_u32 s40, s14, 0x5420000
s_addc_u32 s41, s15, 0
SM 42,0x43800
SM 43,0x20000
v_and_b32 v208, 15, v254
v_mul_u32_u24 v208, 2560, v208
v_lshrrev_b32 v209, 4, v254
v_lshl_add_u32 v208, v209, 4, v208
s_mul_i32 s16, s78, 5120
AS 16,16,1280
W 0
B
VA 215,150528,196
ds_read_b128 v[176:179], v215
W 0
Mvavz 48,80,176
Mvavz 52,84,176
Mvavz 56,88,176
Mvavz 60,92,176
Mvavz 64,96,176
Mvavz 68,100,176
Mvavz 72,104,176
Mvavz 76,108,176
Mvavz 80,112,176
Mvavz 84,116,176
s_mov_b32 exec_lo, 0x00030003
s_mov_b32 exec_hi, 0x00030003
Svs 48,208,40,16,0
Svs 52,208,40,16,64
Svs 56,208,40,16,128
Svs 60,208,40,16,192
Svs 64,208,40,16,256
Svs 68,208,40,16,320
Svs 72,208,40,16,384
Svs 76,208,40,16,448
Svs 80,208,40,16,512
Svs 84,208,40,16,576
s_mov_b64 exec, -1
s_waitcnt vmcnt(0)
B
G 24,224
G 25,225
G 26,226
G 27,227
G 28,228
G 29,229
G 30,230
G 31,231
G 32,232
G 33,233
G 100,234
v_and_b32 v160, 7, v254
v_add_u32 v160, s66, v160
v_cmp_ne_u32 vcc, s68, v160
v_cmp_ne_u32 s[16:17], s69, v160
N 4
s_and_b64 exec, vcc, s[16:17]
buffer_store_dwordx2 v[24:25], v100, s[52:55], s65 offen offset:0 sc1
buffer_store_dwordx2 v[26:27], v100, s[52:55], s65 offen offset:64 sc1
buffer_store_dwordx2 v[28:29], v100, s[52:55], s65 offen offset:128 sc1
buffer_store_dwordx2 v[30:31], v100, s[52:55], s65 offen offset:192 sc1
buffer_store_dwordx2 v[32:33], v100, s[52:55], s65 offen offset:256 sc1
s_mov_b64 exec, -1
s_cmp_lt_u32 s78, 20
s_cbranch_scc0 PE2%=
B
s_lshl_b32 s16, s78, 8
v_lshlrev_b32 v200, 2, v254
AS 16,16,143360
buffer_load_dword v0, v200, s[40:43], s16 offen sc1
AS 16,16,5120
buffer_load_dword v1, v200, s[40:43], s16 offen sc1
AS 16,16,5120
buffer_load_dword v2, v200, s[40:43], s16 offen sc1
AS 16,16,5120
buffer_load_dword v3, v200, s[40:43], s16 offen sc1
AS 16,16,5120
buffer_load_dword v4, v200, s[40:43], s16 offen sc1
AS 16,16,5120
buffer_load_dword v5, v200, s[40:43], s16 offen sc1
AS 16,16,5120
buffer_load_dword v6, v200, s[40:43], s16 offen sc1
AS 16,16,5120
buffer_load_dword v7, v200, s[40:43], s16 offen sc1
AS 16,16,5120
buffer_load_dword v8, v200, s[40:43], s16 offen sc1
AS 16,16,5120
buffer_load_dword v9, v200, s[40:43], s16 offen sc1
AS 16,16,5120
buffer_load_dword v10, v200, s[40:43], s16 offen sc1
AS 16,16,5120
buffer_load_dword v11, v200, s[40:43], s16 offen sc1
AS 16,16,5120
buffer_load_dword v12, v200, s[40:43], s16 offen sc1
AS 16,16,5120
buffer_load_dword v13, v200, s[40:43], s16 offen sc1
AS 16,16,5120
s_waitcnt vmcnt(12)
Fad 0,0,1
s_waitcnt vmcnt(11)
Fad 0,0,2
s_waitcnt vmcnt(10)
Fad 0,0,3
s_waitcnt vmcnt(9)
Fad 0,0,4
s_waitcnt vmcnt(8)
Fad 0,0,5
s_waitcnt vmcnt(7)
Fad 0,0,6
s_waitcnt vmcnt(6)
Fad 0,0,7
s_waitcnt vmcnt(5)
Fad 0,0,8
s_waitcnt vmcnt(4)
Fad 0,0,9
s_waitcnt vmcnt(3)
Fad 0,0,10
s_waitcnt vmcnt(2)
Fad 0,0,11
s_waitcnt vmcnt(1)
Fad 0,0,12
s_waitcnt vmcnt(0)
Fad 0,0,13
VA 201,151808,200
ds_write_b32 v201, v0 offset:512
W 0
B
PE2%=:
OE2%=:
s_branch FM2_DONE%=
XN2_SPIN%=:
SM 20,0
XN2_L%=:
s_sleep 2
global_load_dword v246, v247, s[14:15] sc1
s_waitcnt vmcnt(0)
v_cmp_ne_u32 vcc, s79, v246
N 4
s_cbranch_vccz XN2_OK%=
AS 20,20,1
s_cmp_lt_u32 s20, 65536
s_cbranch_scc1 XN2_L%=
s_branch XN2_OK%=
GOSLOW2%=:
s_cmp_eq_u32 s17, 0
s_cbranch_scc1 SBACK2%=
s_getpc_b64 s[22:23]
SRET2%=:
s_add_u32 s22, s22, SBACK2%=-SRET2%=
s_addc_u32 s23, s23, 0
s_getpc_b64 s[16:17]
SJ2%=:
s_add_u32 s16, s16, SLOW%=-SJ2%=
s_addc_u32 s17, s17, 0
s_setpc_b64 s[16:17]
SBACK2%=:
s_and_b32 s16, s10, 7
s_lshr_b32 s41, s16, 1
s_and_b32 s16, s16, 1
s_lshl_b32 s16, s16, 3
s_lshr_b32 s17, s10, 3
s_lshr_b32 s18, s17, 2
s_add_u32 s39, s16, s18
s_and_b32 s40, s17, 3
s_lshl_b32 s16, s41, 2
s_add_u32 s43, s16, s40
s_mov_b64 s[24:25], s[50:51]
SM 26,0x500000
SM 27,0x20000
s_lshl_b32 s16, s41, 10
s_lshl_b32 s17, s11, 5
s_add_u32 s16, s16, s17
s_mul_i32 s16, s16, 1280
s_mul_i32 s17, s40, 320
s_add_u32 s36, s16, s17
s_waitcnt vmcnt(0)
B
s_getpc_b64 s[16:17]
SK2%=:
s_add_u32 s16, s16, MDONE2%=-SK2%=
s_addc_u32 s17, s17, -1
s_setpc_b64 s[16:17]
FM2_DONE%=:
s_getpc_b64 s[16:17]
FJ6%=:
s_add_u32 s16, s16, FM_END%=-FJ6%=
s_addc_u32 s17, s17, 0
s_setpc_b64 s[16:17]
FM_W3%=:
s_waitcnt vmcnt(20)
B
Rv 0,140,0
T 32,144,0
T 34,144,1024
Rv 4,140,2048
T 36,145,0
T 38,145,1024
Rv 8,140,4096
T 40,146,0
T 42,146,1024
Rv 12,140,6144
T 44,147,0
T 46,147,1024
Rv 16,140,8192
T 48,148,0
T 50,148,1024
Rv 20,140,10240
T 52,149,0
T 54,149,1024
Rv 24,140,12288
T 56,150,0
T 58,150,1024
Rv 28,140,14336
T 60,151,0
T 62,151,1024
WV 16
B
AS 79,79,1
Mavvz 0,0,32
Mavvz 4,0,36
Rv 64,141,0
Mavvz 8,0,40
T 96,144,16384
Mavvz 12,0,44
v_dot2c_f32_bf16 v181, v168, v180
v_dot2c_f32_bf16 v182, v168, v168
Mavvz 16,0,48
T 98,144,17408
Mavvz 20,0,52
v_dot2c_f32_bf16 v181, v169, v180
v_dot2c_f32_bf16 v182, v169, v169
Mavvz 24,0,56
Rv 68,141,2048
v_dot2c_f32_bf16 v181, v170, v180
Mavvz 28,0,60
AM 35,0
Mavvz 60,4,60
v_dot2c_f32_bf16 v182, v170, v170
D 236,28,33,0
Mavvz 56,4,56
T 100,145,16384
v_dot2c_f32_bf16 v181, v171, v180
Mavvz 52,4,52
T 102,145,17408
Mavvz 48,4,48
v_dot2c_f32_bf16 v182, v171, v171
v_dot2c_f32_bf16 v181, v172, v180
Mavvz 44,4,44
Rv 72,141,4096
Mavvz 40,4,40
v_dot2c_f32_bf16 v182, v172, v172
Mavvz 36,4,36
T 104,146,16384
v_dot2c_f32_bf16 v181, v173, v180
Mavvz 32,4,32
v_dot2c_f32_bf16 v182, v173, v173
D 237,28,33,1024
Mavvz 64,8,32
T 106,146,17408
Mavvz 68,8,36
v_dot2c_f32_bf16 v181, v174, v180
v_dot2c_f32_bf16 v182, v174, v174
Mavvz 72,8,40
Rv 76,141,6144
Mavvz 76,8,44
v_dot2c_f32_bf16 v181, v175, v180
Mavvz 80,8,48
T 108,147,16384
Mavvz 84,8,52
v_dot2c_f32_bf16 v182, v175, v175
v_dot2c_f32_bf16 v181, v176, v180
Mavvz 88,8,56
v_dot2c_f32_bf16 v182, v176, v176
D 238,28,33,2048
Mavvz 92,8,60
T 110,147,17408
v_dot2c_f32_bf16 v181, v177, v180
Mavvz 124,12,60
Rv 80,141,8192
Mavvz 120,12,56
T 112,148,16384
Mavvz 116,12,52
v_dot2c_f32_bf16 v182, v177, v177
v_dot2c_f32_bf16 v181, v178, v180
Mavvz 112,12,48
T 114,148,17408
Mavvz 108,12,44
v_dot2c_f32_bf16 v182, v178, v178
v_dot2c_f32_bf16 v181, v179, v180
Mavvz 104,12,40
D 239,28,33,3072
Mavvz 100,12,36
v_dot2c_f32_bf16 v182, v179, v179
AS 33,33,262144
Mavvz 96,12,32
Rv 84,141,10240
N 3
Mavvz 128,16,32
T 116,149,16384
v_add_f32_dpp v181, v181, v181 row_ror:8 row_mask:0xf bank_mask:0xf
Mavvz 132,16,36
T 118,149,17408
Mavvz 136,16,40
v_add_f32_dpp v182, v182, v182 row_ror:8 row_mask:0xf bank_mask:0xf
MV 186,181
Mavvz 140,16,44
AM 34,69632
Mavvz 144,16,48
MV 187,182
D 132,24,32,0
Mavvz 148,16,52
Rv 88,141,12288
N 1
Mavvz 152,16,56
T 120,150,16384
v_permlane16_swap_b32 v181, v186
Mavvz 156,16,60
T 122,150,17408
Mavvz 188,20,60
v_permlane16_swap_b32 v182, v187
N 1
Mavvz 184,20,56
Rv 92,141,14336
Mavvz 180,20,52
Fad 181,181,186
Mavvz 176,20,48
Fad 182,182,187
D 133,24,32,1024
Mavvz 172,20,44
T 124,151,16384
MV 186,181
Mavvz 168,20,40
T 126,151,17408
Mavvz 164,20,36
MV 187,182
Mavvz 160,20,32
N 1
Mavvz 192,24,32
v_permlane32_swap_b32 v181, v186
Mavvz 196,24,36
v_permlane32_swap_b32 v182, v187
Mavvz 200,24,40
D 134,24,32,2048
Mavvz 204,24,44
N 1
Mavvz 208,24,48
Fad 181,181,186
Mavvz 212,24,52
Fad 182,182,187
Mavvz 216,24,56
MV 186,181
Mavvz 220,24,60
MV 187,182
Mavvz 252,28,60
ds_write_b64 v185, v[186:187]
Mavvz 248,28,56
D 135,24,32,3072
Mavvz 244,28,52
AS 32,32,128
Mavvz 240,28,48
Mavvz 236,28,44
Mavvz 232,28,40
Mavvz 228,28,36
Mavvz 224,28,32
WV 12
B
Mavvc 0,64,96
Mavvc 4,64,100
Rv 0,140,32768
Mavvc 8,64,104
T 32,144,32768
Mavvc 12,64,108
Mavvc 16,64,112
T 34,144,33792
Mavvc 20,64,116
Mavvc 24,64,120
Rv 4,140,34816
Mavvc 28,64,124
AM 35,16384
Mavvc 60,68,124
D 236,28,33,0
Mavvc 56,68,120
T 36,145,32768
Mavvc 52,68,116
T 38,145,33792
Mavvc 48,68,112
Mavvc 44,68,108
Rv 8,140,36864
Mavvc 40,68,104
Mavvc 36,68,100
T 40,146,32768
Mavvc 32,68,96
D 237,28,33,1024
Mavvc 64,72,96
T 42,146,33792
Mavvc 68,72,100
Mavvc 72,72,104
Rv 12,140,38912
Mavvc 76,72,108
Mavvc 80,72,112
T 44,147,32768
Mavvc 84,72,116
Mavvc 88,72,120
D 238,28,33,2048
Mavvc 92,72,124
T 46,147,33792
Mavvc 124,76,124
Rv 16,140,40960
Mavvc 120,76,120
T 48,148,32768
Mavvc 116,76,116
Mavvc 112,76,112
T 50,148,33792
Mavvc 108,76,108
Mavvc 104,76,104
D 239,28,33,3072
Mavvc 100,76,100
AS 33,33,262144
Mavvc 96,76,96
Rv 20,140,43008
Mavvc 128,80,96
T 52,149,32768
Mavvc 132,80,100
T 54,149,33792
Mavvc 136,80,104
Mavvc 140,80,108
AM 34,0
Mavvc 144,80,112
D 128,24,32,0
Mavvc 148,80,116
Rv 24,140,45056
Mavvc 152,80,120
T 56,150,32768
Mavvc 156,80,124
T 58,150,33792
Mavvc 188,84,124
Mavvc 184,84,120
Rv 28,140,47104
Mavvc 180,84,116
Mavvc 176,84,112
D 129,24,32,1024
Mavvc 172,84,108
T 60,151,32768
Mavvc 168,84,104
T 62,151,33792
Mavvc 164,84,100
Mavvc 160,84,96
Mavvc 192,88,96
Mavvc 196,88,100
Mavvc 200,88,104
D 130,24,32,2048
Mavvc 204,88,108
Mavvc 208,88,112
Mavvc 212,88,116
Mavvc 216,88,120
Mavvc 220,88,124
Mavvc 252,92,124
Mavvc 248,92,120
D 131,24,32,3072
Mavvc 244,92,116
ds_read_b64 v[188:189], v184 offset:0
Mavvc 240,92,112
ds_read_b64 v[190:191], v184 offset:64
Mavvc 236,92,108
ds_read_b64 v[192:193], v184 offset:128
Mavvc 232,92,104
ds_read_b64 v[194:195], v184 offset:192
Mavvc 228,92,100
Mavvc 224,92,96
WV 12
B
Mavvc 0,0,32
Mavvc 4,0,36
Rv 64,141,32768
Fad 181,188,190
Mavvc 8,0,40
T 96,144,0
Fad 182,189,191
Mavvc 12,0,44
Fad 186,192,194
Fad 187,193,195
Mavvc 16,0,48
T 98,144,1024
Fad 181,181,186
Mavvc 20,0,52
Fad 182,182,187
v_mul_f32 v181, 0x3acccccd, v181
Mavvc 24,0,56
Rv 68,141,34816
v_mul_f32 v182, 0x3acccccd, v182
Mavvc 28,0,60
v_fma_f32 v182, -v181, v181, v182
AM 35,32768
Mavvc 60,4,60
v_max_f32 v182, 0, v182
D 236,28,33,0
Mavvc 56,4,56
T 100,145,0
v_add_f32 v182, 0x3727c5ac, v182
Mavvc 52,4,52
T 102,145,1024
v_rsq_f32 v182, v182
Mavvc 48,4,48
N 0
v_mul_f32 v182, 0x3e0293ee, v182
Mavvc 44,4,44
Rv 72,141,36864
Fmu 181,181,182
Mavvc 40,4,40
v_xor_b32 v181, 0x80000000, v181
LO 196,168
Mavvc 36,4,36
T 104,146,0
HI 197,168
Mavvc 32,4,32
FM 196,196,182,181
D 237,28,33,1024
Mavvc 64,8,32
T 106,146,1024
FM 197,197,182,181
Mavvc 68,8,36
C 168,196,197
LO 196,169
Mavvc 72,8,40
Rv 76,141,38912
HI 197,169
Mavvc 76,8,44
FM 196,196,182,181
FM 197,197,182,181
Mavvc 80,8,48
T 108,147,0
C 169,196,197
Mavvc 84,8,52
LO 196,170
HI 197,170
Mavvc 88,8,56
FM 196,196,182,181
D 238,28,33,2048
Mavvc 92,8,60
T 110,147,1024
FM 197,197,182,181
Mavvc 124,12,60
Rv 80,141,40960
C 170,196,197
Mavvc 120,12,56
T 112,148,0
LO 196,171
Mavvc 116,12,52
HI 197,171
FM 196,196,182,181
Mavvc 112,12,48
T 114,148,1024
FM 197,197,182,181
Mavvc 108,12,44
C 171,196,197
LO 196,172
Mavvc 104,12,40
HI 197,172
D 239,28,33,3072
Mavvc 100,12,36
FM 196,196,182,181
AS 33,33,262144
Mavvc 96,12,32
Rv 84,141,43008
FM 197,197,182,181
Mavvc 128,16,32
T 116,149,0
C 172,196,197
Mavvc 132,16,36
T 118,149,1024
LO 196,173
Mavvc 136,16,40
HI 197,173
FM 196,196,182,181
Mavvc 140,16,44
FM 197,197,182,181
AM 34,4096
Mavvc 144,16,48
C 173,196,197
D 132,24,32,0
Mavvc 148,16,52
Rv 88,141,45056
LO 196,174
Mavvc 152,16,56
T 120,150,0
HI 197,174
Mavvc 156,16,60
T 122,150,1024
FM 196,196,182,181
Mavvc 188,20,60
FM 197,197,182,181
C 174,196,197
Mavvc 184,20,56
Rv 92,141,47104
LO 196,175
Mavvc 180,20,52
HI 197,175
FM 196,196,182,181
)A"
R"A(Mavvc 176,20,48
FM 197,197,182,181
D 133,24,32,1024
Mavvc 172,20,44
T 124,151,0
C 175,196,197
Mavvc 168,20,40
T 126,151,1024
LO 196,176
Mavvc 164,20,36
HI 197,176
Mavvc 160,20,32
FM 196,196,182,181
Mavvc 192,24,32
FM 197,197,182,181
Mavvc 196,24,36
C 176,196,197
LO 196,177
Mavvc 200,24,40
HI 197,177
D 134,24,32,2048
Mavvc 204,24,44
FM 196,196,182,181
Mavvc 208,24,48
FM 197,197,182,181
Mavvc 212,24,52
C 177,196,197
Mavvc 216,24,56
LO 196,178
Mavvc 220,24,60
HI 197,178
Mavvc 252,28,60
FM 196,196,182,181
FM 197,197,182,181
Mavvc 248,28,56
C 178,196,197
D 135,24,32,3072
Mavvc 244,28,52
LO 196,179
AS 32,32,128
Mavvc 240,28,48
HI 197,179
Mavvc 236,28,44
FM 196,196,182,181
Mavvc 232,28,40
FM 197,197,182,181
Mavvc 228,28,36
C 179,196,197
Mavvc 224,28,32
s_lshr_b32 s18, s10, 4
s_mul_i32 s18, s18, 163840
s_lshr_b32 s19, s10, 1
s_and_b32 s19, s19, 7
s_lshl_b32 s19, s19, 10
s_add_u32 s18, s18, s19
s_and_b32 s19, s10, 1
s_lshl_b32 s19, s19, 7
s_add_u32 s18, s18, s19
AS 19,18,65536
Svs 168,205,48,18,0
Svs 172,205,48,19,0
Svs 176,206,48,18,0
WV 15
B
Mavvc 0,64,96
Mavvc 4,64,100
Rv 0,142,0
Mavvc 8,64,104
T 32,144,16384
Mavvc 12,64,108
Mavvc 16,64,112
T 34,144,17408
Mavvc 20,64,116
Mavvc 24,64,120
Rv 4,142,2048
Mavvc 28,64,124
AM 35,0
Mavvc 60,68,124
D 236,28,33,0
Mavvc 56,68,120
T 36,145,16384
Mavvc 52,68,116
T 38,145,17408
Mavvc 48,68,112
Mavvc 44,68,108
Rv 8,142,4096
Mavvc 40,68,104
Mavvc 36,68,100
T 40,146,16384
Mavvc 32,68,96
D 237,28,33,1024
Mavvc 64,72,96
T 42,146,17408
Mavvc 68,72,100
Mavvc 72,72,104
Rv 12,142,6144
Mavvc 76,72,108
Mavvc 80,72,112
T 44,147,16384
Mavvc 84,72,116
Mavvc 88,72,120
D 238,28,33,2048
Mavvc 92,72,124
T 46,147,17408
Mavvc 124,76,124
Rv 16,142,8192
Mavvc 120,76,120
T 48,148,16384
Mavvc 116,76,116
Mavvc 112,76,112
T 50,148,17408
Mavvc 108,76,108
Mavvc 104,76,104
D 239,28,33,3072
Mavvc 100,76,100
AS 33,33,262144
Mavvc 96,76,96
Rv 20,142,10240
Mavvc 128,80,96
T 52,149,16384
Mavvc 132,80,100
T 54,149,17408
Mavvc 136,80,104
Mavvc 140,80,108
AM 34,32768
Mavvc 144,80,112
D 128,24,32,0
Mavvc 148,80,116
Rv 24,142,12288
Mavvc 152,80,120
T 56,150,16384
Mavvc 156,80,124
T 58,150,17408
Mavvc 188,84,124
Mavvc 184,84,120
Rv 28,142,14336
Mavvc 180,84,116
Mavvc 176,84,112
D 129,24,32,1024
Mavvc 172,84,108
T 60,151,16384
Mavvc 168,84,104
T 62,151,17408
Mavvc 164,84,100
Mavvc 160,84,96
Mavvc 192,88,96
Mavvc 196,88,100
Mavvc 200,88,104
D 130,24,32,2048
Mavvc 204,88,108
Mavvc 208,88,112
Mavvc 212,88,116
Mavvc 216,88,120
Mavvc 220,88,124
Mavvc 252,92,124
Mavvc 248,92,120
D 131,24,32,3072
Mavvc 244,92,116
Mavvc 240,92,112
Mavvc 236,92,108
Mavvc 232,92,104
Mavvc 228,92,100
Mavvc 224,92,96
WV 15
B
Mavvc 0,0,32
Mavvc 4,0,36
Rv 64,143,0
Mavvc 8,0,40
T 96,144,32768
Mavvc 12,0,44
Mavvc 16,0,48
T 98,144,33792
Mavvc 20,0,52
Mavvc 24,0,56
Rv 68,143,2048
Mavvc 28,0,60
AM 35,16384
Mavvc 60,4,60
D 236,28,33,0
Mavvc 56,4,56
T 100,145,32768
Mavvc 52,4,52
T 102,145,33792
Mavvc 48,4,48
Mavvc 44,4,44
Rv 72,143,4096
Mavvc 40,4,40
Mavvc 36,4,36
T 104,146,32768
Mavvc 32,4,32
D 237,28,33,1024
Mavvc 64,8,32
T 106,146,33792
Mavvc 68,8,36
Mavvc 72,8,40
Rv 76,143,6144
Mavvc 76,8,44
Mavvc 80,8,48
T 108,147,32768
Mavvc 84,8,52
Mavvc 88,8,56
D 238,28,33,2048
Mavvc 92,8,60
T 110,147,33792
Mavvc 124,12,60
Rv 80,143,8192
Mavvc 120,12,56
T 112,148,32768
Mavvc 116,12,52
Mavvc 112,12,48
T 114,148,33792
Mavvc 108,12,44
Mavvc 104,12,40
D 239,28,33,3072
Mavvc 100,12,36
AS 33,33,262144
Mavvc 96,12,32
Rv 84,143,10240
Mavvc 128,16,32
T 116,149,32768
Mavvc 132,16,36
T 118,149,33792
Mavvc 136,16,40
Mavvc 140,16,44
AM 34,36864
Mavvc 144,16,48
D 132,24,32,0
Mavvc 148,16,52
Rv 88,143,12288
Mavvc 152,16,56
T 120,150,32768
Mavvc 156,16,60
T 122,150,33792
Mavvc 188,20,60
Mavvc 184,20,56
Rv 92,143,14336
Mavvc 180,20,52
Mavvc 176,20,48
D 133,24,32,1024
Mavvc 172,20,44
T 124,151,32768
Mavvc 168,20,40
T 126,151,33792
Mavvc 164,20,36
Mavvc 160,20,32
Mavvc 192,24,32
Mavvc 196,24,36
Mavvc 200,24,40
D 134,24,32,2048
Mavvc 204,24,44
Mavvc 208,24,48
Mavvc 212,24,52
Mavvc 216,24,56
Mavvc 220,24,60
Mavvc 252,28,60
Mavvc 248,28,56
D 135,24,32,3072
Mavvc 244,28,52
AS 32,32,128
Mavvc 240,28,48
Mavvc 236,28,44
Mavvc 232,28,40
Mavvc 228,28,36
Mavvc 224,28,32
WV 12
B
Mavvc 0,64,96
Mavvc 4,64,100
Rv 0,140,0
Mavvc 8,64,104
T 32,144,0
Mavvc 12,64,108
Mavvc 16,64,112
T 34,144,1024
Mavvc 20,64,116
Mavvc 24,64,120
Rv 4,140,2048
Mavvc 28,64,124
AM 35,32768
Mavvc 60,68,124
D 236,28,33,0
Mavvc 56,68,120
T 36,145,0
Mavvc 52,68,116
T 38,145,1024
Mavvc 48,68,112
Mavvc 44,68,108
Rv 8,140,4096
Mavvc 40,68,104
Mavvc 36,68,100
T 40,146,0
Mavvc 32,68,96
D 237,28,33,1024
Mavvc 64,72,96
T 42,146,1024
Mavvc 68,72,100
Mavvc 72,72,104
Rv 12,140,6144
Mavvc 76,72,108
Mavvc 80,72,112
T 44,147,0
Mavvc 84,72,116
Mavvc 88,72,120
D 238,28,33,2048
Mavvc 92,72,124
T 46,147,1024
Mavvc 124,76,124
Rv 16,140,8192
Mavvc 120,76,120
T 48,148,0
Mavvc 116,76,116
Mavvc 112,76,112
T 50,148,1024
Mavvc 108,76,108
Mavvc 104,76,104
D 239,28,33,3072
Mavvc 100,76,100
AS 33,33,262144
Mavvc 96,76,96
Rv 20,140,10240
Mavvc 128,80,96
T 52,149,0
Mavvc 132,80,100
T 54,149,1024
Mavvc 136,80,104
Mavvc 140,80,108
AM 34,65536
Mavvc 144,80,112
D 128,24,32,0
Mavvc 148,80,116
Rv 24,140,12288
Mavvc 152,80,120
T 56,150,0
Mavvc 156,80,124
T 58,150,1024
Mavvc 188,84,124
Mavvc 184,84,120
Rv 28,140,14336
Mavvc 180,84,116
Mavvc 176,84,112
D 129,24,32,1024
Mavvc 172,84,108
T 60,151,0
Mavvc 168,84,104
T 62,151,1024
Mavvc 164,84,100
Mavvc 160,84,96
Mavvc 192,88,96
Mavvc 196,88,100
Mavvc 200,88,104
D 130,24,32,2048
Mavvc 204,88,108
Mavvc 208,88,112
Mavvc 212,88,116
Mavvc 216,88,120
Mavvc 220,88,124
Mavvc 252,92,124
Mavvc 248,92,120
D 131,24,32,3072
Mavvc 244,92,116
Mavvc 240,92,112
Mavvc 236,92,108
Mavvc 232,92,104
Mavvc 228,92,100
Mavvc 224,92,96
WV 12
B
Mavvc 0,0,32
Mavvc 4,0,36
Rv 64,141,0
Mavvc 8,0,40
T 96,144,16384
Mavvc 12,0,44
Mavvc 16,0,48
T 98,144,17408
Mavvc 20,0,52
Mavvc 24,0,56
Rv 68,141,2048
Mavvc 28,0,60
AM 35,0
Mavvc 60,4,60
D 236,28,33,0
Mavvc 56,4,56
T 100,145,16384
Mavvc 52,4,52
T 102,145,17408
Mavvc 48,4,48
Mavvc 44,4,44
Rv 72,141,4096
Mavvc 40,4,40
Mavvc 36,4,36
T 104,146,16384
Mavvc 32,4,32
D 237,28,33,1024
Mavvc 64,8,32
T 106,146,17408
Mavvc 68,8,36
Mavvc 72,8,40
Rv 76,141,6144
Mavvc 76,8,44
Mavvc 80,8,48
T 108,147,16384
Mavvc 84,8,52
Mavvc 88,8,56
D 238,28,33,2048
Mavvc 92,8,60
T 110,147,17408
Mavvc 124,12,60
Rv 80,141,8192
Mavvc 120,12,56
T 112,148,16384
Mavvc 116,12,52
Mavvc 112,12,48
T 114,148,17408
Mavvc 108,12,44
Mavvc 104,12,40
D 239,28,33,3072
Mavvc 100,12,36
AS 33,33,262144
Mavvc 96,12,32
Rv 84,141,10240
Mavvc 128,16,32
T 116,149,16384
Mavvc 132,16,36
T 118,149,17408
Mavvc 136,16,40
Mavvc 140,16,44
AM 34,69632
Mavvc 144,16,48
D 132,24,32,0
Mavvc 148,16,52
Rv 88,141,12288
Mavvc 152,16,56
T 120,150,16384
Mavvc 156,16,60
T 122,150,17408
Mavvc 188,20,60
Mavvc 184,20,56
Rv 92,141,14336
Mavvc 180,20,52
Mavvc 176,20,48
D 133,24,32,1024
Mavvc 172,20,44
T 124,151,16384
Mavvc 168,20,40
T 126,151,17408
Mavvc 164,20,36
Mavvc 160,20,32
Mavvc 192,24,32
Mavvc 196,24,36
Mavvc 200,24,40
D 134,24,32,2048
Mavvc 204,24,44
Mavvc 208,24,48
Mavvc 212,24,52
Mavvc 216,24,56
Mavvc 220,24,60
Mavvc 252,28,60
Mavvc 248,28,56
D 135,24,32,3072
Mavvc 244,28,52
AS 32,32,128
Mavvc 240,28,48
Mavvc 236,28,44
Mavvc 232,28,40
Mavvc 228,28,36
Mavvc 224,28,32
WV 12
B
s_getpc_b64 s[16:17]
1:
s_add_u32 s16, s16, QS3%=-1b
s_addc_u32 s17, s17, 0
s_lshr_b32 s21, s10, 3
v_lshlrev_b32 v249, 5, v254
v_add_u32 v249, s21, v249
v_min_u32 v249, ((QE3%=-QS3%=)>>7), v249
v_lshlrev_b32 v249, 7, v249
global_load_dword v248, v249, s[16:17]
Mavvc 0,64,96
Mavvc 4,64,100
Rv 0,140,32768
Mavvc 8,64,104
T 32,144,32768
Mavvc 12,64,108
Mavvc 16,64,112
T 34,144,33792
Mavvc 20,64,116
Mavvc 24,64,120
Rv 4,140,34816
Mavvc 28,64,124
AM 35,16384
Mavvc 60,68,124
D 236,28,33,0
Mavvc 56,68,120
T 36,145,32768
Mavvc 52,68,116
T 38,145,33792
Mavvc 48,68,112
Mavvc 44,68,108
Rv 8,140,36864
Mavvc 40,68,104
Mavvc 36,68,100
T 40,146,32768
Mavvc 32,68,96
D 237,28,33,1024
Mavvc 64,72,96
T 42,146,33792
Mavvc 68,72,100
Mavvc 72,72,104
Rv 12,140,38912
Mavvc 76,72,108
Mavvc 80,72,112
T 44,147,32768
Mavvc 84,72,116
Mavvc 88,72,120
D 238,28,33,2048
Mavvc 92,72,124
T 46,147,33792
Mavvc 124,76,124
Rv 16,140,40960
Mavvc 120,76,120
T 48,148,32768
Mavvc 116,76,116
Mavvc 112,76,112
T 50,148,33792
Mavvc 108,76,108
Mavvc 104,76,104
D 239,28,33,3072
Mavvc 100,76,100
AS 33,33,262144
Mavvc 96,76,96
Rv 20,140,43008
Mavvc 128,80,96
T 52,149,32768
Mavvc 132,80,100
T 54,149,33792
Mavvc 136,80,104
Mavvc 140,80,108
AM 34,0
Mavvc 144,80,112
D 128,24,32,0
Mavvc 148,80,116
Rv 24,140,45056
Mavvc 152,80,120
T 56,150,32768
Mavvc 156,80,124
T 58,150,33792
Mavvc 188,84,124
Mavvc 184,84,120
Rv 28,140,47104
Mavvc 180,84,116
Mavvc 176,84,112
D 129,24,32,1024
Mavvc 172,84,108
T 60,151,32768
Mavvc 168,84,104
T 62,151,33792
Mavvc 164,84,100
Mavvc 160,84,96
Mavvc 192,88,96
Mavvc 196,88,100
Mavvc 200,88,104
D 130,24,32,2048
Mavvc 204,88,108
Mavvc 208,88,112
Mavvc 212,88,116
Mavvc 216,88,120
Mavvc 220,88,124
Mavvc 252,92,124
Mavvc 248,92,120
D 131,24,32,3072
Mavvc 244,92,116
Mavvc 240,92,112
Mavvc 236,92,108
Mavvc 232,92,104
Mavvc 228,92,100
Mavvc 224,92,96
WV 13
B
Mavvc 0,0,32
Mavvc 4,0,36
Rv 64,141,32768
Mavvc 8,0,40
T 96,144,0
Mavvc 12,0,44
Mavvc 16,0,48
T 98,144,1024
Mavvc 20,0,52
Mavvc 24,0,56
Rv 68,141,34816
Mavvc 28,0,60
AM 35,32768
Mavvc 60,4,60
D 236,28,33,0
Mavvc 56,4,56
T 100,145,0
Mavvc 52,4,52
T 102,145,1024
Mavvc 48,4,48
Mavvc 44,4,44
Rv 72,141,36864
Mavvc 40,4,40
Mavvc 36,4,36
T 104,146,0
Mavvc 32,4,32
D 237,28,33,1024
Mavvc 64,8,32
T 106,146,1024
Mavvc 68,8,36
Mavvc 72,8,40
Rv 76,141,38912
Mavvc 76,8,44
Mavvc 80,8,48
T 108,147,0
Mavvc 84,8,52
Mavvc 88,8,56
D 238,28,33,2048
Mavvc 92,8,60
T 110,147,1024
Mavvc 124,12,60
Rv 80,141,40960
Mavvc 120,12,56
T 112,148,0
Mavvc 116,12,52
Mavvc 112,12,48
T 114,148,1024
Mavvc 108,12,44
Mavvc 104,12,40
D 239,28,33,3072
Mavvc 100,12,36
AS 33,33,262144
Mavvc 96,12,32
Rv 84,141,43008
Mavvc 128,16,32
T 116,149,0
Mavvc 132,16,36
T 118,149,1024
Mavvc 136,16,40
Mavvc 140,16,44
AM 34,4096
Mavvc 144,16,48
D 132,24,32,0
Mavvc 148,16,52
Rv 88,141,45056
Mavvc 152,16,56
T 120,150,0
Mavvc 156,16,60
T 122,150,1024
Mavvc 188,20,60
Mavvc 184,20,56
Rv 92,141,47104
Mavvc 180,20,52
Mavvc 176,20,48
D 133,24,32,1024
Mavvc 172,20,44
T 124,151,0
Mavvc 168,20,40
T 126,151,1024
Mavvc 164,20,36
Mavvc 160,20,32
Mavvc 192,24,32
Mavvc 196,24,36
Mavvc 200,24,40
D 134,24,32,2048
Mavvc 204,24,44
Mavvc 208,24,48
Mavvc 212,24,52
Mavvc 216,24,56
Mavvc 220,24,60
Mavvc 252,28,60
Mavvc 248,28,56
D 135,24,32,3072
Mavvc 244,28,52
AS 32,32,128
Mavvc 240,28,48
Mavvc 236,28,44
Mavvc 232,28,40
Mavvc 228,28,36
Mavvc 224,28,32
WV 12
B
s_sub_u32 s23, 15, s13
s_cmp_ge_u32 s11, 2
s_cselect_b32 s23, s13, s23
s_lshl_b32 s21, s23, 8
AS 21,21,0
v_mov_b32 v247, s21
v_min_u32 v246, 15, v254
v_lshl_add_u32 v247, v246, 2, v247
global_load_dword v246, v247, s[14:15] sc1
Mavvc 0,64,96
Mavvc 4,64,100
Rv 0,142,0
Mavvc 8,64,104
T 32,144,16384
Mavvc 12,64,108
Mavvc 16,64,112
T 34,144,17408
Mavvc 20,64,116
Mavvc 24,64,120
Rv 4,142,2048
Mavvc 28,64,124
AM 35,0
Mavvc 60,68,124
D 236,28,33,0
Mavvc 56,68,120
T 36,145,16384
Mavvc 52,68,116
T 38,145,17408
Mavvc 48,68,112
Mavvc 44,68,108
Rv 8,142,4096
Mavvc 40,68,104
Mavvc 36,68,100
T 40,146,16384
Mavvc 32,68,96
D 237,28,33,1024
Mavvc 64,72,96
T 42,146,17408
Mavvc 68,72,100
Mavvc 72,72,104
Rv 12,142,6144
Mavvc 76,72,108
Mavvc 80,72,112
T 44,147,16384
Mavvc 84,72,116
Mavvc 88,72,120
D 238,28,33,2048
Mavvc 92,72,124
T 46,147,17408
Mavvc 124,76,124
Rv 16,142,8192
Mavvc 120,76,120
T 48,148,16384
Mavvc 116,76,116
Mavvc 112,76,112
T 50,148,17408
Mavvc 108,76,108
Mavvc 104,76,104
D 239,28,33,3072
Mavvc 100,76,100
AS 33,33,262144
Mavvc 96,76,96
Rv 20,142,10240
Mavvc 128,80,96
T 52,149,16384
Mavvc 132,80,100
T 54,149,17408
Mavvc 136,80,104
Mavvc 140,80,108
AM 34,32768
Mavvc 144,80,112
D 128,24,32,0
Mavvc 148,80,116
Rv 24,142,12288
Mavvc 152,80,120
T 56,150,16384
Mavvc 156,80,124
T 58,150,17408
Mavvc 188,84,124
Mavvc 184,84,120
Rv 28,142,14336
Mavvc 180,84,116
Mavvc 176,84,112
D 129,24,32,1024
)A"
R"A(Mavvc 172,84,108
T 60,151,16384
Mavvc 168,84,104
T 62,151,17408
Mavvc 164,84,100
Mavvc 160,84,96
Mavvc 192,88,96
Mavvc 196,88,100
Mavvc 200,88,104
D 130,24,32,2048
Mavvc 204,88,108
Mavvc 208,88,112
Mavvc 212,88,116
Mavvc 216,88,120
Mavvc 220,88,124
Mavvc 252,92,124
Mavvc 248,92,120
D 131,24,32,3072
Mavvc 244,92,116
Mavvc 240,92,112
Mavvc 236,92,108
Mavvc 232,92,104
Mavvc 228,92,100
Mavvc 224,92,96
WV 13
B
Mavvc 0,0,32
Mavvc 4,0,36
Rv 64,143,0
Mavvc 8,0,40
T 96,144,32768
Mavvc 12,0,44
Mavvc 16,0,48
T 98,144,33792
Mavvc 20,0,52
Mavvc 24,0,56
Rv 68,143,2048
Mavvc 28,0,60
AM 35,16384
Mavvc 60,4,60
D 236,28,33,0
Mavvc 56,4,56
T 100,145,32768
Mavvc 52,4,52
T 102,145,33792
Mavvc 48,4,48
Mavvc 44,4,44
Rv 72,143,4096
Mavvc 40,4,40
Mavvc 36,4,36
T 104,146,32768
Mavvc 32,4,32
D 237,28,33,1024
Mavvc 64,8,32
T 106,146,33792
Mavvc 68,8,36
Mavvc 72,8,40
Rv 76,143,6144
Mavvc 76,8,44
Mavvc 80,8,48
T 108,147,32768
Mavvc 84,8,52
Mavvc 88,8,56
D 238,28,33,2048
Mavvc 92,8,60
T 110,147,33792
Mavvc 124,12,60
Rv 80,143,8192
Mavvc 120,12,56
T 112,148,32768
Mavvc 116,12,52
Mavvc 112,12,48
T 114,148,33792
Mavvc 108,12,44
Mavvc 104,12,40
D 239,28,33,3072
Mavvc 100,12,36
AS 33,33,262144
Mavvc 96,12,32
Rv 84,143,10240
Mavvc 128,16,32
T 116,149,32768
Mavvc 132,16,36
T 118,149,33792
Mavvc 136,16,40
Mavvc 140,16,44
AM 34,36864
Mavvc 144,16,48
D 132,24,32,0
Mavvc 148,16,52
Rv 88,143,12288
Mavvc 152,16,56
T 120,150,32768
Mavvc 156,16,60
T 122,150,33792
Mavvc 188,20,60
Mavvc 184,20,56
Rv 92,143,14336
Mavvc 180,20,52
Mavvc 176,20,48
D 133,24,32,1024
Mavvc 172,20,44
T 124,151,32768
Mavvc 168,20,40
T 126,151,33792
Mavvc 164,20,36
Mavvc 160,20,32
Mavvc 192,24,32
Mavvc 196,24,36
Mavvc 200,24,40
D 134,24,32,2048
Mavvc 204,24,44
Mavvc 208,24,48
Mavvc 212,24,52
Mavvc 216,24,56
Mavvc 220,24,60
Mavvc 252,28,60
Mavvc 248,28,56
D 135,24,32,3072
Mavvc 244,28,52
AS 32,32,128
Mavvc 240,28,48
Mavvc 236,28,44
Mavvc 232,28,40
Mavvc 228,28,36
Mavvc 224,28,32
WV 12
B
v_cmp_ne_u32 vcc, s79, v246
N 4
s_cbranch_vccnz XN3_SPIN%=
XN3_OK%=:
Mavvc 0,64,96
Mavvc 4,64,100
Rv 0,140,0
Mavvc 8,64,104
T 32,144,0
Mavvc 12,64,108
Mavvc 16,64,112
T 34,144,1024
Mavvc 20,64,116
Mavvc 24,64,120
Rv 4,140,2048
Mavvc 28,64,124
AM 35,32768
Mavvc 60,68,124
D 236,28,33,0
Mavvc 56,68,120
T 36,145,0
s_and_b32 s21, s12, 3
Mavvc 52,68,116
T 38,145,1024
Mavvc 48,68,112
s_mul_i32 s21, s21, 40960
Mavvc 44,68,108
Rv 8,140,4096
Mavvc 40,68,104
s_add_u32 s21, s21, s53
Mavvc 36,68,100
T 40,146,0
Mavvc 32,68,96
AS 21,21,16384
D 237,28,33,1024
Mavvc 64,72,96
T 42,146,1024
Mavvc 68,72,100
v_min_u32 v249, 63, v254
Mavvc 72,72,104
Rv 12,140,6144
Mavvc 76,72,108
v_lshlrev_b32 v249, 7, v249
Mavvc 80,72,112
T 44,147,0
Mavvc 84,72,116
buffer_load_dword v248, v249, s[48:51], s21 offen sc1
Mavvc 88,72,120
D 238,28,33,2048
Mavvc 92,72,124
T 46,147,1024
Mavvc 124,76,124
Rv 16,140,8192
Mavvc 120,76,120
T 48,148,0
Mavvc 116,76,116
Mavvc 112,76,112
T 50,148,1024
Mavvc 108,76,108
Mavvc 104,76,104
D 239,28,33,3072
Mavvc 100,76,100
AS 33,33,262144
Mavvc 96,76,96
Rv 20,140,10240
Mavvc 128,80,96
T 52,149,0
Mavvc 132,80,100
T 54,149,1024
Mavvc 136,80,104
Mavvc 140,80,108
Rv 24,140,12288
Mavvc 144,80,112
Mavvc 148,80,116
T 56,150,0
Mavvc 152,80,120
Mavvc 156,80,124
T 58,150,1024
AS 55,53,0
M0 90112
Mavvc 188,84,124
Ds 243,48,55,0
Mavvc 184,84,120
Rv 28,140,14336
Mavvc 180,84,116
Mavvc 176,84,112
T 60,151,0
Mavvc 172,84,108
Mavvc 168,84,104
T 62,151,1024
Mavvc 164,84,100
Ds 243,48,55,1024
Mavvc 160,84,96
Mavvc 192,88,96
Mavvc 196,88,100
Mavvc 200,88,104
Mavvc 204,88,108
Mavvc 208,88,112
Mavvc 212,88,116
Ds 243,48,55,2048
Mavvc 216,88,120
Mavvc 220,88,124
Mavvc 252,92,124
Mavvc 248,92,120
Mavvc 244,92,116
Mavvc 240,92,112
Ds 243,48,55,3072
Mavvc 236,92,108
Mavvc 232,92,104
Mavvc 228,92,100
Mavvc 224,92,96
WV 13
B
Mavvc 0,0,32
Mavvc 4,0,36
Rv 64,141,0
Mavvc 8,0,40
T 96,144,16384
Mavvc 12,0,44
Mavvc 16,0,48
T 98,144,17408
Mavvc 20,0,52
Mavvc 24,0,56
Rv 68,141,2048
Mavvc 28,0,60
AM 35,0
Mavvc 60,4,60
D 236,28,33,0
Mavvc 56,4,56
T 100,145,16384
s_and_b32 s21, s12, 3
Mavvc 52,4,52
T 102,145,17408
Mavvc 48,4,48
s_mul_i32 s21, s21, 40960
Mavvc 44,4,44
Rv 72,141,4096
Mavvc 40,4,40
s_add_u32 s21, s21, s53
Mavvc 36,4,36
T 104,146,16384
Mavvc 32,4,32
AS 21,21,24576
D 237,28,33,1024
Mavvc 64,8,32
T 106,146,17408
Mavvc 68,8,36
v_min_u32 v249, 63, v254
Mavvc 72,8,40
Rv 76,141,6144
Mavvc 76,8,44
v_lshlrev_b32 v249, 7, v249
Mavvc 80,8,48
T 108,147,16384
Mavvc 84,8,52
buffer_load_dword v248, v249, s[48:51], s21 offen sc1
Mavvc 88,8,56
D 238,28,33,2048
Mavvc 92,8,60
T 110,147,17408
Mavvc 124,12,60
Rv 80,141,8192
Mavvc 120,12,56
T 112,148,16384
Mavvc 116,12,52
Mavvc 112,12,48
T 114,148,17408
Mavvc 108,12,44
Mavvc 104,12,40
D 239,28,33,3072
Mavvc 100,12,36
AS 33,33,262144
Mavvc 96,12,32
Rv 84,141,10240
Mavvc 128,16,32
T 116,149,16384
Mavvc 132,16,36
T 118,149,17408
Mavvc 136,16,40
Mavvc 140,16,44
Rv 88,141,12288
Mavvc 144,16,48
Mavvc 148,16,52
T 120,150,16384
Mavvc 152,16,56
Mavvc 156,16,60
T 122,150,17408
AS 55,53,8192
M0 94208
Mavvc 188,20,60
Ds 243,48,55,0
Mavvc 184,20,56
Rv 92,141,14336
Mavvc 180,20,52
Mavvc 176,20,48
T 124,151,16384
Mavvc 172,20,44
Mavvc 168,20,40
T 126,151,17408
Mavvc 164,20,36
Ds 243,48,55,1024
Mavvc 160,20,32
Mavvc 192,24,32
Mavvc 196,24,36
Mavvc 200,24,40
Mavvc 204,24,44
Mavvc 208,24,48
Mavvc 212,24,52
Ds 243,48,55,2048
Mavvc 216,24,56
Mavvc 220,24,60
Mavvc 252,28,60
Mavvc 248,28,56
Mavvc 244,28,52
Mavvc 240,28,48
Ds 243,48,55,3072
Mavvc 236,28,44
Mavvc 232,28,40
Mavvc 228,28,36
Mavvc 224,28,32
WV 13
B
Mavvc 0,64,96
Mavvc 4,64,100
Rv 0,140,32768
Mavvc 8,64,104
T 32,144,32768
Mavvc 12,64,108
Mavvc 16,64,112
T 34,144,33792
Mavvc 20,64,116
Mavvc 24,64,120
Rv 4,140,34816
Mavvc 28,64,124
s_add_u32 s55, s52, 0x0
M0 126976
Mavvc 60,68,124
T 36,145,32768
D 236,44,55,0
Mavvc 56,68,120
s_and_b32 s21, s12, 3
Mavvc 52,68,116
T 38,145,33792
Mavvc 48,68,112
s_mul_i32 s21, s21, 40960
Mavvc 44,68,108
Rv 8,140,36864
Mavvc 40,68,104
s_add_u32 s21, s21, s53
Mavvc 36,68,100
T 40,146,32768
Mavvc 32,68,96
AS 21,21,32768
D 237,44,55,1024
Mavvc 64,72,96
T 42,146,33792
Mavvc 68,72,100
v_min_u32 v249, 31, v254
Mavvc 72,72,104
Rv 12,140,38912
Mavvc 76,72,108
v_lshlrev_b32 v249, 7, v249
Mavvc 80,72,112
T 44,147,32768
Mavvc 84,72,116
buffer_load_dword v248, v249, s[48:51], s21 offen sc1
Mavvc 88,72,120
T 46,147,33792
D 238,44,55,2048
Mavvc 92,72,124
Mavvc 124,76,124
Rv 16,140,40960
Mavvc 120,76,120
T 48,148,32768
Mavvc 116,76,116
Mavvc 112,76,112
T 50,148,33792
Mavvc 108,76,108
Mavvc 104,76,104
Rv 20,140,43008
Mavvc 100,76,100
D 239,44,55,3072
Mavvc 96,76,96
T 52,149,32768
Mavvc 128,80,96
Mavvc 132,80,100
T 54,149,33792
Mavvc 136,80,104
Mavvc 140,80,108
Rv 24,140,45056
Mavvc 144,80,112
AS 55,53,16384
M0 24576
Mavvc 148,80,116
T 56,150,32768
Ds 243,48,55,0
Mavvc 152,80,120
Mavvc 156,80,124
T 58,150,33792
Mavvc 188,84,124
Mavvc 184,84,120
Rv 28,140,47104
Mavvc 180,84,116
Mavvc 176,84,112
T 60,151,32768
Mavvc 172,84,108
Ds 243,48,55,1024
Mavvc 168,84,104
T 62,151,33792
Mavvc 164,84,100
Mavvc 160,84,96
Mavvc 192,88,96
Mavvc 196,88,100
Mavvc 200,88,104
Mavvc 204,88,108
Ds 243,48,55,2048
Mavvc 208,88,112
Mavvc 212,88,116
Mavvc 216,88,120
Mavvc 220,88,124
Mavvc 252,92,124
Mavvc 248,92,120
Mavvc 244,92,116
Mavvc 240,92,112
Ds 243,48,55,3072
Mavvc 236,92,108
Mavvc 232,92,104
Mavvc 228,92,100
Mavvc 224,92,96
WV 13
B
Mavvc 0,0,32
Mavvc 4,0,36
Rv 64,141,32768
Mavvc 8,0,40
T 96,144,0
Mavvc 12,0,44
Mavvc 16,0,48
T 98,144,1024
Mavvc 20,0,52
Mavvc 24,0,56
Rv 68,141,34816
Mavvc 28,0,60
s_add_u32 s55, s52, 0x80000
M0 143360
Mavvc 60,4,60
T 100,145,0
D 236,44,55,0
Mavvc 56,4,56
Mavvc 52,4,52
T 102,145,1024
Mavvc 48,4,48
Mavvc 44,4,44
Rv 72,141,36864
Mavvc 40,4,40
Mavvc 36,4,36
T 104,146,0
Mavvc 32,4,32
D 237,44,55,1024
Mavvc 64,8,32
T 106,146,1024
Mavvc 68,8,36
Mavvc 72,8,40
Rv 76,141,38912
Mavvc 76,8,44
Mavvc 80,8,48
T 108,147,0
Mavvc 84,8,52
Mavvc 88,8,56
T 110,147,1024
D 238,44,55,2048
Mavvc 92,8,60
Mavvc 124,12,60
Rv 80,141,40960
Mavvc 120,12,56
T 112,148,0
Mavvc 116,12,52
Mavvc 112,12,48
T 114,148,1024
Mavvc 108,12,44
Mavvc 104,12,40
Rv 84,141,43008
Mavvc 100,12,36
D 239,44,55,3072
Mavvc 96,12,32
T 116,149,0
Mavvc 128,16,32
Mavvc 132,16,36
T 118,149,1024
Mavvc 136,16,40
Mavvc 140,16,44
Rv 88,141,45056
Mavvc 144,16,48
AS 55,53,24576
M0 28672
Mavvc 148,16,52
T 120,150,0
Ds 243,48,55,0
Mavvc 152,16,56
Mavvc 156,16,60
T 122,150,1024
Mavvc 188,20,60
Mavvc 184,20,56
Rv 92,141,47104
Mavvc 180,20,52
Mavvc 176,20,48
T 124,151,0
Mavvc 172,20,44
Ds 243,48,55,1024
Mavvc 168,20,40
T 126,151,1024
Mavvc 164,20,36
Mavvc 160,20,32
Mavvc 192,24,32
Mavvc 196,24,36
Mavvc 200,24,40
Mavvc 204,24,44
Ds 243,48,55,2048
Mavvc 208,24,48
Mavvc 212,24,52
Mavvc 216,24,56
Mavvc 220,24,60
Mavvc 252,28,60
Mavvc 248,28,56
Mavvc 244,28,52
Mavvc 240,28,48
Ds 243,48,55,3072
Mavvc 236,28,44
Mavvc 232,28,40
Mavvc 228,28,36
Mavvc 224,28,32
WV 12
B
Mavvc 0,64,96
Mavvc 4,64,100
Mavvc 8,64,104
Mavvc 12,64,108
Mavvc 16,64,112
Rv 192,245,0
Mavvc 20,64,116
s_add_u32 s55, s52, 0x100000
M0 110592
Mavvc 24,64,120
D 236,44,55,0
Mavvc 28,64,124
Rv 196,245,1024
Mavvc 60,68,124
Mavvc 56,68,120
Mavvc 52,68,116
Rv 200,245,2048
Mavvc 48,68,112
D 237,44,55,1024
Mavvc 44,68,108
Mavvc 40,68,104
Mavvc 36,68,100
Rv 204,245,3072
Mavvc 32,68,96
Mavvc 64,72,96
D 238,44,55,2048
Mavvc 68,72,100
T 128,240,16384
Mavvc 72,72,104
T 130,240,17408
Mavvc 76,72,108
Mavvc 80,72,112
X 242,32,240
T 132,242,16384
Mavvc 84,72,116
T 134,242,17408
D 239,44,55,3072
Mavvc 88,72,120
Mavvc 92,72,124
Mavvc 124,76,124
AS 55,53,32768
M0 57344
Mavvc 120,76,120
X 241,64,240
Ds 243,48,55,0
Mavvc 116,76,116
T 136,241,16384
Mavvc 112,76,112
T 138,241,17408
Mavvc 108,76,108
X 242,96,240
Mavvc 104,76,104
T 140,242,16384
T 142,242,17408
Mavvc 100,76,100
Ds 243,48,55,1024
Mavvc 96,76,96
X 241,128,240
Mavvc 128,80,96
T 144,241,16384
Mavvc 132,80,100
T 146,241,17408
Mavvc 136,80,104
X 242,160,240
T 148,242,16384
Mavvc 140,80,108
T 150,242,17408
Ds 243,48,55,2048
Mavvc 144,80,112
Mavvc 148,80,116
X 241,192,240
Mavvc 152,80,120
T 152,241,16384
Mavvc 156,80,124
T 154,241,17408
Mavvc 188,84,124
Ds 243,48,55,3072
Mavvc 184,84,120
X 242,224,240
Mavvc 180,84,116
T 156,242,16384
Mavvc 176,84,112
T 158,242,17408
Mavvc 172,84,108
AS 55,53,40960
M0 61440
Mavvc 168,84,104
Ds 243,48,55,0
Mavvc 164,84,100
Mavvc 160,84,96
Mavvc 192,88,96
Mavvc 196,88,100
Mavvc 200,88,104
Ds 243,48,55,1024
Mavvc 204,88,108
Mavvc 208,88,112
Mavvc 212,88,116
Mavvc 216,88,120
Ds 243,48,55,2048
Mavvc 220,88,124
Mavvc 252,92,124
Mavvc 248,92,120
Mavvc 244,92,116
Mavvc 240,92,112
Ds 243,48,55,3072
Mavvc 236,92,108
Mavvc 232,92,104
Mavvc 228,92,100
Mavvc 224,92,96
s_add_u32 s36, s14, 0x1400000
s_addc_u32 s37, s15, 0
SM 38,0x1000000
SM 39,0x20000
s_lshl_b32 s16, s12, 19
s_lshl_b32 s17, s13, 16
s_add_u32 s40, s16, s17
AS 40,40,32768
s_add_u32 s24, s14, 0x400000
s_addc_u32 s25, s15, 0
SM 26,0x2000000
SM 27,0x20000
s_add_u32 s28, s14, 0x1400000
s_addc_u32 s29, s15, 0
SM 30,0x1000000
SM 31,0x20000
s_lshl_b32 s16, s12, 19
s_lshl_b32 s17, s11, 12
s_add_u32 s56, s16, s17
s_mov_b32 s57, s56
AS 58,17,0
AS 59,17,65536
AS 56,56,32768
QS3%=:
W 0
Mvvvz 0,128,192
Rv 208,245,4096
Mvvvz 4,128,196
Rv 212,245,5120
Mvvvz 8,128,200
Mvvvz 12,128,204
Rv 216,245,6144
Mvvvz 28,132,204
Mvvvz 24,132,200
Rv 220,245,7168
Mvvvz 20,132,196
Mvvvz 16,132,192
T 160,240,24576
Mvvvz 32,136,192
T 162,240,25600
Mvvvz 36,136,196
X 242,32,240
Mvvvz 40,136,200
T 164,242,24576
Mvvvz 44,136,204
T 166,242,25600
Mvvvz 60,140,204
X 241,64,240
Mvvvz 56,140,200
T 168,241,24576
Mvvvz 52,140,196
T 170,241,25600
Mvvvz 48,140,192
X 242,96,240
Mvvvz 64,144,192
T 172,242,24576
Mvvvz 68,144,196
T 174,242,25600
Mvvvz 72,144,200
X 241,128,240
Mvvvz 76,144,204
T 176,241,24576
Mvvvz 92,148,204
T 178,241,25600
Mvvvz 88,148,200
X 242,160,240
Mvvvz 84,148,196
T 180,242,24576
Mvvvz 80,148,192
T 182,242,25600
Mvvvz 96,152,192
X 241,192,240
Mvvvz 100,152,196
T 184,241,24576
Mvvvz 104,152,200
T 186,241,25600
Mvvvz 108,152,204
X 242,224,240
Mvvvz 124,156,204
T 188,242,24576
Mvvvz 120,156,200
T 190,242,25600
Mvvvz 116,156,196
)A"
R"A(Mvvvz 112,156,192
WV 12
B
Mvvvc 0,160,208
Rv 192,244,0
Mvvvc 4,160,212
Rv 196,244,1024
Mvvvc 8,160,216
s_add_u32 s55, s52, 0x180000
M0 126976
Mvvvc 12,160,220
D 236,44,55,0
Mvvvc 28,164,220
Rv 200,244,2048
Mvvvc 24,164,216
Rv 204,244,3072
Mvvvc 20,164,212
D 237,44,55,1024
Mvvvc 16,164,208
T 128,240,32768
T 130,240,33792
Mvvvc 32,168,208
D 238,44,55,2048
Mvvvc 36,168,212
X 242,32,240
T 132,242,32768
Mvvvc 40,168,216
T 134,242,33792
D 239,44,55,3072
Mvvvc 44,168,220
X 241,64,240
T 136,241,32768
T 138,241,33792
Mvvvc 60,172,220
s_add_u32 s55, s52, 0x140000
M0 118784
Mvvvc 56,172,216
X 242,96,240
D 236,44,55,0
Mvvvc 52,172,212
T 140,242,32768
T 142,242,33792
Mvvvc 48,172,208
X 241,128,240
D 237,44,55,1024
Mvvvc 64,176,208
T 144,241,32768
T 146,241,33792
Mvvvc 68,176,212
X 242,160,240
D 238,44,55,2048
Mvvvc 72,176,216
T 148,242,32768
Mvvvc 76,176,220
T 150,242,33792
X 241,192,240
Mvvvc 92,180,220
T 152,241,32768
T 154,241,33792
D 239,44,55,3072
Mvvvc 88,180,216
X 242,224,240
AS 55,53,49152
M0 90112
Mvvvc 84,180,212
T 156,242,32768
Ds 243,48,55,0
Mvvvc 80,180,208
T 158,242,33792
Mvvvc 96,184,208
Ds 243,48,55,1024
Mvvvc 100,184,212
Mvvvc 104,184,216
Mvvvc 108,184,220
Ds 243,48,55,2048
Mvvvc 124,188,220
Mvvvc 120,188,216
Ds 243,48,55,3072
Mvvvc 116,188,212
Mvvvc 112,188,208
W 0
Mvvvc 0,128,192
Rv 208,244,4096
Mvvvc 4,128,196
Rv 212,244,5120
Mvvvc 8,128,200
AS 55,54,49152
M0 73728
Mvvvc 12,128,204
Ds 243,48,55,0
Mvvvc 28,132,204
Rv 216,244,6144
Mvvvc 24,132,200
Rv 220,244,7168
Mvvvc 20,132,196
Ds 243,48,55,1024
Mvvvc 16,132,192
T 160,240,40960
T 162,240,41984
Mvvvc 32,136,192
Ds 243,48,55,2048
Mvvvc 36,136,196
X 242,32,240
T 164,242,40960
Mvvvc 40,136,200
T 166,242,41984
Ds 243,48,55,3072
Mvvvc 44,136,204
X 241,64,240
T 168,241,40960
T 170,241,41984
Mvvvc 60,140,204
AS 55,53,57344
M0 94208
Mvvvc 56,140,200
X 242,96,240
Ds 243,48,55,0
Mvvvc 52,140,196
T 172,242,40960
T 174,242,41984
Mvvvc 48,140,192
X 241,128,240
Ds 243,48,55,1024
Mvvvc 64,144,192
T 176,241,40960
T 178,241,41984
Mvvvc 68,144,196
X 242,160,240
Ds 243,48,55,2048
Mvvvc 72,144,200
T 180,242,40960
Mvvvc 76,144,204
T 182,242,41984
X 241,192,240
Mvvvc 92,148,204
T 184,241,40960
T 186,241,41984
Ds 243,48,55,3072
Mvvvc 88,148,200
X 242,224,240
AS 55,54,57344
M0 77824
Mvvvc 84,148,196
T 188,242,40960
Ds 243,48,55,0
Mvvvc 80,148,192
T 190,242,41984
Mvvvc 96,152,192
Ds 243,48,55,1024
Mvvvc 100,152,196
Mvvvc 104,152,200
Mvvvc 108,152,204
Ds 243,48,55,2048
Mvvvc 124,156,204
Mvvvc 120,156,200
Ds 243,48,55,3072
Mvvvc 116,156,196
Mvvvc 112,156,192
WV 24
B
Mvvvc 0,160,208
Rv 192,244,32768
Mvvvc 4,160,212
Rv 196,244,33792
Mvvvc 8,160,216
s_add_u32 s55, s52, 0x200000
M0 143360
Mvvvc 12,160,220
D 236,44,55,0
Mvvvc 28,164,220
Rv 200,244,34816
Mvvvc 24,164,216
Rv 204,244,35840
Mvvvc 20,164,212
D 237,44,55,1024
Mvvvc 16,164,208
T 128,240,0
T 130,240,1024
Mvvvc 32,168,208
D 238,44,55,2048
Mvvvc 36,168,212
X 242,32,240
T 132,242,0
Mvvvc 40,168,216
T 134,242,1024
D 239,44,55,3072
Mvvvc 44,168,220
X 241,64,240
T 136,241,0
T 138,241,1024
Mvvvc 60,172,220
s_add_u32 s55, s52, 0x1c0000
M0 135168
Mvvvc 56,172,216
X 242,96,240
D 236,44,55,0
Mvvvc 52,172,212
T 140,242,0
T 142,242,1024
Mvvvc 48,172,208
X 241,128,240
D 237,44,55,1024
Mvvvc 64,176,208
T 144,241,0
T 146,241,1024
Mvvvc 68,176,212
X 242,160,240
D 238,44,55,2048
Mvvvc 72,176,216
T 148,242,0
Mvvvc 76,176,220
T 150,242,1024
X 241,192,240
Mvvvc 92,180,220
T 152,241,0
T 154,241,1024
D 239,44,55,3072
Mvvvc 88,180,216
X 242,224,240
AS 55,53,65536
M0 24576
Mvvvc 84,180,212
T 156,242,0
Ds 243,48,55,0
Mvvvc 80,180,208
T 158,242,1024
Mvvvc 96,184,208
Ds 243,48,55,1024
Mvvvc 100,184,212
Mvvvc 104,184,216
Mvvvc 108,184,220
Ds 243,48,55,2048
Mvvvc 124,188,220
Mvvvc 120,188,216
Ds 243,48,55,3072
Mvvvc 116,188,212
Mvvvc 112,188,208
W 0
Mvvvc 0,128,192
Rv 208,244,36864
Mvvvc 4,128,196
Rv 212,244,37888
Mvvvc 8,128,200
AS 55,54,65536
M0 8192
Mvvvc 12,128,204
Ds 243,48,55,0
Mvvvc 28,132,204
Rv 216,244,38912
Mvvvc 24,132,200
Rv 220,244,39936
Mvvvc 20,132,196
Ds 243,48,55,1024
Mvvvc 16,132,192
T 160,240,8192
T 162,240,9216
Mvvvc 32,136,192
Ds 243,48,55,2048
Mvvvc 36,136,196
X 242,32,240
T 164,242,8192
Mvvvc 40,136,200
T 166,242,9216
Ds 243,48,55,3072
Mvvvc 44,136,204
X 241,64,240
T 168,241,8192
T 170,241,9216
Mvvvc 60,140,204
AS 55,53,73728
M0 28672
Mvvvc 56,140,200
X 242,96,240
Ds 243,48,55,0
Mvvvc 52,140,196
T 172,242,8192
T 174,242,9216
Mvvvc 48,140,192
X 241,128,240
Ds 243,48,55,1024
Mvvvc 64,144,192
T 176,241,8192
T 178,241,9216
Mvvvc 68,144,196
X 242,160,240
Ds 243,48,55,2048
Mvvvc 72,144,200
T 180,242,8192
Mvvvc 76,144,204
T 182,242,9216
X 241,192,240
Mvvvc 92,148,204
T 184,241,8192
T 186,241,9216
Ds 243,48,55,3072
Mvvvc 88,148,200
X 242,224,240
AS 55,54,73728
M0 12288
Mvvvc 84,148,196
T 188,242,8192
Ds 243,48,55,0
Mvvvc 80,148,192
T 190,242,9216
Mvvvc 96,152,192
Ds 243,48,55,1024
Mvvvc 100,152,196
Mvvvc 104,152,200
Mvvvc 108,152,204
Ds 243,48,55,2048
Mvvvc 124,156,204
Mvvvc 120,156,200
Ds 243,48,55,3072
Mvvvc 116,156,196
Mvvvc 112,156,192
WV 24
B
Mvvvc 0,160,208
Rv 192,245,0
Mvvvc 4,160,212
Rv 196,245,1024
Mvvvc 8,160,216
s_add_u32 s55, s52, 0x280000
M0 110592
Mvvvc 12,160,220
D 236,44,55,0
Mvvvc 28,164,220
Rv 200,245,2048
Mvvvc 24,164,216
Rv 204,245,3072
Mvvvc 20,164,212
D 237,44,55,1024
Mvvvc 16,164,208
T 128,240,16384
T 130,240,17408
Mvvvc 32,168,208
D 238,44,55,2048
Mvvvc 36,168,212
X 242,32,240
T 132,242,16384
Mvvvc 40,168,216
T 134,242,17408
D 239,44,55,3072
Mvvvc 44,168,220
X 241,64,240
T 136,241,16384
T 138,241,17408
Mvvvc 60,172,220
s_add_u32 s55, s52, 0x240000
M0 102400
Mvvvc 56,172,216
X 242,96,240
D 236,44,55,0
Mvvvc 52,172,212
T 140,242,16384
T 142,242,17408
Mvvvc 48,172,208
X 241,128,240
D 237,44,55,1024
Mvvvc 64,176,208
T 144,241,16384
T 146,241,17408
Mvvvc 68,176,212
X 242,160,240
D 238,44,55,2048
Mvvvc 72,176,216
T 148,242,16384
Mvvvc 76,176,220
T 150,242,17408
X 241,192,240
Mvvvc 92,180,220
T 152,241,16384
T 154,241,17408
D 239,44,55,3072
Mvvvc 88,180,216
X 242,224,240
AS 55,53,81920
M0 57344
Mvvvc 84,180,212
T 156,242,16384
Ds 243,48,55,0
Mvvvc 80,180,208
T 158,242,17408
Mvvvc 96,184,208
Ds 243,48,55,1024
Mvvvc 100,184,212
Mvvvc 104,184,216
Mvvvc 108,184,220
Ds 243,48,55,2048
Mvvvc 124,188,220
Mvvvc 120,188,216
Ds 243,48,55,3072
Mvvvc 116,188,212
Mvvvc 112,188,208
W 0
Mvvvc 0,128,192
Rv 208,245,4096
Mvvvc 4,128,196
Rv 212,245,5120
Mvvvc 8,128,200
AS 55,54,81920
M0 40960
Mvvvc 12,128,204
Ds 243,48,55,0
Mvvvc 28,132,204
Rv 216,245,6144
Mvvvc 24,132,200
Rv 220,245,7168
Mvvvc 20,132,196
Ds 243,48,55,1024
Mvvvc 16,132,192
T 160,240,24576
T 162,240,25600
Mvvvc 32,136,192
Ds 243,48,55,2048
Mvvvc 36,136,196
X 242,32,240
T 164,242,24576
Mvvvc 40,136,200
T 166,242,25600
Ds 243,48,55,3072
Mvvvc 44,136,204
X 241,64,240
T 168,241,24576
T 170,241,25600
Mvvvc 60,140,204
AS 55,53,90112
M0 61440
Mvvvc 56,140,200
X 242,96,240
Ds 243,48,55,0
Mvvvc 52,140,196
T 172,242,24576
T 174,242,25600
Mvvvc 48,140,192
X 241,128,240
Ds 243,48,55,1024
Mvvvc 64,144,192
T 176,241,24576
T 178,241,25600
Mvvvc 68,144,196
X 242,160,240
Ds 243,48,55,2048
Mvvvc 72,144,200
T 180,242,24576
Mvvvc 76,144,204
T 182,242,25600
X 241,192,240
Mvvvc 92,148,204
T 184,241,24576
T 186,241,25600
Ds 243,48,55,3072
Mvvvc 88,148,200
X 242,224,240
AS 55,54,90112
M0 45056
Mvvvc 84,148,196
T 188,242,24576
Ds 243,48,55,0
Mvvvc 80,148,192
T 190,242,25600
Mvvvc 96,152,192
Ds 243,48,55,1024
Mvvvc 100,152,196
Mvvvc 104,152,200
Mvvvc 108,152,204
Ds 243,48,55,2048
Mvvvc 124,156,204
Mvvvc 120,156,200
Ds 243,48,55,3072
Mvvvc 116,156,196
Mvvvc 112,156,192
WV 24
B
Mvvvc 0,160,208
Rv 192,244,0
Mvvvc 4,160,212
Rv 196,244,1024
Mvvvc 8,160,216
s_add_u32 s55, s52, 0x300000
M0 126976
Mvvvc 12,160,220
D 236,44,55,0
Mvvvc 28,164,220
Rv 200,244,2048
Mvvvc 24,164,216
Rv 204,244,3072
Mvvvc 20,164,212
D 237,44,55,1024
Mvvvc 16,164,208
T 128,240,32768
T 130,240,33792
Mvvvc 32,168,208
D 238,44,55,2048
Mvvvc 36,168,212
X 242,32,240
T 132,242,32768
Mvvvc 40,168,216
T 134,242,33792
D 239,44,55,3072
Mvvvc 44,168,220
X 241,64,240
T 136,241,32768
T 138,241,33792
Mvvvc 60,172,220
s_add_u32 s55, s52, 0x2c0000
M0 118784
Mvvvc 56,172,216
X 242,96,240
D 236,44,55,0
Mvvvc 52,172,212
T 140,242,32768
T 142,242,33792
Mvvvc 48,172,208
X 241,128,240
D 237,44,55,1024
Mvvvc 64,176,208
T 144,241,32768
T 146,241,33792
Mvvvc 68,176,212
X 242,160,240
D 238,44,55,2048
Mvvvc 72,176,216
T 148,242,32768
Mvvvc 76,176,220
T 150,242,33792
X 241,192,240
Mvvvc 92,180,220
T 152,241,32768
T 154,241,33792
D 239,44,55,3072
Mvvvc 88,180,216
X 242,224,240
AS 55,53,98304
M0 90112
Mvvvc 84,180,212
T 156,242,32768
Ds 243,48,55,0
Mvvvc 80,180,208
T 158,242,33792
Mvvvc 96,184,208
Ds 243,48,55,1024
Mvvvc 100,184,212
Mvvvc 104,184,216
Mvvvc 108,184,220
Ds 243,48,55,2048
Mvvvc 124,188,220
Mvvvc 120,188,216
Ds 243,48,55,3072
Mvvvc 116,188,212
Mvvvc 112,188,208
W 0
Mvvvc 0,128,192
Rv 208,244,4096
Mvvvc 4,128,196
Rv 212,244,5120
Mvvvc 8,128,200
AS 55,54,98304
M0 73728
Mvvvc 12,128,204
Ds 243,48,55,0
Mvvvc 28,132,204
Rv 216,244,6144
Mvvvc 24,132,200
Rv 220,244,7168
Mvvvc 20,132,196
Ds 243,48,55,1024
Mvvvc 16,132,192
T 160,240,40960
T 162,240,41984
Mvvvc 32,136,192
Ds 243,48,55,2048
Mvvvc 36,136,196
X 242,32,240
T 164,242,40960
Mvvvc 40,136,200
T 166,242,41984
Ds 243,48,55,3072
Mvvvc 44,136,204
X 241,64,240
T 168,241,40960
T 170,241,41984
Mvvvc 60,140,204
AS 55,53,106496
M0 94208
Mvvvc 56,140,200
X 242,96,240
Ds 243,48,55,0
Mvvvc 52,140,196
T 172,242,40960
T 174,242,41984
Mvvvc 48,140,192
X 241,128,240
Ds 243,48,55,1024
Mvvvc 64,144,192
T 176,241,40960
T 178,241,41984
Mvvvc 68,144,196
X 242,160,240
Ds 243,48,55,2048
Mvvvc 72,144,200
T 180,242,40960
Mvvvc 76,144,204
T 182,242,41984
X 241,192,240
Mvvvc 92,148,204
T 184,241,40960
T 186,241,41984
Ds 243,48,55,3072
Mvvvc 88,148,200
X 242,224,240
AS 55,54,106496
M0 77824
Mvvvc 84,148,196
T 188,242,40960
Ds 243,48,55,0
Mvvvc 80,148,192
T 190,242,41984
Mvvvc 96,152,192
Ds 243,48,55,1024
Mvvvc 100,152,196
Mvvvc 104,152,200
Mvvvc 108,152,204
Ds 243,48,55,2048
Mvvvc 124,156,204
Mvvvc 120,156,200
Ds 243,48,55,3072
Mvvvc 116,156,196
Mvvvc 112,156,192
WV 24
B
Mvvvc 0,160,208
Rv 192,244,32768
G 224,0
Mvvvc 4,160,212
Rv 196,244,33792
G 225,1
Mvvvc 8,160,216
G 226,2
G 227,3
Mvvvc 12,160,220
Rv 200,244,34816
G 228,32
Mvvvc 28,164,220
G 229,33
G 230,34
Mvvvc 24,164,216
Rv 204,244,35840
G 231,35
Mvvvc 20,164,212
C 232,224,225
C 233,226,227
Mvvvc 16,164,208
T 128,240,0
C 234,228,229
Mvvvc 32,168,208
T 130,240,1024
C 235,230,231
Mvvvc 36,168,212
X 242,32,240
Svs 232,243,36,40,0
Mvvvc 40,168,216
T 132,242,0
G 224,4
Mvvvc 44,168,220
T 134,242,1024
G 225,5
Mvvvc 60,172,220
X 241,64,240
G 226,6
Mvvvc 56,172,216
T 136,241,0
G 227,7
Mvvvc 52,172,212
T 138,241,1024
G 228,36
Mvvvc 48,172,208
X 242,96,240
G 229,37
Mvvvc 64,176,208
T 140,242,0
G 230,38
Mvvvc 68,176,212
T 142,242,1024
G 231,39
Mvvvc 72,176,216
X 241,128,240
C 232,224,225
Mvvvc 76,176,220
T 144,241,0
C 233,226,227
Mvvvc 92,180,220
T 146,241,1024
C 234,228,229
Mvvvc 88,180,216
X 242,160,240
C 235,230,231
Mvvvc 84,180,212
T 148,242,0
Svs 232,243,36,40,1024
Mvvvc 80,180,208
T 150,242,1024
G 224,8
Mvvvc 96,184,208
X 241,192,240
G 225,9
Mvvvc 100,184,212
T 152,241,0
G 226,10
Mvvvc 104,184,216
T 154,241,1024
G 227,11
Mvvvc 108,184,220
X 242,224,240
G 228,40
Mvvvc 124,188,220
T 156,242,0
G 229,41
Mvvvc 120,188,216
T 158,242,1024
G 230,42
Mvvvc 116,188,212
G 231,43
Mvvvc 112,188,208
C 232,224,225
C 233,226,227
C 234,228,229
C 235,230,231
Svs 232,243,36,40,2048
G 224,12
G 225,13
G 226,14
G 227,15
G 228,44
G 229,45
G 230,46
G 231,47
C 232,224,225
C 233,226,227
C 234,228,229
C 235,230,231
Svs 232,243,36,40,3072
W 0
Mvvvc 0,128,192
Rv 208,244,36864
AS 40,40,4096
Mvvvc 4,128,196
Rv 212,244,37888
G 224,16
Mvvvc 8,128,200
G 225,17
G 226,18
Mvvvc 12,128,204
Rv 216,244,38912
G 227,19
Mvvvc 28,132,204
G 228,48
G 229,49
Mvvvc 24,132,200
Rv 220,244,39936
G 230,50
Mvvvc 20,132,196
G 231,51
C 232,224,225
Mvvvc 16,132,192
T 160,240,8192
C 233,226,227
Mvvvc 32,136,192
T 162,240,9216
C 234,228,229
Mvvvc 36,136,196
X 242,32,240
C 235,230,231
Mvvvc 40,136,200
)A"
R"A(T 164,242,8192
Svs 232,243,36,40,0
Mvvvc 44,136,204
T 166,242,9216
G 224,20
Mvvvc 60,140,204
X 241,64,240
G 225,21
Mvvvc 56,140,200
T 168,241,8192
G 226,22
Mvvvc 52,140,196
T 170,241,9216
G 227,23
Mvvvc 48,140,192
X 242,96,240
G 228,52
Mvvvc 64,144,192
T 172,242,8192
G 229,53
Mvvvc 68,144,196
T 174,242,9216
G 230,54
Mvvvc 72,144,200
X 241,128,240
G 231,55
Mvvvc 76,144,204
T 176,241,8192
C 232,224,225
Mvvvc 92,148,204
T 178,241,9216
C 233,226,227
Mvvvc 88,148,200
X 242,160,240
C 234,228,229
Mvvvc 84,148,196
T 180,242,8192
C 235,230,231
Mvvvc 80,148,192
T 182,242,9216
Svs 232,243,36,40,1024
Mvvvc 96,152,192
X 241,192,240
G 224,24
Mvvvc 100,152,196
T 184,241,8192
G 225,25
Mvvvc 104,152,200
T 186,241,9216
G 226,26
Mvvvc 108,152,204
X 242,224,240
G 227,27
Mvvvc 124,156,204
T 188,242,8192
G 228,56
Mvvvc 120,156,200
T 190,242,9216
G 229,57
Mvvvc 116,156,196
G 230,58
Mvvvc 112,156,192
G 231,59
C 232,224,225
C 233,226,227
C 234,228,229
C 235,230,231
Svs 232,243,36,40,2048
G 224,28
G 225,29
G 226,30
G 227,31
G 228,60
G 229,61
G 230,62
G 231,63
C 232,224,225
C 233,226,227
C 234,228,229
C 235,230,231
Svs 232,243,36,40,3072
WV 8
B
s_getpc_b64 s[16:17]
1:
s_add_u32 s16, s16, QE3%=-1b
s_addc_u32 s17, s17, 0
s_lshr_b32 s21, s10, 3
v_lshlrev_b32 v249, 5, v254
v_add_u32 v249, s21, v249
v_min_u32 v249, ((AE3%=-QE3%=)>>7), v249
v_lshlrev_b32 v249, 7, v249
global_load_dword v248, v249, s[16:17]
Mvvvc 0,160,208
Rv 192,245,0
AS 40,40,4096
Mvvvc 4,160,212
Rv 196,245,1024
G 224,64
Mvvvc 8,160,216
G 225,65
G 226,66
Mvvvc 12,160,220
Rv 200,245,2048
Mvvvc 28,164,220
G 227,67
G 228,96
Mvvvc 24,164,216
Rv 204,245,3072
G 229,97
Mvvvc 20,164,212
G 230,98
G 231,99
Mvvvc 16,164,208
T 128,240,16384
C 232,224,225
Mvvvc 32,168,208
T 130,240,17408
C 233,226,227
Mvvvc 36,168,212
X 242,32,240
C 234,228,229
Mvvvc 40,168,216
T 132,242,16384
C 235,230,231
Mvvvc 44,168,220
T 134,242,17408
Svs 232,243,36,40,0
Mvvvc 60,172,220
X 241,64,240
G 224,68
Mvvvc 56,172,216
T 136,241,16384
G 225,69
Mvvvc 52,172,212
T 138,241,17408
G 226,70
Mvvvc 48,172,208
X 242,96,240
G 227,71
Mvvvc 64,176,208
T 140,242,16384
G 228,100
Mvvvc 68,176,212
T 142,242,17408
G 229,101
Mvvvc 72,176,216
X 241,128,240
G 230,102
Mvvvc 76,176,220
T 144,241,16384
G 231,103
Mvvvc 92,180,220
T 146,241,17408
C 232,224,225
Mvvvc 88,180,216
X 242,160,240
C 233,226,227
Mvvvc 84,180,212
T 148,242,16384
C 234,228,229
Mvvvc 80,180,208
T 150,242,17408
C 235,230,231
Mvvvc 96,184,208
X 241,192,240
Svs 232,243,36,40,1024
Mvvvc 100,184,212
T 152,241,16384
G 224,72
Mvvvc 104,184,216
T 154,241,17408
G 225,73
Mvvvc 108,184,220
X 242,224,240
G 226,74
Mvvvc 124,188,220
T 156,242,16384
G 227,75
Mvvvc 120,188,216
T 158,242,17408
G 228,104
Mvvvc 116,188,212
G 229,105
Mvvvc 112,188,208
G 230,106
G 231,107
C 232,224,225
C 233,226,227
C 234,228,229
C 235,230,231
Svs 232,243,36,40,2048
W 0
s_load_dwordx2 s[42:43], s[8:9], 40
Mvvvc 0,128,192
Rv 208,245,4096
G 224,76
Mvvvc 4,128,196
Rv 212,245,5120
G 225,77
Mvvvc 8,128,200
G 226,78
G 227,79
Mvvvc 12,128,204
Rv 216,245,6144
G 228,108
Mvvvc 28,132,204
G 229,109
G 230,110
Mvvvc 24,132,200
Rv 220,245,7168
G 231,111
Mvvvc 20,132,196
C 232,224,225
C 233,226,227
Mvvvc 16,132,192
T 160,240,24576
C 234,228,229
Mvvvc 32,136,192
T 162,240,25600
C 235,230,231
Mvvvc 36,136,196
X 242,32,240
Svs 232,243,36,40,3072
Mvvvc 40,136,200
T 164,242,24576
AS 40,40,4096
Mvvvc 44,136,204
T 166,242,25600
G 224,80
Mvvvc 60,140,204
X 241,64,240
G 225,81
Mvvvc 56,140,200
T 168,241,24576
G 226,82
Mvvvc 52,140,196
T 170,241,25600
G 227,83
Mvvvc 48,140,192
X 242,96,240
G 228,112
Mvvvc 64,144,192
T 172,242,24576
G 229,113
Mvvvc 68,144,196
T 174,242,25600
G 230,114
Mvvvc 72,144,200
X 241,128,240
G 231,115
Mvvvc 76,144,204
T 176,241,24576
C 232,224,225
Mvvvc 92,148,204
T 178,241,25600
C 233,226,227
Mvvvc 88,148,200
X 242,160,240
C 234,228,229
Mvvvc 84,148,196
T 180,242,24576
C 235,230,231
Mvvvc 80,148,192
T 182,242,25600
Svs 232,243,36,40,0
Mvvvc 96,152,192
X 241,192,240
G 224,84
Mvvvc 100,152,196
T 184,241,24576
G 225,85
Mvvvc 104,152,200
T 186,241,25600
G 226,86
Mvvvc 108,152,204
X 242,224,240
G 227,87
Mvvvc 124,156,204
T 188,242,24576
G 228,116
Mvvvc 120,156,200
T 190,242,25600
G 229,117
Mvvvc 116,156,196
G 230,118
Mvvvc 112,156,192
G 231,119
C 232,224,225
C 233,226,227
C 234,228,229
C 235,230,231
Svs 232,243,36,40,1024
G 224,88
G 225,89
G 226,90
G 227,91
G 228,120
G 229,121
G 230,122
G 231,123
C 232,224,225
C 233,226,227
C 234,228,229
C 235,230,231
Svs 232,243,36,40,2048
W 0
B
Mvvvc 0,160,208
Rv 192,244,0
G 224,92
Mvvvc 4,160,212
Rv 196,244,1024
G 225,93
Mvvvc 8,160,216
G 226,94
G 227,95
Mvvvc 12,160,220
Rv 200,244,2048
Mvvvc 28,164,220
G 228,124
G 229,125
Mvvvc 24,164,216
Rv 204,244,3072
G 230,126
Mvvvc 20,164,212
G 231,127
C 232,224,225
Mvvvc 16,164,208
T 128,240,32768
C 233,226,227
Mvvvc 32,168,208
T 130,240,33792
C 234,228,229
Mvvvc 36,168,212
X 242,32,240
C 235,230,231
Mvvvc 40,168,216
T 132,242,32768
Svs 232,243,36,40,3072
Mvvvc 44,168,220
T 134,242,33792
AS 40,40,4096
Mvvvc 60,172,220
X 241,64,240
G 224,128
Mvvvc 56,172,216
T 136,241,32768
G 225,129
Mvvvc 52,172,212
T 138,241,33792
G 226,130
Mvvvc 48,172,208
X 242,96,240
G 227,131
Mvvvc 64,176,208
T 140,242,32768
G 228,160
Mvvvc 68,176,212
T 142,242,33792
G 229,161
Mvvvc 72,176,216
X 241,128,240
G 230,162
Mvvvc 76,176,220
T 144,241,32768
G 231,163
Mvvvc 92,180,220
T 146,241,33792
C 232,224,225
Mvvvc 88,180,216
X 242,160,240
C 233,226,227
Mvvvc 84,180,212
T 148,242,32768
C 234,228,229
Mvvvc 80,180,208
T 150,242,33792
C 235,230,231
Mvvvc 96,184,208
X 241,192,240
Svs 232,243,36,40,0
Mvvvc 100,184,212
T 152,241,32768
G 224,132
Mvvvc 104,184,216
T 154,241,33792
G 225,133
Mvvvc 108,184,220
X 242,224,240
G 226,134
Mvvvc 124,188,220
T 156,242,32768
G 227,135
Mvvvc 120,188,216
T 158,242,33792
G 228,164
Mvvvc 116,188,212
G 229,165
Mvvvc 112,188,208
G 230,166
G 231,167
C 232,224,225
C 233,226,227
C 234,228,229
C 235,230,231
Svs 232,243,36,40,1024
W 0
Mvvvc 0,128,192
Rv 208,244,4096
G 224,136
Mvvvc 4,128,196
Rv 212,244,5120
G 225,137
Mvvvc 8,128,200
G 226,138
G 227,139
Mvvvc 12,128,204
Rv 216,244,6144
G 228,168
Mvvvc 28,132,204
G 229,169
G 230,170
Mvvvc 24,132,200
Rv 220,244,7168
G 231,171
Mvvvc 20,132,196
C 232,224,225
C 233,226,227
Mvvvc 16,132,192
T 160,240,40960
C 234,228,229
Mvvvc 32,136,192
T 162,240,41984
C 235,230,231
Mvvvc 36,136,196
X 242,32,240
Svs 232,243,36,40,2048
Mvvvc 40,136,200
T 164,242,40960
G 224,140
Mvvvc 44,136,204
T 166,242,41984
G 225,141
Mvvvc 60,140,204
X 241,64,240
G 226,142
Mvvvc 56,140,200
T 168,241,40960
G 227,143
Mvvvc 52,140,196
T 170,241,41984
G 228,172
Mvvvc 48,140,192
X 242,96,240
G 229,173
Mvvvc 64,144,192
T 172,242,40960
G 230,174
Mvvvc 68,144,196
T 174,242,41984
G 231,175
Mvvvc 72,144,200
X 241,128,240
C 232,224,225
Mvvvc 76,144,204
T 176,241,40960
C 233,226,227
Mvvvc 92,148,204
T 178,241,41984
C 234,228,229
Mvvvc 88,148,200
X 242,160,240
C 235,230,231
Mvvvc 84,148,196
T 180,242,40960
Svs 232,243,36,40,3072
Mvvvc 80,148,192
T 182,242,41984
AS 40,40,4096
Mvvvc 96,152,192
X 241,192,240
G 224,144
Mvvvc 100,152,196
T 184,241,40960
G 225,145
Mvvvc 104,152,200
T 186,241,41984
G 226,146
Mvvvc 108,152,204
X 242,224,240
G 227,147
Mvvvc 124,156,204
T 188,242,40960
G 228,176
Mvvvc 120,156,200
T 190,242,41984
G 229,177
Mvvvc 116,156,196
G 230,178
Mvvvc 112,156,192
G 231,179
C 232,224,225
C 233,226,227
C 234,228,229
C 235,230,231
Svs 232,243,36,40,0
G 224,148
G 225,149
G 226,150
G 227,151
G 228,180
G 229,181
G 230,182
G 231,183
C 232,224,225
C 233,226,227
C 234,228,229
C 235,230,231
Svs 232,243,36,40,1024
W 0
B
Mvvvc 0,160,208
Rv 192,244,32768
G 224,152
Mvvvc 4,160,212
Rv 196,244,33792
G 225,153
Mvvvc 8,160,216
G 226,154
G 227,155
Mvvvc 12,160,220
Rv 200,244,34816
Mvvvc 28,164,220
G 228,184
G 229,185
Mvvvc 24,164,216
Rv 204,244,35840
G 230,186
Mvvvc 20,164,212
G 231,187
C 232,224,225
Mvvvc 16,164,208
T 128,240,0
C 233,226,227
Mvvvc 32,168,208
T 130,240,1024
C 234,228,229
Mvvvc 36,168,212
X 242,32,240
C 235,230,231
Mvvvc 40,168,216
T 132,242,0
Svs 232,243,36,40,2048
Mvvvc 44,168,220
T 134,242,1024
G 224,156
Mvvvc 60,172,220
X 241,64,240
G 225,157
Mvvvc 56,172,216
T 136,241,0
G 226,158
Mvvvc 52,172,212
T 138,241,1024
G 227,159
Mvvvc 48,172,208
X 242,96,240
G 228,188
Mvvvc 64,176,208
T 140,242,0
G 229,189
Mvvvc 68,176,212
T 142,242,1024
G 230,190
Mvvvc 72,176,216
X 241,128,240
G 231,191
Mvvvc 76,176,220
T 144,241,0
C 232,224,225
Mvvvc 92,180,220
T 146,241,1024
C 233,226,227
Mvvvc 88,180,216
X 242,160,240
C 234,228,229
Mvvvc 84,180,212
T 148,242,0
C 235,230,231
Mvvvc 80,180,208
T 150,242,1024
Svs 232,243,36,40,3072
Mvvvc 96,184,208
X 241,192,240
AS 40,40,4096
Mvvvc 100,184,212
T 152,241,0
G 224,192
Mvvvc 104,184,216
T 154,241,1024
G 225,193
Mvvvc 108,184,220
X 242,224,240
G 226,194
Mvvvc 124,188,220
T 156,242,0
G 227,195
Mvvvc 120,188,216
T 158,242,1024
G 228,224
Mvvvc 116,188,212
G 229,225
Mvvvc 112,188,208
G 230,226
G 231,227
C 232,224,225
C 233,226,227
C 234,228,229
C 235,230,231
Svs 232,243,36,40,0
W 0
Mvvvc 0,128,192
Rv 208,244,36864
G 224,196
Mvvvc 4,128,196
Rv 212,244,37888
G 225,197
Mvvvc 8,128,200
G 226,198
G 227,199
Mvvvc 12,128,204
Rv 216,244,38912
G 228,228
Mvvvc 28,132,204
G 229,229
G 230,230
Mvvvc 24,132,200
Rv 220,244,39936
G 231,231
Mvvvc 20,132,196
C 232,224,225
C 233,226,227
Mvvvc 16,132,192
T 160,240,8192
C 234,228,229
Mvvvc 32,136,192
T 162,240,9216
C 235,230,231
Mvvvc 36,136,196
X 242,32,240
Svs 232,243,36,40,1024
Mvvvc 40,136,200
T 164,242,8192
G 224,200
Mvvvc 44,136,204
T 166,242,9216
G 225,201
Mvvvc 60,140,204
X 241,64,240
G 226,202
Mvvvc 56,140,200
T 168,241,8192
G 227,203
Mvvvc 52,140,196
T 170,241,9216
G 228,232
Mvvvc 48,140,192
X 242,96,240
G 229,233
Mvvvc 64,144,192
T 172,242,8192
G 230,234
Mvvvc 68,144,196
T 174,242,9216
G 231,235
Mvvvc 72,144,200
X 241,128,240
C 232,224,225
Mvvvc 76,144,204
T 176,241,8192
C 233,226,227
Mvvvc 92,148,204
T 178,241,9216
C 234,228,229
Mvvvc 88,148,200
X 242,160,240
C 235,230,231
Mvvvc 84,148,196
T 180,242,8192
Svs 232,243,36,40,2048
Mvvvc 80,148,192
T 182,242,9216
G 224,204
Mvvvc 96,152,192
X 241,192,240
G 225,205
Mvvvc 100,152,196
T 184,241,8192
G 226,206
Mvvvc 104,152,200
T 186,241,9216
G 227,207
Mvvvc 108,152,204
X 242,224,240
G 228,236
Mvvvc 124,156,204
T 188,242,8192
G 229,237
Mvvvc 120,156,200
T 190,242,9216
G 230,238
Mvvvc 116,156,196
G 231,239
Mvvvc 112,156,192
C 232,224,225
C 233,226,227
C 234,228,229
C 235,230,231
Svs 232,243,36,40,3072
AS 40,40,4096
G 224,208
G 225,209
G 226,210
G 227,211
G 228,240
G 229,241
G 230,242
G 231,243
C 232,224,225
C 233,226,227
C 234,228,229
C 235,230,231
Svs 232,243,36,40,0
W 0
B
Mvvvc 0,160,208
Rv 192,245,0
G 224,212
Mvvvc 4,160,212
Rv 196,245,1024
G 225,213
Mvvvc 8,160,216
G 226,214
G 227,215
Mvvvc 12,160,220
Rv 200,245,2048
Mvvvc 28,164,220
G 228,244
G 229,245
Mvvvc 24,164,216
Rv 204,245,3072
G 230,246
Mvvvc 20,164,212
G 231,247
C 232,224,225
Mvvvc 16,164,208
T 128,240,16384
C 233,226,227
Mvvvc 32,168,208
T 130,240,17408
C 234,228,229
Mvvvc 36,168,212
X 242,32,240
C 235,230,231
Mvvvc 40,168,216
T 132,242,16384
Svs 232,243,36,40,1024
Mvvvc 44,168,220
T 134,242,17408
G 224,216
Mvvvc 60,172,220
X 241,64,240
G 225,217
Mvvvc 56,172,216
T 136,241,16384
G 226,218
Mvvvc 52,172,212
T 138,241,17408
G 227,219
Mvvvc 48,172,208
X 242,96,240
G 228,248
Mvvvc 64,176,208
T 140,242,16384
G 229,249
Mvvvc 68,176,212
T 142,242,17408
G 230,250
Mvvvc 72,176,216
X 241,128,240
G 231,251
Mvvvc 76,176,220
T 144,241,16384
C 232,224,225
Mvvvc 92,180,220
T 146,241,17408
C 233,226,227
Mvvvc 88,180,216
X 242,160,240
C 234,228,229
Mvvvc 84,180,212
T 148,242,16384
C 235,230,231
Mvvvc 80,180,208
T 150,242,17408
Svs 232,243,36,40,2048
Mvvvc 96,184,208
X 241,192,240
G 224,220
Mvvvc 100,184,212
T 152,241,16384
G 225,221
Mvvvc 104,184,216
T 154,241,17408
G 226,222
Mvvvc 108,184,220
X 242,224,240
G 227,223
Mvvvc 124,188,220
T 156,242,16384
G 228,252
Mvvvc 120,188,216
T 158,242,17408
G 229,253
Mvvvc 116,188,212
G 230,254
Mvvvc 112,188,208
G 231,255
C 232,224,225
C 233,226,227
C 234,228,229
C 235,230,231
Svs 232,243,36,40,3072
W 0
Mvvvc 0,128,192
)A"
R"A(Rv 208,245,4096
Mvvvc 4,128,196
Rv 212,245,5120
Mvvvc 8,128,200
Mvvvc 12,128,204
Rv 216,245,6144
Mvvvc 28,132,204
Mvvvc 24,132,200
Rv 220,245,7168
Mvvvc 20,132,196
Mvvvc 16,132,192
T 160,240,24576
Mvvvc 32,136,192
T 162,240,25600
Mvvvc 36,136,196
X 242,32,240
Mvvvc 40,136,200
T 164,242,24576
Mvvvc 44,136,204
T 166,242,25600
Mvvvc 60,140,204
X 241,64,240
Mvvvc 56,140,200
T 168,241,24576
Mvvvc 52,140,196
T 170,241,25600
Mvvvc 48,140,192
X 242,96,240
Mvvvc 64,144,192
T 172,242,24576
Mvvvc 68,144,196
T 174,242,25600
Mvvvc 72,144,200
X 241,128,240
Mvvvc 76,144,204
T 176,241,24576
Mvvvc 92,148,204
T 178,241,25600
Mvvvc 88,148,200
X 242,160,240
Mvvvc 84,148,196
T 180,242,24576
Mvvvc 80,148,192
T 182,242,25600
Mvvvc 96,152,192
X 241,192,240
Mvvvc 100,152,196
T 184,241,24576
Mvvvc 104,152,200
T 186,241,25600
Mvvvc 108,152,204
X 242,224,240
Mvvvc 124,156,204
T 188,242,24576
Mvvvc 120,156,200
T 190,242,25600
Mvvvc 116,156,196
Mvvvc 112,156,192
W 0
B
Mvvvc 0,160,208
Mvvvc 4,160,212
Mvvvc 8,160,216
Mvvvc 12,160,220
Mvvvc 28,164,220
Mvvvc 24,164,216
Mvvvc 20,164,212
Mvvvc 16,164,208
Mvvvc 32,168,208
Mvvvc 36,168,212
Mvvvc 40,168,216
Mvvvc 44,168,220
Mvvvc 60,172,220
Mvvvc 56,172,216
Mvvvc 52,172,212
Mvvvc 48,172,208
Mvvvc 64,176,208
Mvvvc 68,176,212
Mvvvc 72,176,216
Mvvvc 76,176,220
Mvvvc 92,180,220
Mvvvc 88,180,216
Mvvvc 84,180,212
Mvvvc 80,180,208
Mvvvc 96,184,208
Mvvvc 100,184,212
Mvvvc 104,184,216
Mvvvc 108,184,220
Mvvvc 124,188,220
Mvvvc 120,188,216
Mvvvc 116,188,212
Mvvvc 112,188,208
QE3%=:
WV 0
B
VA 224,147456,243
C 128,0,1
C 129,2,3
C 130,16,17
C 131,18,19
DWv 224,128,0
C 132,32,33
C 133,34,35
C 134,48,49
C 135,50,51
DWv 224,132,1024
C 136,64,65
C 137,66,67
C 138,80,81
C 139,82,83
DWv 224,136,2048
C 140,96,97
C 141,98,99
C 142,112,113
C 143,114,115
DWv 224,140,3072
C 144,4,5
C 145,6,7
C 146,20,21
C 147,22,23
DWv 224,144,4096
C 148,36,37
C 149,38,39
C 150,52,53
C 151,54,55
DWv 224,148,5120
C 152,68,69
C 153,70,71
C 154,84,85
C 155,86,87
DWv 224,152,6144
C 156,100,101
C 157,102,103
C 158,116,117
C 159,118,119
DWv 224,156,7168
C 128,8,9
C 129,10,11
C 130,24,25
C 131,26,27
DWv 224,128,8192
C 132,40,41
C 133,42,43
C 134,56,57
C 135,58,59
DWv 224,132,9216
C 136,72,73
C 137,74,75
C 138,88,89
C 139,90,91
DWv 224,136,10240
C 140,104,105
C 141,106,107
C 142,120,121
C 143,122,123
DWv 224,140,11264
C 144,12,13
C 145,14,15
C 146,28,29
C 147,30,31
DWv 224,144,12288
C 148,44,45
C 149,46,47
C 150,60,61
C 151,62,63
DWv 224,148,13312
C 152,76,77
C 153,78,79
C 154,92,93
C 155,94,95
DWv 224,152,14336
C 156,108,109
C 157,110,111
C 158,124,125
C 159,126,127
DWv 224,156,15360
W 0
B
Ra 128,224,0
Ra 132,224,1024
Ra 136,224,2048
Ra 140,224,3072
Ra 144,224,4096
Ra 148,224,5120
Ra 152,224,6144
Ra 156,224,7168
Ra 160,224,8192
Ra 164,224,9216
Ra 168,224,10240
Ra 172,224,11264
Ra 176,224,12288
Ra 180,224,13312
Ra 184,224,14336
Ra 188,224,15360
VA 225,114688,243
Ra 192,225,0
Ra 196,225,1024
Ra 200,225,2048
Ra 204,225,3072
Ra 208,225,4096
Ra 212,225,5120
Ra 216,225,6144
Ra 220,225,7168
Ra 224,225,8192
Ra 228,225,9216
Ra 232,225,10240
Ra 236,225,11264
Ra 240,225,12288
Ra 244,225,13312
Ra 248,225,14336
Ra 252,225,15360
s_add_u32 s32, s14, 0x2400000
s_addc_u32 s33, s15, 0
SM 34,0x1000000
SM 35,0x20000
v_lshlrev_b32 v196, 4, v254
VA 197,0,196
VA 198,65536,196
v_mov_b32 v199, 0xff800000
v_mov_b32 v192, 0x3f803f80
v_mov_b32 v193, 0x3f803f80
v_mov_b32 v194, 0x3f803f80
v_mov_b32 v195, 0x3f803f80
v_and_b32 v200, 15, v254
v_lshrrev_b32 v201, 4, v254
v_lshlrev_b32 v201, 2, v201
VA 202,0,201
v_cmp_le_u32 s[62:63], v202, v200
VA 202,1,201
v_cmp_le_u32 s[64:65], v202, v200
VA 202,2,201
v_cmp_le_u32 s[66:67], v202, v200
VA 202,3,201
v_cmp_le_u32 s[68:69], v202, v200
s_lshl_b32 s61, s13, 1
AS 61,61,2
SM 60,0
s_mov_b64 s[70:71], 0
s_mov_b64 s[50:51], s[42:43]
VA 237,0,254
v_mul_u32_u24 v238, 3277, v237
v_lshrrev_b32 v238, 16, v238
v_mul_u32_u24 v239, 20, v238
v_sub_u32 v239, v237, v239
v_lshrrev_b32 v240, 1, v239
v_lshrrev_b32 v241, 2, v238
v_and_b32 v241, 1, v241
v_xor_b32 v240, v240, v241
v_and_b32 v239, 1, v239
v_lshlrev_b32 v239, 4, v239
v_lshl_add_u32 v239, v240, 5, v239
v_mul_u32_u24 v238, 1280, v238
v_add_u32 v238, v238, v239
MV 232,238
VA 237,64,254
v_mul_u32_u24 v238, 3277, v237
v_lshrrev_b32 v238, 16, v238
v_mul_u32_u24 v239, 20, v238
v_sub_u32 v239, v237, v239
v_lshrrev_b32 v240, 1, v239
v_lshrrev_b32 v241, 2, v238
v_and_b32 v241, 1, v241
v_xor_b32 v240, v240, v241
v_and_b32 v239, 1, v239
v_lshlrev_b32 v239, 4, v239
v_lshl_add_u32 v239, v240, 5, v239
v_mul_u32_u24 v238, 1280, v238
v_add_u32 v238, v238, v239
v_subrev_u32 v233, 1024, v238
VA 237,128,254
v_mul_u32_u24 v238, 3277, v237
v_lshrrev_b32 v238, 16, v238
v_mul_u32_u24 v239, 20, v238
v_sub_u32 v239, v237, v239
v_lshrrev_b32 v240, 1, v239
v_lshrrev_b32 v241, 2, v238
v_and_b32 v241, 1, v241
v_xor_b32 v240, v240, v241
v_and_b32 v239, 1, v239
v_lshlrev_b32 v239, 4, v239
v_lshl_add_u32 v239, v240, 5, v239
v_mul_u32_u24 v238, 1280, v238
v_add_u32 v238, v238, v239
v_subrev_u32 v234, 2048, v238
VA 237,192,254
v_mul_u32_u24 v238, 3277, v237
v_lshrrev_b32 v238, 16, v238
v_mul_u32_u24 v239, 20, v238
v_sub_u32 v239, v237, v239
v_lshrrev_b32 v240, 1, v239
v_lshrrev_b32 v241, 2, v238
v_and_b32 v241, 1, v241
v_xor_b32 v240, v240, v241
v_and_b32 v239, 1, v239
v_lshlrev_b32 v239, 4, v239
v_lshl_add_u32 v239, v240, 5, v239
v_mul_u32_u24 v238, 1280, v238
v_add_u32 v238, v238, v239
v_subrev_u32 v235, 3072, v238
VA 237,256,254
v_mul_u32_u24 v238, 3277, v237
v_lshrrev_b32 v238, 16, v238
v_mul_u32_u24 v239, 20, v238
v_sub_u32 v239, v237, v239
v_lshrrev_b32 v240, 1, v239
v_lshrrev_b32 v241, 2, v238
v_and_b32 v241, 1, v241
v_xor_b32 v240, v240, v241
v_and_b32 v239, 1, v239
v_lshlrev_b32 v239, 4, v239
v_lshl_add_u32 v239, v240, 5, v239
v_mul_u32_u24 v238, 1280, v238
v_add_u32 v238, v238, v239
MV 236,238
B
AM 59,0
N 0
Ds 196,28,57,0
Ds 196,28,57,1024
Ds 196,28,57,2048
Ds 196,28,57,3072
AS 57,57,16384
s_cmp_eq_u32 s13, 0
s_cbranch_scc0 AT3_M1%=
SM 73,0x0
SM 74,0x0
SM 75,0x8000
SM 76,0x0
SM 77,0x0
SM 78,0x0
s_branch AT3_MD%=
AT3_M1%=:
s_cmp_eq_u32 s13, 1
s_cbranch_scc0 AT3_M2%=
SM 73,0x0
SM 74,0x0
SM 75,0x8000
SM 76,0x2
SM 77,0x0
SM 78,0x0
s_branch AT3_MD%=
AT3_M2%=:
s_cmp_eq_u32 s13, 2
s_cbranch_scc0 AT3_M3%=
SM 73,0x0
SM 74,0x0
SM 75,0x8000
SM 76,0x0
SM 77,0x0
SM 78,0x8
s_branch AT3_MD%=
AT3_M3%=:
s_cmp_eq_u32 s13, 3
s_cbranch_scc0 AT3_M4%=
SM 73,0x0
SM 74,0x0
SM 75,0x8000
SM 76,0x0
SM 77,0x0
SM 78,0x20
s_branch AT3_MD%=
AT3_M4%=:
s_cmp_eq_u32 s13, 4
s_cbranch_scc0 AT3_M5%=
SM 73,0x0
SM 74,0x0
SM 75,0x8000
SM 76,0x0
SM 77,0x0
SM 78,0x80
s_branch AT3_MD%=
AT3_M5%=:
s_cmp_eq_u32 s13, 5
s_cbranch_scc0 AT3_M6%=
SM 73,0x0
SM 74,0x0
SM 75,0x8000
SM 76,0x0
SM 77,0x0
SM 78,0x200
s_branch AT3_MD%=
AT3_M6%=:
s_cmp_eq_u32 s13, 6
s_cbranch_scc0 AT3_M7%=
SM 73,0x8000
SM 74,0x0
SM 75,0x0
SM 76,0x0
SM 77,0x0
SM 78,0x800
s_branch AT3_MD%=
AT3_M7%=:
SM 73,0x0
SM 74,0x20000
SM 75,0x0
SM 76,0x0
SM 77,0x4000
SM 78,0x2000
AT3_MD%=:
WV 4
B
Rv 144,197,0
Rv 148,197,1024
Rv 152,197,2048
Rv 156,197,3072
Rv 160,197,4096
Rv 164,197,5120
Rv 168,197,6144
W 5
s_cmp_eq_u32 s13, 0
s_cbranch_scc1 AT3_B0%=
s_branch AT3_B10%=
XAT3_B0%=:
AS 60,60,1
AT3_B0%=:
Mvvaz 0,144,128
Mvvaz 8,144,144
Mvvaz 16,144,160
Rv 172,197,7168
Mvvaz 24,144,176
Mvvac 24,148,180
Mvvac 16,148,164
Rv 144,197,8192
Mvvac 8,148,148
Mvvac 0,148,132
W 5
Mvvac 0,152,136
Mvvac 8,152,152
Rv 148,197,9216
Mvvac 16,152,168
Mvvac 24,152,184
Mvvac 24,156,188
Mvvac 16,156,172
Rv 152,197,10240
Mvvac 8,156,156
Mvvac 0,156,140
W 5
Mvvaz 4,160,128
Mvvaz 12,160,144
Rv 156,197,11264
Mvvaz 20,160,160
N 2
v_max3_f32 v200, v0, v1, v2
Fma 200,200,3
v_max3_f32 v202, v8, v9, v10
Fma 202,202,11
Mvvaz 28,160,176
v_max3_f32 v204, v16, v17, v18
Fma 204,204,19
v_max3_f32 v206, v24, v25, v26
Fma 206,206,27
Mvvac 28,164,180
MV 201,200
MV 203,202
MV 205,204
MV 207,206
Mvvac 20,164,164
Rv 160,197,12288
v_permlane16_swap_b32 v200, v201
v_permlane16_swap_b32 v202, v203
v_permlane16_swap_b32 v204, v205
Mvvac 12,164,148
v_permlane16_swap_b32 v206, v207
Fma 200,200,201
Fma 202,202,203
Fma 204,204,205
Mvvac 4,164,132
Fma 206,206,207
MV 201,200
MV 203,202
MV 205,204
W 5
Mvvac 4,168,136
MV 207,206
v_permlane32_swap_b32 v200, v201
v_permlane32_swap_b32 v202, v203
Mvvac 12,168,152
v_permlane32_swap_b32 v204, v205
v_permlane32_swap_b32 v206, v207
Fma 200,200,201
Fma 202,202,203
Mvvac 20,168,168
Rv 164,197,13312
Fma 204,204,205
Fma 206,206,207
v_sub_f32 v128, 0, v200
v_sub_f32 v129, 0, v200
Mvvac 28,168,184
v_sub_f32 v130, 0, v200
v_sub_f32 v131, 0, v200
v_sub_f32 v132, 0, v202
Mvvac 28,172,188
v_sub_f32 v133, 0, v202
v_sub_f32 v134, 0, v202
v_sub_f32 v135, 0, v202
v_sub_f32 v136, 0, v204
Mvvac 20,172,172
v_sub_f32 v137, 0, v204
v_sub_f32 v138, 0, v204
v_sub_f32 v139, 0, v204
v_sub_f32 v140, 0, v206
Mvvac 12,172,156
v_sub_f32 v141, 0, v206
v_sub_f32 v142, 0, v206
v_sub_f32 v143, 0, v206
Mvvac 4,172,140
Rv 168,197,14336
W 5
Mvvan 32,144,128,128
Mvvan 40,144,144,132
Mvvan 48,144,160,136
Mvvan 56,144,176,140
Mvvac 56,148,180
Rv 172,197,15360
Mvvac 48,148,164
Fad 0,0,128
Mvvac 40,148,148
E 0
Mvvac 32,148,132
Fad 1,1,128
W 4
Mvvac 32,152,136
E 1
Mvvac 40,152,152
Fad 2,2,128
Mvvac 48,152,168
E 2
Mvvac 56,152,184
C 0,0,1
Fad 3,3,128
Mvvac 56,156,188
E 3
AS 56,56,12288
SM 58,40960
Mvvac 48,156,172
Fad 4,4,128
W 0
B
Mvvac 40,156,156
E 4
Mvvac 32,156,140
Fad 5,5,128
W 15
Mvvan 36,160,128,128
E 5
Mvvan 44,160,144,132
C 1,2,3
Fad 6,6,128
Mvvan 52,160,160,136
E 6
Mvvan 60,160,176,140
C 2,4,5
Fad 7,7,128
AM 58,0
Mvvac 60,164,180
E 7
Ds 196,24,56,0
Mvvac 52,164,164
Fad 8,8,132
Mvvac 44,164,148
E 8
Mvvac 36,164,132
Fad 9,9,132
W 15
Mvvac 36,168,136
E 9
Mvvac 44,168,152
C 3,6,7
Fad 10,10,132
Mvvac 52,168,168
E 10
Mvvac 60,168,184
C 8,8,9
Fad 11,11,132
Ds 196,24,56,1024
Mvvac 60,172,188
E 11
Mvvac 52,172,172
Fad 12,12,132
Mvvac 44,172,156
E 12
Mvvac 36,172,140
Fad 13,13,132
E 13
C 9,10,11
Fad 14,14,132
E 14
Ds 196,24,56,2048
C 10,12,13
Fad 15,15,132
E 15
Fad 16,16,136
E 16
Fad 17,17,136
E 17
C 11,14,15
Fad 18,18,136
Ds 196,24,56,3072
E 18
C 16,16,17
Fad 19,19,136
AS 56,56,4096
E 19
Fad 20,20,136
E 20
Fad 21,21,136
E 21
C 17,18,19
Fad 22,22,136
E 22
C 18,20,21
Fad 23,23,136
AM 58,4096
E 23
Ds 196,24,56,0
Fad 24,24,140
E 24
Fad 25,25,140
E 25
C 19,22,23
Fad 26,26,140
E 26
C 24,24,25
Fad 27,27,140
Ds 196,24,56,1024
E 27
Fad 28,28,140
E 28
Fad 29,29,140
E 29
C 25,26,27
Fad 30,30,140
E 30
Ds 196,24,56,2048
Fad 31,31,140
E 31
C 26,28,29
E 32
E 33
C 27,30,31
Ds 196,24,56,3072
E 34
C 32,32,33
AS 56,56,28672
E 35
W 0
B
Rv 144,197,16384
E 36
Rv 148,197,17408
E 37
C 33,34,35
AM 59,16384
E 38
Ds 196,28,57,0
Rv 152,197,18432
C 34,36,37
E 39
Rv 156,197,19456
E 40
Ds 196,28,57,1024
E 41
Rv 160,197,20480
C 35,38,39
E 42
Ds 196,28,57,2048
C 40,40,41
Rv 164,197,21504
E 43
E 44
Ds 196,28,57,3072
Rv 168,197,22528
AS 57,57,16384
W 5
XAT3_B1%=:
AS 60,60,1
AT3_B1%=:
Mvvan 64,144,128,128
E 45
Mvvan 72,144,144,132
C 41,42,43
Mvvan 80,144,160,136
Rv 172,197,23552
E 46
Mvvan 88,144,176,140
C 42,44,45
Mvvac 88,148,180
E 47
Mvvac 80,148,164
Rv 144,197,24576
Mvvac 72,148,148
E 48
Mvvac 64,148,132
W 5
Mvvac 64,152,136
E 49
Mvvac 72,152,152
Rv 148,197,25600
C 43,46,47
Mvvac 80,152,168
E 50
Mvvac 88,152,184
C 48,48,49
Mvvac 88,156,188
E 51
Mvvac 80,156,172
Rv 152,197,26624
Mvvac 72,156,156
E 52
Mvvac 64,156,140
W 5
E 53
C 49,50,51
Mvvan 76,160,144,132
Rv 156,197,27648
Mvvan 84,160,160,136
E 54
Mvvan 92,160,176,140
C 50,52,53
Mvvac 92,164,180
E 55
Mvvac 84,164,164
Rv 160,197,28672
Mvvac 76,164,148
E 56
C 51,54,55
W 5
E 57
Mvvac 76,168,152
Mvvac 84,168,168
Rv 164,197,29696
E 58
Mvvac 92,168,184
C 56,56,57
Mvvac 92,172,188
E 59
Mvvac 84,172,172
Mvvac 76,172,156
E 60
Rv 168,197,30720
W 5
E 61
C 57,58,59
Mvvan 112,144,160,136
E 62
Mvvan 120,144,176,140
C 58,60,61
Mvvac 120,148,180
Rv 172,197,31744
E 63
Mvvac 112,148,164
K 64,199,64,62
E 64
K 65,199,65,64
W 4
E 65
C 59,62,63
K 66,199,66,66
Mvvac 112,152,168
E 66
Mvvac 120,152,184
K 67,199,67,68
Mvvac 120,156,188
E 67
Mvvac 112,156,172
WV 12
B
Rv 144,198,0
C 64,64,65
W 15
Rv 148,198,1024
C 65,66,67
v_mov_b32 v66, 0
)A"
R"A(Mvvan 124,160,176,140
Rv 152,198,2048
AM 58,16384
Mvvac 124,164,180
Ds 196,24,56,0
Rv 156,198,3072
E 72
W 15
E 73
Rv 160,198,4096
v_mov_b32 v67, 0
E 74
Mvvac 124,168,184
Ds 196,24,56,1024
Mvvac 124,172,188
Rv 164,198,5120
E 75
C 72,72,73
K 76,199,76,62
E 76
C 73,74,75
Rv 168,198,6144
K 77,199,77,64
W 5
Mavvz 0,144,0
E 77
Mavvz 4,144,8
K 78,199,78,66
Mavvz 8,144,16
Rv 172,198,7168
E 78
Ds 196,24,56,2048
Mavvz 12,144,24
Mvvvz 176,192,0
K 79,199,79,68
Mavvz 28,148,24
E 79
Mavvz 24,148,16
Rv 144,198,8192
C 74,76,77
Mavvz 20,148,8
E 80
Mavvz 16,148,0
W 5
Mavvz 32,152,0
E 81
Mavvz 36,152,8
Rv 148,198,9216
C 75,78,79
Ds 196,24,56,3072
Mavvz 40,152,16
E 82
Mavvz 44,152,24
C 80,80,81
AS 56,56,4096
Mavvz 60,156,24
E 83
Mavvz 56,156,16
Rv 152,198,10240
Mvvvz 180,192,8
Mavvz 52,156,8
E 84
Mavvz 48,156,0
W 5
Mavvz 64,160,0
E 85
Mavvz 68,160,8
Rv 156,198,11264
Mvvvz 184,192,16
C 81,82,83
Mavvz 72,160,16
E 86
Mavvz 76,160,24
C 82,84,85
AM 58,20480
Mavvz 92,164,24
E 87
Ds 196,24,56,0
Mavvz 88,164,16
Rv 160,198,12288
Mavvz 84,164,8
E 88
Mavvz 80,164,0
Mvvvz 188,192,24
W 5
Mavvz 96,168,0
E 89
Mavvz 100,168,8
C 83,86,87
Mavvz 104,168,16
Rv 164,198,13312
E 90
Mavvz 108,168,24
C 88,88,89
Ds 196,24,56,1024
Mavvz 124,172,24
E 91
Mavvz 120,172,16
Mavvz 116,172,8
E 92
Mavvz 112,172,0
Rv 168,198,14336
W 5
Mavvc 0,144,32
E 93
Mavvc 4,144,40
C 89,90,91
Mavvc 8,144,48
E 94
Ds 196,24,56,2048
Mavvc 12,144,56
C 90,92,93
Mavvc 28,148,56
Rv 172,198,15360
E 95
Mavvc 24,148,48
Mvvvc 176,192,32
Mavvc 20,148,40
C 91,94,95
Mavvc 16,148,32
W 4
Mavvc 32,152,32
Mavvc 36,152,40
Mvvvc 180,192,40
Ds 196,24,56,3072
Mavvc 40,152,48
Mavvc 44,152,56
AS 56,56,28672
Mavvc 60,156,56
Mavvc 56,156,48
WV 12
B
AS 57,57,12288
SM 59,106496
VA 197,32768,197
Mavvc 52,156,40
Rv 144,197,0
Mavvc 48,156,32
W 15
Mavvc 64,160,32
Rv 148,197,1024
Mvvvc 184,192,48
Mavvc 68,160,40
AM 59,0
Mavvc 72,160,48
Ds 196,28,57,0
Mavvc 76,160,56
Rv 152,197,2048
Mavvc 92,164,56
Mavvc 88,164,48
Mavvc 84,164,40
Rv 156,197,3072
Ds 196,28,57,1024
Mavvc 80,164,32
W 15
Mavvc 96,168,32
Mvvvc 188,192,56
Mavvc 100,168,40
Rv 160,197,4096
Mavvc 104,168,48
Ds 196,28,57,2048
Mavvc 108,168,56
Mavvc 124,172,56
Rv 164,197,5120
Mavvc 120,172,48
Mavvc 116,172,40
Ds 196,28,57,3072
Mavvc 112,172,32
Rv 168,197,6144
W 5
XAT3_B2%=:
AS 60,60,1
AT3_B2%=:
Mvvaz 0,144,192
AS 57,57,4096
Mvvaz 8,144,208
Mvvaz 16,144,224
Rv 172,197,7168
Mvvaz 24,144,240
Mvvac 24,148,244
Mvvac 16,148,228
Rv 144,197,8192
K 112,199,112,62
Mvvac 8,148,212
E 112
Mvvac 0,148,196
K 113,199,113,64
AM 59,4096
W 5
Mvvac 0,152,200
E 113
Ds 196,28,57,0
Mvvac 8,152,216
Rv 148,197,9216
K 114,199,114,66
Mvvac 16,152,232
E 114
Mvvac 24,152,248
K 115,199,115,68
Mvvac 24,156,252
E 115
Mvvac 16,156,236
Rv 152,197,10240
C 112,112,113
Mvvac 8,156,220
C 113,114,115
Mvvac 0,156,204
W 5
Mvvaz 4,160,192
Mvvaz 12,160,208
Rv 156,197,11264
Ds 196,28,57,1024
Mvvaz 20,160,224
N 1
v_max3_f32 v200, v0, v1, v2
Fma 200,200,3
v_max3_f32 v202, v8, v9, v10
Fma 202,202,11
Mvvaz 28,160,240
v_max3_f32 v204, v16, v17, v18
Fma 204,204,19
v_max3_f32 v206, v24, v25, v26
Fma 206,206,27
Mvvac 28,164,244
MV 201,200
MV 203,202
MV 205,204
MV 207,206
Mvvac 20,164,228
Rv 160,197,12288
v_permlane16_swap_b32 v200, v201
v_permlane16_swap_b32 v202, v203
v_permlane16_swap_b32 v204, v205
v_mov_b32 v114, 0
Mvvac 12,164,212
E 120
v_permlane16_swap_b32 v206, v207
Fma 200,200,201
Fma 202,202,203
Fma 204,204,205
Mvvac 4,164,196
Fma 206,206,207
MV 201,200
MV 203,202
MV 205,204
W 5
Mvvac 4,168,200
E 121
MV 207,206
v_permlane32_swap_b32 v200, v201
v_permlane32_swap_b32 v202, v203
Mvvac 12,168,216
v_permlane32_swap_b32 v204, v205
v_permlane32_swap_b32 v206, v207
Fma 200,200,201
Fma 202,202,203
v_mov_b32 v115, 0
Mvvac 20,168,232
Rv 164,197,13312
E 122
Fma 204,204,205
Fma 206,206,207
v_sub_f32 v128, 0, v200
v_sub_f32 v129, 0, v200
Ds 196,28,57,2048
Mvvac 28,168,248
v_sub_f32 v130, 0, v200
v_sub_f32 v131, 0, v200
v_sub_f32 v132, 0, v202
C 120,120,121
Mvvac 28,172,252
E 123
v_sub_f32 v133, 0, v202
v_sub_f32 v134, 0, v202
v_sub_f32 v135, 0, v202
v_sub_f32 v136, 0, v204
Mvvac 20,172,236
v_sub_f32 v137, 0, v204
v_sub_f32 v138, 0, v204
v_sub_f32 v139, 0, v204
v_sub_f32 v140, 0, v206
K 124,199,124,62
Mvvac 12,172,220
E 124
v_sub_f32 v141, 0, v206
v_sub_f32 v142, 0, v206
v_sub_f32 v143, 0, v206
Mvvac 4,172,204
Rv 168,197,14336
K 125,199,125,64
W 5
Mvvan 32,144,192,128
E 125
Mvvan 40,144,208,132
C 121,122,123
K 126,199,126,66
Mvvan 48,144,224,136
E 126
Mvvan 56,144,240,140
C 122,124,125
K 127,199,127,68
Ds 196,28,57,3072
Mvvac 56,148,244
Rv 172,197,15360
E 127
Mvvac 48,148,228
Fad 0,0,128
AS 57,57,28672
Mvvac 40,148,212
E 0
Mvvac 32,148,196
Fad 1,1,128
W 4
Mvvac 32,152,200
E 1
Mvvac 40,152,216
C 123,126,127
Fad 2,2,128
Mvvac 48,152,232
E 2
Mvvac 56,152,248
C 0,0,1
Fad 3,3,128
Mvvac 56,156,252
E 3
Mvvac 48,156,236
Fad 4,4,128
WV 16
B
Mvvac 40,156,220
Rv 144,198,16384
E 4
Mvvac 32,156,204
Fad 5,5,128
W 15
Mvvan 36,160,192,128
Rv 148,198,17408
E 5
Mvvan 44,160,208,132
C 1,2,3
Fad 6,6,128
Mvvan 52,160,224,136
E 6
Mvvan 60,160,240,140
Rv 152,198,18432
C 2,4,5
Fad 7,7,128
AM 58,0
Mvvac 60,164,244
E 7
Ds 196,24,56,0
Mvvac 52,164,228
Fad 8,8,132
Mvvac 44,164,212
Rv 156,198,19456
E 8
Mvvac 36,164,196
Fad 9,9,132
W 15
Mvvac 36,168,200
E 9
Mvvac 44,168,216
Rv 160,198,20480
C 3,6,7
Fad 10,10,132
Mvvac 52,168,232
E 10
Mvvac 60,168,248
C 8,8,9
Fad 11,11,132
Ds 196,24,56,1024
Mvvac 60,172,252
Rv 164,198,21504
E 11
Mvvac 52,172,236
Fad 12,12,132
Mvvac 44,172,220
E 12
Mvvac 36,172,204
Rv 168,198,22528
Fad 13,13,132
W 5
Mavvc 0,144,64
E 13
Mavvc 4,144,72
C 9,10,11
Fad 14,14,132
Mavvc 8,144,80
Rv 172,198,23552
E 14
Ds 196,24,56,2048
Mavvc 12,144,88
C 10,12,13
Fad 15,15,132
Mavvc 28,148,88
E 15
Mavvc 24,148,80
Rv 144,198,24576
Mvvvc 176,192,64
Fad 16,16,136
Mavvc 20,148,72
E 16
Mavvc 16,148,64
Fad 17,17,136
W 5
Mavvc 32,152,64
E 17
Mavvc 36,152,72
Rv 148,198,25600
C 11,14,15
Fad 18,18,136
Ds 196,24,56,3072
Mavvc 40,152,80
E 18
Mavvc 44,152,88
C 16,16,17
Fad 19,19,136
AS 56,56,4096
Mavvc 60,156,88
E 19
Mavvc 56,156,80
Rv 152,198,26624
Mvvvc 180,192,72
Fad 20,20,136
Mavvc 52,156,72
E 20
Mavvc 48,156,64
Fad 21,21,136
W 5
Mavvc 64,160,64
E 21
Mvvvc 184,192,80
Mavvc 68,160,72
Rv 156,198,27648
C 17,18,19
Fad 22,22,136
Mavvc 72,160,80
E 22
Mavvc 76,160,88
C 18,20,21
Fad 23,23,136
AM 58,4096
Mavvc 92,164,88
E 23
Ds 196,24,56,0
Mavvc 88,164,80
Rv 160,198,28672
Fad 24,24,140
Mavvc 84,164,72
E 24
Mavvc 80,164,64
Mvvvc 188,192,88
Fad 25,25,140
W 5
Mavvc 96,168,64
E 25
Mavvc 100,168,72
C 19,22,23
Fad 26,26,140
Mavvc 104,168,80
Rv 164,198,29696
E 26
Mavvc 108,168,88
C 24,24,25
Fad 27,27,140
Ds 196,24,56,1024
Mavvc 124,172,88
E 27
Mavvc 120,172,80
Fad 28,28,140
Mavvc 116,172,72
E 28
Mavvc 112,172,64
Rv 168,198,30720
Fad 29,29,140
W 5
E 29
C 25,26,27
Fad 30,30,140
Mavvc 8,144,112
E 30
Ds 196,24,56,2048
Mavvc 12,144,120
Fad 31,31,140
Mavvc 28,148,120
Rv 172,198,31744
E 31
Mavvc 24,148,112
C 26,28,29
E 32
W 4
E 33
C 27,30,31
Ds 196,24,56,3072
Mavvc 40,152,112
E 34
Mavvc 44,152,120
C 32,32,33
AS 56,56,28672
Mavvc 60,156,120
E 35
Mavvc 56,156,112
WV 16
B
Rv 144,197,16384
E 36
W 15
Rv 148,197,17408
E 37
Mvvvc 184,192,112
C 33,34,35
AM 59,16384
Mavvc 72,160,112
E 38
Ds 196,28,57,0
Mavvc 76,160,120
Rv 152,197,18432
C 34,36,37
Mavvc 92,164,120
E 39
Mavvc 88,164,112
Rv 156,197,19456
E 40
Ds 196,28,57,1024
Mvvvc 188,192,120
W 15
E 41
Rv 160,197,20480
C 35,38,39
Mavvc 104,168,112
E 42
Ds 196,28,57,2048
Mavvc 108,168,120
C 40,40,41
Mavvc 124,172,120
Rv 164,197,21504
E 43
Mavvc 120,172,112
E 44
Ds 196,28,57,3072
Rv 168,197,22528
W 5
XAT3_B3%=:
AS 60,60,1
AT3_B3%=:
s_mov_b32 s16, s13
s_lshr_b32 s17, s16, 1
s_lshl_b32 s17, s17, 21
s_and_b32 s16, s16, 1
s_lshl_b32 s16, s16, 13
s_add_u32 s17, s17, s16
s_lshl_b32 s16, s12, 16
s_add_u32 s17, s17, s16
AS 72,17,4096
N 7
v_mov_b32 v231, 0x5d800000
v_cmp_nle_f32 vcc, v176, v231
N 3
s_or_b64 s[70:71], s[70:71], vcc
v_cmp_nle_f32 vcc, v180, v231
N 3
s_or_b64 s[70:71], s[70:71], vcc
v_cmp_nle_f32 vcc, v184, v231
N 3
s_or_b64 s[70:71], s[70:71], vcc
v_cmp_nle_f32 vcc, v188, v231
N 3
s_or_b64 s[70:71], s[70:71], vcc
s_cmp_lg_u64 s[70:71], 0
s_cselect_b32 s16, 1, 0
s_mov_b64 s[70:71], 0
v_mov_b32 v200, s16
v_mov_b32 v201, 149524
ds_write_b32 v201, v200
v_rcp_f32 v224, v176
v_rcp_f32 v226, v180
v_rcp_f32 v228, v184
v_rcp_f32 v176, v188
G 200,0
G 204,16
G 201,1
G 205,17
G 202,2
G 206,18
G 203,3
G 207,19
PM 200,200,224
PM 202,202,224
PM 204,204,224
PM 206,206,224
C 208,200,201
C 209,202,203
C 210,204,205
C 211,206,207
Svs 208,196,32,72,0
G 212,4
G 216,20
G 213,5
G 217,21
G 214,6
G 218,22
G 215,7
G 219,23
PM 212,212,226
PM 214,214,226
PM 216,216,226
PM 218,218,226
C 220,212,213
C 221,214,215
C 222,216,217
C 223,218,219
Svs 220,196,32,72,1024
G 200,8
G 204,24
G 201,9
G 205,25
G 202,10
G 206,26
G 203,11
G 207,27
PM 200,200,228
PM 202,202,228
PM 204,204,228
PM 206,206,228
C 208,200,201
C 209,202,203
C 210,204,205
C 211,206,207
Svs 208,196,32,72,2048
G 212,12
G 216,28
G 213,13
G 217,29
G 214,14
G 218,30
G 215,15
G 219,31
PM 212,212,176
PM 214,214,176
PM 216,216,176
PM 218,218,176
C 220,212,213
C 221,214,215
C 222,216,217
C 223,218,219
Svs 220,196,32,72,3072
AS 72,72,16384
G 200,32
G 204,48
G 201,33
G 205,49
G 202,34
G 206,50
G 203,35
G 207,51
PM 200,200,224
PM 202,202,224
PM 204,204,224
PM 206,206,224
C 208,200,201
C 209,202,203
C 210,204,205
C 211,206,207
Svs 208,196,32,72,0
G 212,36
G 216,52
G 213,37
G 217,53
G 214,38
G 218,54
G 215,39
G 219,55
PM 212,212,226
PM 214,214,226
PM 216,216,226
PM 218,218,226
C 220,212,213
C 221,214,215
C 222,216,217
C 223,218,219
Svs 220,196,32,72,1024
G 200,40
G 204,56
G 201,41
G 205,57
G 202,42
G 206,58
G 203,43
G 207,59
PM 200,200,228
PM 202,202,228
PM 204,204,228
PM 206,206,228
C 208,200,201
C 209,202,203
C 210,204,205
C 211,206,207
Svs 208,196,32,72,2048
G 212,44
G 216,60
G 213,45
G 217,61
G 214,46
G 218,62
G 215,47
G 219,63
PM 212,212,176
PM 214,214,176
PM 216,216,176
PM 218,218,176
C 220,212,213
C 221,214,215
C 222,216,217
C 223,218,219
Svs 220,196,32,72,3072
AS 72,72,16384
G 200,64
G 204,80
G 201,65
G 205,81
G 202,66
G 206,82
G 203,67
G 207,83
PM 200,200,224
PM 202,202,224
PM 204,204,224
PM 206,206,224
C 208,200,201
C 209,202,203
C 210,204,205
C 211,206,207
Svs 208,196,32,72,0
G 212,68
G 216,84
G 213,69
G 217,85
G 214,70
G 218,86
G 215,71
G 219,87
PM 212,212,226
PM 214,214,226
PM 216,216,226
PM 218,218,226
C 220,212,213
C 221,214,215
C 222,216,217
C 223,218,219
Svs 220,196,32,72,1024
G 200,72
G 204,88
G 201,73
G 205,89
G 202,74
G 206,90
G 203,75
G 207,91
PM 200,200,228
PM 202,202,228
PM 204,204,228
PM 206,206,228
C 208,200,201
C 209,202,203
C 210,204,205
C 211,206,207
Svs 208,196,32,72,2048
G 212,76
G 216,92
G 213,77
G 217,93
G 214,78
G 218,94
G 215,79
G 219,95
PM 212,212,176
PM 214,214,176
PM 216,216,176
PM 218,218,176
C 220,212,213
C 221,214,215
C 222,216,217
C 223,218,219
Svs 220,196,32,72,3072
AS 72,72,16384
G 200,96
G 204,112
G 201,97
G 205,113
G 202,98
G 206,114
G 203,99
G 207,115
PM 200,200,224
PM 202,202,224
PM 204,204,224
PM 206,206,224
C 208,200,201
C 209,202,203
C 210,204,205
C 211,206,207
Svs 208,196,32,72,0
G 212,100
G 216,116
G 213,101
G 217,117
G 214,102
G 218,118
G 215,103
G 219,119
PM 212,212,226
PM 214,214,226
PM 216,216,226
PM 218,218,226
C 220,212,213
C 221,214,215
C 222,216,217
C 223,218,219
Svs 220,196,32,72,1024
G 200,104
G 204,120
G 201,105
G 205,121
G 202,106
G 206,122
G 203,107
G 207,123
PM 200,200,228
PM 202,202,228
PM 204,204,228
PM 206,206,228
C 208,200,201
C 209,202,203
C 210,204,205
C 211,206,207
Svs 208,196,32,72,2048
G 212,108
G 216,124
G 213,109
G 217,125
G 214,110
G 218,126
G 215,111
G 219,127
PM 212,212,176
PM 214,214,176
PM 216,216,176
PM 218,218,176
C 220,212,213
C 221,214,215
C 222,216,217
C 223,218,219
Svs 220,196,32,72,3072
s_bitcmp1_b32 s73, s60
s_cbranch_scc1 XAT3_B16%=
s_bitcmp1_b32 s74, s60
s_cbranch_scc1 XAT3_B19%=
XAT3_B4%=:
AS 60,60,1
AT3_B4%=:
Mvvan 64,144,192,128
AS 57,57,4096
E 45
Mvvan 72,144,208,132
C 41,42,43
Mvvan 80,144,224,136
Rv 172,197,23552
E 46
Mvvan 88,144,240,140
C 42,44,45
Mvvac 88,148,244
E 47
)A"
R"A(Mvvac 80,148,228
Rv 144,197,24576
Mvvac 72,148,212
E 48
Mvvac 64,148,196
AM 59,20480
W 6
Mvvac 64,152,200
E 49
Ds 196,28,57,0
Mvvac 72,152,216
Rv 148,197,25600
C 43,46,47
Mvvac 80,152,232
E 50
Mvvac 88,152,248
C 48,48,49
Mvvac 88,156,252
E 51
Mvvac 80,156,236
Rv 152,197,26624
Mvvac 72,156,220
E 52
Mvvac 64,156,204
W 6
Mvvan 68,160,192,128
E 53
Mvvan 76,160,208,132
Rv 156,197,27648
C 49,50,51
Ds 196,28,57,1024
Mvvan 84,160,224,136
E 54
Mvvan 92,160,240,140
C 50,52,53
Mvvac 92,164,244
E 55
Mvvac 84,164,228
Rv 160,197,28672
Mvvac 76,164,212
E 56
Mvvac 68,164,196
W 5
Mvvac 68,168,200
E 57
Mvvac 76,168,216
C 51,54,55
Mvvac 84,168,232
Rv 164,197,29696
E 58
Ds 196,28,57,2048
Mvvac 92,168,248
C 56,56,57
Mvvac 92,172,252
E 59
Mvvac 84,172,236
Mvvac 76,172,220
E 60
Mvvac 68,172,204
Rv 168,197,30720
W 5
Mvvan 96,144,192,128
E 61
Mvvan 104,144,208,132
C 57,58,59
Mvvan 112,144,224,136
E 62
Mvvan 120,144,240,140
C 58,60,61
Ds 196,28,57,3072
Mvvac 120,148,244
Rv 172,197,31744
E 63
Mvvac 112,148,228
AS 57,57,28672
Mvvac 104,148,212
E 64
Mvvac 96,148,196
W 4
Mvvac 96,152,200
E 65
Mvvac 104,152,216
C 59,62,63
Mvvac 112,152,232
E 66
Mvvac 120,152,248
C 64,64,65
Mvvac 120,156,252
E 67
Mvvac 112,156,236
WV 32
B
VA 198,32768,198
Mvvac 104,156,220
Rv 144,198,0
E 68
Mvvac 96,156,204
W 15
Mvvan 100,160,192,128
Rv 148,198,1024
E 69
Mvvan 108,160,208,132
C 65,66,67
Mvvan 116,160,224,136
E 70
Mvvan 124,160,240,140
Rv 152,198,2048
C 66,68,69
AM 58,16384
Mvvac 124,164,244
E 71
Ds 196,24,56,0
Mvvac 116,164,228
Mvvac 108,164,212
Rv 156,198,3072
E 72
Mvvac 100,164,196
W 15
Mvvac 100,168,200
E 73
Mvvac 108,168,216
Rv 160,198,4096
C 67,70,71
Mvvac 116,168,232
E 74
Mvvac 124,168,248
C 72,72,73
Ds 196,24,56,1024
Mvvac 124,172,252
Rv 164,198,5120
E 75
Mvvac 116,172,236
Mvvac 108,172,220
E 76
Mvvac 100,172,204
Rv 168,198,6144
W 5
Mavvz 0,144,0
E 77
Mavvz 4,144,8
C 73,74,75
Mavvz 8,144,16
Rv 172,198,7168
E 78
Ds 196,24,56,2048
Mavvz 12,144,24
C 74,76,77
Mavvz 28,148,24
E 79
Mavvz 24,148,16
Rv 144,198,8192
Mvvvz 176,192,0
Mavvz 20,148,8
E 80
Mavvz 16,148,0
W 5
Mavvz 32,152,0
E 81
Mavvz 36,152,8
Rv 148,198,9216
C 75,78,79
Ds 196,24,56,3072
Mavvz 40,152,16
E 82
Mavvz 44,152,24
C 80,80,81
AS 56,56,4096
Mavvz 60,156,24
E 83
Mavvz 56,156,16
Rv 152,198,10240
Mvvvz 180,192,8
Mavvz 52,156,8
E 84
Mavvz 48,156,0
W 5
Mavvz 64,160,0
E 85
Mavvz 68,160,8
Rv 156,198,11264
Mvvvz 184,192,16
C 81,82,83
Mavvz 72,160,16
E 86
Mavvz 76,160,24
C 82,84,85
AM 58,20480
Mavvz 92,164,24
E 87
Ds 196,24,56,0
Mavvz 88,164,16
Rv 160,198,12288
Mavvz 84,164,8
E 88
Mavvz 80,164,0
Mvvvz 188,192,24
W 5
Mavvz 96,168,0
E 89
Mavvz 100,168,8
C 83,86,87
Mavvz 104,168,16
Rv 164,198,13312
E 90
Mavvz 108,168,24
C 88,88,89
Ds 196,24,56,1024
Mavvz 124,172,24
E 91
Mavvz 120,172,16
Mavvz 116,172,8
E 92
Mavvz 112,172,0
Rv 168,198,14336
W 5
Mavvc 0,144,32
E 93
Mavvc 4,144,40
C 89,90,91
Mavvc 8,144,48
E 94
Ds 196,24,56,2048
Mavvc 12,144,56
C 90,92,93
Mavvc 28,148,56
Rv 172,198,15360
E 95
Mavvc 24,148,48
Mvvvc 176,192,32
Mavvc 20,148,40
E 96
Mavvc 16,148,32
W 4
Mavvc 32,152,32
E 97
Mavvc 36,152,40
Mvvvc 180,192,40
C 91,94,95
Ds 196,24,56,3072
Mavvc 40,152,48
E 98
Mavvc 44,152,56
C 96,96,97
AS 56,56,28672
Mavvc 60,156,56
E 99
Mavvc 56,156,48
WV 32
B
Mavvc 52,156,40
Rv 144,197,0
E 100
Mavvc 48,156,32
W 15
Mavvc 64,160,32
Rv 148,197,1024
E 101
Mavvc 68,160,40
Mvvvc 184,192,48
C 97,98,99
AM 59,0
Mavvc 72,160,48
E 102
Ds 196,28,57,0
Mavvc 76,160,56
Rv 152,197,2048
C 98,100,101
Mavvc 92,164,56
E 103
Mavvc 88,164,48
Mavvc 84,164,40
Rv 156,197,3072
E 104
Ds 196,28,57,1024
Mavvc 80,164,32
Mvvvc 188,192,56
W 15
Mavvc 96,168,32
E 105
Mavvc 100,168,40
Rv 160,197,4096
C 99,102,103
Mavvc 104,168,48
E 106
Ds 196,28,57,2048
Mavvc 108,168,56
C 104,104,105
Mavvc 124,172,56
Rv 164,197,5120
E 107
Mavvc 120,172,48
Mavvc 116,172,40
E 108
Ds 196,28,57,3072
Mavvc 112,172,32
Rv 168,197,6144
W 5
XAT3_B5%=:
AS 60,60,1
AT3_B5%=:
Mvvan 0,144,192,128
AS 57,57,4096
E 109
Mvvan 8,144,208,132
C 105,106,107
Mvvan 16,144,224,136
Rv 172,197,7168
E 110
Mvvan 24,144,240,140
C 106,108,109
Mvvac 24,148,244
E 111
Mvvac 16,148,228
Rv 144,197,8192
Mvvac 8,148,212
E 112
Mvvac 0,148,196
AM 59,4096
W 5
Mvvac 0,152,200
E 113
Ds 196,28,57,0
Mvvac 8,152,216
Rv 148,197,9216
C 107,110,111
Mvvac 16,152,232
E 114
Mvvac 24,152,248
C 112,112,113
Mvvac 24,156,252
E 115
Mvvac 16,156,236
Rv 152,197,10240
Mvvac 8,156,220
E 116
Mvvac 0,156,204
W 5
Mvvan 4,160,192,128
E 117
Mvvan 12,160,208,132
Rv 156,197,11264
C 113,114,115
Ds 196,28,57,1024
Mvvan 20,160,224,136
E 118
Mvvan 28,160,240,140
C 114,116,117
Mvvac 28,164,244
E 119
Mvvac 20,164,228
Rv 160,197,12288
Mvvac 12,164,212
E 120
Mvvac 4,164,196
W 5
Mvvac 4,168,200
E 121
Mvvac 12,168,216
C 115,118,119
Mvvac 20,168,232
Rv 164,197,13312
E 122
Ds 196,28,57,2048
Mvvac 28,168,248
C 120,120,121
Mvvac 28,172,252
E 123
Mvvac 20,172,236
Mvvac 12,172,220
E 124
Mvvac 4,172,204
Rv 168,197,14336
W 5
Mvvan 32,144,192,128
E 125
Mvvan 40,144,208,132
C 121,122,123
Mvvan 48,144,224,136
E 126
Mvvan 56,144,240,140
C 122,124,125
Ds 196,28,57,3072
Mvvac 56,148,244
Rv 172,197,15360
E 127
Mvvac 48,148,228
AS 57,57,28672
Mvvac 40,148,212
E 0
Mvvac 32,148,196
W 4
Mvvac 32,152,200
E 1
Mvvac 40,152,216
C 123,126,127
Mvvac 48,152,232
E 2
Mvvac 56,152,248
C 0,0,1
Mvvac 56,156,252
E 3
Mvvac 48,156,236
WV 16
B
Mvvac 40,156,220
Rv 144,198,16384
E 4
Mvvac 32,156,204
W 15
Mvvan 36,160,192,128
Rv 148,198,17408
E 5
Mvvan 44,160,208,132
C 1,2,3
Mvvan 52,160,224,136
E 6
Mvvan 60,160,240,140
Rv 152,198,18432
C 2,4,5
AM 58,0
Mvvac 60,164,244
E 7
Ds 196,24,56,0
Mvvac 52,164,228
Mvvac 44,164,212
Rv 156,198,19456
E 8
Mvvac 36,164,196
W 15
Mvvac 36,168,200
E 9
Mvvac 44,168,216
Rv 160,198,20480
C 3,6,7
Mvvac 52,168,232
E 10
Mvvac 60,168,248
C 8,8,9
Ds 196,24,56,1024
Mvvac 60,172,252
Rv 164,198,21504
E 11
Mvvac 52,172,236
Mvvac 44,172,220
E 12
Mvvac 36,172,204
Rv 168,198,22528
W 5
Mavvc 0,144,64
E 13
Mavvc 4,144,72
C 9,10,11
Mavvc 8,144,80
Rv 172,198,23552
E 14
Ds 196,24,56,2048
Mavvc 12,144,88
C 10,12,13
Mavvc 28,148,88
E 15
Mavvc 24,148,80
Rv 144,198,24576
Mvvvc 176,192,64
Mavvc 20,148,72
E 16
Mavvc 16,148,64
W 5
Mavvc 32,152,64
E 17
Mavvc 36,152,72
Rv 148,198,25600
C 11,14,15
Ds 196,24,56,3072
Mavvc 40,152,80
E 18
Mavvc 44,152,88
C 16,16,17
AS 56,56,4096
Mavvc 60,156,88
E 19
Mavvc 56,156,80
Rv 152,198,26624
Mvvvc 180,192,72
Mavvc 52,156,72
E 20
Mavvc 48,156,64
W 5
Mavvc 64,160,64
E 21
Mavvc 68,160,72
Rv 156,198,27648
Mvvvc 184,192,80
C 17,18,19
Mavvc 72,160,80
E 22
Mavvc 76,160,88
C 18,20,21
AM 58,4096
Mavvc 92,164,88
E 23
Ds 196,24,56,0
Mavvc 88,164,80
Rv 160,198,28672
Mavvc 84,164,72
E 24
Mavvc 80,164,64
Mvvvc 188,192,88
W 5
Mavvc 96,168,64
E 25
Mavvc 100,168,72
C 19,22,23
Mavvc 104,168,80
Rv 164,198,29696
E 26
Mavvc 108,168,88
C 24,24,25
Ds 196,24,56,1024
Mavvc 124,172,88
E 27
Mavvc 120,172,80
Mavvc 116,172,72
E 28
Mavvc 112,172,64
Rv 168,198,30720
W 5
Mavvc 0,144,96
E 29
Mavvc 4,144,104
C 25,26,27
Mavvc 8,144,112
E 30
Ds 196,24,56,2048
Mavvc 12,144,120
C 26,28,29
Mavvc 28,148,120
Rv 172,198,31744
E 31
Mavvc 24,148,112
Mvvvc 176,192,96
Mavvc 20,148,104
E 32
Mavvc 16,148,96
W 4
Mavvc 32,152,96
E 33
Mavvc 36,152,104
Mvvvc 180,192,104
C 27,30,31
Ds 196,24,56,3072
Mavvc 40,152,112
E 34
Mavvc 44,152,120
C 32,32,33
AS 56,56,28672
Mavvc 60,156,120
E 35
Mavvc 56,156,112
WV 16
B
Mavvc 52,156,104
Rv 144,197,16384
E 36
Mavvc 48,156,96
W 15
Mavvc 64,160,96
Rv 148,197,17408
E 37
Mavvc 68,160,104
Mvvvc 184,192,112
C 33,34,35
AM 59,16384
Mavvc 72,160,112
E 38
Ds 196,28,57,0
Mavvc 76,160,120
Rv 152,197,18432
C 34,36,37
Mavvc 92,164,120
E 39
Mavvc 88,164,112
Mavvc 84,164,104
Rv 156,197,19456
E 40
Ds 196,28,57,1024
Mavvc 80,164,96
Mvvvc 188,192,120
W 15
Mavvc 96,168,96
E 41
Mavvc 100,168,104
Rv 160,197,20480
C 35,38,39
Mavvc 104,168,112
E 42
Ds 196,28,57,2048
Mavvc 108,168,120
C 40,40,41
Mavvc 124,172,120
Rv 164,197,21504
E 43
Mavvc 120,172,112
Mavvc 116,172,104
E 44
Ds 196,28,57,3072
Mavvc 112,172,96
Rv 168,197,22528
W 5
s_bitcmp1_b32 s75, s60
s_cbranch_scc1 XAT3_B7%=
XAT3_B6%=:
AS 60,60,1
AT3_B6%=:
Mvvan 64,144,192,128
AS 57,57,4096
E 45
Mvvan 72,144,208,132
C 41,42,43
Mvvan 80,144,224,136
Rv 172,197,23552
E 46
Mvvan 88,144,240,140
C 42,44,45
Mvvac 88,148,244
E 47
Mvvac 80,148,228
Rv 144,197,24576
Mvvac 72,148,212
E 48
Mvvac 64,148,196
AM 59,20480
W 5
Mvvac 64,152,200
E 49
Ds 196,28,57,0
Mvvac 72,152,216
Rv 148,197,25600
C 43,46,47
Mvvac 80,152,232
E 50
Mvvac 88,152,248
C 48,48,49
Mvvac 88,156,252
E 51
Mvvac 80,156,236
Rv 152,197,26624
Mvvac 72,156,220
E 52
Mvvac 64,156,204
W 5
Mvvan 68,160,192,128
E 53
Mvvan 76,160,208,132
Rv 156,197,27648
C 49,50,51
Ds 196,28,57,1024
Mvvan 84,160,224,136
E 54
Mvvan 92,160,240,140
C 50,52,53
Mvvac 92,164,244
E 55
Mvvac 84,164,228
Rv 160,197,28672
Mvvac 76,164,212
E 56
Mvvac 68,164,196
W 5
Mvvac 68,168,200
E 57
Mvvac 76,168,216
C 51,54,55
Mvvac 84,168,232
Rv 164,197,29696
E 58
Ds 196,28,57,2048
Mvvac 92,168,248
C 56,56,57
Mvvac 92,172,252
E 59
Mvvac 84,172,236
Mvvac 76,172,220
E 60
Mvvac 68,172,204
Rv 168,197,30720
W 5
Mvvan 96,144,192,128
E 61
Mvvan 104,144,208,132
C 57,58,59
Mvvan 112,144,224,136
E 62
Mvvan 120,144,240,140
C 58,60,61
Ds 196,28,57,3072
Mvvac 120,148,244
Rv 172,197,31744
E 63
Mvvac 112,148,228
AS 57,57,28672
Mvvac 104,148,212
E 64
Mvvac 96,148,196
W 4
Mvvac 96,152,200
E 65
Mvvac 104,152,216
C 59,62,63
Mvvac 112,152,232
E 66
Mvvac 120,152,248
C 64,64,65
Mvvac 120,156,252
E 67
Mvvac 112,156,236
WV 16
B
Mvvac 104,156,220
Rv 144,198,0
E 68
Mvvac 96,156,204
W 15
Mvvan 100,160,192,128
Rv 148,198,1024
E 69
Mvvan 108,160,208,132
C 65,66,67
Mvvan 116,160,224,136
E 70
Mvvan 124,160,240,140
Rv 152,198,2048
C 66,68,69
AM 58,16384
Mvvac 124,164,244
E 71
Ds 196,24,56,0
Mvvac 116,164,228
Mvvac 108,164,212
Rv 156,198,3072
E 72
Mvvac 100,164,196
W 15
Mvvac 100,168,200
E 73
Mvvac 108,168,216
Rv 160,198,4096
C 67,70,71
Mvvac 116,168,232
E 74
Mvvac 124,168,248
C 72,72,73
Ds 196,24,56,1024
Mvvac 124,172,252
Rv 164,198,5120
E 75
Mvvac 116,172,236
Mvvac 108,172,220
E 76
Mvvac 100,172,204
Rv 168,198,6144
W 5
Mavvc 0,144,0
E 77
Mavvc 4,144,8
C 73,74,75
Mavvc 8,144,16
Rv 172,198,7168
E 78
Ds 196,24,56,2048
Mavvc 12,144,24
C 74,76,77
Mavvc 28,148,24
E 79
Mavvc 24,148,16
Rv 144,198,8192
Mvvvc 176,192,0
Mavvc 20,148,8
E 80
Mavvc 16,148,0
W 5
Mavvc 32,152,0
E 81
Mavvc 36,152,8
Rv 148,198,9216
C 75,78,79
Ds 196,24,56,3072
Mavvc 40,152,16
E 82
Mavvc 44,152,24
C 80,80,81
AS 56,56,4096
Mavvc 60,156,24
E 83
Mavvc 56,156,16
Rv 152,198,10240
Mvvvc 180,192,8
Mavvc 52,156,8
E 84
Mavvc 48,156,0
W 5
Mavvc 64,160,0
E 85
Mavvc 68,160,8
Rv 156,198,11264
Mvvvc 184,192,16
C 81,82,83
Mavvc 72,160,16
E 86
Mavvc 76,160,24
C 82,84,85
AM 58,20480
Mavvc 92,164,24
E 87
Ds 196,24,56,0
Mavvc 88,164,16
Rv 160,198,12288
Mavvc 84,164,8
E 88
Mavvc 80,164,0
Mvvvc 188,192,24
W 5
Mavvc 96,168,0
E 89
Mavvc 100,168,8
C 83,86,87
Mavvc 104,168,16
Rv 164,198,13312
E 90
Mavvc 108,168,24
C 88,88,89
Ds 196,24,56,1024
Mavvc 124,172,24
E 91
Mavvc 120,172,16
Mavvc 116,172,8
E 92
Mavvc 112,172,0
Rv 168,198,14336
W 5
Mavvc 0,144,32
E 93
Mavvc 4,144,40
C 89,90,91
Mavvc 8,144,48
E 94
Ds 196,24,56,2048
Mavvc 12,144,56
C 90,92,93
Mavvc 28,148,56
Rv 172,198,15360
E 95
Mavvc 24,148,48
Mvvvc 176,192,32
Mavvc 20,148,40
E 96
Mavvc 16,148,32
W 4
Mavvc 32,152,32
E 97
Mavvc 36,152,40
Mvvvc 180,192,40
C 91,94,95
Ds 196,24,56,3072
Mavvc 40,152,48
E 98
Mavvc 44,152,56
C 96,96,97
AS 56,56,28672
Mavvc 60,156,56
E 99
Mavvc 56,156,48
WV 16
B
Mavvc 52,156,40
Rv 144,197,0
E 100
Mavvc 48,156,32
W 15
Mavvc 64,160,32
Rv 148,197,1024
E 101
Mavvc 68,160,40
Mvvvc 184,192,48
C 97,98,99
AM 59,0
Mavvc 72,160,48
E 102
Ds 196,28,57,0
Mavvc 76,160,56
Rv 152,197,2048
C 98,100,101
Mavvc 92,164,56
E 103
Mavvc 88,164,48
Mavvc 84,164,40
Rv 156,197,3072
E 104
Ds 196,28,57,1024
Mavvc 80,164,32
Mvvvc 188,192,56
W 15
Mavvc 96,168,32
E 105
Mavvc 100,168,40
Rv 160,197,4096
C 99,102,103
Mavvc 104,168,48
E 106
Ds 196,28,57,2048
Mavvc 108,168,56
C 104,104,105
Mavvc 124,172,56
Rv 164,197,5120
E 107
Mavvc 120,172,48
Mavvc 116,172,40
E 108
Ds 196,28,57,3072
Mavvc 112,172,32
Rv 168,197,6144
W 5
s_branch XAT3_B5%=
)A"
R"A(XAT3_B7%=:
AS 60,60,1
AT3_B7%=:
Mvvan 64,144,192,128
AS 57,57,4096
E 45
Mvvan 72,144,208,132
C 41,42,43
Mvvan 80,144,224,136
Rv 172,197,23552
E 46
Mvvan 88,144,240,140
C 42,44,45
Mvvac 88,148,244
E 47
Mvvac 80,148,228
Rv 144,197,24576
Mvvac 72,148,212
E 48
Mvvac 64,148,196
AM 59,20480
W 5
Mvvac 64,152,200
E 49
Ds 196,28,57,0
Mvvac 72,152,216
Rv 148,197,25600
C 43,46,47
Mvvac 80,152,232
E 50
Mvvac 88,152,248
C 48,48,49
Mvvac 88,156,252
E 51
Mvvac 80,156,236
Rv 152,197,26624
Mvvac 72,156,220
E 52
Mvvac 64,156,204
W 5
Mvvan 68,160,192,128
E 53
Mvvan 76,160,208,132
Rv 156,197,27648
C 49,50,51
Ds 196,28,57,1024
Mvvan 84,160,224,136
E 54
Mvvan 92,160,240,140
C 50,52,53
Mvvac 92,164,244
E 55
Mvvac 84,164,228
Rv 160,197,28672
Mvvac 76,164,212
E 56
Mvvac 68,164,196
W 5
Mvvac 68,168,200
E 57
Mvvac 76,168,216
C 51,54,55
Mvvac 84,168,232
Rv 164,197,29696
E 58
Ds 196,28,57,2048
Mvvac 92,168,248
C 56,56,57
Mvvac 92,172,252
E 59
Mvvac 84,172,236
Mvvac 76,172,220
E 60
Mvvac 68,172,204
Rv 168,197,30720
W 5
Mvvan 96,144,192,128
E 61
Mvvan 104,144,208,132
C 57,58,59
Mvvan 112,144,224,136
E 62
Mvvan 120,144,240,140
C 58,60,61
Ds 196,28,57,3072
Mvvac 120,148,244
Rv 172,197,31744
E 63
Mvvac 112,148,228
AS 57,57,28672
Mvvac 104,148,212
E 64
Mvvac 96,148,196
W 4
Mvvac 96,152,200
E 65
Mvvac 104,152,216
C 59,62,63
Mvvac 112,152,232
E 66
Mvvac 120,152,248
C 64,64,65
Mvvac 120,156,252
E 67
Mvvac 112,156,236
WV 16
B
Mvvac 104,156,220
Rv 144,198,0
E 68
Mvvac 96,156,204
W 15
Mvvan 100,160,192,128
Rv 148,198,1024
E 69
Mvvan 108,160,208,132
C 65,66,67
Mvvan 116,160,224,136
E 70
Mvvan 124,160,240,140
Rv 152,198,2048
C 66,68,69
Mvvac 124,164,244
E 71
Mvvac 116,164,228
Mvvac 108,164,212
Rv 156,198,3072
E 72
Mvvac 100,164,196
W 15
Mvvac 100,168,200
E 73
Mvvac 108,168,216
Rv 160,198,4096
C 67,70,71
Mvvac 116,168,232
E 74
Mvvac 124,168,248
C 72,72,73
Mvvac 124,172,252
Rv 164,198,5120
E 75
Mvvac 116,172,236
Mvvac 108,172,220
E 76
Mvvac 100,172,204
Rv 168,198,6144
W 5
Mavvc 0,144,0
E 77
Mavvc 4,144,8
C 73,74,75
Mavvc 8,144,16
Rv 172,198,7168
E 78
Mavvc 12,144,24
C 74,76,77
Mavvc 28,148,24
E 79
Mavvc 24,148,16
Rv 144,198,8192
Mvvvc 176,192,0
Mavvc 20,148,8
E 80
Mavvc 16,148,0
W 5
Mavvc 32,152,0
E 81
Mavvc 36,152,8
Rv 148,198,9216
C 75,78,79
Mavvc 40,152,16
E 82
Mavvc 44,152,24
C 80,80,81
Mavvc 60,156,24
E 83
Mavvc 56,156,16
Rv 152,198,10240
Mvvvc 180,192,8
Mavvc 52,156,8
E 84
Mavvc 48,156,0
W 5
Mavvc 64,160,0
E 85
Mavvc 68,160,8
Rv 156,198,11264
Mvvvc 184,192,16
C 81,82,83
Mavvc 72,160,16
E 86
Mavvc 76,160,24
C 82,84,85
Mavvc 92,164,24
E 87
Mavvc 88,164,16
Rv 160,198,12288
Mavvc 84,164,8
E 88
Mavvc 80,164,0
Mvvvc 188,192,24
W 5
Mavvc 96,168,0
E 89
Mavvc 100,168,8
C 83,86,87
Mavvc 104,168,16
Rv 164,198,13312
E 90
Mavvc 108,168,24
C 88,88,89
Mavvc 124,172,24
E 91
Mavvc 120,172,16
Mavvc 116,172,8
E 92
Mavvc 112,172,0
Rv 168,198,14336
W 5
Mavvc 0,144,32
E 93
Mavvc 4,144,40
C 89,90,91
Mavvc 8,144,48
E 94
Mavvc 12,144,56
C 90,92,93
Mavvc 28,148,56
Rv 172,198,15360
E 95
Mavvc 24,148,48
Mvvvc 176,192,32
Mavvc 20,148,40
E 96
Mavvc 16,148,32
W 4
Mavvc 32,152,32
E 97
Mavvc 36,152,40
Mvvvc 180,192,40
C 91,94,95
Mavvc 40,152,48
E 98
Mavvc 44,152,56
C 96,96,97
Mavvc 60,156,56
E 99
Mavvc 56,156,48
WV 8
B
Mavvc 52,156,40
Rv 144,197,0
E 100
Mavvc 48,156,32
W 15
Mavvc 64,160,32
Rv 148,197,1024
E 101
Mavvc 68,160,40
Mvvvc 184,192,48
C 97,98,99
AM 59,0
Mavvc 72,160,48
E 102
Ds 196,28,57,0
Mavvc 76,160,56
Rv 152,197,2048
C 98,100,101
Mavvc 92,164,56
E 103
Mavvc 88,164,48
Mavvc 84,164,40
Rv 156,197,3072
E 104
Ds 196,28,57,1024
Mavvc 80,164,32
Mvvvc 188,192,56
W 15
Mavvc 96,168,32
E 105
Mavvc 100,168,40
Rv 160,197,4096
C 99,102,103
Mavvc 104,168,48
E 106
Ds 196,28,57,2048
Mavvc 108,168,56
C 104,104,105
Mavvc 124,172,56
Rv 164,197,5120
E 107
Mavvc 120,172,48
Mavvc 116,172,40
E 108
Ds 196,28,57,3072
Mavvc 112,172,32
Rv 168,197,6144
W 5
XAT3_B8%=:
AS 60,60,1
AT3_B8%=:
Mvvan 0,144,192,128
AS 57,57,4096
E 109
Mvvan 8,144,208,132
C 105,106,107
Mvvan 16,144,224,136
Rv 172,197,7168
E 110
Mvvan 24,144,240,140
C 106,108,109
Mvvac 24,148,244
E 111
Mvvac 16,148,228
Rv 144,197,8192
Mvvac 8,148,212
E 112
Mvvac 0,148,196
AM 59,4096
W 5
Mvvac 0,152,200
E 113
Ds 196,28,57,0
Mvvac 8,152,216
Rv 148,197,9216
C 107,110,111
Mvvac 16,152,232
E 114
Mvvac 24,152,248
C 112,112,113
Mvvac 24,156,252
E 115
Mvvac 16,156,236
Rv 152,197,10240
Mvvac 8,156,220
E 116
Mvvac 0,156,204
W 5
E 117
C 113,114,115
Mvvan 12,160,208,132
Rv 156,197,11264
Ds 196,28,57,1024
Mvvan 20,160,224,136
E 118
Mvvan 28,160,240,140
C 114,116,117
Mvvac 28,164,244
E 119
Mvvac 20,164,228
Rv 160,197,12288
Mvvac 12,164,212
E 120
C 115,118,119
W 5
E 121
Mvvac 12,168,216
Mvvac 20,168,232
Rv 164,197,13312
E 122
Ds 196,28,57,2048
Mvvac 28,168,248
C 120,120,121
Mvvac 28,172,252
E 123
Mvvac 20,172,236
Mvvac 12,172,220
E 124
Rv 168,197,14336
W 5
E 125
C 121,122,123
Mvvan 48,144,224,136
E 126
Mvvan 56,144,240,140
C 122,124,125
Ds 196,28,57,3072
Mvvac 56,148,244
Rv 172,197,15360
E 127
Mvvac 48,148,228
K 0,199,0,62
AS 57,57,28672
E 0
K 1,199,1,64
W 4
E 1
C 123,126,127
K 2,199,2,66
Mvvac 48,152,232
E 2
Mvvac 56,152,248
K 3,199,3,68
Mvvac 56,156,252
E 3
Mvvac 48,156,236
WV 8
B
Rv 144,198,16384
C 0,0,1
W 15
Rv 148,198,17408
C 1,2,3
v_mov_b32 v2, 0
Mvvan 60,160,240,140
Rv 152,198,18432
Mvvac 60,164,244
Rv 156,198,19456
E 8
W 15
E 9
Rv 160,198,20480
v_mov_b32 v3, 0
E 10
Mvvac 60,168,248
Mvvac 60,172,252
Rv 164,198,21504
E 11
C 8,8,9
K 12,199,12,62
E 12
C 9,10,11
Rv 168,198,22528
K 13,199,13,64
W 5
Mavvc 0,144,64
E 13
Mavvc 4,144,72
K 14,199,14,66
Mavvc 8,144,80
Rv 172,198,23552
E 14
Mavvc 12,144,88
Mvvvc 176,192,64
K 15,199,15,68
Mavvc 28,148,88
E 15
Mavvc 24,148,80
Rv 144,198,24576
C 10,12,13
Mavvc 20,148,72
E 16
Mavvc 16,148,64
W 5
Mavvc 32,152,64
E 17
Mavvc 36,152,72
Rv 148,198,25600
C 11,14,15
Mavvc 40,152,80
E 18
Mavvc 44,152,88
C 16,16,17
Mavvc 60,156,88
E 19
Mavvc 56,156,80
Rv 152,198,26624
Mvvvc 180,192,72
Mavvc 52,156,72
E 20
Mavvc 48,156,64
W 5
Mavvc 64,160,64
E 21
Mavvc 68,160,72
Rv 156,198,27648
Mvvvc 184,192,80
C 17,18,19
Mavvc 72,160,80
E 22
Mavvc 76,160,88
C 18,20,21
Mavvc 92,164,88
E 23
Mavvc 88,164,80
Rv 160,198,28672
Mavvc 84,164,72
E 24
Mavvc 80,164,64
Mvvvc 188,192,88
W 5
Mavvc 96,168,64
E 25
Mavvc 100,168,72
C 19,22,23
Mavvc 104,168,80
Rv 164,198,29696
E 26
Mavvc 108,168,88
C 24,24,25
Mavvc 124,172,88
E 27
Mavvc 120,172,80
Mavvc 116,172,72
E 28
Mavvc 112,172,64
Rv 168,198,30720
W 5
Mavvc 0,144,96
E 29
Mavvc 4,144,104
C 25,26,27
Mavvc 8,144,112
E 30
Mavvc 12,144,120
C 26,28,29
Mavvc 28,148,120
Rv 172,198,31744
E 31
Mavvc 24,148,112
Mvvvc 176,192,96
Mavvc 20,148,104
C 27,30,31
Mavvc 16,148,96
W 4
Mavvc 32,152,96
Mavvc 36,152,104
Mvvvc 180,192,104
Mavvc 40,152,112
Mavvc 44,152,120
Mavvc 60,156,120
Mavvc 56,156,112
W 0
B
Mavvc 52,156,104
Mavvc 48,156,96
W 15
Mavvc 64,160,96
Mvvvc 184,192,112
Mavvc 68,160,104
Mavvc 72,160,112
Mavvc 76,160,120
Mavvc 92,164,120
Mavvc 88,164,112
Mavvc 84,164,104
Mavvc 80,164,96
W 15
Mavvc 96,168,96
Mvvvc 188,192,120
Mavvc 100,168,104
Mavvc 104,168,112
Mavvc 108,168,120
Mavvc 124,172,120
Mavvc 120,172,112
Mavvc 116,172,104
Mavvc 112,172,96
XAT3_B9%=:
AS 60,60,1
AT3_B9%=:
K 48,199,48,62
E 48
K 49,199,49,64
E 49
K 50,199,50,66
E 50
K 51,199,51,68
E 51
C 48,48,49
C 49,50,51
v_mov_b32 v50, 0
E 56
E 57
v_mov_b32 v51, 0
E 58
C 56,56,57
E 59
K 60,199,60,62
E 60
K 61,199,61,64
E 61
C 57,58,59
K 62,199,62,66
E 62
K 63,199,63,68
E 63
C 58,60,61
C 59,62,63
WV 0
B
Rv 144,198,0
Rv 148,198,1024
Rv 152,198,2048
Rv 156,198,3072
Rv 160,198,4096
Rv 164,198,5120
Rv 168,198,6144
W 5
Mavvc 0,144,0
Mavvc 4,144,8
Mavvc 8,144,16
Rv 172,198,7168
Mvvvc 176,192,0
Mavvc 12,144,24
Mavvc 28,148,24
Mavvc 24,148,16
Rv 144,198,8192
Mavvc 20,148,8
Mavvc 16,148,0
W 5
Mavvc 32,152,0
Mavvc 36,152,8
Rv 148,198,9216
Mvvvc 180,192,8
Mavvc 40,152,16
Mavvc 44,152,24
Mavvc 60,156,24
Mavvc 56,156,16
Rv 152,198,10240
Mavvc 52,156,8
Mavvc 48,156,0
W 5
Mavvc 64,160,0
Mvvvc 184,192,16
Mavvc 68,160,8
Rv 156,198,11264
Mavvc 72,160,16
Mavvc 76,160,24
Mavvc 92,164,24
Mavvc 88,164,16
Rv 160,198,12288
Mavvc 84,164,8
Mavvc 80,164,0
W 5
Mavvc 96,168,0
Mvvvc 188,192,24
Mavvc 100,168,8
Mavvc 104,168,16
Rv 164,198,13312
Mavvc 108,168,24
Mavvc 124,172,24
Mavvc 120,172,16
Mavvc 116,172,8
Mavvc 112,172,0
Rv 168,198,14336
W 5
Mavvc 8,144,48
Mavvc 12,144,56
Mavvc 28,148,56
Rv 172,198,15360
Mavvc 24,148,48
W 4
Mavvc 40,152,48
Mavvc 44,152,56
Mavvc 60,156,56
Mavvc 56,156,48
W 0
B
W 15
Mvvvc 184,192,48
Mavvc 72,160,48
Mavvc 76,160,56
Mavvc 92,164,56
Mavvc 88,164,48
W 15
Mvvvc 188,192,56
Mavvc 104,168,48
Mavvc 108,168,56
Mavvc 124,172,56
Mavvc 120,172,48
s_branch XAT3_FIN%=
XAT3_B10%=:
AS 60,60,1
AT3_B10%=:
Mvvaz 0,144,128
Mvvaz 8,144,144
Mvvaz 16,144,160
Rv 172,197,7168
Mvvaz 24,144,176
Mvvac 24,148,180
Mvvac 16,148,164
Rv 144,197,8192
Mvvac 8,148,148
Mvvac 0,148,132
W 5
Mvvac 0,152,136
Mvvac 8,152,152
Rv 148,197,9216
Mvvac 16,152,168
Mvvac 24,152,184
Mvvac 24,156,188
Mvvac 16,156,172
Rv 152,197,10240
Mvvac 8,156,156
Mvvac 0,156,140
W 5
Mvvaz 4,160,128
Mvvaz 12,160,144
Rv 156,197,11264
Mvvaz 20,160,160
N 2
v_max3_f32 v200, v0, v1, v2
Fma 200,200,3
v_max3_f32 v202, v8, v9, v10
Fma 202,202,11
Mvvaz 28,160,176
v_max3_f32 v204, v16, v17, v18
Fma 204,204,19
v_max3_f32 v206, v24, v25, v26
Fma 206,206,27
Mvvac 28,164,180
MV 201,200
MV 203,202
MV 205,204
MV 207,206
Mvvac 20,164,164
Rv 160,197,12288
v_permlane16_swap_b32 v200, v201
v_permlane16_swap_b32 v202, v203
v_permlane16_swap_b32 v204, v205
Mvvac 12,164,148
v_permlane16_swap_b32 v206, v207
Fma 200,200,201
Fma 202,202,203
Fma 204,204,205
Mvvac 4,164,132
Fma 206,206,207
MV 201,200
MV 203,202
MV 205,204
W 5
Mvvac 4,168,136
MV 207,206
v_permlane32_swap_b32 v200, v201
v_permlane32_swap_b32 v202, v203
Mvvac 12,168,152
v_permlane32_swap_b32 v204, v205
v_permlane32_swap_b32 v206, v207
Fma 200,200,201
Fma 202,202,203
Mvvac 20,168,168
Rv 164,197,13312
Fma 204,204,205
Fma 206,206,207
v_sub_f32 v128, 0, v200
v_sub_f32 v129, 0, v200
Mvvac 28,168,184
v_sub_f32 v130, 0, v200
v_sub_f32 v131, 0, v200
v_sub_f32 v132, 0, v202
Mvvac 28,172,188
v_sub_f32 v133, 0, v202
v_sub_f32 v134, 0, v202
v_sub_f32 v135, 0, v202
v_sub_f32 v136, 0, v204
Mvvac 20,172,172
v_sub_f32 v137, 0, v204
v_sub_f32 v138, 0, v204
v_sub_f32 v139, 0, v204
v_sub_f32 v140, 0, v206
Mvvac 12,172,156
v_sub_f32 v141, 0, v206
v_sub_f32 v142, 0, v206
v_sub_f32 v143, 0, v206
Mvvac 4,172,140
Rv 168,197,14336
W 5
Mvvan 32,144,128,128
Mvvan 40,144,144,132
Mvvan 48,144,160,136
Mvvan 56,144,176,140
Mvvac 56,148,180
Rv 172,197,15360
Mvvac 48,148,164
Fad 0,0,128
Mvvac 40,148,148
E 0
Mvvac 32,148,132
Fad 1,1,128
W 4
Mvvac 32,152,136
E 1
Mvvac 40,152,152
Fad 2,2,128
Mvvac 48,152,168
E 2
Mvvac 56,152,184
C 0,0,1
Fad 3,3,128
Mvvac 56,156,188
E 3
Mvvac 48,156,172
Fad 4,4,128
W 0
B
Mvvac 40,156,156
E 4
Mvvac 32,156,140
Fad 5,5,128
W 15
Mvvan 36,160,128,128
E 5
Mvvan 44,160,144,132
C 1,2,3
Fad 6,6,128
Mvvan 52,160,160,136
E 6
Mvvan 60,160,176,140
C 2,4,5
Fad 7,7,128
Mvvac 60,164,180
E 7
Mvvac 52,164,164
Fad 8,8,132
Mvvac 44,164,148
E 8
Mvvac 36,164,132
Fad 9,9,132
W 15
Mvvac 36,168,136
E 9
AM 58,0
Mvvac 44,168,152
C 3,6,7
Fad 10,10,132
Ds 196,24,56,0
Mvvac 52,168,168
E 10
Mvvac 60,168,184
C 8,8,9
Fad 11,11,132
Mvvac 60,172,188
E 11
Mvvac 52,172,172
Fad 12,12,132
Mvvac 44,172,156
E 12
Mvvac 36,172,140
Fad 13,13,132
E 13
C 9,10,11
Fad 14,14,132
E 14
C 10,12,13
Fad 15,15,132
E 15
Fad 16,16,136
E 16
Fad 17,17,136
Ds 196,24,56,1024
E 17
C 11,14,15
Fad 18,18,136
E 18
C 16,16,17
Fad 19,19,136
E 19
Fad 20,20,136
E 20
Fad 21,21,136
E 21
C 17,18,19
Fad 22,22,136
E 22
C 18,20,21
Fad 23,23,136
E 23
Fad 24,24,140
Ds 196,24,56,2048
E 24
Fad 25,25,140
E 25
C 19,22,23
Fad 26,26,140
E 26
C 24,24,25
Fad 27,27,140
E 27
Fad 28,28,140
E 28
Fad 29,29,140
E 29
C 25,26,27
Fad 30,30,140
E 30
Fad 31,31,140
Ds 196,24,56,3072
E 31
C 26,28,29
AS 56,56,16384
E 32
E 33
C 27,30,31
E 34
C 32,32,33
E 35
W 0
B
Rv 144,197,16384
E 36
Rv 148,197,17408
E 37
C 33,34,35
AM 59,16384
E 38
Ds 196,28,57,0
Rv 152,197,18432
C 34,36,37
E 39
Rv 156,197,19456
E 40
Ds 196,28,57,1024
E 41
Rv 160,197,20480
C 35,38,39
)A"
R"A(E 42
Ds 196,28,57,2048
C 40,40,41
Rv 164,197,21504
E 43
E 44
Ds 196,28,57,3072
Rv 168,197,22528
AS 57,57,16384
W 5
XAT3_B11%=:
AS 60,60,1
AT3_B11%=:
Mvvan 64,144,128,128
E 45
Mvvan 72,144,144,132
C 41,42,43
Mvvan 80,144,160,136
Rv 172,197,23552
E 46
Mvvan 88,144,176,140
C 42,44,45
Mvvac 88,148,180
E 47
Mvvac 80,148,164
Rv 144,197,24576
Mvvac 72,148,148
E 48
Mvvac 64,148,132
W 5
Mvvac 64,152,136
E 49
Mvvac 72,152,152
Rv 148,197,25600
C 43,46,47
Mvvac 80,152,168
E 50
Mvvac 88,152,184
C 48,48,49
Mvvac 88,156,188
E 51
Mvvac 80,156,172
Rv 152,197,26624
Mvvac 72,156,156
E 52
Mvvac 64,156,140
W 5
Mvvan 68,160,128,128
E 53
Mvvan 76,160,144,132
Rv 156,197,27648
C 49,50,51
Mvvan 84,160,160,136
E 54
Mvvan 92,160,176,140
C 50,52,53
Mvvac 92,164,180
E 55
Mvvac 84,164,164
Rv 160,197,28672
Mvvac 76,164,148
E 56
Mvvac 68,164,132
W 5
Mvvac 68,168,136
E 57
Mvvac 76,168,152
C 51,54,55
Mvvac 84,168,168
Rv 164,197,29696
E 58
Mvvac 92,168,184
C 56,56,57
Mvvac 92,172,188
E 59
Mvvac 84,172,172
Mvvac 76,172,156
E 60
Mvvac 68,172,140
Rv 168,197,30720
W 5
Mvvan 96,144,128,128
E 61
Mvvan 104,144,144,132
C 57,58,59
Mvvan 112,144,160,136
E 62
Mvvan 120,144,176,140
C 58,60,61
Mvvac 120,148,180
Rv 172,197,31744
E 63
Mvvac 112,148,164
Mvvac 104,148,148
E 64
Mvvac 96,148,132
W 4
Mvvac 96,152,136
E 65
Mvvac 104,152,152
C 59,62,63
Mvvac 112,152,168
E 66
Mvvac 120,152,184
C 64,64,65
Mvvac 120,156,188
E 67
Mvvac 112,156,172
WV 8
B
Mvvac 104,156,156
Rv 144,198,0
E 68
Mvvac 96,156,140
W 15
Mvvan 100,160,128,128
Rv 148,198,1024
E 69
Mvvan 108,160,144,132
C 65,66,67
Mvvan 116,160,160,136
E 70
Mvvan 124,160,176,140
Rv 152,198,2048
C 66,68,69
Mvvac 124,164,180
E 71
Mvvac 116,164,164
Mvvac 108,164,148
Rv 156,198,3072
E 72
Mvvac 100,164,132
W 15
Mvvac 100,168,136
E 73
AM 58,16384
Mvvac 108,168,152
Rv 160,198,4096
C 67,70,71
Ds 196,24,56,0
Mvvac 116,168,168
E 74
Mvvac 124,168,184
C 72,72,73
Mvvac 124,172,188
Rv 164,198,5120
E 75
Mvvac 116,172,172
Mvvac 108,172,156
E 76
Mvvac 100,172,140
Rv 168,198,6144
W 5
Mavvz 0,144,0
E 77
Mavvz 4,144,8
C 73,74,75
Mavvz 8,144,16
Rv 172,198,7168
E 78
Mavvz 12,144,24
C 74,76,77
Mavvz 28,148,24
E 79
Mavvz 24,148,16
Rv 144,198,8192
Mvvvz 176,192,0
Mavvz 20,148,8
E 80
Mavvz 16,148,0
Ds 196,24,56,1024
W 5
Mavvz 32,152,0
E 81
Mavvz 36,152,8
Rv 148,198,9216
C 75,78,79
Mavvz 40,152,16
E 82
Mavvz 44,152,24
C 80,80,81
Mavvz 60,156,24
E 83
Mavvz 56,156,16
Rv 152,198,10240
Mvvvz 180,192,8
Mavvz 52,156,8
E 84
Mavvz 48,156,0
W 5
Mavvz 64,160,0
E 85
Mavvz 68,160,8
Rv 156,198,11264
Mvvvz 184,192,16
C 81,82,83
Mavvz 72,160,16
E 86
Mavvz 76,160,24
C 82,84,85
Mavvz 92,164,24
E 87
Mavvz 88,164,16
Rv 160,198,12288
Ds 196,24,56,2048
Mavvz 84,164,8
E 88
Mavvz 80,164,0
Mvvvz 188,192,24
W 5
Mavvz 96,168,0
E 89
Mavvz 100,168,8
C 83,86,87
Mavvz 104,168,16
Rv 164,198,13312
E 90
Mavvz 108,168,24
C 88,88,89
Mavvz 124,172,24
E 91
Mavvz 120,172,16
Mavvz 116,172,8
E 92
Mavvz 112,172,0
Rv 168,198,14336
W 5
Mavvc 0,144,32
E 93
Mavvc 4,144,40
C 89,90,91
Mavvc 8,144,48
E 94
Mavvc 12,144,56
C 90,92,93
Ds 196,24,56,3072
Mavvc 28,148,56
Rv 172,198,15360
E 95
Mavvc 24,148,48
Mvvvc 176,192,32
AS 56,56,16384
Mavvc 20,148,40
E 96
Mavvc 16,148,32
W 4
Mavvc 32,152,32
E 97
Mavvc 36,152,40
Mvvvc 180,192,40
C 91,94,95
Mavvc 40,152,48
E 98
Mavvc 44,152,56
C 96,96,97
Mavvc 60,156,56
E 99
Mavvc 56,156,48
WV 8
B
Mavvc 52,156,40
Rv 144,197,0
E 100
Mavvc 48,156,32
W 15
Mavvc 64,160,32
Rv 148,197,1024
E 101
Mavvc 68,160,40
Mvvvc 184,192,48
C 97,98,99
AM 59,0
Mavvc 72,160,48
E 102
Ds 196,28,57,0
Mavvc 76,160,56
Rv 152,197,2048
C 98,100,101
Mavvc 92,164,56
E 103
Mavvc 88,164,48
Mavvc 84,164,40
Rv 156,197,3072
E 104
Ds 196,28,57,1024
Mavvc 80,164,32
Mvvvc 188,192,56
W 15
Mavvc 96,168,32
E 105
Mavvc 100,168,40
Rv 160,197,4096
C 99,102,103
Mavvc 104,168,48
E 106
Ds 196,28,57,2048
Mavvc 108,168,56
C 104,104,105
Mavvc 124,172,56
Rv 164,197,5120
E 107
Mavvc 120,172,48
Mavvc 116,172,40
E 108
Ds 196,28,57,3072
Mavvc 112,172,32
Rv 168,197,6144
AS 57,57,16384
W 5
s_bitcmp1_b32 s76, s60
s_cbranch_scc1 XAT3_B12%=
s_branch XAT3_B14%=
XAT3_B12%=:
AS 60,60,1
AT3_B12%=:
Mvvan 0,144,128,128
E 109
Mvvan 8,144,144,132
C 105,106,107
Mvvan 16,144,160,136
Rv 172,197,7168
E 110
Mvvan 24,144,176,140
C 106,108,109
Mvvac 24,148,180
E 111
Mvvac 16,148,164
Rv 144,197,8192
Mvvac 8,148,148
E 112
Mvvac 0,148,132
W 5
Mvvac 0,152,136
E 113
Mvvac 8,152,152
Rv 148,197,9216
C 107,110,111
Mvvac 16,152,168
E 114
Mvvac 24,152,184
C 112,112,113
Mvvac 24,156,188
E 115
Mvvac 16,156,172
Rv 152,197,10240
Mvvac 8,156,156
E 116
Mvvac 0,156,140
W 5
Mvvan 4,160,128,128
E 117
Mvvan 12,160,144,132
Rv 156,197,11264
C 113,114,115
Mvvan 20,160,160,136
E 118
Mvvan 28,160,176,140
C 114,116,117
Mvvac 28,164,180
E 119
Mvvac 20,164,164
Rv 160,197,12288
Mvvac 12,164,148
E 120
Mvvac 4,164,132
W 5
Mvvac 4,168,136
E 121
Mvvac 12,168,152
C 115,118,119
Mvvac 20,168,168
Rv 164,197,13312
E 122
Mvvac 28,168,184
C 120,120,121
Mvvac 28,172,188
E 123
Mvvac 20,172,172
Mvvac 12,172,156
E 124
Mvvac 4,172,140
Rv 168,197,14336
W 5
Mvvan 32,144,128,128
E 125
Mvvan 40,144,144,132
C 121,122,123
Mvvan 48,144,160,136
E 126
Mvvan 56,144,176,140
C 122,124,125
Mvvac 56,148,180
Rv 172,197,15360
E 127
Mvvac 48,148,164
Mvvac 40,148,148
E 0
Mvvac 32,148,132
W 4
Mvvac 32,152,136
E 1
Mvvac 40,152,152
C 123,126,127
Mvvac 48,152,168
E 2
Mvvac 56,152,184
C 0,0,1
Mvvac 56,156,188
E 3
AS 56,56,12288
SM 58,40960
Mvvac 48,156,172
WV 8
B
Mvvac 40,156,156
Rv 144,198,16384
E 4
Mvvac 32,156,140
W 15
Mvvan 36,160,128,128
Rv 148,198,17408
E 5
Mvvan 44,160,144,132
C 1,2,3
Mvvan 52,160,160,136
E 6
Mvvan 60,160,176,140
Rv 152,198,18432
C 2,4,5
AM 58,0
Mvvac 60,164,180
E 7
Ds 196,24,56,0
Mvvac 52,164,164
Mvvac 44,164,148
Rv 156,198,19456
E 8
Mvvac 36,164,132
W 15
Mvvac 36,168,136
E 9
Mvvac 44,168,152
Rv 160,198,20480
C 3,6,7
Mvvac 52,168,168
E 10
Mvvac 60,168,184
C 8,8,9
Ds 196,24,56,1024
Mvvac 60,172,188
Rv 164,198,21504
E 11
Mvvac 52,172,172
Mvvac 44,172,156
E 12
Mvvac 36,172,140
Rv 168,198,22528
W 5
Mavvc 0,144,64
E 13
Mavvc 4,144,72
C 9,10,11
Mavvc 8,144,80
Rv 172,198,23552
E 14
Ds 196,24,56,2048
Mavvc 12,144,88
C 10,12,13
Mavvc 28,148,88
E 15
Mavvc 24,148,80
Rv 144,198,24576
Mvvvc 176,192,64
Mavvc 20,148,72
E 16
Mavvc 16,148,64
W 5
Mavvc 32,152,64
E 17
Mavvc 36,152,72
Rv 148,198,25600
C 11,14,15
Ds 196,24,56,3072
Mavvc 40,152,80
E 18
Mavvc 44,152,88
C 16,16,17
AS 56,56,4096
Mavvc 60,156,88
E 19
Mavvc 56,156,80
Rv 152,198,26624
Mvvvc 180,192,72
Mavvc 52,156,72
E 20
Mavvc 48,156,64
W 5
Mavvc 64,160,64
E 21
Mavvc 68,160,72
Rv 156,198,27648
Mvvvc 184,192,80
C 17,18,19
Mavvc 72,160,80
E 22
Mavvc 76,160,88
C 18,20,21
AM 58,4096
Mavvc 92,164,88
E 23
Ds 196,24,56,0
Mavvc 88,164,80
Rv 160,198,28672
Mavvc 84,164,72
E 24
Mavvc 80,164,64
Mvvvc 188,192,88
W 5
Mavvc 96,168,64
E 25
Mavvc 100,168,72
C 19,22,23
Mavvc 104,168,80
Rv 164,198,29696
E 26
Mavvc 108,168,88
C 24,24,25
Ds 196,24,56,1024
Mavvc 124,172,88
E 27
Mavvc 120,172,80
Mavvc 116,172,72
E 28
Mavvc 112,172,64
Rv 168,198,30720
W 5
Mavvc 0,144,96
E 29
Mavvc 4,144,104
C 25,26,27
Mavvc 8,144,112
E 30
Ds 196,24,56,2048
Mavvc 12,144,120
C 26,28,29
Mavvc 28,148,120
Rv 172,198,31744
E 31
Mavvc 24,148,112
Mvvvc 176,192,96
Mavvc 20,148,104
E 32
Mavvc 16,148,96
W 4
Mavvc 32,152,96
E 33
Mavvc 36,152,104
Mvvvc 180,192,104
C 27,30,31
Ds 196,24,56,3072
Mavvc 40,152,112
E 34
Mavvc 44,152,120
C 32,32,33
AS 56,56,28672
Mavvc 60,156,120
E 35
Mavvc 56,156,112
WV 12
B
Mavvc 52,156,104
Rv 144,197,16384
E 36
Mavvc 48,156,96
W 15
Mavvc 64,160,96
Rv 148,197,17408
E 37
Mavvc 68,160,104
Mvvvc 184,192,112
C 33,34,35
AM 59,16384
Mavvc 72,160,112
E 38
Ds 196,28,57,0
Mavvc 76,160,120
Rv 152,197,18432
C 34,36,37
Mavvc 92,164,120
E 39
Mavvc 88,164,112
Mavvc 84,164,104
Rv 156,197,19456
E 40
Ds 196,28,57,1024
Mavvc 80,164,96
Mvvvc 188,192,120
W 15
Mavvc 96,168,96
E 41
Mavvc 100,168,104
Rv 160,197,20480
C 35,38,39
Mavvc 104,168,112
E 42
Ds 196,28,57,2048
Mavvc 108,168,120
C 40,40,41
Mavvc 124,172,120
Rv 164,197,21504
E 43
Mavvc 120,172,112
Mavvc 116,172,104
E 44
Ds 196,28,57,3072
Mavvc 112,172,96
Rv 168,197,22528
AS 57,57,16384
W 5
s_bitcmp1_b32 s77, s60
s_cbranch_scc1 XAT3_B17%=
XAT3_B13%=:
AS 60,60,1
AT3_B13%=:
Mvvan 64,144,128,128
E 45
Mvvan 72,144,144,132
C 41,42,43
Mvvan 80,144,160,136
Rv 172,197,23552
E 46
Mvvan 88,144,176,140
C 42,44,45
Mvvac 88,148,180
E 47
Mvvac 80,148,164
Rv 144,197,24576
Mvvac 72,148,148
E 48
Mvvac 64,148,132
W 5
Mvvac 64,152,136
E 49
Mvvac 72,152,152
Rv 148,197,25600
C 43,46,47
Mvvac 80,152,168
E 50
Mvvac 88,152,184
C 48,48,49
Mvvac 88,156,188
E 51
Mvvac 80,156,172
Rv 152,197,26624
Mvvac 72,156,156
E 52
Mvvac 64,156,140
W 5
E 53
C 49,50,51
Mvvan 76,160,144,132
Rv 156,197,27648
Mvvan 84,160,160,136
E 54
Mvvan 92,160,176,140
C 50,52,53
Mvvac 92,164,180
E 55
Mvvac 84,164,164
Rv 160,197,28672
Mvvac 76,164,148
E 56
C 51,54,55
W 5
E 57
Mvvac 76,168,152
Mvvac 84,168,168
Rv 164,197,29696
E 58
Mvvac 92,168,184
C 56,56,57
Mvvac 92,172,188
E 59
Mvvac 84,172,172
Mvvac 76,172,156
E 60
Rv 168,197,30720
W 5
E 61
C 57,58,59
Mvvan 112,144,160,136
E 62
Mvvan 120,144,176,140
C 58,60,61
Mvvac 120,148,180
Rv 172,197,31744
E 63
Mvvac 112,148,164
K 64,199,64,62
E 64
K 65,199,65,64
W 4
E 65
C 59,62,63
K 66,199,66,66
Mvvac 112,152,168
E 66
Mvvac 120,152,184
K 67,199,67,68
Mvvac 120,156,188
E 67
Mvvac 112,156,172
WV 12
B
Rv 144,198,0
C 64,64,65
W 15
Rv 148,198,1024
C 65,66,67
v_mov_b32 v66, 0
Mvvan 124,160,176,140
Rv 152,198,2048
AM 58,16384
Mvvac 124,164,180
Ds 196,24,56,0
Rv 156,198,3072
E 72
W 15
E 73
Rv 160,198,4096
v_mov_b32 v67, 0
E 74
Mvvac 124,168,184
Ds 196,24,56,1024
Mvvac 124,172,188
Rv 164,198,5120
E 75
C 72,72,73
K 76,199,76,62
E 76
C 73,74,75
Rv 168,198,6144
K 77,199,77,64
W 5
Mavvc 0,144,0
E 77
Mavvc 4,144,8
K 78,199,78,66
Mavvc 8,144,16
Rv 172,198,7168
E 78
Ds 196,24,56,2048
Mavvc 12,144,24
Mvvvc 176,192,0
K 79,199,79,68
Mavvc 28,148,24
E 79
Mavvc 24,148,16
Rv 144,198,8192
C 74,76,77
Mavvc 20,148,8
E 80
Mavvc 16,148,0
W 5
Mavvc 32,152,0
E 81
Mavvc 36,152,8
Rv 148,198,9216
C 75,78,79
Ds 196,24,56,3072
Mavvc 40,152,16
E 82
Mavvc 44,152,24
C 80,80,81
AS 56,56,4096
Mavvc 60,156,24
E 83
Mavvc 56,156,16
Rv 152,198,10240
Mvvvc 180,192,8
Mavvc 52,156,8
E 84
Mavvc 48,156,0
W 5
Mavvc 64,160,0
E 85
Mavvc 68,160,8
Rv 156,198,11264
Mvvvc 184,192,16
C 81,82,83
Mavvc 72,160,16
E 86
Mavvc 76,160,24
C 82,84,85
AM 58,20480
Mavvc 92,164,24
E 87
Ds 196,24,56,0
Mavvc 88,164,16
Rv 160,198,12288
Mavvc 84,164,8
E 88
Mavvc 80,164,0
Mvvvc 188,192,24
W 5
Mavvc 96,168,0
E 89
Mavvc 100,168,8
C 83,86,87
Mavvc 104,168,16
Rv 164,198,13312
E 90
Mavvc 108,168,24
C 88,88,89
Ds 196,24,56,1024
Mavvc 124,172,24
E 91
Mavvc 120,172,16
Mavvc 116,172,8
E 92
Mavvc 112,172,0
Rv 168,198,14336
W 5
Mavvc 0,144,32
E 93
Mavvc 4,144,40
C 89,90,91
Mavvc 8,144,48
E 94
Ds 196,24,56,2048
Mavvc 12,144,56
C 90,92,93
Mavvc 28,148,56
Rv 172,198,15360
E 95
Mavvc 24,148,48
Mvvvc 176,192,32
Mavvc 20,148,40
C 91,94,95
Mavvc 16,148,32
W 4
Mavvc 32,152,32
Mavvc 36,152,40
Mvvvc 180,192,40
Ds 196,24,56,3072
Mavvc 40,152,48
Mavvc 44,152,56
AS 56,56,28672
Mavvc 60,156,56
Mavvc 56,156,48
WV 12
B
AS 57,57,12288
SM 59,106496
VA 197,32768,197
Mavvc 52,156,40
Rv 144,197,0
Mavvc 48,156,32
W 15
Mavvc 64,160,32
Rv 148,197,1024
Mvvvc 184,192,48
Mavvc 68,160,40
AM 59,0
Mavvc 72,160,48
Ds 196,28,57,0
Mavvc 76,160,56
Rv 152,197,2048
Mavvc 92,164,56
Mavvc 88,164,48
Mavvc 84,164,40
Rv 156,197,3072
Ds 196,28,57,1024
Mavvc 80,164,32
W 15
Mavvc 96,168,32
Mvvvc 188,192,56
Mavvc 100,168,40
Rv 160,197,4096
Mavvc 104,168,48
Ds 196,28,57,2048
Mavvc 108,168,56
Mavvc 124,172,56
Rv 164,197,5120
Mavvc 120,172,48
Mavvc 116,172,40
Ds 196,28,57,3072
Mavvc 112,172,32
Rv 168,197,6144
W 5
s_branch XAT3_B2%=
XAT3_B14%=:
AS 60,60,1
AT3_B14%=:
Mvvan 0,144,128,128
E 109
Mvvan 8,144,144,132
C 105,106,107
Mvvan 16,144,160,136
Rv 172,197,7168
E 110
Mvvan 24,144,176,140
C 106,108,109
Mvvac 24,148,180
E 111
Mvvac 16,148,164
Rv 144,197,8192
Mvvac 8,148,148
E 112
Mvvac 0,148,132
W 5
Mvvac 0,152,136
E 113
Mvvac 8,152,152
Rv 148,197,9216
C 107,110,111
Mvvac 16,152,168
E 114
Mvvac 24,152,184
C 112,112,113
Mvvac 24,156,188
)A"
R"A(E 115
Mvvac 16,156,172
Rv 152,197,10240
Mvvac 8,156,156
E 116
Mvvac 0,156,140
W 5
Mvvan 4,160,128,128
E 117
Mvvan 12,160,144,132
Rv 156,197,11264
C 113,114,115
Mvvan 20,160,160,136
E 118
Mvvan 28,160,176,140
C 114,116,117
Mvvac 28,164,180
E 119
Mvvac 20,164,164
Rv 160,197,12288
Mvvac 12,164,148
E 120
Mvvac 4,164,132
W 5
Mvvac 4,168,136
E 121
Mvvac 12,168,152
C 115,118,119
Mvvac 20,168,168
Rv 164,197,13312
E 122
Mvvac 28,168,184
C 120,120,121
Mvvac 28,172,188
E 123
Mvvac 20,172,172
Mvvac 12,172,156
E 124
Mvvac 4,172,140
Rv 168,197,14336
W 5
Mvvan 32,144,128,128
E 125
Mvvan 40,144,144,132
C 121,122,123
Mvvan 48,144,160,136
E 126
Mvvan 56,144,176,140
C 122,124,125
Mvvac 56,148,180
Rv 172,197,15360
E 127
Mvvac 48,148,164
Mvvac 40,148,148
E 0
Mvvac 32,148,132
W 4
Mvvac 32,152,136
E 1
Mvvac 40,152,152
C 123,126,127
Mvvac 48,152,168
E 2
Mvvac 56,152,184
C 0,0,1
Mvvac 56,156,188
E 3
Mvvac 48,156,172
WV 8
B
Mvvac 40,156,156
Rv 144,198,16384
E 4
Mvvac 32,156,140
W 15
Mvvan 36,160,128,128
Rv 148,198,17408
E 5
Mvvan 44,160,144,132
C 1,2,3
Mvvan 52,160,160,136
E 6
Mvvan 60,160,176,140
Rv 152,198,18432
C 2,4,5
Mvvac 60,164,180
E 7
Mvvac 52,164,164
Mvvac 44,164,148
Rv 156,198,19456
E 8
Mvvac 36,164,132
W 15
Mvvac 36,168,136
E 9
AM 58,0
Mvvac 44,168,152
Rv 160,198,20480
C 3,6,7
Ds 196,24,56,0
Mvvac 52,168,168
E 10
Mvvac 60,168,184
C 8,8,9
Mvvac 60,172,188
Rv 164,198,21504
E 11
Mvvac 52,172,172
Mvvac 44,172,156
E 12
Mvvac 36,172,140
Rv 168,198,22528
W 5
Mavvc 0,144,64
E 13
Mavvc 4,144,72
C 9,10,11
Mavvc 8,144,80
Rv 172,198,23552
E 14
Mavvc 12,144,88
C 10,12,13
Mavvc 28,148,88
E 15
Mavvc 24,148,80
Rv 144,198,24576
Mvvvc 176,192,64
Mavvc 20,148,72
E 16
Mavvc 16,148,64
Ds 196,24,56,1024
W 5
Mavvc 32,152,64
E 17
Mavvc 36,152,72
Rv 148,198,25600
C 11,14,15
Mavvc 40,152,80
E 18
Mavvc 44,152,88
C 16,16,17
Mavvc 60,156,88
E 19
Mavvc 56,156,80
Rv 152,198,26624
Mvvvc 180,192,72
Mavvc 52,156,72
E 20
Mavvc 48,156,64
W 5
Mavvc 64,160,64
E 21
Mavvc 68,160,72
Rv 156,198,27648
Mvvvc 184,192,80
C 17,18,19
Mavvc 72,160,80
E 22
Mavvc 76,160,88
C 18,20,21
Mavvc 92,164,88
E 23
Mavvc 88,164,80
Rv 160,198,28672
Ds 196,24,56,2048
Mavvc 84,164,72
E 24
Mavvc 80,164,64
Mvvvc 188,192,88
W 5
Mavvc 96,168,64
E 25
Mavvc 100,168,72
C 19,22,23
Mavvc 104,168,80
Rv 164,198,29696
E 26
Mavvc 108,168,88
C 24,24,25
Mavvc 124,172,88
E 27
Mavvc 120,172,80
Mavvc 116,172,72
E 28
Mavvc 112,172,64
Rv 168,198,30720
W 5
Mavvc 0,144,96
E 29
Mavvc 4,144,104
C 25,26,27
Mavvc 8,144,112
E 30
Mavvc 12,144,120
C 26,28,29
Ds 196,24,56,3072
Mavvc 28,148,120
Rv 172,198,31744
E 31
Mavvc 24,148,112
Mvvvc 176,192,96
AS 56,56,16384
Mavvc 20,148,104
E 32
Mavvc 16,148,96
W 4
Mavvc 32,152,96
E 33
Mavvc 36,152,104
Mvvvc 180,192,104
C 27,30,31
Mavvc 40,152,112
E 34
Mavvc 44,152,120
C 32,32,33
Mavvc 60,156,120
E 35
Mavvc 56,156,112
WV 8
B
Mavvc 52,156,104
Rv 144,197,16384
E 36
Mavvc 48,156,96
W 15
Mavvc 64,160,96
Rv 148,197,17408
E 37
Mavvc 68,160,104
Mvvvc 184,192,112
C 33,34,35
AM 59,16384
Mavvc 72,160,112
E 38
Ds 196,28,57,0
Mavvc 76,160,120
Rv 152,197,18432
C 34,36,37
Mavvc 92,164,120
E 39
Mavvc 88,164,112
Mavvc 84,164,104
Rv 156,197,19456
E 40
Ds 196,28,57,1024
Mavvc 80,164,96
Mvvvc 188,192,120
W 15
Mavvc 96,168,96
E 41
Mavvc 100,168,104
Rv 160,197,20480
C 35,38,39
Mavvc 104,168,112
E 42
Ds 196,28,57,2048
Mavvc 108,168,120
C 40,40,41
Mavvc 124,172,120
Rv 164,197,21504
E 43
Mavvc 120,172,112
Mavvc 116,172,104
E 44
Ds 196,28,57,3072
Mavvc 112,172,96
Rv 168,197,22528
AS 57,57,16384
W 5
XAT3_B15%=:
AS 60,60,1
AT3_B15%=:
Mvvan 64,144,128,128
E 45
Mvvan 72,144,144,132
C 41,42,43
Mvvan 80,144,160,136
Rv 172,197,23552
E 46
Mvvan 88,144,176,140
C 42,44,45
Mvvac 88,148,180
E 47
Mvvac 80,148,164
Rv 144,197,24576
Mvvac 72,148,148
E 48
Mvvac 64,148,132
W 5
Mvvac 64,152,136
E 49
Mvvac 72,152,152
Rv 148,197,25600
C 43,46,47
Mvvac 80,152,168
E 50
Mvvac 88,152,184
C 48,48,49
Mvvac 88,156,188
E 51
Mvvac 80,156,172
Rv 152,197,26624
Mvvac 72,156,156
E 52
Mvvac 64,156,140
W 5
Mvvan 68,160,128,128
E 53
Mvvan 76,160,144,132
Rv 156,197,27648
C 49,50,51
Mvvan 84,160,160,136
E 54
Mvvan 92,160,176,140
C 50,52,53
Mvvac 92,164,180
E 55
Mvvac 84,164,164
Rv 160,197,28672
Mvvac 76,164,148
E 56
Mvvac 68,164,132
W 5
Mvvac 68,168,136
E 57
Mvvac 76,168,152
C 51,54,55
Mvvac 84,168,168
Rv 164,197,29696
E 58
Mvvac 92,168,184
C 56,56,57
Mvvac 92,172,188
E 59
Mvvac 84,172,172
Mvvac 76,172,156
E 60
Mvvac 68,172,140
Rv 168,197,30720
W 5
Mvvan 96,144,128,128
E 61
Mvvan 104,144,144,132
C 57,58,59
Mvvan 112,144,160,136
E 62
Mvvan 120,144,176,140
C 58,60,61
Mvvac 120,148,180
Rv 172,197,31744
E 63
Mvvac 112,148,164
Mvvac 104,148,148
E 64
Mvvac 96,148,132
W 4
Mvvac 96,152,136
E 65
Mvvac 104,152,152
C 59,62,63
Mvvac 112,152,168
E 66
Mvvac 120,152,184
C 64,64,65
Mvvac 120,156,188
E 67
Mvvac 112,156,172
WV 8
B
Mvvac 104,156,156
Rv 144,198,0
E 68
Mvvac 96,156,140
W 15
Mvvan 100,160,128,128
Rv 148,198,1024
E 69
Mvvan 108,160,144,132
C 65,66,67
Mvvan 116,160,160,136
E 70
Mvvan 124,160,176,140
Rv 152,198,2048
C 66,68,69
Mvvac 124,164,180
E 71
Mvvac 116,164,164
Mvvac 108,164,148
Rv 156,198,3072
E 72
Mvvac 100,164,132
W 15
Mvvac 100,168,136
E 73
AM 58,16384
Mvvac 108,168,152
Rv 160,198,4096
C 67,70,71
Ds 196,24,56,0
Mvvac 116,168,168
E 74
Mvvac 124,168,184
C 72,72,73
Mvvac 124,172,188
Rv 164,198,5120
E 75
Mvvac 116,172,172
Mvvac 108,172,156
E 76
Mvvac 100,172,140
Rv 168,198,6144
W 5
Mavvc 0,144,0
E 77
Mavvc 4,144,8
C 73,74,75
Mavvc 8,144,16
Rv 172,198,7168
E 78
Mavvc 12,144,24
C 74,76,77
Mavvc 28,148,24
E 79
Mavvc 24,148,16
Rv 144,198,8192
Mvvvc 176,192,0
Mavvc 20,148,8
E 80
Mavvc 16,148,0
Ds 196,24,56,1024
W 5
Mavvc 32,152,0
E 81
Mavvc 36,152,8
Rv 148,198,9216
C 75,78,79
Mavvc 40,152,16
E 82
Mavvc 44,152,24
C 80,80,81
Mavvc 60,156,24
E 83
Mavvc 56,156,16
Rv 152,198,10240
Mvvvc 180,192,8
Mavvc 52,156,8
E 84
Mavvc 48,156,0
W 5
Mavvc 64,160,0
E 85
Mavvc 68,160,8
Rv 156,198,11264
Mvvvc 184,192,16
C 81,82,83
Mavvc 72,160,16
E 86
Mavvc 76,160,24
C 82,84,85
Mavvc 92,164,24
E 87
Mavvc 88,164,16
Rv 160,198,12288
Ds 196,24,56,2048
Mavvc 84,164,8
E 88
Mavvc 80,164,0
Mvvvc 188,192,24
W 5
Mavvc 96,168,0
E 89
Mavvc 100,168,8
C 83,86,87
Mavvc 104,168,16
Rv 164,198,13312
E 90
Mavvc 108,168,24
C 88,88,89
Mavvc 124,172,24
E 91
Mavvc 120,172,16
Mavvc 116,172,8
E 92
Mavvc 112,172,0
Rv 168,198,14336
W 5
Mavvc 0,144,32
E 93
Mavvc 4,144,40
C 89,90,91
Mavvc 8,144,48
E 94
Mavvc 12,144,56
C 90,92,93
Ds 196,24,56,3072
Mavvc 28,148,56
Rv 172,198,15360
E 95
Mavvc 24,148,48
Mvvvc 176,192,32
AS 56,56,16384
Mavvc 20,148,40
E 96
Mavvc 16,148,32
W 4
Mavvc 32,152,32
E 97
Mavvc 36,152,40
Mvvvc 180,192,40
C 91,94,95
Mavvc 40,152,48
E 98
Mavvc 44,152,56
C 96,96,97
Mavvc 60,156,56
E 99
Mavvc 56,156,48
WV 8
B
Mavvc 52,156,40
Rv 144,197,0
E 100
Mavvc 48,156,32
W 15
Mavvc 64,160,32
Rv 148,197,1024
E 101
Mavvc 68,160,40
Mvvvc 184,192,48
C 97,98,99
AM 59,0
Mavvc 72,160,48
E 102
Ds 196,28,57,0
Mavvc 76,160,56
Rv 152,197,2048
C 98,100,101
Mavvc 92,164,56
E 103
Mavvc 88,164,48
Mavvc 84,164,40
Rv 156,197,3072
E 104
Ds 196,28,57,1024
Mavvc 80,164,32
Mvvvc 188,192,56
W 15
Mavvc 96,168,32
E 105
Mavvc 100,168,40
Rv 160,197,4096
C 99,102,103
Mavvc 104,168,48
E 106
Ds 196,28,57,2048
Mavvc 108,168,56
C 104,104,105
Mavvc 124,172,56
Rv 164,197,5120
E 107
Mavvc 120,172,48
Mavvc 116,172,40
E 108
Ds 196,28,57,3072
Mavvc 112,172,32
Rv 168,197,6144
AS 57,57,16384
W 5
s_bitcmp1_b32 s78, s60
s_cbranch_scc1 XAT3_B12%=
s_branch XAT3_B14%=
XAT3_B16%=:
AS 60,60,1
AT3_B16%=:
Mvvan 64,144,192,128
AS 57,57,4096
E 45
Mvvan 72,144,208,132
C 41,42,43
Mvvan 80,144,224,136
Rv 172,197,23552
E 46
Mvvan 88,144,240,140
C 42,44,45
Mvvac 88,148,244
E 47
Mvvac 80,148,228
Rv 144,197,24576
Mvvac 72,148,212
E 48
Mvvac 64,148,196
AM 59,20480
W 6
Mvvac 64,152,200
E 49
Ds 196,28,57,0
Mvvac 72,152,216
Rv 148,197,25600
C 43,46,47
Mvvac 80,152,232
E 50
Mvvac 88,152,248
C 48,48,49
Mvvac 88,156,252
E 51
Mvvac 80,156,236
Rv 152,197,26624
Mvvac 72,156,220
E 52
Mvvac 64,156,204
W 6
Mvvan 68,160,192,128
E 53
Mvvan 76,160,208,132
Rv 156,197,27648
C 49,50,51
Ds 196,28,57,1024
Mvvan 84,160,224,136
E 54
Mvvan 92,160,240,140
C 50,52,53
Mvvac 92,164,244
E 55
Mvvac 84,164,228
Rv 160,197,28672
Mvvac 76,164,212
E 56
Mvvac 68,164,196
W 5
Mvvac 68,168,200
E 57
Mvvac 76,168,216
C 51,54,55
Mvvac 84,168,232
Rv 164,197,29696
E 58
Ds 196,28,57,2048
Mvvac 92,168,248
C 56,56,57
Mvvac 92,172,252
E 59
Mvvac 84,172,236
Mvvac 76,172,220
E 60
Mvvac 68,172,204
Rv 168,197,30720
W 5
Mvvan 96,144,192,128
E 61
Mvvan 104,144,208,132
C 57,58,59
Mvvan 112,144,224,136
E 62
Mvvan 120,144,240,140
C 58,60,61
Ds 196,28,57,3072
Mvvac 120,148,244
Rv 172,197,31744
E 63
Mvvac 112,148,228
AS 57,57,28672
Mvvac 104,148,212
E 64
Mvvac 96,148,196
W 4
Mvvac 96,152,200
E 65
Mvvac 104,152,216
C 59,62,63
Mvvac 112,152,232
E 66
Mvvac 120,152,248
C 64,64,65
Mvvac 120,156,252
E 67
Mvvac 112,156,236
WV 32
B
VA 198,32768,198
Mvvac 104,156,220
Rv 144,198,0
E 68
Mvvac 96,156,204
W 15
Mvvan 100,160,192,128
Rv 148,198,1024
E 69
Mvvan 108,160,208,132
C 65,66,67
Mvvan 116,160,224,136
E 70
Mvvan 124,160,240,140
Rv 152,198,2048
C 66,68,69
Mvvac 124,164,244
E 71
Mvvac 116,164,228
Mvvac 108,164,212
Rv 156,198,3072
E 72
Mvvac 100,164,196
W 15
Mvvac 100,168,200
E 73
Mvvac 108,168,216
Rv 160,198,4096
C 67,70,71
Mvvac 116,168,232
E 74
Mvvac 124,168,248
C 72,72,73
Mvvac 124,172,252
Rv 164,198,5120
E 75
Mvvac 116,172,236
Mvvac 108,172,220
E 76
Mvvac 100,172,204
Rv 168,198,6144
W 5
Mavvz 0,144,0
E 77
Mavvz 4,144,8
C 73,74,75
Mavvz 8,144,16
Rv 172,198,7168
E 78
Mavvz 12,144,24
C 74,76,77
Mavvz 28,148,24
E 79
Mavvz 24,148,16
Rv 144,198,8192
Mvvvz 176,192,0
Mavvz 20,148,8
E 80
Mavvz 16,148,0
W 5
Mavvz 32,152,0
E 81
Mavvz 36,152,8
Rv 148,198,9216
C 75,78,79
Mavvz 40,152,16
E 82
Mavvz 44,152,24
C 80,80,81
Mavvz 60,156,24
E 83
Mavvz 56,156,16
Rv 152,198,10240
Mvvvz 180,192,8
Mavvz 52,156,8
E 84
Mavvz 48,156,0
W 5
Mavvz 64,160,0
E 85
Mavvz 68,160,8
Rv 156,198,11264
Mvvvz 184,192,16
C 81,82,83
Mavvz 72,160,16
E 86
Mavvz 76,160,24
C 82,84,85
Mavvz 92,164,24
E 87
Mavvz 88,164,16
Rv 160,198,12288
Mavvz 84,164,8
E 88
Mavvz 80,164,0
Mvvvz 188,192,24
W 5
Mavvz 96,168,0
E 89
Mavvz 100,168,8
C 83,86,87
Mavvz 104,168,16
Rv 164,198,13312
E 90
Mavvz 108,168,24
C 88,88,89
Mavvz 124,172,24
E 91
Mavvz 120,172,16
Mavvz 116,172,8
E 92
Mavvz 112,172,0
Rv 168,198,14336
W 5
Mavvc 0,144,32
E 93
Mavvc 4,144,40
C 89,90,91
Mavvc 8,144,48
E 94
Mavvc 12,144,56
C 90,92,93
Mavvc 28,148,56
Rv 172,198,15360
E 95
Mavvc 24,148,48
Mvvvc 176,192,32
Mavvc 20,148,40
E 96
Mavvc 16,148,32
W 4
Mavvc 32,152,32
E 97
Mavvc 36,152,40
Mvvvc 180,192,40
C 91,94,95
Mavvc 40,152,48
E 98
Mavvc 44,152,56
C 96,96,97
Mavvc 60,156,56
E 99
Mavvc 56,156,48
WV 24
B
Mavvc 52,156,40
Rv 144,197,0
E 100
Mavvc 48,156,32
W 15
Mavvc 64,160,32
Rv 148,197,1024
E 101
Mavvc 68,160,40
Mvvvc 184,192,48
C 97,98,99
AM 59,0
Mavvc 72,160,48
E 102
Ds 196,28,57,0
Mavvc 76,160,56
Rv 152,197,2048
C 98,100,101
Mavvc 92,164,56
E 103
Mavvc 88,164,48
Mavvc 84,164,40
Rv 156,197,3072
E 104
Ds 196,28,57,1024
Mavvc 80,164,32
Mvvvc 188,192,56
W 15
Mavvc 96,168,32
E 105
Mavvc 100,168,40
Rv 160,197,4096
C 99,102,103
Mavvc 104,168,48
E 106
Ds 196,28,57,2048
Mavvc 108,168,56
C 104,104,105
Mavvc 124,172,56
Rv 164,197,5120
E 107
Mavvc 120,172,48
Mavvc 116,172,40
E 108
Ds 196,28,57,3072
Mavvc 112,172,32
Rv 168,197,6144
W 5
s_branch XAT3_B8%=
XAT3_B17%=:
AS 60,60,1
AT3_B17%=:
Mvvan 64,144,128,128
E 45
Mvvan 72,144,144,132
C 41,42,43
Mvvan 80,144,160,136
Rv 172,197,23552
E 46
Mvvan 88,144,176,140
C 42,44,45
Mvvac 88,148,180
E 47
Mvvac 80,148,164
Rv 144,197,24576
Mvvac 72,148,148
E 48
Mvvac 64,148,132
W 5
Mvvac 64,152,136
E 49
Mvvac 72,152,152
Rv 148,197,25600
C 43,46,47
Mvvac 80,152,168
E 50
Mvvac 88,152,184
C 48,48,49
Mvvac 88,156,188
E 51
Mvvac 80,156,172
Rv 152,197,26624
Mvvac 72,156,156
E 52
Mvvac 64,156,140
W 5
E 53
C 49,50,51
Mvvan 76,160,144,132
Rv 156,197,27648
Mvvan 84,160,160,136
E 54
Mvvan 92,160,176,140
C 50,52,53
Mvvac 92,164,180
E 55
Mvvac 84,164,164
Rv 160,197,28672
Mvvac 76,164,148
E 56
C 51,54,55
W 5
E 57
Mvvac 76,168,152
Mvvac 84,168,168
Rv 164,197,29696
)A"
R"A(E 58
Mvvac 92,168,184
C 56,56,57
Mvvac 92,172,188
E 59
Mvvac 84,172,172
Mvvac 76,172,156
E 60
Rv 168,197,30720
W 5
E 61
C 57,58,59
Mvvan 112,144,160,136
E 62
Mvvan 120,144,176,140
C 58,60,61
Mvvac 120,148,180
Rv 172,197,31744
E 63
Mvvac 112,148,164
K 64,199,64,62
E 64
K 65,199,65,64
W 4
E 65
C 59,62,63
K 66,199,66,66
Mvvac 112,152,168
E 66
Mvvac 120,152,184
K 67,199,67,68
Mvvac 120,156,188
E 67
Mvvac 112,156,172
WV 12
B
Rv 144,198,0
C 64,64,65
W 15
Rv 148,198,1024
C 65,66,67
v_mov_b32 v66, 0
Mvvan 124,160,176,140
Rv 152,198,2048
Mvvac 124,164,180
Rv 156,198,3072
E 72
W 15
E 73
Rv 160,198,4096
v_mov_b32 v67, 0
E 74
Mvvac 124,168,184
Mvvac 124,172,188
Rv 164,198,5120
E 75
C 72,72,73
K 76,199,76,62
E 76
C 73,74,75
Rv 168,198,6144
K 77,199,77,64
W 5
Mavvc 0,144,0
E 77
Mavvc 4,144,8
K 78,199,78,66
Mavvc 8,144,16
Rv 172,198,7168
E 78
Mavvc 12,144,24
Mvvvc 176,192,0
K 79,199,79,68
Mavvc 28,148,24
E 79
Mavvc 24,148,16
Rv 144,198,8192
C 74,76,77
Mavvc 20,148,8
E 80
Mavvc 16,148,0
W 5
Mavvc 32,152,0
E 81
Mavvc 36,152,8
Rv 148,198,9216
C 75,78,79
Mavvc 40,152,16
E 82
Mavvc 44,152,24
C 80,80,81
Mavvc 60,156,24
E 83
Mavvc 56,156,16
Rv 152,198,10240
Mvvvc 180,192,8
Mavvc 52,156,8
E 84
Mavvc 48,156,0
W 5
Mavvc 64,160,0
E 85
Mavvc 68,160,8
Rv 156,198,11264
Mvvvc 184,192,16
C 81,82,83
Mavvc 72,160,16
E 86
Mavvc 76,160,24
C 82,84,85
Mavvc 92,164,24
E 87
Mavvc 88,164,16
Rv 160,198,12288
Mavvc 84,164,8
E 88
Mavvc 80,164,0
Mvvvc 188,192,24
W 5
Mavvc 96,168,0
E 89
Mavvc 100,168,8
C 83,86,87
Mavvc 104,168,16
Rv 164,198,13312
E 90
Mavvc 108,168,24
C 88,88,89
Mavvc 124,172,24
E 91
Mavvc 120,172,16
Mavvc 116,172,8
E 92
Mavvc 112,172,0
Rv 168,198,14336
W 5
Mavvc 0,144,32
E 93
Mavvc 4,144,40
C 89,90,91
Mavvc 8,144,48
E 94
Mavvc 12,144,56
C 90,92,93
Mavvc 28,148,56
Rv 172,198,15360
E 95
Mavvc 24,148,48
Mvvvc 176,192,32
Mavvc 20,148,40
C 91,94,95
Mavvc 16,148,32
W 4
Mavvc 32,152,32
Mavvc 36,152,40
Mvvvc 180,192,40
Mavvc 40,152,48
Mavvc 44,152,56
Mavvc 60,156,56
Mavvc 56,156,48
WV 4
B
AS 57,57,12288
SM 59,106496
VA 197,32768,197
Mavvc 52,156,40
Rv 144,197,0
Mavvc 48,156,32
W 15
Mavvc 64,160,32
Rv 148,197,1024
Mvvvc 184,192,48
Mavvc 68,160,40
AM 59,0
Mavvc 72,160,48
Ds 196,28,57,0
Mavvc 76,160,56
Rv 152,197,2048
Mavvc 92,164,56
Mavvc 88,164,48
Mavvc 84,164,40
Rv 156,197,3072
Ds 196,28,57,1024
Mavvc 80,164,32
W 15
Mavvc 96,168,32
Mvvvc 188,192,56
Mavvc 100,168,40
Rv 160,197,4096
Mavvc 104,168,48
Ds 196,28,57,2048
Mavvc 108,168,56
Mavvc 124,172,56
Rv 164,197,5120
Mavvc 120,172,48
Mavvc 116,172,40
Ds 196,28,57,3072
Mavvc 112,172,32
Rv 168,197,6144
W 5
XAT3_B18%=:
AS 60,60,1
AT3_B18%=:
Mvvaz 0,144,192
AS 57,57,4096
Mvvaz 8,144,208
Mvvaz 16,144,224
Rv 172,197,7168
Mvvaz 24,144,240
Mvvac 24,148,244
Mvvac 16,148,228
Rv 144,197,8192
K 112,199,112,62
Mvvac 8,148,212
E 112
Mvvac 0,148,196
K 113,199,113,64
AM 59,4096
W 5
Mvvac 0,152,200
E 113
Ds 196,28,57,0
Mvvac 8,152,216
Rv 148,197,9216
K 114,199,114,66
Mvvac 16,152,232
E 114
Mvvac 24,152,248
K 115,199,115,68
Mvvac 24,156,252
E 115
Mvvac 16,156,236
Rv 152,197,10240
C 112,112,113
Mvvac 8,156,220
Mvvac 0,156,204
W 5
C 113,114,115
Mvvaz 12,160,208
Rv 156,197,11264
Ds 196,28,57,1024
Mvvaz 20,160,224
N 1
K 0,199,0,62
K 1,199,1,64
K 2,199,2,66
K 3,199,3,68
v_mov_b32 v114, 0
Mvvaz 28,160,240
v_max3_f32 v200, v0, v1, v2
Fma 200,200,3
v_max3_f32 v202, v8, v9, v10
Fma 202,202,11
Mvvac 28,164,244
v_max3_f32 v204, v16, v17, v18
Fma 204,204,19
v_max3_f32 v206, v24, v25, v26
Fma 206,206,27
Mvvac 20,164,228
Rv 160,197,12288
MV 201,200
MV 203,202
MV 205,204
MV 207,206
Mvvac 12,164,212
E 120
v_permlane16_swap_b32 v200, v201
v_permlane16_swap_b32 v202, v203
v_permlane16_swap_b32 v204, v205
v_permlane16_swap_b32 v206, v207
Fma 200,200,201
Fma 202,202,203
Fma 204,204,205
Fma 206,206,207
v_mov_b32 v115, 0
W 5
E 121
MV 201,200
MV 203,202
MV 205,204
MV 207,206
Mvvac 12,168,216
v_permlane32_swap_b32 v200, v201
v_permlane32_swap_b32 v202, v203
v_permlane32_swap_b32 v204, v205
v_permlane32_swap_b32 v206, v207
Mvvac 20,168,232
Rv 164,197,13312
E 122
Fma 200,200,201
Fma 202,202,203
Fma 204,204,205
Fma 206,206,207
Ds 196,28,57,2048
Mvvac 28,168,248
v_sub_f32 v128, 0, v200
v_sub_f32 v129, 0, v200
v_sub_f32 v130, 0, v200
v_sub_f32 v131, 0, v200
C 120,120,121
Mvvac 28,172,252
E 123
v_sub_f32 v132, 0, v202
v_sub_f32 v133, 0, v202
v_sub_f32 v134, 0, v202
v_sub_f32 v135, 0, v202
Mvvac 20,172,236
v_sub_f32 v136, 0, v204
v_sub_f32 v137, 0, v204
v_sub_f32 v138, 0, v204
v_sub_f32 v139, 0, v204
K 124,199,124,62
Mvvac 12,172,220
E 124
v_sub_f32 v140, 0, v206
v_sub_f32 v141, 0, v206
v_sub_f32 v142, 0, v206
v_sub_f32 v143, 0, v206
Rv 168,197,14336
K 125,199,125,64
W 5
E 125
C 121,122,123
K 126,199,126,66
Mvvan 48,144,224,136
E 126
Mvvan 56,144,240,140
K 127,199,127,68
Ds 196,28,57,3072
Mvvac 56,148,244
Rv 172,197,15360
E 127
Mvvac 48,148,228
Fad 0,0,128
AS 57,57,28672
E 0
C 122,124,125
Fad 1,1,128
W 4
E 1
C 123,126,127
Fad 2,2,128
Mvvac 48,152,232
E 2
Mvvac 56,152,248
Fad 3,3,128
Mvvac 56,156,252
E 3
Mvvac 48,156,236
WV 8
B
Rv 144,198,16384
C 0,0,1
W 15
Rv 148,198,17408
C 1,2,3
v_mov_b32 v2, 0
Mvvan 60,160,240,140
Rv 152,198,18432
Mvvac 60,164,244
Fad 8,8,132
Rv 156,198,19456
E 8
Fad 9,9,132
W 15
E 9
Rv 160,198,20480
v_mov_b32 v3, 0
Fad 10,10,132
E 10
C 8,8,9
Mvvac 60,168,248
Fad 11,11,132
Mvvac 60,172,252
Rv 164,198,21504
E 11
Fad 12,12,132
K 12,199,12,62
E 12
C 9,10,11
Rv 168,198,22528
Fad 13,13,132
K 13,199,13,64
W 5
Mavvc 0,144,64
E 13
Mavvc 4,144,72
Fad 14,14,132
K 14,199,14,66
Mavvc 8,144,80
Rv 172,198,23552
E 14
Mvvvc 176,192,64
Mavvc 12,144,88
Fad 15,15,132
K 15,199,15,68
Mavvc 28,148,88
E 15
Mavvc 24,148,80
Rv 144,198,24576
C 10,12,13
Fad 16,16,136
Mavvc 20,148,72
E 16
Mavvc 16,148,64
Fad 17,17,136
W 5
Mavvc 32,152,64
E 17
Mavvc 36,152,72
Rv 148,198,25600
C 11,14,15
Fad 18,18,136
Mavvc 40,152,80
E 18
Mavvc 44,152,88
C 16,16,17
Fad 19,19,136
Mavvc 60,156,88
E 19
Mavvc 56,156,80
Rv 152,198,26624
Mvvvc 180,192,72
Fad 20,20,136
Mavvc 52,156,72
E 20
Mavvc 48,156,64
Fad 21,21,136
W 5
Mavvc 64,160,64
E 21
Mvvvc 184,192,80
Mavvc 68,160,72
Rv 156,198,27648
C 17,18,19
Fad 22,22,136
Mavvc 72,160,80
E 22
Mavvc 76,160,88
C 18,20,21
Fad 23,23,136
Mavvc 92,164,88
E 23
Mavvc 88,164,80
Rv 160,198,28672
Fad 24,24,140
Mavvc 84,164,72
E 24
Mavvc 80,164,64
Mvvvc 188,192,88
Fad 25,25,140
W 5
Mavvc 96,168,64
E 25
Mavvc 100,168,72
C 19,22,23
Fad 26,26,140
Mavvc 104,168,80
Rv 164,198,29696
E 26
Mavvc 108,168,88
C 24,24,25
Fad 27,27,140
Mavvc 124,172,88
E 27
Mavvc 120,172,80
Fad 28,28,140
Mavvc 116,172,72
E 28
Mavvc 112,172,64
Rv 168,198,30720
Fad 29,29,140
W 5
E 29
C 25,26,27
Fad 30,30,140
Mavvc 8,144,112
E 30
Mavvc 12,144,120
Fad 31,31,140
Mavvc 28,148,120
Rv 172,198,31744
E 31
Mavvc 24,148,112
C 26,28,29
W 4
C 27,30,31
Mavvc 40,152,112
Mavvc 44,152,120
Mavvc 60,156,120
Mavvc 56,156,112
W 0
B
W 15
Mvvvc 184,192,112
Mavvc 72,160,112
Mavvc 76,160,120
Mavvc 92,164,120
Mavvc 88,164,112
W 15
Mvvvc 188,192,120
Mavvc 104,168,112
Mavvc 108,168,120
Mavvc 124,172,120
Mavvc 120,172,112
s_branch XAT3_B3%=
XAT3_B19%=:
AS 60,60,1
AT3_B19%=:
K 48,199,48,62
E 48
K 49,199,49,64
E 49
K 50,199,50,66
E 50
K 51,199,51,68
E 51
C 48,48,49
C 49,50,51
v_mov_b32 v50, 0
E 56
E 57
v_mov_b32 v51, 0
E 58
C 56,56,57
E 59
K 60,199,60,62
E 60
K 61,199,61,64
E 61
C 57,58,59
K 62,199,62,66
E 62
K 63,199,63,68
E 63
C 58,60,61
C 59,62,63
WV 0
B
VA 198,32768,198
Rv 144,198,0
Rv 148,198,1024
Rv 152,198,2048
Rv 156,198,3072
Rv 160,198,4096
Rv 164,198,5120
Rv 168,198,6144
W 5
Mavvz 0,144,0
Mavvz 4,144,8
Mavvz 8,144,16
Rv 172,198,7168
Mvvvz 176,192,0
Mavvz 12,144,24
Mavvz 28,148,24
Mavvz 24,148,16
Rv 144,198,8192
Mavvz 20,148,8
Mavvz 16,148,0
W 5
Mavvz 32,152,0
Mavvz 36,152,8
Rv 148,198,9216
Mvvvz 180,192,8
Mavvz 40,152,16
Mavvz 44,152,24
Mavvz 60,156,24
Mavvz 56,156,16
Rv 152,198,10240
Mavvz 52,156,8
Mavvz 48,156,0
W 5
Mavvz 64,160,0
Mvvvz 184,192,16
Mavvz 68,160,8
Rv 156,198,11264
Mavvz 72,160,16
Mavvz 76,160,24
Mavvz 92,164,24
Mavvz 88,164,16
Rv 160,198,12288
Mavvz 84,164,8
Mavvz 80,164,0
W 5
Mavvz 96,168,0
Mvvvz 188,192,24
Mavvz 100,168,8
Mavvz 104,168,16
Rv 164,198,13312
Mavvz 108,168,24
Mavvz 124,172,24
Mavvz 120,172,16
Mavvz 116,172,8
Mavvz 112,172,0
Rv 168,198,14336
W 5
Mavvc 8,144,48
Mavvc 12,144,56
Mavvc 28,148,56
Rv 172,198,15360
Mavvc 24,148,48
W 4
Mavvc 40,152,48
Mavvc 44,152,56
Mavvc 60,156,56
Mavvc 56,156,48
W 0
B
W 15
Mvvvc 184,192,48
Mavvc 72,160,48
Mavvc 76,160,56
Mavvc 92,164,56
Mavvc 88,164,48
W 15
Mvvvc 188,192,56
Mavvc 104,168,48
Mavvc 108,168,56
Mavvc 124,172,56
Mavvc 120,172,48
XAT3_FIN%=:
AS 60,60,1
AT3_FIN%=:
N 7
WV 0
B
VA 200,49152,196
DWa 200,0,0
DWa 200,4,1024
DWa 200,8,2048
DWa 200,12,3072
DWa 200,16,4096
DWa 200,20,5120
DWa 200,24,6144
DWa 200,28,7168
DWa 200,32,8192
DWa 200,36,9216
DWa 200,40,10240
DWa 200,44,11264
DWa 200,48,12288
DWa 200,52,13312
DWa 200,56,14336
DWa 200,60,15360
v_mov_b32 v231, 0x5d800000
v_cmp_nle_f32 vcc, v176, v231
N 3
s_or_b64 s[70:71], s[70:71], vcc
v_cmp_nle_f32 vcc, v180, v231
N 3
s_or_b64 s[70:71], s[70:71], vcc
v_cmp_nle_f32 vcc, v184, v231
N 3
s_or_b64 s[70:71], s[70:71], vcc
v_cmp_nle_f32 vcc, v188, v231
N 3
s_or_b64 s[70:71], s[70:71], vcc
s_cmp_lg_u64 s[70:71], 0
s_cselect_b32 s16, 1, 0
v_mov_b32 v202, s16
v_mov_b32 v203, 149516
ds_write_b32 v203, v202
MV 204,128
MV 208,176
MV 205,132
MV 209,180
MV 206,136
MV 210,184
MV 207,140
MV 211,188
VA 201,161792,196
ds_write_b128 v201, v[204:207]
DWv 201,208,1024
s_sub_u32 s16, 15, s13
s_lshr_b32 s17, s16, 1
s_lshl_b32 s17, s17, 21
s_and_b32 s16, s16, 1
s_lshl_b32 s16, s16, 13
s_add_u32 s17, s17, s16
s_lshl_b32 s16, s12, 16
s_add_u32 s17, s17, s16
AS 72,17,4096
AS 72,72,32768
W 0
B
VA 201,157696,196
ds_read_b128 v[212:215], v201
Rv 216,201,1024
v_mov_b32 v203, 149504
ds_read_b128 v[64:67], v203
ds_read_b64 v[68:69], v203 offset:16
VA 200,16384,196
Rv 0,200,0
Rv 4,200,1024
Rv 8,200,2048
Rv 12,200,3072
Rv 16,200,4096
Rv 20,200,5120
Rv 24,200,6144
Rv 28,200,7168
Rv 32,200,8192
Rv 36,200,9216
Rv 40,200,10240
Rv 44,200,11264
Rv 48,200,12288
Rv 52,200,13312
Rv 56,200,14336
Rv 60,200,15360
v_mov_b32 v231, 0x5d800000
W 15
v_min_f32 v201, v204, v212
Fsu 204,201,204
Fsu 212,201,212
E 204
E 212
Fmu 202,208,204
W 15
Ffm 202,216,212
v_rcp_f32 v202, v202
N 0
Fmu 220,204,202
Fmu 222,212,202
v_min_f32 v201, v205, v213
Fsu 205,201,205
Fsu 213,201,213
E 205
E 213
Fmu 202,209,205
Ffm 202,217,213
v_rcp_f32 v202, v202
N 0
Fmu 230,205,202
Fmu 224,213,202
v_min_f32 v201, v206, v214
Fsu 206,201,206
Fsu 214,201,214
E 206
E 214
Fmu 202,210,206
Ffm 202,218,214
v_rcp_f32 v202, v202
N 0
Fmu 128,206,202
Fmu 226,214,202
v_min_f32 v201, v207, v215
Fsu 207,201,207
Fsu 215,201,215
E 207
E 215
Fmu 202,211,207
Ffm 202,219,215
v_rcp_f32 v202, v202
N 0
Fmu 130,207,202
Fmu 228,215,202
W 15
v_or_b32 v64, v64, v65
v_or_b32 v66, v66, v67
v_or_b32 v64, v64, v66
W 15
v_or_b32 v68, v68, v69
v_or_b32 v65, v64, v68
N 0
v_readfirstlane_b32 s16, v65
v_readfirstlane_b32 s17, v68
N 3
s_cmp_lg_u32 s16, 0
s_cbranch_scc1 GOSLOW3%=
FASTM3%=:
G 64,64
G 68,80
G 65,65
G 69,81
G 66,66
G 70,82
G 67,67
G 71,83
PM 64,64,220
PM 66,66,220
PM 68,68,220
PM 70,70,220
W 15
v_pk_fma_f32 v[64:65], v[0:1], v[222:223], v[64:65] op_sel_hi:[1,0,1]
W 11
v_pk_fma_f32 v[68:69], v[16:17], v[222:223], v[68:69] op_sel_hi:[1,0,1]
v_pk_fma_f32 v[66:67], v[2:3], v[222:223], v[66:67] op_sel_hi:[1,0,1]
v_pk_fma_f32 v[70:71], v[18:19], v[222:223], v[70:71] op_sel_hi:[1,0,1]
C 72,64,65
C 73,66,67
C 74,68,69
C 75,70,71
Svs 72,196,32,72,0
G 76,68
G 80,84
G 77,69
G 81,85
G 78,70
G 82,86
G 79,71
G 83,87
PM 76,76,230
PM 78,78,230
PM 80,80,230
PM 82,82,230
v_pk_fma_f32 v[76:77], v[4:5], v[224:225], v[76:77] op_sel_hi:[1,0,1]
W 10
v_pk_fma_f32 v[80:81], v[20:21], v[224:225], v[80:81] op_sel_hi:[1,0,1]
v_pk_fma_f32 v[78:79], v[6:7], v[224:225], v[78:79] op_sel_hi:[1,0,1]
v_pk_fma_f32 v[82:83], v[22:23], v[224:225], v[82:83] op_sel_hi:[1,0,1]
C 84,76,77
C 85,78,79
C 86,80,81
C 87,82,83
Svs 84,196,32,72,1024
G 88,72
G 92,88
G 89,73
G 93,89
G 90,74
G 94,90
G 91,75
G 95,91
PM 88,88,128
PM 90,90,128
PM 92,92,128
PM 94,94,128
v_pk_fma_f32 v[88:89], v[8:9], v[226:227], v[88:89] op_sel_hi:[1,0,1]
)A"
R"A(W 9
v_pk_fma_f32 v[92:93], v[24:25], v[226:227], v[92:93] op_sel_hi:[1,0,1]
v_pk_fma_f32 v[90:91], v[10:11], v[226:227], v[90:91] op_sel_hi:[1,0,1]
v_pk_fma_f32 v[94:95], v[26:27], v[226:227], v[94:95] op_sel_hi:[1,0,1]
C 96,88,89
C 97,90,91
C 98,92,93
C 99,94,95
Svs 96,196,32,72,2048
G 100,76
G 104,92
G 101,77
G 105,93
G 102,78
G 106,94
G 103,79
G 107,95
PM 100,100,130
PM 102,102,130
PM 104,104,130
PM 106,106,130
v_pk_fma_f32 v[100:101], v[12:13], v[228:229], v[100:101] op_sel_hi:[1,0,1]
W 8
v_pk_fma_f32 v[104:105], v[28:29], v[228:229], v[104:105] op_sel_hi:[1,0,1]
v_pk_fma_f32 v[102:103], v[14:15], v[228:229], v[102:103] op_sel_hi:[1,0,1]
v_pk_fma_f32 v[106:107], v[30:31], v[228:229], v[106:107] op_sel_hi:[1,0,1]
C 108,100,101
C 109,102,103
C 110,104,105
C 111,106,107
Svs 108,196,32,72,3072
AS 72,72,16384
G 64,96
G 68,112
G 65,97
G 69,113
G 66,98
G 70,114
G 67,99
G 71,115
PM 64,64,220
PM 66,66,220
PM 68,68,220
PM 70,70,220
W 7
v_pk_fma_f32 v[64:65], v[32:33], v[222:223], v[64:65] op_sel_hi:[1,0,1]
W 3
v_pk_fma_f32 v[68:69], v[48:49], v[222:223], v[68:69] op_sel_hi:[1,0,1]
v_pk_fma_f32 v[66:67], v[34:35], v[222:223], v[66:67] op_sel_hi:[1,0,1]
v_pk_fma_f32 v[70:71], v[50:51], v[222:223], v[70:71] op_sel_hi:[1,0,1]
C 72,64,65
C 73,66,67
C 74,68,69
C 75,70,71
Svs 72,196,32,72,0
G 76,100
G 80,116
G 77,101
G 81,117
G 78,102
G 82,118
G 79,103
G 83,119
PM 76,76,230
PM 78,78,230
PM 80,80,230
PM 82,82,230
v_pk_fma_f32 v[76:77], v[36:37], v[224:225], v[76:77] op_sel_hi:[1,0,1]
W 2
v_pk_fma_f32 v[80:81], v[52:53], v[224:225], v[80:81] op_sel_hi:[1,0,1]
v_pk_fma_f32 v[78:79], v[38:39], v[224:225], v[78:79] op_sel_hi:[1,0,1]
v_pk_fma_f32 v[82:83], v[54:55], v[224:225], v[82:83] op_sel_hi:[1,0,1]
C 84,76,77
C 85,78,79
C 86,80,81
C 87,82,83
Svs 84,196,32,72,1024
G 88,104
G 92,120
G 89,105
G 93,121
G 90,106
G 94,122
G 91,107
G 95,123
PM 88,88,128
PM 90,90,128
PM 92,92,128
PM 94,94,128
v_pk_fma_f32 v[88:89], v[40:41], v[226:227], v[88:89] op_sel_hi:[1,0,1]
W 1
v_pk_fma_f32 v[92:93], v[56:57], v[226:227], v[92:93] op_sel_hi:[1,0,1]
v_pk_fma_f32 v[90:91], v[42:43], v[226:227], v[90:91] op_sel_hi:[1,0,1]
v_pk_fma_f32 v[94:95], v[58:59], v[226:227], v[94:95] op_sel_hi:[1,0,1]
C 96,88,89
C 97,90,91
C 98,92,93
C 99,94,95
Svs 96,196,32,72,2048
G 100,108
G 104,124
G 101,109
G 105,125
G 102,110
G 106,126
G 103,111
G 107,127
PM 100,100,130
PM 102,102,130
PM 104,104,130
PM 106,106,130
v_pk_fma_f32 v[100:101], v[44:45], v[228:229], v[100:101] op_sel_hi:[1,0,1]
W 0
v_pk_fma_f32 v[104:105], v[60:61], v[228:229], v[104:105] op_sel_hi:[1,0,1]
v_pk_fma_f32 v[102:103], v[46:47], v[228:229], v[102:103] op_sel_hi:[1,0,1]
v_pk_fma_f32 v[106:107], v[62:63], v[228:229], v[106:107] op_sel_hi:[1,0,1]
C 108,100,101
C 109,102,103
C 110,104,105
C 111,106,107
Svs 108,196,32,72,3072
s_getpc_b64 s[16:17]
1:
s_add_u32 s16, s16, AE3%=-1b
s_addc_u32 s17, s17, 0
s_lshr_b32 s21, s10, 3
v_lshlrev_b32 v249, 5, v254
v_add_u32 v249, s21, v249
v_min_u32 v249, ((OE3%=-AE3%=)>>7), v249
v_lshlrev_b32 v249, 7, v249
global_load_dword v248, v249, s[16:17]
s_and_b32 s16, s10, 7
s_lshr_b32 s41, s16, 1
s_and_b32 s16, s16, 1
s_lshl_b32 s16, s16, 3
s_lshr_b32 s17, s10, 3
s_lshr_b32 s18, s17, 2
s_add_u32 s39, s16, s18
s_and_b32 s40, s17, 3
s_lshl_b32 s16, s41, 2
s_add_u32 s43, s16, s40
s_mov_b64 s[24:25], s[50:51]
SM 26,0x500000
SM 27,0x20000
s_lshl_b32 s16, s41, 10
s_lshl_b32 s17, s11, 5
s_add_u32 s16, s16, s17
s_mul_i32 s16, s16, 1280
s_mul_i32 s17, s40, 320
s_add_u32 s36, s16, s17
AE3%=:
s_waitcnt vmcnt(0)
MDONE3%=:
s_sub_u32 s23, 15, s13
s_lshl_b32 s23, s23, 2
s_lshr_b32 s22, s12, 3
s_add_u32 s23, s23, s22
s_and_b32 s19, s12, 7
s_lshl_b32 s19, s19, 2
AS 19,19,3
s_lshl_b32 s21, s23, 8
AS 21,21,32768
s_lshl_b32 s22, s19, 2
s_add_u32 s21, s21, s22
v_mov_b32 v247, s21
v_mov_b32 v246, s79
s_mov_b64 exec, 1
global_store_dword v247, v246, s[14:15] sc1
s_mov_b64 exec, -1
N 3
AS 42,36,0
M0 96256
N 0
D 232,24,42,0
D 233,24,42,1024
D 234,24,42,2048
D 235,24,42,3072
M0 100352
N 0
D 236,24,42,0
AS 42,36,20480
M0 101376
N 0
D 232,24,42,0
D 233,24,42,1024
D 234,24,42,2048
D 235,24,42,3072
M0 105472
N 0
D 236,24,42,0
s_add_u32 s28, s14, 0x2400000
s_addc_u32 s29, s15, 0
SM 30,0x1000000
SM 31,0x20000
s_add_u32 s32, s14, 0x3400000
s_addc_u32 s33, s15, 0
SM 34,0xa00000
SM 35,0x20000
v_lshrrev_b32 v117, 4, v254
v_and_b32 v118, 15, v254
v_lshrrev_b32 v118, 2, v118
v_lshl_add_u32 v118, v117, 2, v118
v_mul_u32_u24 v118, 320, v118
v_and_b32 v119, 3, v254
v_lshl_add_u32 v118, v119, 3, v118
v_and_b32 v117, 1, v117
v_lshlrev_b32 v117, 5, v117
v_add_u32 v119, v118, v117
v_sub_u32 v120, v118, v117
VA 122,65536,119
VA 123,65536,120
VA 124,106496,119
VA 125,106496,120
VA 126,0,119
VA 127,0,120
s_lshr_b32 s16, s39, 1
s_lshl_b32 s16, s16, 21
s_lshl_b32 s17, s41, 19
s_add_u32 s16, s16, s17
s_and_b32 s17, s39, 1
s_lshl_b32 s17, s17, 13
s_add_u32 s16, s16, s17
s_lshl_b32 s17, s11, 11
s_add_u32 s37, s16, s17
s_lshl_b32 s16, s41, 4
s_add_u32 s16, s16, s39
s_lshl_b32 s16, s16, 3
s_lshl_b32 s17, s11, 1
s_add_u32 s16, s16, s17
s_mul_i32 s16, s16, 20480
s_mul_i32 s17, s40, 5120
s_add_u32 s38, s16, s17
s_load_dwordx2 s[44:45], s[8:9], 0
SM 46,0x280000
SM 47,0x20000
s_load_dwordx2 s[48:49], s[8:9], 48
SM 50,0x500
SM 51,0x20000
s_load_dwordx2 s[52:53], s[8:9], 80
SM 54,0x280000
SM 55,0x20000
v_lshrrev_b32 v104, 5, v254
v_lshlrev_b32 v104, 5, v104
v_lshrrev_b32 v105, 3, v254
v_and_b32 v105, 3, v105
v_lshl_add_u32 v101, v105, 3, v104
v_and_b32 v106, 7, v254
v_mul_u32_u24 v106, 1280, v106
v_add_u32 v100, v106, v101
s_lshl_b32 s16, s39, 7
s_lshl_b32 s17, s43, 3
s_add_u32 s16, s16, s17
s_mov_b32 s66, s16
s_mul_i32 s16, s16, 1280
s_mul_i32 s17, s11, 320
s_add_u32 s65, s16, s17
s_mov_b32 s67, s17
v_lshrrev_b32 v104, 5, v254
v_mul_u32_u24 v104, 81920, v104
v_lshlrev_b32 v104, 6, v104
v_and_b32 v106, 7, v254
v_lshl_add_u32 v106, v105, 4, v106
s_and_b32 s16, s43, 1
s_lshl_b32 s16, s16, 3
v_add_u32 v106, s16, v106
v_lshl_add_u32 v102, v106, 4, v104
s_lshl_b32 s16, s39, 3
s_lshr_b32 s17, s43, 1
s_add_u32 s16, s16, s17
s_mul_i32 s16, s16, 20480
s_mul_i32 s17, s11, 5120
s_add_u32 s64, s16, s17
W 0
s_load_dwordx2 s[70:71], s[8:9], 56
s_load_dwordx2 s[76:77], s[8:9], 64
s_load_dwordx2 s[72:73], s[8:9], 72
SM 74,0x21c000
SM 75,0x20000
W 0
B
s_lshr_b32 s78, s10, 1
s_and_b32 s16, s10, 1
s_cselect_b32 s16, 54, s78
s_cmp_lt_u32 s16, 54
s_cbranch_scc0 OMN3%=
s_mov_b64 s[56:57], s[70:71]
SM 58,0x21c000
SM 59,0x20000
s_mov_b64 s[60:61], s[76:77]
SM 62,0x21c000
SM 63,0x20000
v_lshrrev_b32 v99, 2, v254
v_mul_u32_u24 v99, 3456, v99
v_and_b32 v98, 3, v254
v_lshl_add_u32 v99, v98, 4, v99
s_lshl_b32 s22, s78, 6
AS 22,22,1658880
AS 21,37,0
Las 80,196,28,21,0
Las 84,196,28,21,1024
AS 21,37,16384
Las 88,196,28,21,0
Las 92,196,28,21,1024
AS 21,37,32768
Las 96,196,28,21,0
Las 100,196,28,21,1024
AS 21,37,49152
Las 104,196,28,21,0
Las 108,196,28,21,1024
AS 21,37,65536
Las 112,196,28,21,0
Las 116,196,28,21,1024
AS 21,37,81920
Las 120,196,28,21,0
Las 124,196,28,21,1024
AS 42,36,163840
M0 137216
N 0
D 232,24,42,0
D 233,24,42,1024
D 234,24,42,2048
D 235,24,42,3072
M0 141312
N 0
D 236,24,42,0
AS 42,36,184320
M0 142336
N 0
D 232,24,42,0
D 233,24,42,1024
D 234,24,42,2048
D 235,24,42,3072
M0 146432
N 0
D 236,24,42,0
WV 22
B
T 0,122,0
T 2,122,5120
T 4,123,32
T 6,123,5152
T 8,122,64
T 10,122,5184
T 12,123,96
T 14,123,5216
T 16,122,128
T 18,122,5248
T 20,123,160
T 22,123,5280
T 24,122,192
T 26,122,5312
T 28,123,224
T 30,123,5344
T 32,122,256
T 34,122,5376
T 36,123,288
T 38,123,5408
WV 20
Mavaz 0,0,80
T 40,122,10240
T 42,122,15360
Mavaz 4,0,84
AS 42,36,327680
M0 30720
Mavaz 12,4,84
T 44,123,10272
D 232,24,42,0
Mavaz 8,4,80
T 46,123,15392
Mavaz 16,8,80
T 48,122,10304
T 50,122,15424
Mavaz 20,8,84
T 52,123,10336
D 233,24,42,1024
Mavaz 28,12,84
T 54,123,15456
Mavaz 24,12,80
T 56,122,10368
T 58,122,15488
Mavaz 32,16,80
D 234,24,42,2048
Mavaz 36,16,84
T 60,123,10400
T 62,123,15520
Mavaz 44,20,84
T 64,122,10432
D 235,24,42,3072
Mavaz 40,20,80
T 66,122,15552
T 68,123,10464
Mavaz 48,24,80
T 70,123,15584
M0 34816
Mavaz 52,24,84
T 72,122,10496
D 236,24,42,0
Mavaz 60,28,84
T 74,122,15616
AS 21,37,98304
Mavaz 56,28,80
T 76,123,10528
Las 128,196,28,21,0
Mavaz 64,32,80
T 78,123,15648
Mavaz 68,32,84
Mavaz 76,36,84
Las 132,196,28,21,1024
Mavaz 72,36,80
WV 25
Mavac 0,40,88
T 0,122,20480
T 2,122,25600
Mavac 4,40,92
AS 42,36,348160
M0 35840
Mavac 12,44,92
T 4,123,20512
D 232,24,42,0
Mavac 8,44,88
T 6,123,25632
Mavac 16,48,88
T 8,122,20544
T 10,122,25664
Mavac 20,48,92
T 12,123,20576
D 233,24,42,1024
Mavac 28,52,92
T 14,123,25696
Mavac 24,52,88
T 16,122,20608
T 18,122,25728
Mavac 32,56,88
D 234,24,42,2048
Mavac 36,56,92
T 20,123,20640
T 22,123,25760
Mavac 44,60,92
T 24,122,20672
D 235,24,42,3072
Mavac 40,60,88
T 26,122,25792
T 28,123,20704
Mavac 48,64,88
T 30,123,25824
M0 39936
Mavac 52,64,92
T 32,122,20736
D 236,24,42,0
Mavac 60,68,92
T 34,122,25856
AS 21,37,114688
Mavac 56,68,88
T 36,123,20768
Las 136,196,28,21,0
Mavac 64,72,88
T 38,123,25888
Mavac 68,72,92
Mavac 76,76,92
Las 140,196,28,21,1024
Mavac 72,76,88
WV 30
Mavac 0,0,96
T 40,122,30720
Mavac 4,0,100
T 42,122,35840
Mavac 12,4,100
T 44,123,30752
Mavac 8,4,96
T 46,123,35872
T 48,122,30784
Mavac 16,8,96
T 50,122,35904
AS 21,37,131072
Mavac 20,8,100
T 52,123,30816
Las 80,196,28,21,0
Mavac 28,12,100
T 54,123,35936
Mavac 24,12,96
T 56,122,30848
Mavac 32,16,96
T 58,122,35968
Mavac 36,16,100
T 60,123,30880
Mavac 44,20,100
T 62,123,36000
T 64,122,30912
Mavac 40,20,96
T 66,122,36032
Las 84,196,28,21,1024
Mavac 48,24,96
T 68,123,30944
Mavac 52,24,100
T 70,123,36064
Mavac 60,28,100
T 72,122,30976
Mavac 56,28,96
T 74,122,36096
Mavac 64,32,96
T 76,123,31008
T 78,123,36128
Mavac 68,32,100
La 144,99,56,22,0
Mavac 76,36,100
Mavac 72,36,96
WV 17
B
Mavac 0,40,104
T 0,124,0
T 2,124,5120
Mavac 4,40,108
AS 42,36,491520
M0 96256
Mavac 12,44,108
T 4,125,32
D 232,24,42,0
Mavac 8,44,104
T 6,125,5152
T 8,124,64
Mavac 16,48,104
D 233,24,42,1024
Mavac 20,48,108
T 10,124,5184
Mavac 28,52,108
T 12,125,96
T 14,125,5216
Mavac 24,52,104
T 16,124,128
D 234,24,42,2048
Mavac 32,56,104
T 18,124,5248
T 20,125,160
Mavac 36,56,108
T 22,125,5280
D 235,24,42,3072
Mavac 44,60,108
T 24,124,192
M0 100352
Mavac 40,60,104
T 26,124,5312
D 236,24,42,0
Mavac 48,64,104
T 28,125,224
AS 21,37,147456
Mavac 52,64,108
T 30,125,5344
Las 88,196,28,21,0
Mavac 60,68,108
T 32,124,256
Mavac 56,68,104
T 34,124,5376
T 36,125,288
Mavac 64,72,104
T 38,125,5408
Las 92,196,28,21,1024
Mavac 68,72,108
Mavac 76,76,108
La 148,99,60,22,0
Mavac 72,76,104
W 0
Mavac 0,0,112
T 40,124,10240
Mavac 4,0,116
T 42,124,15360
Mavac 12,4,116
T 44,125,10272
Mavac 8,4,112
T 46,125,15392
T 48,124,10304
Mavac 16,8,112
T 50,124,15424
AS 21,37,163840
Mavac 20,8,116
T 52,125,10336
Las 96,196,28,21,0
Mavac 28,12,116
T 54,125,15456
Mavac 24,12,112
T 56,124,10368
Mavac 32,16,112
T 58,124,15488
Mavac 36,16,116
T 60,125,10400
Mavac 44,20,116
T 62,125,15520
T 64,124,10432
Mavac 40,20,112
T 66,124,15552
Las 100,196,28,21,1024
Mavac 48,24,112
T 68,125,10464
Mavac 52,24,116
T 70,125,15584
Mavac 60,28,116
T 72,124,10496
Mavac 56,28,112
T 74,124,15616
T 76,125,10528
Mavac 64,32,112
T 78,125,15648
AS 22,22,55296
Mavac 68,32,116
La 152,99,56,22,0
Mavac 76,36,116
Mavac 72,36,112
W 0
Mavac 0,40,120
T 0,124,20480
T 2,124,25600
Mavac 4,40,124
AS 42,36,512000
M0 101376
Mavac 12,44,124
T 4,125,20512
D 232,24,42,0
Mavac 8,44,120
T 6,125,25632
T 8,124,20544
Mavac 16,48,120
D 233,24,42,1024
Mavac 20,48,124
T 10,124,25664
Mavac 28,52,124
T 12,125,20576
T 14,125,25696
Mavac 24,52,120
T 16,124,20608
D 234,24,42,2048
Mavac 32,56,120
T 18,124,25728
T 20,125,20640
Mavac 36,56,124
T 22,125,25760
D 235,24,42,3072
Mavac 44,60,124
T 24,124,20672
M0 105472
Mavac 40,60,120
T 26,124,25792
D 236,24,42,0
Mavac 48,64,120
T 28,125,20704
AS 21,37,180224
Mavac 52,64,124
T 30,125,25824
Las 104,196,28,21,0
Mavac 60,68,124
T 32,124,20736
Mavac 56,68,120
T 34,124,25856
T 36,125,20768
Mavac 64,72,120
T 38,125,25888
Las 108,196,28,21,1024
Mavac 68,72,124
Mavac 76,76,124
La 156,99,60,22,0
Mavac 72,76,120
WV 29
Mavac 0,0,128
T 40,124,30720
)A"
R"A(Mavac 4,0,132
T 42,124,35840
Mavac 12,4,132
T 44,125,30752
Mavac 8,4,128
T 46,125,35872
Mavac 16,8,128
T 48,124,30784
Mavac 20,8,132
T 50,124,35904
T 52,125,30816
Mavac 28,12,132
T 54,125,35936
AS 21,37,196608
Mavac 24,12,128
T 56,124,30848
Las 112,196,28,21,0
Mavac 32,16,128
T 58,124,35968
Mavac 36,16,132
T 60,125,30880
Mavac 44,20,132
T 62,125,36000
Mavac 40,20,128
T 64,124,30912
Mavac 48,24,128
T 66,124,36032
Mavac 52,24,132
T 68,125,30944
Mavac 60,28,132
T 70,125,36064
Mavac 56,28,128
T 72,124,30976
T 74,124,36096
Mavac 64,32,128
T 76,125,31008
Las 116,196,28,21,1024
Mavac 68,32,132
T 78,125,36128
Mavac 76,36,132
Mavac 72,36,128
WV 24
B
Mavac 0,40,136
T 0,126,0
T 2,126,5120
Mavac 4,40,140
AS 42,36,655360
M0 137216
Mavac 12,44,140
T 4,127,32
D 232,24,42,0
Mavac 8,44,136
T 6,127,5152
T 8,126,64
Mavac 16,48,136
D 233,24,42,1024
Mavac 20,48,140
T 10,126,5184
Mavac 28,52,140
T 12,127,96
T 14,127,5216
Mavac 24,52,136
T 16,126,128
D 234,24,42,2048
Mavac 32,56,136
T 18,126,5248
T 20,127,160
Mavac 36,56,140
T 22,127,5280
D 235,24,42,3072
Mavac 44,60,140
T 24,126,192
M0 141312
Mavac 40,60,136
T 26,126,5312
D 236,24,42,0
Mavac 48,64,136
T 28,127,224
AS 21,37,212992
Mavac 52,64,140
T 30,127,5344
Las 120,196,28,21,0
Mavac 60,68,140
T 32,126,256
Mavac 56,68,136
T 34,126,5376
T 36,127,288
Mavac 64,72,136
T 38,127,5408
Las 124,196,28,21,1024
Mavac 68,72,140
AS 22,22,55296
Mavac 76,76,140
La 160,99,56,22,0
Mavac 72,76,136
WV 30
Mavac 0,0,80
T 40,126,10240
Mavac 4,0,84
T 42,126,15360
Mavac 12,4,84
T 44,127,10272
Mavac 8,4,80
T 46,127,15392
T 48,126,10304
Mavac 16,8,80
T 50,126,15424
AS 21,37,229376
Mavac 20,8,84
T 52,127,10336
Las 128,196,28,21,0
Mavac 28,12,84
T 54,127,15456
Mavac 24,12,80
T 56,126,10368
Mavac 32,16,80
T 58,126,15488
Mavac 36,16,84
T 60,127,10400
Mavac 44,20,84
T 62,127,15520
T 64,126,10432
Mavac 40,20,80
T 66,126,15552
Las 132,196,28,21,1024
Mavac 48,24,80
T 68,127,10464
Mavac 52,24,84
T 70,127,15584
Mavac 60,28,84
T 72,126,10496
Mavac 56,28,80
T 74,126,15616
Mavac 64,32,80
T 76,127,10528
T 78,127,15648
Mavac 68,32,84
La 164,99,60,22,0
Mavac 76,36,84
Mavac 72,36,80
WV 25
Mavac 0,40,88
T 0,126,20480
T 2,126,25600
Mavac 4,40,92
AS 42,36,675840
M0 142336
Mavac 12,44,92
T 4,127,20512
D 232,24,42,0
Mavac 8,44,88
T 6,127,25632
T 8,126,20544
Mavac 16,48,88
D 233,24,42,1024
Mavac 20,48,92
T 10,126,25664
Mavac 28,52,92
T 12,127,20576
T 14,127,25696
Mavac 24,52,88
T 16,126,20608
D 234,24,42,2048
Mavac 32,56,88
T 18,126,25728
T 20,127,20640
Mavac 36,56,92
T 22,127,25760
D 235,24,42,3072
Mavac 44,60,92
T 24,126,20672
M0 146432
Mavac 40,60,88
T 26,126,25792
D 236,24,42,0
Mavac 48,64,88
T 28,127,20704
AS 21,37,245760
Mavac 52,64,92
T 30,127,25824
Las 136,196,28,21,0
Mavac 60,68,92
T 32,126,20736
Mavac 56,68,88
T 34,126,25856
T 36,127,20768
Mavac 64,72,88
T 38,127,25888
Las 140,196,28,21,1024
Mavac 68,72,92
AS 22,22,55296
Mavac 76,76,92
La 168,99,56,22,0
Mavac 72,76,88
WV 30
Mavac 0,0,96
T 40,126,30720
Mavac 4,0,100
T 42,126,35840
Mavac 12,4,100
T 44,127,30752
Mavac 8,4,96
T 46,127,35872
Mavac 16,8,96
T 48,126,30784
Mavac 20,8,100
T 50,126,35904
T 52,127,30816
Mavac 28,12,100
T 54,127,35936
AS 21,37,262144
Mavac 24,12,96
T 56,126,30848
Las 80,196,28,21,0
Mavac 32,16,96
T 58,126,35968
Mavac 36,16,100
T 60,127,30880
Mavac 44,20,100
T 62,127,36000
Mavac 40,20,96
T 64,126,30912
Mavac 48,24,96
T 66,126,36032
Mavac 52,24,100
T 68,127,30944
Mavac 60,28,100
T 70,127,36064
Mavac 56,28,96
T 72,126,30976
T 74,126,36096
Mavac 64,32,96
T 76,127,31008
Las 84,196,28,21,1024
Mavac 68,32,100
T 78,127,36128
Mavac 76,36,100
Mavac 72,36,96
WV 24
B
Mavac 0,40,104
T 0,122,0
T 2,122,5120
Mavac 4,40,108
AS 42,36,819200
M0 30720
Mavac 12,44,108
T 4,123,32
D 232,24,42,0
Mavac 8,44,104
T 6,123,5152
T 8,122,64
Mavac 16,48,104
D 233,24,42,1024
Mavac 20,48,108
T 10,122,5184
Mavac 28,52,108
T 12,123,96
T 14,123,5216
Mavac 24,52,104
T 16,122,128
D 234,24,42,2048
Mavac 32,56,104
T 18,122,5248
T 20,123,160
Mavac 36,56,108
T 22,123,5280
D 235,24,42,3072
Mavac 44,60,108
T 24,122,192
M0 34816
Mavac 40,60,104
T 26,122,5312
D 236,24,42,0
Mavac 48,64,104
T 28,123,224
AS 21,37,278528
Mavac 52,64,108
T 30,123,5344
Las 88,196,28,21,0
Mavac 60,68,108
T 32,122,256
Mavac 56,68,104
T 34,122,5376
T 36,123,288
Mavac 64,72,104
T 38,123,5408
Las 92,196,28,21,1024
Mavac 68,72,108
Mavac 76,76,108
La 172,99,60,22,0
Mavac 72,76,104
WV 29
Mavac 0,0,112
T 40,122,10240
Mavac 4,0,116
T 42,122,15360
Mavac 12,4,116
T 44,123,10272
Mavac 8,4,112
T 46,123,15392
T 48,122,10304
Mavac 16,8,112
T 50,122,15424
AS 21,37,294912
Mavac 20,8,116
T 52,123,10336
Las 96,196,28,21,0
Mavac 28,12,116
T 54,123,15456
Mavac 24,12,112
T 56,122,10368
Mavac 32,16,112
T 58,122,15488
Mavac 36,16,116
T 60,123,10400
Mavac 44,20,116
T 62,123,15520
T 64,122,10432
Mavac 40,20,112
T 66,122,15552
Las 100,196,28,21,1024
Mavac 48,24,112
T 68,123,10464
Mavac 52,24,116
T 70,123,15584
Mavac 60,28,116
T 72,122,10496
Mavac 56,28,112
T 74,122,15616
T 76,123,10528
Mavac 64,32,112
T 78,123,15648
AS 22,22,55296
Mavac 68,32,116
La 176,99,56,22,0
Mavac 76,36,116
Mavac 72,36,112
WV 25
Mavac 0,40,120
T 0,122,20480
T 2,122,25600
Mavac 4,40,124
AS 42,36,839680
M0 35840
Mavac 12,44,124
T 4,123,20512
D 232,24,42,0
Mavac 8,44,120
T 6,123,25632
T 8,122,20544
Mavac 16,48,120
D 233,24,42,1024
Mavac 20,48,124
T 10,122,25664
Mavac 28,52,124
T 12,123,20576
T 14,123,25696
Mavac 24,52,120
T 16,122,20608
D 234,24,42,2048
Mavac 32,56,120
T 18,122,25728
T 20,123,20640
Mavac 36,56,124
T 22,123,25760
D 235,24,42,3072
Mavac 44,60,124
T 24,122,20672
M0 39936
Mavac 40,60,120
T 26,122,25792
D 236,24,42,0
Mavac 48,64,120
T 28,123,20704
AS 21,37,311296
Mavac 52,64,124
T 30,123,25824
Las 104,196,28,21,0
Mavac 60,68,124
T 32,122,20736
Mavac 56,68,120
T 34,122,25856
T 36,123,20768
Mavac 64,72,120
T 38,123,25888
Las 108,196,28,21,1024
Mavac 68,72,124
Mavac 76,76,124
La 180,99,60,22,0
Mavac 72,76,120
WV 30
Mavac 0,0,128
T 40,122,30720
Mavac 4,0,132
T 42,122,35840
Mavac 12,4,132
T 44,123,30752
Mavac 8,4,128
T 46,123,35872
Mavac 16,8,128
T 48,122,30784
Mavac 20,8,132
T 50,122,35904
T 52,123,30816
Mavac 28,12,132
T 54,123,35936
AS 21,37,327680
Mavac 24,12,128
T 56,122,30848
Las 112,196,28,21,0
Mavac 32,16,128
T 58,122,35968
Mavac 36,16,132
T 60,123,30880
Mavac 44,20,132
T 62,123,36000
Mavac 40,20,128
T 64,122,30912
Mavac 48,24,128
T 66,122,36032
Mavac 52,24,132
T 68,123,30944
Mavac 60,28,132
T 70,123,36064
Mavac 56,28,128
T 72,122,30976
T 74,122,36096
Mavac 64,32,128
T 76,123,31008
Las 116,196,28,21,1024
Mavac 68,32,132
T 78,123,36128
Mavac 76,36,132
Mavac 72,36,128
WV 24
B
Mavac 0,40,136
T 0,124,0
T 2,124,5120
Mavac 4,40,140
AS 42,36,983040
M0 96256
Mavac 12,44,140
T 4,125,32
D 232,24,42,0
Mavac 8,44,136
T 6,125,5152
T 8,124,64
Mavac 16,48,136
D 233,24,42,1024
Mavac 20,48,140
T 10,124,5184
Mavac 28,52,140
T 12,125,96
T 14,125,5216
Mavac 24,52,136
T 16,124,128
D 234,24,42,2048
Mavac 32,56,136
T 18,124,5248
T 20,125,160
Mavac 36,56,140
T 22,125,5280
D 235,24,42,3072
Mavac 44,60,140
T 24,124,192
M0 100352
Mavac 40,60,136
T 26,124,5312
D 236,24,42,0
Mavac 48,64,136
T 28,125,224
AS 21,37,344064
Mavac 52,64,140
T 30,125,5344
Las 120,196,28,21,0
Mavac 60,68,140
T 32,124,256
Mavac 56,68,136
T 34,124,5376
T 36,125,288
Mavac 64,72,136
T 38,125,5408
Las 124,196,28,21,1024
Mavac 68,72,140
AS 22,22,55296
Mavac 76,76,140
La 184,99,56,22,0
Mavac 72,76,136
WV 29
Mavac 0,0,80
T 40,124,10240
Mavac 4,0,84
T 42,124,15360
Mavac 12,4,84
T 44,125,10272
Mavac 8,4,80
T 46,125,15392
T 48,124,10304
Mavac 16,8,80
T 50,124,15424
AS 21,37,360448
Mavac 20,8,84
T 52,125,10336
Las 128,196,28,21,0
Mavac 28,12,84
T 54,125,15456
Mavac 24,12,80
T 56,124,10368
Mavac 32,16,80
T 58,124,15488
Mavac 36,16,84
T 60,125,10400
Mavac 44,20,84
T 62,125,15520
T 64,124,10432
Mavac 40,20,80
T 66,124,15552
Las 132,196,28,21,1024
Mavac 48,24,80
T 68,125,10464
Mavac 52,24,84
T 70,125,15584
Mavac 60,28,84
T 72,124,10496
Mavac 56,28,80
T 74,124,15616
Mavac 64,32,80
T 76,125,10528
T 78,125,15648
Mavac 68,32,84
La 188,99,60,22,0
Mavac 76,36,84
Mavac 72,36,80
WV 25
Mavac 0,40,88
T 0,124,20480
T 2,124,25600
Mavac 4,40,92
AS 42,36,1003520
M0 101376
Mavac 12,44,92
T 4,125,20512
D 232,24,42,0
Mavac 8,44,88
T 6,125,25632
T 8,124,20544
Mavac 16,48,88
D 233,24,42,1024
Mavac 20,48,92
T 10,124,25664
Mavac 28,52,92
T 12,125,20576
T 14,125,25696
Mavac 24,52,88
T 16,124,20608
D 234,24,42,2048
Mavac 32,56,88
T 18,124,25728
T 20,125,20640
Mavac 36,56,92
T 22,125,25760
D 235,24,42,3072
Mavac 44,60,92
T 24,124,20672
M0 105472
Mavac 40,60,88
T 26,124,25792
D 236,24,42,0
Mavac 48,64,88
T 28,125,20704
AS 21,37,376832
Mavac 52,64,92
T 30,125,25824
Las 136,196,28,21,0
Mavac 60,68,92
T 32,124,20736
Mavac 56,68,88
T 34,124,25856
T 36,125,20768
Mavac 64,72,88
T 38,125,25888
Las 140,196,28,21,1024
Mavac 68,72,92
AS 22,22,55296
Mavac 76,76,92
La 192,99,56,22,0
Mavac 72,76,88
WV 30
Mavac 0,0,96
T 40,124,30720
Mavac 4,0,100
T 42,124,35840
Mavac 12,4,100
T 44,125,30752
Mavac 8,4,96
T 46,125,35872
T 48,124,30784
Mavac 16,8,96
T 50,124,35904
AS 21,37,393216
Mavac 20,8,100
T 52,125,30816
Las 80,196,28,21,0
Mavac 28,12,100
T 54,125,35936
Mavac 24,12,96
T 56,124,30848
Mavac 32,16,96
T 58,124,35968
Mavac 36,16,100
T 60,125,30880
Mavac 44,20,100
T 62,125,36000
T 64,124,30912
Mavac 40,20,96
T 66,124,36032
Las 84,196,28,21,1024
Mavac 48,24,96
T 68,125,30944
Mavac 52,24,100
T 70,125,36064
Mavac 60,28,100
T 72,124,30976
Mavac 56,28,96
T 74,124,36096
Mavac 64,32,96
T 76,125,31008
T 78,125,36128
Mavac 68,32,100
La 196,99,60,22,0
Mavac 76,36,100
Mavac 72,36,96
WV 25
B
Mavac 0,40,104
T 0,126,0
T 2,126,5120
Mavac 4,40,108
AS 42,36,1146880
M0 137216
Mavac 12,44,108
T 4,127,32
D 232,24,42,0
Mavac 8,44,104
T 6,127,5152
Mavac 16,48,104
T 8,126,64
T 10,126,5184
Mavac 20,48,108
T 12,127,96
D 233,24,42,1024
Mavac 28,52,108
T 14,127,5216
Mavac 24,52,104
T 16,126,128
T 18,126,5248
Mavac 32,56,104
D 234,24,42,2048
Mavac 36,56,108
T 20,127,160
T 22,127,5280
Mavac 44,60,108
T 24,126,192
D 235,24,42,3072
Mavac 40,60,104
T 26,126,5312
T 28,127,224
Mavac 48,64,104
T 30,127,5344
M0 141312
Mavac 52,64,108
T 32,126,256
D 236,24,42,0
Mavac 60,68,108
T 34,126,5376
AS 21,37,409600
Mavac 56,68,104
T 36,127,288
Las 88,196,28,21,0
Mavac 64,72,104
T 38,127,5408
Mavac 68,72,108
Mavac 76,76,108
Las 92,196,28,21,1024
Mavac 72,76,104
WV 29
Mavac 0,0,112
T 40,126,10240
Mavac 4,0,116
T 42,126,15360
Mavac 12,4,116
T 44,127,10272
Mavac 8,4,112
T 46,127,15392
T 48,126,10304
Mavac 16,8,112
T 50,126,15424
AS 21,37,425984
Mavac 20,8,116
T 52,127,10336
Las 96,196,28,21,0
Mavac 28,12,116
T 54,127,15456
Mavac 24,12,112
T 56,126,10368
Mavac 32,16,112
T 58,126,15488
Mavac 36,16,116
T 60,127,10400
Mavac 44,20,116
T 62,127,15520
T 64,126,10432
Mavac 40,20,112
T 66,126,15552
Las 100,196,28,21,1024
Mavac 48,24,112
T 68,127,10464
Mavac 52,24,116
T 70,127,15584
Mavac 60,28,116
T 72,126,10496
Mavac 56,28,112
T 74,126,15616
T 76,127,10528
Mavac 64,32,112
T 78,127,15648
AS 22,22,55296
Mavac 68,32,116
La 200,99,56,22,0
Mavac 76,36,116
Mavac 72,36,112
WV 25
Mavac 0,40,120
T 0,126,20480
T 2,126,25600
Mavac 4,40,124
AS 42,36,1167360
M0 142336
Mavac 12,44,124
T 4,127,20512
D 232,24,42,0
Mavac 8,44,120
T 6,127,25632
T 8,126,20544
Mavac 16,48,120
D 233,24,42,1024
Mavac 20,48,124
T 10,126,25664
Mavac 28,52,124
T 12,127,20576
T 14,127,25696
Mavac 24,52,120
T 16,126,20608
D 234,24,42,2048
Mavac 32,56,120
T 18,126,25728
T 20,127,20640
Mavac 36,56,124
T 22,127,25760
D 235,24,42,3072
Mavac 44,60,124
T 24,126,20672
M0 146432
Mavac 40,60,120
T 26,126,25792
D 236,24,42,0
Mavac 48,64,120
T 28,127,20704
AS 21,37,442368
Mavac 52,64,124
T 30,127,25824
Las 104,196,28,21,0
Mavac 60,68,124
T 32,126,20736
Mavac 56,68,120
T 34,126,25856
T 36,127,20768
Mavac 64,72,120
T 38,127,25888
Las 108,196,28,21,1024
Mavac 68,72,124
Mavac 76,76,124
La 204,99,60,22,0
Mavac 72,76,120
WV 30
Mavac 0,0,128
T 40,126,30720
Mavac 4,0,132
T 42,126,35840
Mavac 12,4,132
T 44,127,30752
Mavac 8,4,128
T 46,127,35872
T 48,126,30784
Mavac 16,8,128
T 50,126,35904
AS 21,37,458752
Mavac 20,8,132
T 52,127,30816
Las 112,196,28,21,0
)A"
R"A(Mavac 28,12,132
T 54,127,35936
Mavac 24,12,128
T 56,126,30848
Mavac 32,16,128
T 58,126,35968
Mavac 36,16,132
T 60,127,30880
Mavac 44,20,132
T 62,127,36000
T 64,126,30912
Mavac 40,20,128
T 66,126,36032
Las 116,196,28,21,1024
Mavac 48,24,128
T 68,127,30944
Mavac 52,24,132
T 70,127,36064
Mavac 60,28,132
T 72,126,30976
Mavac 56,28,128
T 74,126,36096
T 76,127,31008
Mavac 64,32,128
T 78,127,36128
AS 22,22,55296
Mavac 68,32,132
La 208,99,56,22,0
Mavac 76,36,132
Mavac 72,36,128
WV 25
B
Mavac 0,40,136
T 0,122,0
Mavac 4,40,140
T 2,122,5120
Mavac 12,44,140
T 4,123,32
Mavac 8,44,136
T 6,123,5152
Mavac 16,48,136
T 8,122,64
Mavac 20,48,140
T 10,122,5184
T 12,123,96
Mavac 28,52,140
T 14,123,5216
AS 21,37,475136
Mavac 24,52,136
T 16,122,128
Las 120,196,28,21,0
Mavac 32,56,136
T 18,122,5248
Mavac 36,56,140
T 20,123,160
Mavac 44,60,140
T 22,123,5280
Mavac 40,60,136
T 24,122,192
Mavac 48,64,136
T 26,122,5312
Mavac 52,64,140
T 28,123,224
Mavac 60,68,140
T 30,123,5344
Mavac 56,68,136
T 32,122,256
T 34,122,5376
Mavac 64,72,136
T 36,123,288
Las 124,196,28,21,1024
Mavac 68,72,140
T 38,123,5408
Mavac 76,76,140
Mavac 72,76,136
WV 24
Mavac 0,0,80
T 40,122,10240
Mavac 4,0,84
T 42,122,15360
Mavac 12,4,84
T 44,123,10272
Mavac 8,4,80
T 46,123,15392
T 48,122,10304
Mavac 16,8,80
T 50,122,15424
AS 21,37,491520
Mavac 20,8,84
T 52,123,10336
Las 128,196,28,21,0
Mavac 28,12,84
T 54,123,15456
Mavac 24,12,80
T 56,122,10368
Mavac 32,16,80
T 58,122,15488
Mavac 36,16,84
T 60,123,10400
Mavac 44,20,84
T 62,123,15520
T 64,122,10432
Mavac 40,20,80
T 66,122,15552
Las 132,196,28,21,1024
Mavac 48,24,80
T 68,123,10464
Mavac 52,24,84
T 70,123,15584
Mavac 60,28,84
T 72,122,10496
Mavac 56,28,80
T 74,122,15616
Mavac 64,32,80
T 76,123,10528
T 78,123,15648
Mavac 68,32,84
La 212,99,60,22,0
Mavac 76,36,84
Mavac 72,36,80
WV 19
Mavac 0,40,88
T 0,122,20480
Mavac 4,40,92
T 2,122,25600
Mavac 12,44,92
T 4,123,20512
Mavac 8,44,88
T 6,123,25632
T 8,122,20544
Mavac 16,48,88
T 10,122,25664
AS 21,37,507904
Mavac 20,48,92
T 12,123,20576
Las 136,196,28,21,0
Mavac 28,52,92
T 14,123,25696
Mavac 24,52,88
T 16,122,20608
Mavac 32,56,88
T 18,122,25728
Mavac 36,56,92
T 20,123,20640
Mavac 44,60,92
T 22,123,25760
T 24,122,20672
Mavac 40,60,88
T 26,122,25792
Las 140,196,28,21,1024
Mavac 48,64,88
T 28,123,20704
Mavac 52,64,92
T 30,123,25824
Mavac 60,68,92
T 32,122,20736
Mavac 56,68,88
T 34,122,25856
T 36,123,20768
Mavac 64,72,88
T 38,123,25888
AS 22,22,55296
Mavac 68,72,92
La 216,99,56,22,0
Mavac 76,76,92
Mavac 72,76,88
WV 20
Mavac 0,0,96
T 40,122,30720
Mavac 4,0,100
T 42,122,35840
Mavac 12,4,100
T 44,123,30752
Mavac 8,4,96
T 46,123,35872
Mavac 16,8,96
T 48,122,30784
Mavac 20,8,100
T 50,122,35904
Mavac 28,12,100
T 52,123,30816
Mavac 24,12,96
T 54,123,35936
Mavac 32,16,96
T 56,122,30848
Mavac 36,16,100
T 58,122,35968
Mavac 44,20,100
T 60,123,30880
Mavac 40,20,96
T 62,123,36000
Mavac 48,24,96
T 64,122,30912
Mavac 52,24,100
T 66,122,36032
T 68,123,30944
Mavac 60,28,100
T 70,123,36064
La 220,99,60,22,0
Mavac 56,28,96
T 72,122,30976
Mavac 64,32,96
T 74,122,36096
Mavac 68,32,100
T 76,123,31008
Mavac 76,36,100
T 78,123,36128
Mavac 72,36,96
WV 13
B
Mavac 0,40,104
T 0,124,0
Mavac 4,40,108
T 2,124,5120
Mavac 12,44,108
T 4,125,32
Mavac 8,44,104
T 6,125,5152
Mavac 16,48,104
T 8,124,64
Mavac 20,48,108
T 10,124,5184
Mavac 28,52,108
T 12,125,96
Mavac 24,52,104
T 14,125,5216
Mavac 32,56,104
T 16,124,128
Mavac 36,56,108
T 18,124,5248
Mavac 44,60,108
T 20,125,160
Mavac 40,60,104
T 22,125,5280
Mavac 48,64,104
T 24,124,192
Mavac 52,64,108
T 26,124,5312
Mavac 60,68,108
T 28,125,224
Mavac 56,68,104
T 30,125,5344
Mavac 64,72,104
T 32,124,256
Mavac 68,72,108
T 34,124,5376
Mavac 76,76,108
T 36,125,288
Mavac 72,76,104
T 38,125,5408
WV 10
Mavac 0,0,112
T 40,124,10240
Mavac 4,0,116
T 42,124,15360
Mavac 12,4,116
T 44,125,10272
Mavac 8,4,112
T 46,125,15392
Mavac 16,8,112
T 48,124,10304
Mavac 20,8,116
T 50,124,15424
Mavac 28,12,116
T 52,125,10336
Mavac 24,12,112
T 54,125,15456
Mavac 32,16,112
T 56,124,10368
Mavac 36,16,116
T 58,124,15488
Mavac 44,20,116
T 60,125,10400
Mavac 40,20,112
T 62,125,15520
Mavac 48,24,112
T 64,124,10432
Mavac 52,24,116
T 66,124,15552
Mavac 60,28,116
T 68,125,10464
Mavac 56,28,112
T 70,125,15584
Mavac 64,32,112
T 72,124,10496
Mavac 68,32,116
T 74,124,15616
Mavac 76,36,116
T 76,125,10528
Mavac 72,36,112
T 78,125,15648
WV 7
Mavac 0,40,120
T 0,124,20480
Mavac 4,40,124
T 2,124,25600
Mavac 12,44,124
T 4,125,20512
Mavac 8,44,120
T 6,125,25632
Mavac 16,48,120
T 8,124,20544
Mavac 20,48,124
T 10,124,25664
Mavac 28,52,124
T 12,125,20576
Mavac 24,52,120
T 14,125,25696
Mavac 32,56,120
T 16,124,20608
Mavac 36,56,124
T 18,124,25728
Mavac 44,60,124
T 20,125,20640
Mavac 40,60,120
T 22,125,25760
Mavac 48,64,120
T 24,124,20672
Mavac 52,64,124
T 26,124,25792
Mavac 60,68,124
T 28,125,20704
Mavac 56,68,120
T 30,125,25824
Mavac 64,72,120
T 32,124,20736
Mavac 68,72,124
T 34,124,25856
Mavac 76,76,124
T 36,125,20768
Mavac 72,76,120
T 38,125,25888
WV 5
Mavac 0,0,128
T 40,124,30720
Mavac 4,0,132
T 42,124,35840
Mavac 12,4,132
T 44,125,30752
Mavac 8,4,128
T 46,125,35872
Mavac 16,8,128
T 48,124,30784
Mavac 20,8,132
T 50,124,35904
Mavac 28,12,132
T 52,125,30816
Mavac 24,12,128
T 54,125,35936
Mavac 32,16,128
T 56,124,30848
Mavac 36,16,132
T 58,124,35968
Mavac 44,20,132
T 60,125,30880
Mavac 40,20,128
T 62,125,36000
Mavac 48,24,128
T 64,124,30912
Mavac 52,24,132
T 66,124,36032
Mavac 60,28,132
T 68,125,30944
Mavac 56,28,128
T 70,125,36064
Mavac 64,32,128
T 72,124,30976
Mavac 68,32,132
T 74,124,36096
Mavac 76,36,132
T 76,125,31008
Mavac 72,36,128
T 78,125,36128
WV 2
B
Mavac 0,40,136
Mavac 4,40,140
Mavac 12,44,140
Mavac 8,44,136
Mavac 16,48,136
Mavac 20,48,140
Mavac 28,52,140
Mavac 24,52,136
Mavac 32,56,136
Mavac 36,56,140
Mavac 44,60,140
Mavac 40,60,136
Mavac 48,64,136
Mavac 52,64,140
Mavac 60,68,140
Mavac 56,68,136
Mavac 64,72,136
Mavac 68,72,140
Mavac 76,76,140
Mavac 72,76,136
s_branch OMD3%=
OMN3%=:
AS 21,37,0
Las 80,196,28,21,0
Las 84,196,28,21,1024
AS 21,37,16384
Las 88,196,28,21,0
Las 92,196,28,21,1024
AS 21,37,32768
Las 96,196,28,21,0
Las 100,196,28,21,1024
AS 21,37,49152
Las 104,196,28,21,0
Las 108,196,28,21,1024
AS 21,37,65536
Las 112,196,28,21,0
Las 116,196,28,21,1024
AS 21,37,81920
Las 120,196,28,21,0
Las 124,196,28,21,1024
AS 42,36,163840
M0 137216
N 0
D 232,24,42,0
D 233,24,42,1024
D 234,24,42,2048
D 235,24,42,3072
M0 141312
N 0
D 236,24,42,0
AS 42,36,184320
M0 142336
N 0
D 232,24,42,0
D 233,24,42,1024
D 234,24,42,2048
D 235,24,42,3072
M0 146432
N 0
D 236,24,42,0
WV 22
B
T 0,122,0
T 2,122,5120
T 4,123,32
T 6,123,5152
T 8,122,64
T 10,122,5184
T 12,123,96
T 14,123,5216
T 16,122,128
T 18,122,5248
T 20,123,160
T 22,123,5280
T 24,122,192
T 26,122,5312
T 28,123,224
T 30,123,5344
T 32,122,256
T 34,122,5376
T 36,123,288
T 38,123,5408
WV 20
Mavaz 0,0,80
T 40,122,10240
T 42,122,15360
Mavaz 4,0,84
AS 42,36,327680
M0 30720
Mavaz 12,4,84
T 44,123,10272
D 232,24,42,0
Mavaz 8,4,80
T 46,123,15392
Mavaz 16,8,80
T 48,122,10304
T 50,122,15424
Mavaz 20,8,84
T 52,123,10336
D 233,24,42,1024
Mavaz 28,12,84
T 54,123,15456
Mavaz 24,12,80
T 56,122,10368
T 58,122,15488
Mavaz 32,16,80
D 234,24,42,2048
Mavaz 36,16,84
T 60,123,10400
T 62,123,15520
Mavaz 44,20,84
T 64,122,10432
D 235,24,42,3072
Mavaz 40,20,80
T 66,122,15552
T 68,123,10464
Mavaz 48,24,80
T 70,123,15584
M0 34816
Mavaz 52,24,84
T 72,122,10496
D 236,24,42,0
Mavaz 60,28,84
T 74,122,15616
AS 21,37,98304
Mavaz 56,28,80
T 76,123,10528
Las 128,196,28,21,0
Mavaz 64,32,80
T 78,123,15648
Mavaz 68,32,84
Mavaz 76,36,84
Las 132,196,28,21,1024
Mavaz 72,36,80
WV 25
Mavac 0,40,88
T 0,122,20480
T 2,122,25600
Mavac 4,40,92
AS 42,36,348160
M0 35840
Mavac 12,44,92
T 4,123,20512
D 232,24,42,0
Mavac 8,44,88
T 6,123,25632
Mavac 16,48,88
T 8,122,20544
T 10,122,25664
Mavac 20,48,92
T 12,123,20576
D 233,24,42,1024
Mavac 28,52,92
T 14,123,25696
Mavac 24,52,88
T 16,122,20608
T 18,122,25728
Mavac 32,56,88
D 234,24,42,2048
Mavac 36,56,92
T 20,123,20640
T 22,123,25760
Mavac 44,60,92
T 24,122,20672
D 235,24,42,3072
Mavac 40,60,88
T 26,122,25792
T 28,123,20704
Mavac 48,64,88
T 30,123,25824
M0 39936
Mavac 52,64,92
T 32,122,20736
D 236,24,42,0
Mavac 60,68,92
T 34,122,25856
AS 21,37,114688
Mavac 56,68,88
T 36,123,20768
Las 136,196,28,21,0
Mavac 64,72,88
T 38,123,25888
Mavac 68,72,92
Mavac 76,76,92
Las 140,196,28,21,1024
Mavac 72,76,88
WV 30
Mavac 0,0,96
T 40,122,30720
Mavac 4,0,100
T 42,122,35840
Mavac 12,4,100
T 44,123,30752
Mavac 8,4,96
T 46,123,35872
Mavac 16,8,96
T 48,122,30784
Mavac 20,8,100
T 50,122,35904
T 52,123,30816
Mavac 28,12,100
T 54,123,35936
AS 21,37,131072
Mavac 24,12,96
T 56,122,30848
Las 80,196,28,21,0
Mavac 32,16,96
T 58,122,35968
Mavac 36,16,100
T 60,123,30880
Mavac 44,20,100
T 62,123,36000
Mavac 40,20,96
T 64,122,30912
Mavac 48,24,96
T 66,122,36032
Mavac 52,24,100
T 68,123,30944
Mavac 60,28,100
T 70,123,36064
Mavac 56,28,96
T 72,122,30976
T 74,122,36096
Mavac 64,32,96
T 76,123,31008
Las 84,196,28,21,1024
Mavac 68,32,100
T 78,123,36128
Mavac 76,36,100
Mavac 72,36,96
WV 16
B
Mavac 0,40,104
T 0,124,0
T 2,124,5120
Mavac 4,40,108
AS 42,36,491520
M0 96256
Mavac 12,44,108
T 4,125,32
D 232,24,42,0
Mavac 8,44,104
T 6,125,5152
Mavac 16,48,104
T 8,124,64
T 10,124,5184
Mavac 20,48,108
T 12,125,96
D 233,24,42,1024
Mavac 28,52,108
T 14,125,5216
Mavac 24,52,104
T 16,124,128
T 18,124,5248
Mavac 32,56,104
D 234,24,42,2048
Mavac 36,56,108
T 20,125,160
T 22,125,5280
Mavac 44,60,108
T 24,124,192
D 235,24,42,3072
Mavac 40,60,104
T 26,124,5312
T 28,125,224
Mavac 48,64,104
T 30,125,5344
M0 100352
Mavac 52,64,108
T 32,124,256
D 236,24,42,0
Mavac 60,68,108
T 34,124,5376
AS 21,37,147456
Mavac 56,68,104
T 36,125,288
Las 88,196,28,21,0
Mavac 64,72,104
T 38,125,5408
Mavac 68,72,108
Mavac 76,76,108
Las 92,196,28,21,1024
Mavac 72,76,104
W 0
Mavac 0,0,112
T 40,124,10240
Mavac 4,0,116
T 42,124,15360
Mavac 12,4,116
T 44,125,10272
Mavac 8,4,112
T 46,125,15392
Mavac 16,8,112
T 48,124,10304
Mavac 20,8,116
T 50,124,15424
T 52,125,10336
Mavac 28,12,116
T 54,125,15456
AS 21,37,163840
Mavac 24,12,112
T 56,124,10368
Las 96,196,28,21,0
Mavac 32,16,112
T 58,124,15488
Mavac 36,16,116
T 60,125,10400
Mavac 44,20,116
T 62,125,15520
Mavac 40,20,112
T 64,124,10432
Mavac 48,24,112
T 66,124,15552
Mavac 52,24,116
T 68,125,10464
Mavac 60,28,116
T 70,125,15584
Mavac 56,28,112
T 72,124,10496
T 74,124,15616
Mavac 64,32,112
T 76,125,10528
Las 100,196,28,21,1024
Mavac 68,32,116
T 78,125,15648
Mavac 76,36,116
Mavac 72,36,112
W 0
Mavac 0,40,120
T 0,124,20480
T 2,124,25600
Mavac 4,40,124
AS 42,36,512000
M0 101376
Mavac 12,44,124
T 4,125,20512
D 232,24,42,0
Mavac 8,44,120
T 6,125,25632
Mavac 16,48,120
T 8,124,20544
T 10,124,25664
Mavac 20,48,124
T 12,125,20576
D 233,24,42,1024
Mavac 28,52,124
T 14,125,25696
Mavac 24,52,120
T 16,124,20608
T 18,124,25728
Mavac 32,56,120
D 234,24,42,2048
Mavac 36,56,124
T 20,125,20640
T 22,125,25760
Mavac 44,60,124
T 24,124,20672
D 235,24,42,3072
Mavac 40,60,120
T 26,124,25792
T 28,125,20704
Mavac 48,64,120
T 30,125,25824
M0 105472
Mavac 52,64,124
T 32,124,20736
D 236,24,42,0
Mavac 60,68,124
T 34,124,25856
AS 21,37,180224
Mavac 56,68,120
T 36,125,20768
Las 104,196,28,21,0
Mavac 64,72,120
T 38,125,25888
Mavac 68,72,124
Mavac 76,76,124
Las 108,196,28,21,1024
Mavac 72,76,120
WV 25
Mavac 0,0,128
T 40,124,30720
Mavac 4,0,132
T 42,124,35840
Mavac 12,4,132
T 44,125,30752
Mavac 8,4,128
T 46,125,35872
Mavac 16,8,128
T 48,124,30784
Mavac 20,8,132
T 50,124,35904
T 52,125,30816
Mavac 28,12,132
T 54,125,35936
AS 21,37,196608
Mavac 24,12,128
T 56,124,30848
Las 112,196,28,21,0
Mavac 32,16,128
T 58,124,35968
Mavac 36,16,132
T 60,125,30880
Mavac 44,20,132
T 62,125,36000
Mavac 40,20,128
T 64,124,30912
Mavac 48,24,128
T 66,124,36032
Mavac 52,24,132
T 68,125,30944
Mavac 60,28,132
T 70,125,36064
Mavac 56,28,128
T 72,124,30976
T 74,124,36096
Mavac 64,32,128
T 76,125,31008
Las 116,196,28,21,1024
Mavac 68,32,132
T 78,125,36128
Mavac 76,36,132
Mavac 72,36,128
WV 20
B
Mavac 0,40,136
T 0,126,0
T 2,126,5120
Mavac 4,40,140
AS 42,36,655360
M0 137216
Mavac 12,44,140
T 4,127,32
D 232,24,42,0
Mavac 8,44,136
T 6,127,5152
Mavac 16,48,136
)A"
R"A(T 8,126,64
T 10,126,5184
Mavac 20,48,140
T 12,127,96
D 233,24,42,1024
Mavac 28,52,140
T 14,127,5216
Mavac 24,52,136
T 16,126,128
T 18,126,5248
Mavac 32,56,136
D 234,24,42,2048
Mavac 36,56,140
T 20,127,160
T 22,127,5280
Mavac 44,60,140
T 24,126,192
D 235,24,42,3072
Mavac 40,60,136
T 26,126,5312
T 28,127,224
Mavac 48,64,136
T 30,127,5344
M0 141312
Mavac 52,64,140
T 32,126,256
D 236,24,42,0
Mavac 60,68,140
T 34,126,5376
AS 21,37,212992
Mavac 56,68,136
T 36,127,288
Las 120,196,28,21,0
Mavac 64,72,136
T 38,127,5408
Mavac 68,72,140
Mavac 76,76,140
Las 124,196,28,21,1024
Mavac 72,76,136
WV 25
Mavac 0,0,80
T 40,126,10240
Mavac 4,0,84
T 42,126,15360
Mavac 12,4,84
T 44,127,10272
Mavac 8,4,80
T 46,127,15392
Mavac 16,8,80
T 48,126,10304
Mavac 20,8,84
T 50,126,15424
T 52,127,10336
Mavac 28,12,84
T 54,127,15456
AS 21,37,229376
Mavac 24,12,80
T 56,126,10368
Las 128,196,28,21,0
Mavac 32,16,80
T 58,126,15488
Mavac 36,16,84
T 60,127,10400
Mavac 44,20,84
T 62,127,15520
Mavac 40,20,80
T 64,126,10432
Mavac 48,24,80
T 66,126,15552
Mavac 52,24,84
T 68,127,10464
Mavac 60,28,84
T 70,127,15584
Mavac 56,28,80
T 72,126,10496
T 74,126,15616
Mavac 64,32,80
T 76,127,10528
Las 132,196,28,21,1024
Mavac 68,32,84
T 78,127,15648
Mavac 76,36,84
Mavac 72,36,80
WV 20
Mavac 0,40,88
T 0,126,20480
T 2,126,25600
Mavac 4,40,92
AS 42,36,675840
M0 142336
Mavac 12,44,92
T 4,127,20512
D 232,24,42,0
Mavac 8,44,88
T 6,127,25632
Mavac 16,48,88
T 8,126,20544
T 10,126,25664
Mavac 20,48,92
T 12,127,20576
D 233,24,42,1024
Mavac 28,52,92
T 14,127,25696
Mavac 24,52,88
T 16,126,20608
T 18,126,25728
Mavac 32,56,88
D 234,24,42,2048
Mavac 36,56,92
T 20,127,20640
T 22,127,25760
Mavac 44,60,92
T 24,126,20672
D 235,24,42,3072
Mavac 40,60,88
T 26,126,25792
T 28,127,20704
Mavac 48,64,88
T 30,127,25824
M0 146432
Mavac 52,64,92
T 32,126,20736
D 236,24,42,0
Mavac 60,68,92
T 34,126,25856
AS 21,37,245760
Mavac 56,68,88
T 36,127,20768
Las 136,196,28,21,0
Mavac 64,72,88
T 38,127,25888
Mavac 68,72,92
Mavac 76,76,92
Las 140,196,28,21,1024
Mavac 72,76,88
WV 25
Mavac 0,0,96
T 40,126,30720
Mavac 4,0,100
T 42,126,35840
Mavac 12,4,100
T 44,127,30752
Mavac 8,4,96
T 46,127,35872
Mavac 16,8,96
T 48,126,30784
Mavac 20,8,100
T 50,126,35904
T 52,127,30816
Mavac 28,12,100
T 54,127,35936
AS 21,37,262144
Mavac 24,12,96
T 56,126,30848
Las 80,196,28,21,0
Mavac 32,16,96
T 58,126,35968
Mavac 36,16,100
T 60,127,30880
Mavac 44,20,100
T 62,127,36000
Mavac 40,20,96
T 64,126,30912
Mavac 48,24,96
T 66,126,36032
Mavac 52,24,100
T 68,127,30944
Mavac 60,28,100
T 70,127,36064
Mavac 56,28,96
T 72,126,30976
T 74,126,36096
Mavac 64,32,96
T 76,127,31008
Las 84,196,28,21,1024
Mavac 68,32,100
T 78,127,36128
Mavac 76,36,100
Mavac 72,36,96
WV 20
B
Mavac 0,40,104
T 0,122,0
T 2,122,5120
Mavac 4,40,108
AS 42,36,819200
M0 30720
Mavac 12,44,108
T 4,123,32
D 232,24,42,0
Mavac 8,44,104
T 6,123,5152
Mavac 16,48,104
T 8,122,64
T 10,122,5184
Mavac 20,48,108
T 12,123,96
D 233,24,42,1024
Mavac 28,52,108
T 14,123,5216
Mavac 24,52,104
T 16,122,128
T 18,122,5248
Mavac 32,56,104
D 234,24,42,2048
Mavac 36,56,108
T 20,123,160
T 22,123,5280
Mavac 44,60,108
T 24,122,192
D 235,24,42,3072
Mavac 40,60,104
T 26,122,5312
T 28,123,224
Mavac 48,64,104
T 30,123,5344
M0 34816
Mavac 52,64,108
T 32,122,256
D 236,24,42,0
Mavac 60,68,108
T 34,122,5376
AS 21,37,278528
Mavac 56,68,104
T 36,123,288
Las 88,196,28,21,0
Mavac 64,72,104
T 38,123,5408
Mavac 68,72,108
Mavac 76,76,108
Las 92,196,28,21,1024
Mavac 72,76,104
WV 25
Mavac 0,0,112
T 40,122,10240
Mavac 4,0,116
T 42,122,15360
Mavac 12,4,116
T 44,123,10272
Mavac 8,4,112
T 46,123,15392
Mavac 16,8,112
T 48,122,10304
Mavac 20,8,116
T 50,122,15424
T 52,123,10336
Mavac 28,12,116
T 54,123,15456
AS 21,37,294912
Mavac 24,12,112
T 56,122,10368
Las 96,196,28,21,0
Mavac 32,16,112
T 58,122,15488
Mavac 36,16,116
T 60,123,10400
Mavac 44,20,116
T 62,123,15520
Mavac 40,20,112
T 64,122,10432
Mavac 48,24,112
T 66,122,15552
Mavac 52,24,116
T 68,123,10464
Mavac 60,28,116
T 70,123,15584
Mavac 56,28,112
T 72,122,10496
T 74,122,15616
Mavac 64,32,112
T 76,123,10528
Las 100,196,28,21,1024
Mavac 68,32,116
T 78,123,15648
Mavac 76,36,116
Mavac 72,36,112
WV 20
Mavac 0,40,120
T 0,122,20480
T 2,122,25600
Mavac 4,40,124
AS 42,36,839680
M0 35840
Mavac 12,44,124
T 4,123,20512
D 232,24,42,0
Mavac 8,44,120
T 6,123,25632
Mavac 16,48,120
T 8,122,20544
T 10,122,25664
Mavac 20,48,124
T 12,123,20576
D 233,24,42,1024
Mavac 28,52,124
T 14,123,25696
Mavac 24,52,120
T 16,122,20608
T 18,122,25728
Mavac 32,56,120
D 234,24,42,2048
Mavac 36,56,124
T 20,123,20640
T 22,123,25760
Mavac 44,60,124
T 24,122,20672
D 235,24,42,3072
Mavac 40,60,120
T 26,122,25792
T 28,123,20704
Mavac 48,64,120
T 30,123,25824
M0 39936
Mavac 52,64,124
T 32,122,20736
D 236,24,42,0
Mavac 60,68,124
T 34,122,25856
AS 21,37,311296
Mavac 56,68,120
T 36,123,20768
Las 104,196,28,21,0
Mavac 64,72,120
T 38,123,25888
Mavac 68,72,124
Mavac 76,76,124
Las 108,196,28,21,1024
Mavac 72,76,120
WV 25
Mavac 0,0,128
T 40,122,30720
Mavac 4,0,132
T 42,122,35840
Mavac 12,4,132
T 44,123,30752
Mavac 8,4,128
T 46,123,35872
Mavac 16,8,128
T 48,122,30784
Mavac 20,8,132
T 50,122,35904
T 52,123,30816
Mavac 28,12,132
T 54,123,35936
AS 21,37,327680
Mavac 24,12,128
T 56,122,30848
Las 112,196,28,21,0
Mavac 32,16,128
T 58,122,35968
Mavac 36,16,132
T 60,123,30880
Mavac 44,20,132
T 62,123,36000
Mavac 40,20,128
T 64,122,30912
Mavac 48,24,128
T 66,122,36032
Mavac 52,24,132
T 68,123,30944
Mavac 60,28,132
T 70,123,36064
Mavac 56,28,128
T 72,122,30976
T 74,122,36096
Mavac 64,32,128
T 76,123,31008
Las 116,196,28,21,1024
Mavac 68,32,132
T 78,123,36128
Mavac 76,36,132
Mavac 72,36,128
WV 20
B
Mavac 0,40,136
T 0,124,0
T 2,124,5120
Mavac 4,40,140
AS 42,36,983040
M0 96256
Mavac 12,44,140
T 4,125,32
D 232,24,42,0
Mavac 8,44,136
T 6,125,5152
Mavac 16,48,136
T 8,124,64
T 10,124,5184
Mavac 20,48,140
T 12,125,96
D 233,24,42,1024
Mavac 28,52,140
T 14,125,5216
Mavac 24,52,136
T 16,124,128
T 18,124,5248
Mavac 32,56,136
D 234,24,42,2048
Mavac 36,56,140
T 20,125,160
T 22,125,5280
Mavac 44,60,140
T 24,124,192
D 235,24,42,3072
Mavac 40,60,136
T 26,124,5312
T 28,125,224
Mavac 48,64,136
T 30,125,5344
M0 100352
Mavac 52,64,140
T 32,124,256
D 236,24,42,0
Mavac 60,68,140
T 34,124,5376
AS 21,37,344064
Mavac 56,68,136
T 36,125,288
Las 120,196,28,21,0
Mavac 64,72,136
T 38,125,5408
Mavac 68,72,140
Mavac 76,76,140
Las 124,196,28,21,1024
Mavac 72,76,136
WV 25
Mavac 0,0,80
T 40,124,10240
Mavac 4,0,84
T 42,124,15360
Mavac 12,4,84
T 44,125,10272
Mavac 8,4,80
T 46,125,15392
Mavac 16,8,80
T 48,124,10304
Mavac 20,8,84
T 50,124,15424
T 52,125,10336
Mavac 28,12,84
T 54,125,15456
AS 21,37,360448
Mavac 24,12,80
T 56,124,10368
Las 128,196,28,21,0
Mavac 32,16,80
T 58,124,15488
Mavac 36,16,84
T 60,125,10400
Mavac 44,20,84
T 62,125,15520
Mavac 40,20,80
T 64,124,10432
Mavac 48,24,80
T 66,124,15552
Mavac 52,24,84
T 68,125,10464
Mavac 60,28,84
T 70,125,15584
Mavac 56,28,80
T 72,124,10496
T 74,124,15616
Mavac 64,32,80
T 76,125,10528
Las 132,196,28,21,1024
Mavac 68,32,84
T 78,125,15648
Mavac 76,36,84
Mavac 72,36,80
WV 20
Mavac 0,40,88
T 0,124,20480
T 2,124,25600
Mavac 4,40,92
AS 42,36,1003520
M0 101376
Mavac 12,44,92
T 4,125,20512
D 232,24,42,0
Mavac 8,44,88
T 6,125,25632
Mavac 16,48,88
T 8,124,20544
T 10,124,25664
Mavac 20,48,92
T 12,125,20576
D 233,24,42,1024
Mavac 28,52,92
T 14,125,25696
Mavac 24,52,88
T 16,124,20608
T 18,124,25728
Mavac 32,56,88
D 234,24,42,2048
Mavac 36,56,92
T 20,125,20640
T 22,125,25760
Mavac 44,60,92
T 24,124,20672
D 235,24,42,3072
Mavac 40,60,88
T 26,124,25792
T 28,125,20704
Mavac 48,64,88
T 30,125,25824
M0 105472
Mavac 52,64,92
T 32,124,20736
D 236,24,42,0
Mavac 60,68,92
T 34,124,25856
AS 21,37,376832
Mavac 56,68,88
T 36,125,20768
Las 136,196,28,21,0
Mavac 64,72,88
T 38,125,25888
Mavac 68,72,92
Mavac 76,76,92
Las 140,196,28,21,1024
Mavac 72,76,88
WV 25
Mavac 0,0,96
T 40,124,30720
Mavac 4,0,100
T 42,124,35840
Mavac 12,4,100
T 44,125,30752
Mavac 8,4,96
T 46,125,35872
Mavac 16,8,96
T 48,124,30784
Mavac 20,8,100
T 50,124,35904
T 52,125,30816
Mavac 28,12,100
T 54,125,35936
AS 21,37,393216
Mavac 24,12,96
T 56,124,30848
Las 80,196,28,21,0
Mavac 32,16,96
T 58,124,35968
Mavac 36,16,100
T 60,125,30880
Mavac 44,20,100
T 62,125,36000
Mavac 40,20,96
T 64,124,30912
Mavac 48,24,96
T 66,124,36032
Mavac 52,24,100
T 68,125,30944
Mavac 60,28,100
T 70,125,36064
Mavac 56,28,96
T 72,124,30976
T 74,124,36096
Mavac 64,32,96
T 76,125,31008
Las 84,196,28,21,1024
Mavac 68,32,100
T 78,125,36128
Mavac 76,36,100
Mavac 72,36,96
WV 20
B
Mavac 0,40,104
T 0,126,0
T 2,126,5120
Mavac 4,40,108
AS 42,36,1146880
M0 137216
Mavac 12,44,108
T 4,127,32
D 232,24,42,0
Mavac 8,44,104
T 6,127,5152
Mavac 16,48,104
T 8,126,64
T 10,126,5184
Mavac 20,48,108
T 12,127,96
D 233,24,42,1024
Mavac 28,52,108
T 14,127,5216
Mavac 24,52,104
T 16,126,128
T 18,126,5248
Mavac 32,56,104
D 234,24,42,2048
Mavac 36,56,108
T 20,127,160
T 22,127,5280
Mavac 44,60,108
T 24,126,192
D 235,24,42,3072
Mavac 40,60,104
T 26,126,5312
T 28,127,224
Mavac 48,64,104
T 30,127,5344
M0 141312
Mavac 52,64,108
T 32,126,256
D 236,24,42,0
Mavac 60,68,108
T 34,126,5376
AS 21,37,409600
Mavac 56,68,104
T 36,127,288
Las 88,196,28,21,0
Mavac 64,72,104
T 38,127,5408
Mavac 68,72,108
Mavac 76,76,108
Las 92,196,28,21,1024
Mavac 72,76,104
WV 25
Mavac 0,0,112
T 40,126,10240
Mavac 4,0,116
T 42,126,15360
Mavac 12,4,116
T 44,127,10272
Mavac 8,4,112
T 46,127,15392
Mavac 16,8,112
T 48,126,10304
Mavac 20,8,116
T 50,126,15424
T 52,127,10336
Mavac 28,12,116
T 54,127,15456
AS 21,37,425984
Mavac 24,12,112
T 56,126,10368
Las 96,196,28,21,0
Mavac 32,16,112
T 58,126,15488
Mavac 36,16,116
T 60,127,10400
Mavac 44,20,116
T 62,127,15520
Mavac 40,20,112
T 64,126,10432
Mavac 48,24,112
T 66,126,15552
Mavac 52,24,116
T 68,127,10464
Mavac 60,28,116
T 70,127,15584
Mavac 56,28,112
T 72,126,10496
T 74,126,15616
Mavac 64,32,112
T 76,127,10528
Las 100,196,28,21,1024
Mavac 68,32,116
T 78,127,15648
Mavac 76,36,116
Mavac 72,36,112
WV 20
Mavac 0,40,120
T 0,126,20480
T 2,126,25600
Mavac 4,40,124
AS 42,36,1167360
M0 142336
Mavac 12,44,124
T 4,127,20512
D 232,24,42,0
Mavac 8,44,120
T 6,127,25632
Mavac 16,48,120
T 8,126,20544
T 10,126,25664
Mavac 20,48,124
T 12,127,20576
D 233,24,42,1024
Mavac 28,52,124
T 14,127,25696
Mavac 24,52,120
T 16,126,20608
T 18,126,25728
Mavac 32,56,120
D 234,24,42,2048
Mavac 36,56,124
T 20,127,20640
T 22,127,25760
Mavac 44,60,124
T 24,126,20672
D 235,24,42,3072
Mavac 40,60,120
T 26,126,25792
T 28,127,20704
Mavac 48,64,120
T 30,127,25824
M0 146432
Mavac 52,64,124
T 32,126,20736
D 236,24,42,0
Mavac 60,68,124
T 34,126,25856
AS 21,37,442368
Mavac 56,68,120
T 36,127,20768
Las 104,196,28,21,0
Mavac 64,72,120
T 38,127,25888
Mavac 68,72,124
Mavac 76,76,124
Las 108,196,28,21,1024
Mavac 72,76,120
WV 25
Mavac 0,0,128
T 40,126,30720
Mavac 4,0,132
T 42,126,35840
Mavac 12,4,132
T 44,127,30752
Mavac 8,4,128
T 46,127,35872
Mavac 16,8,128
T 48,126,30784
Mavac 20,8,132
T 50,126,35904
T 52,127,30816
Mavac 28,12,132
T 54,127,35936
AS 21,37,458752
Mavac 24,12,128
T 56,126,30848
Las 112,196,28,21,0
Mavac 32,16,128
T 58,126,35968
Mavac 36,16,132
T 60,127,30880
Mavac 44,20,132
T 62,127,36000
Mavac 40,20,128
T 64,126,30912
Mavac 48,24,128
T 66,126,36032
Mavac 52,24,132
T 68,127,30944
Mavac 60,28,132
T 70,127,36064
Mavac 56,28,128
T 72,126,30976
T 74,126,36096
Mavac 64,32,128
T 76,127,31008
Las 116,196,28,21,1024
Mavac 68,32,132
T 78,127,36128
Mavac 76,36,132
Mavac 72,36,128
WV 20
B
Mavac 0,40,136
T 0,122,0
Mavac 4,40,140
T 2,122,5120
Mavac 12,44,140
T 4,123,32
Mavac 8,44,136
T 6,123,5152
Mavac 16,48,136
T 8,122,64
Mavac 20,48,140
T 10,122,5184
T 12,123,96
Mavac 28,52,140
T 14,123,5216
AS 21,37,475136
Mavac 24,52,136
T 16,122,128
Las 120,196,28,21,0
Mavac 32,56,136
T 18,122,5248
Mavac 36,56,140
T 20,123,160
Mavac 44,60,140
T 22,123,5280
Mavac 40,60,136
T 24,122,192
Mavac 48,64,136
T 26,122,5312
Mavac 52,64,140
T 28,123,224
Mavac 60,68,140
T 30,123,5344
Mavac 56,68,136
T 32,122,256
T 34,122,5376
Mavac 64,72,136
T 36,123,288
Las 124,196,28,21,1024
Mavac 68,72,140
T 38,123,5408
Mavac 76,76,140
Mavac 72,76,136
WV 20
Mavac 0,0,80
T 40,122,10240
)A"
R"A(Mavac 4,0,84
T 42,122,15360
Mavac 12,4,84
T 44,123,10272
Mavac 8,4,80
T 46,123,15392
Mavac 16,8,80
T 48,122,10304
Mavac 20,8,84
T 50,122,15424
T 52,123,10336
Mavac 28,12,84
T 54,123,15456
AS 21,37,491520
Mavac 24,12,80
T 56,122,10368
Las 128,196,28,21,0
Mavac 32,16,80
T 58,122,15488
Mavac 36,16,84
T 60,123,10400
Mavac 44,20,84
T 62,123,15520
Mavac 40,20,80
T 64,122,10432
Mavac 48,24,80
T 66,122,15552
Mavac 52,24,84
T 68,123,10464
Mavac 60,28,84
T 70,123,15584
Mavac 56,28,80
T 72,122,10496
T 74,122,15616
Mavac 64,32,80
T 76,123,10528
Las 132,196,28,21,1024
Mavac 68,32,84
T 78,123,15648
Mavac 76,36,84
Mavac 72,36,80
WV 15
Mavac 0,40,88
T 0,122,20480
Mavac 4,40,92
T 2,122,25600
Mavac 12,44,92
T 4,123,20512
Mavac 8,44,88
T 6,123,25632
Mavac 16,48,88
T 8,122,20544
Mavac 20,48,92
T 10,122,25664
T 12,123,20576
Mavac 28,52,92
T 14,123,25696
AS 21,37,507904
Mavac 24,52,88
T 16,122,20608
Las 136,196,28,21,0
Mavac 32,56,88
T 18,122,25728
Mavac 36,56,92
T 20,123,20640
Mavac 44,60,92
T 22,123,25760
Mavac 40,60,88
T 24,122,20672
Mavac 48,64,88
T 26,122,25792
Mavac 52,64,92
T 28,123,20704
Mavac 60,68,92
T 30,123,25824
Mavac 56,68,88
T 32,122,20736
T 34,122,25856
Mavac 64,72,88
T 36,123,20768
Las 140,196,28,21,1024
Mavac 68,72,92
T 38,123,25888
Mavac 76,76,92
Mavac 72,76,88
WV 15
Mavac 0,0,96
T 40,122,30720
Mavac 4,0,100
T 42,122,35840
Mavac 12,4,100
T 44,123,30752
Mavac 8,4,96
T 46,123,35872
Mavac 16,8,96
T 48,122,30784
Mavac 20,8,100
T 50,122,35904
Mavac 28,12,100
T 52,123,30816
Mavac 24,12,96
T 54,123,35936
Mavac 32,16,96
T 56,122,30848
Mavac 36,16,100
T 58,122,35968
Mavac 44,20,100
T 60,123,30880
Mavac 40,20,96
T 62,123,36000
Mavac 48,24,96
T 64,122,30912
Mavac 52,24,100
T 66,122,36032
Mavac 60,28,100
T 68,123,30944
Mavac 56,28,96
T 70,123,36064
Mavac 64,32,96
T 72,122,30976
Mavac 68,32,100
T 74,122,36096
Mavac 76,36,100
T 76,123,31008
Mavac 72,36,96
T 78,123,36128
WV 8
B
Mavac 0,40,104
T 0,124,0
Mavac 4,40,108
T 2,124,5120
Mavac 12,44,108
T 4,125,32
Mavac 8,44,104
T 6,125,5152
Mavac 16,48,104
T 8,124,64
Mavac 20,48,108
T 10,124,5184
Mavac 28,52,108
T 12,125,96
Mavac 24,52,104
T 14,125,5216
Mavac 32,56,104
T 16,124,128
Mavac 36,56,108
T 18,124,5248
Mavac 44,60,108
T 20,125,160
Mavac 40,60,104
T 22,125,5280
Mavac 48,64,104
T 24,124,192
Mavac 52,64,108
T 26,124,5312
Mavac 60,68,108
T 28,125,224
Mavac 56,68,104
T 30,125,5344
Mavac 64,72,104
T 32,124,256
Mavac 68,72,108
T 34,124,5376
Mavac 76,76,108
T 36,125,288
Mavac 72,76,104
T 38,125,5408
WV 6
Mavac 0,0,112
T 40,124,10240
Mavac 4,0,116
T 42,124,15360
Mavac 12,4,116
T 44,125,10272
Mavac 8,4,112
T 46,125,15392
Mavac 16,8,112
T 48,124,10304
Mavac 20,8,116
T 50,124,15424
Mavac 28,12,116
T 52,125,10336
Mavac 24,12,112
T 54,125,15456
Mavac 32,16,112
T 56,124,10368
Mavac 36,16,116
T 58,124,15488
Mavac 44,20,116
T 60,125,10400
Mavac 40,20,112
T 62,125,15520
Mavac 48,24,112
T 64,124,10432
Mavac 52,24,116
T 66,124,15552
Mavac 60,28,116
T 68,125,10464
Mavac 56,28,112
T 70,125,15584
Mavac 64,32,112
T 72,124,10496
Mavac 68,32,116
T 74,124,15616
Mavac 76,36,116
T 76,125,10528
Mavac 72,36,112
T 78,125,15648
WV 4
Mavac 0,40,120
T 0,124,20480
Mavac 4,40,124
T 2,124,25600
Mavac 12,44,124
T 4,125,20512
Mavac 8,44,120
T 6,125,25632
Mavac 16,48,120
T 8,124,20544
Mavac 20,48,124
T 10,124,25664
Mavac 28,52,124
T 12,125,20576
Mavac 24,52,120
T 14,125,25696
Mavac 32,56,120
T 16,124,20608
Mavac 36,56,124
T 18,124,25728
Mavac 44,60,124
T 20,125,20640
Mavac 40,60,120
T 22,125,25760
Mavac 48,64,120
T 24,124,20672
Mavac 52,64,124
T 26,124,25792
Mavac 60,68,124
T 28,125,20704
Mavac 56,68,120
T 30,125,25824
Mavac 64,72,120
T 32,124,20736
Mavac 68,72,124
T 34,124,25856
Mavac 76,76,124
T 36,125,20768
Mavac 72,76,120
T 38,125,25888
WV 2
Mavac 0,0,128
T 40,124,30720
Mavac 4,0,132
T 42,124,35840
Mavac 12,4,132
T 44,125,30752
Mavac 8,4,128
T 46,125,35872
Mavac 16,8,128
T 48,124,30784
Mavac 20,8,132
T 50,124,35904
Mavac 28,12,132
T 52,125,30816
Mavac 24,12,128
T 54,125,35936
Mavac 32,16,128
T 56,124,30848
Mavac 36,16,132
T 58,124,35968
Mavac 44,20,132
T 60,125,30880
Mavac 40,20,128
T 62,125,36000
Mavac 48,24,128
T 64,124,30912
Mavac 52,24,132
T 66,124,36032
Mavac 60,28,132
T 68,125,30944
Mavac 56,28,128
T 70,125,36064
Mavac 64,32,128
T 72,124,30976
Mavac 68,32,132
T 74,124,36096
Mavac 76,36,132
T 76,125,31008
Mavac 72,36,128
T 78,125,36128
WV 0
B
Mavac 0,40,136
Mavac 4,40,140
Mavac 12,44,140
Mavac 8,44,136
Mavac 16,48,136
Mavac 20,48,140
Mavac 28,52,140
Mavac 24,52,136
Mavac 32,56,136
Mavac 36,56,140
Mavac 44,60,140
Mavac 40,60,136
Mavac 48,64,136
Mavac 52,64,140
Mavac 60,68,140
Mavac 56,68,136
Mavac 64,72,136
Mavac 68,72,140
Mavac 76,76,140
Mavac 72,76,136
OMD3%=:
G 0,0
G 4,8
G 1,1
G 5,9
G 2,2
G 6,10
G 3,3
G 7,11
C 8,0,1
C 9,2,3
C 10,4,5
C 11,6,7
Svs 8,196,32,38,0
G 16,16
G 20,24
G 17,17
G 21,25
G 18,18
G 22,26
G 19,19
G 23,27
C 24,16,17
C 25,18,19
C 26,20,21
C 27,22,23
Svs 24,196,32,38,1024
G 32,32
G 36,40
G 33,33
G 37,41
G 34,34
G 38,42
G 35,35
G 39,43
C 40,32,33
C 41,34,35
C 42,36,37
C 43,38,39
Svs 40,196,32,38,2048
G 48,48
G 52,56
G 49,49
G 53,57
G 50,50
G 54,58
G 51,51
G 55,59
C 56,48,49
C 57,50,51
C 58,52,53
C 59,54,55
Svs 56,196,32,38,3072
G 0,64
G 4,72
G 1,65
G 5,73
G 2,66
G 6,74
G 3,67
G 7,75
C 8,0,1
C 9,2,3
C 10,4,5
C 11,6,7
AS 16,38,4096
Svs 8,196,32,16,0
AS 38,38,20480
G 16,4
G 20,12
G 17,5
G 21,13
G 18,6
G 22,14
G 19,7
G 23,15
C 24,16,17
C 25,18,19
C 26,20,21
C 27,22,23
Svs 24,196,32,38,0
G 32,20
G 36,28
G 33,21
G 37,29
G 34,22
G 38,30
G 35,23
G 39,31
C 40,32,33
C 41,34,35
C 42,36,37
C 43,38,39
Svs 40,196,32,38,1024
G 48,36
G 52,44
G 49,37
G 53,45
G 50,38
G 54,46
G 51,39
G 55,47
C 56,48,49
C 57,50,51
C 58,52,53
C 59,54,55
Svs 56,196,32,38,2048
G 0,52
G 4,60
G 1,53
G 5,61
G 2,54
G 6,62
G 3,55
G 7,63
C 8,0,1
C 9,2,3
C 10,4,5
C 11,6,7
Svs 8,196,32,38,3072
G 16,68
G 20,76
G 17,69
G 21,77
G 18,70
G 22,78
G 19,71
G 23,79
C 24,16,17
C 25,18,19
C 26,20,21
C 27,22,23
AS 16,38,4096
Svs 24,196,32,16,0
buffer_load_dwordx2 v[40:41], v100, s[44:47], s65 offen offset:0
buffer_load_dwordx2 v[42:43], v100, s[44:47], s65 offen offset:64
buffer_load_dwordx2 v[44:45], v100, s[44:47], s65 offen offset:128
buffer_load_dwordx2 v[46:47], v100, s[44:47], s65 offen offset:192
buffer_load_dwordx2 v[48:49], v100, s[44:47], s65 offen offset:256
buffer_load_dwordx2 v[50:51], v101, s[48:51], s67 offen offset:0
buffer_load_dwordx2 v[52:53], v101, s[48:51], s67 offen offset:64
buffer_load_dwordx2 v[54:55], v101, s[48:51], s67 offen offset:128
buffer_load_dwordx2 v[56:57], v101, s[48:51], s67 offen offset:192
buffer_load_dwordx2 v[58:59], v101, s[48:51], s67 offen offset:256
s_mov_b64 s[24:25], s[70:71]
SM 26,0x21c000
SM 27,0x20000
s_mov_b64 s[28:29], s[76:77]
SM 30,0x21c000
SM 31,0x20000
s_waitcnt vmcnt(0)
B
s_and_b32 s16, s10, 1
s_cselect_b32 s16, 54, s78
s_cmp_lt_u32 s16, 54
s_cbranch_scc0 NOST3%=
VA 98,30720,196
DWa 98,144,0
DWa 98,148,40960
DWa 98,152,1024
DWa 98,156,41984
DWa 98,160,2048
DWa 98,164,43008
DWa 98,168,3072
DWa 98,172,44032
DWa 98,176,4096
DWa 98,180,45056
DWa 98,184,5120
DWa 98,188,46080
DWa 98,192,6144
DWa 98,196,47104
DWa 98,200,7168
DWa 98,204,48128
DWa 98,208,8192
DWa 98,212,49152
DWa 98,216,9216
DWa 98,220,50176
NOST3%=:
B
s_and_b32 s16, s10, 1
s_cselect_b32 s16, 54, s78
s_cmp_lt_u32 s16, 54
s_cbranch_scc1 PF3%=
Lvs 0,102,32,64,0
Lvs 8,102,32,64,1024
Lvs 16,102,32,64,2048
Lvs 24,102,32,64,3072
AS 21,64,4096
Lvs 32,102,32,21,0
AS 64,64,2621440
Lvs 4,102,32,64,0
Lvs 12,102,32,64,1024
Lvs 20,102,32,64,2048
Lvs 28,102,32,64,3072
AS 21,64,4096
Lvs 36,102,32,21,0
v_and_b32 v108, 7, v254
v_lshlrev_b32 v108, 4, v108
VA 108,152064,108
s_lshl_b32 s16, s11, 7
v_add_u32 v103, s16, v108
LO 110,50
HI 111,50
LO 112,51
HI 113,51
LO 114,52
HI 115,52
LO 116,53
HI 117,53
LO 118,54
HI 119,54
LO 120,55
HI 121,55
LO 122,56
HI 123,56
LO 124,57
HI 125,57
LO 126,58
HI 127,58
LO 128,59
HI 129,59
PA 104,110,112
PA 104,104,114
PA 104,104,116
PA 104,104,118
PA 104,104,120
PA 104,104,122
PA 104,104,124
PA 104,104,126
PA 104,104,128
Fad 99,104,105
s_waitcnt vmcnt(9)
LO 88,0
HI 89,0
s_waitcnt vmcnt(4)
LO 104,4
HI 105,4
PA 88,88,104
LO 90,1
HI 91,1
LO 104,5
HI 105,5
PA 90,90,104
LO 92,2
HI 93,2
LO 104,6
HI 105,6
PA 92,92,104
LO 94,3
HI 95,3
LO 104,7
HI 105,7
PA 94,94,104
v_permlane32_swap_b32 v88, v92
v_permlane32_swap_b32 v89, v93
v_permlane32_swap_b32 v90, v94
v_permlane32_swap_b32 v91, v95
PA 64,88,92
PA 66,90,94
LO 104,40
HI 105,40
LO 106,41
HI 107,41
PA 64,64,104
PA 66,66,106
LO 88,8
HI 89,8
s_waitcnt vmcnt(3)
LO 104,12
HI 105,12
PA 88,88,104
LO 90,9
HI 91,9
LO 104,13
HI 105,13
PA 90,90,104
LO 92,10
HI 93,10
LO 104,14
HI 105,14
PA 92,92,104
LO 94,11
HI 95,11
LO 104,15
HI 105,15
PA 94,94,104
v_permlane32_swap_b32 v88, v92
v_permlane32_swap_b32 v89, v93
v_permlane32_swap_b32 v90, v94
v_permlane32_swap_b32 v91, v95
PA 68,88,92
PA 70,90,94
LO 104,42
HI 105,42
LO 106,43
HI 107,43
PA 68,68,104
PA 70,70,106
LO 88,16
HI 89,16
s_waitcnt vmcnt(2)
LO 104,20
HI 105,20
PA 88,88,104
LO 90,17
HI 91,17
LO 104,21
HI 105,21
PA 90,90,104
LO 92,18
HI 93,18
LO 104,22
HI 105,22
PA 92,92,104
LO 94,19
HI 95,19
LO 104,23
HI 105,23
PA 94,94,104
v_permlane32_swap_b32 v88, v92
v_permlane32_swap_b32 v89, v93
v_permlane32_swap_b32 v90, v94
v_permlane32_swap_b32 v91, v95
PA 72,88,92
PA 74,90,94
LO 104,44
HI 105,44
LO 106,45
HI 107,45
PA 72,72,104
PA 74,74,106
LO 88,24
HI 89,24
s_waitcnt vmcnt(1)
LO 104,28
HI 105,28
PA 88,88,104
LO 90,25
HI 91,25
LO 104,29
HI 105,29
PA 90,90,104
LO 92,26
HI 93,26
LO 104,30
HI 105,30
PA 92,92,104
LO 94,27
HI 95,27
LO 104,31
HI 105,31
PA 94,94,104
v_permlane32_swap_b32 v88, v92
v_permlane32_swap_b32 v89, v93
v_permlane32_swap_b32 v90, v94
v_permlane32_swap_b32 v91, v95
PA 76,88,92
PA 78,90,94
LO 104,46
HI 105,46
LO 106,47
HI 107,47
PA 76,76,104
PA 78,78,106
LO 88,32
HI 89,32
s_waitcnt vmcnt(0)
LO 104,36
HI 105,36
PA 88,88,104
LO 90,33
HI 91,33
LO 104,37
HI 105,37
PA 90,90,104
LO 92,34
HI 93,34
LO 104,38
HI 105,38
PA 92,92,104
LO 94,35
HI 95,35
LO 104,39
HI 105,39
PA 94,94,104
v_permlane32_swap_b32 v88, v92
v_permlane32_swap_b32 v89, v93
v_permlane32_swap_b32 v90, v94
v_permlane32_swap_b32 v91, v95
PA 80,88,92
PA 82,90,94
LO 104,48
HI 105,48
LO 106,49
HI 107,49
PA 80,80,104
PA 82,82,106
PA 104,64,66
v_pk_mul_f32 v[106:107], v[64:65], v[64:65]
v_pk_mul_f32 v[88:89], v[64:65], v[110:111]
v_pk_fma_f32 v[106:107], v[66:67], v[66:67], v[106:107]
v_pk_fma_f32 v[88:89], v[66:67], v[112:113], v[88:89]
PA 104,104,68
v_pk_fma_f32 v[106:107], v[68:69], v[68:69], v[106:107]
v_pk_fma_f32 v[88:89], v[68:69], v[114:115], v[88:89]
PA 104,104,70
v_pk_fma_f32 v[106:107], v[70:71], v[70:71], v[106:107]
v_pk_fma_f32 v[88:89], v[70:71], v[116:117], v[88:89]
PA 104,104,72
v_pk_fma_f32 v[106:107], v[72:73], v[72:73], v[106:107]
v_pk_fma_f32 v[88:89], v[72:73], v[118:119], v[88:89]
PA 104,104,74
v_pk_fma_f32 v[106:107], v[74:75], v[74:75], v[106:107]
v_pk_fma_f32 v[88:89], v[74:75], v[120:121], v[88:89]
PA 104,104,76
v_pk_fma_f32 v[106:107], v[76:77], v[76:77], v[106:107]
v_pk_fma_f32 v[88:89], v[76:77], v[122:123], v[88:89]
PA 104,104,78
v_pk_fma_f32 v[106:107], v[78:79], v[78:79], v[106:107]
v_pk_fma_f32 v[88:89], v[78:79], v[124:125], v[88:89]
PA 104,104,80
v_pk_fma_f32 v[106:107], v[80:81], v[80:81], v[106:107]
v_pk_fma_f32 v[88:89], v[80:81], v[126:127], v[88:89]
PA 104,104,82
v_pk_fma_f32 v[106:107], v[82:83], v[82:83], v[106:107]
v_pk_fma_f32 v[88:89], v[82:83], v[128:129], v[88:89]
Fad 96,104,105
Fad 97,106,107
Fad 98,88,89
v_add_f32_dpp v96, v96, v96 row_ror:8 row_mask:0xf bank_mask:0xf
v_add_f32_dpp v97, v97, v97 row_ror:8 row_mask:0xf bank_mask:0xf
v_add_f32_dpp v98, v98, v98 row_ror:8 row_mask:0xf bank_mask:0xf
v_add_f32_dpp v99, v99, v99 row_ror:8 row_mask:0xf bank_mask:0xf
MV 104,96
MV 105,97
MV 106,98
MV 107,99
v_permlane16_swap_b32 v96, v104
v_permlane16_swap_b32 v97, v105
v_permlane16_swap_b32 v98, v106
v_permlane16_swap_b32 v99, v107
Fad 96,96,104
Fad 97,97,105
Fad 98,98,106
Fad 99,99,107
MV 104,96
MV 105,97
MV 106,98
MV 107,99
v_permlane32_swap_b32 v96, v104
v_permlane32_swap_b32 v97, v105
v_permlane32_swap_b32 v98, v106
)A"
R"A(v_permlane32_swap_b32 v99, v107
Fad 96,96,104
Fad 97,97,105
Fad 98,98,106
Fad 99,99,107
ds_write_b128 v103, v[96:99]
W 0
B
Rv 0,108,0
Rv 4,108,128
Rv 8,108,256
Rv 12,108,384
W 2
PA 0,0,4
PA 2,2,6
W 0
PA 8,8,12
PA 10,10,14
PA 0,0,8
PA 2,2,10
v_mul_f32 v88, 0x3acccccd, v0
v_mul_f32 v90, 0x3acccccd, v1
v_fma_f32 v90, -v88, v88, v90
v_max_f32 v90, 0, v90
v_add_f32 v91, 0x3727c5ac, v90
v_rsq_f32 v91, v91
v_fma_f32 v92, -v88, v3, v2
Fmu 92,92,91
v_add_f32 v104, 1.0, v91
Fmu 105,88,91
v_xor_b32 v105, 0x80000000, v105
FM 64,64,104,105
FM 65,65,104,105
FM 66,66,104,105
FM 67,67,104,105
C 24,64,65
C 25,66,67
FM 68,68,104,105
FM 69,69,104,105
FM 70,70,104,105
FM 71,71,104,105
C 26,68,69
C 27,70,71
FM 72,72,104,105
FM 73,73,104,105
FM 74,74,104,105
FM 75,75,104,105
C 28,72,73
C 29,74,75
FM 76,76,104,105
FM 77,77,104,105
FM 78,78,104,105
FM 79,79,104,105
C 30,76,77
C 31,78,79
FM 80,80,104,105
FM 81,81,104,105
FM 82,82,104,105
FM 83,83,104,105
C 32,80,81
C 33,82,83
B
v_mov_b32 v162, 151552
ds_read_b32 v160, v162
W 0
v_readfirstlane_b32 s16, v160
N 3
s_and_b32 s68, s16, 0xffff
s_lshr_b32 s69, s16, 16
v_and_b32 v160, 7, v254
v_add_u32 v160, s66, v160
v_cmp_ne_u32 vcc, s68, v160
v_cmp_ne_u32 s[16:17], s69, v160
N 4
s_and_b64 exec, vcc, s[16:17]
buffer_store_dwordx2 v[24:25], v100, s[52:55], s65 offen offset:0 sc1
buffer_store_dwordx2 v[26:27], v100, s[52:55], s65 offen offset:64 sc1
buffer_store_dwordx2 v[28:29], v100, s[52:55], s65 offen offset:128 sc1
buffer_store_dwordx2 v[30:31], v100, s[52:55], s65 offen offset:192 sc1
buffer_store_dwordx2 v[32:33], v100, s[52:55], s65 offen offset:256 sc1
s_mov_b64 exec, -1
s_branch PE3%=
PF3%=:
Lvs 0,102,32,64,0
Lvs 8,102,32,64,1024
Lvs 16,102,32,64,2048
Lvs 24,102,32,64,3072
AS 21,64,4096
Lvs 32,102,32,21,0
AS 64,64,2621440
Lvs 4,102,32,64,0
Lvs 12,102,32,64,1024
Lvs 20,102,32,64,2048
Lvs 28,102,32,64,3072
AS 21,64,4096
Lvs 36,102,32,21,0
v_and_b32 v108, 7, v254
v_lshlrev_b32 v108, 4, v108
VA 108,152064,108
s_lshl_b32 s16, s11, 7
v_add_u32 v103, s16, v108
LO 110,50
HI 111,50
LO 112,51
HI 113,51
LO 114,52
HI 115,52
LO 116,53
HI 117,53
LO 118,54
HI 119,54
LO 120,55
HI 121,55
LO 122,56
HI 123,56
LO 124,57
HI 125,57
LO 126,58
HI 127,58
LO 128,59
HI 129,59
PA 104,110,112
PA 104,104,114
PA 104,104,116
PA 104,104,118
PA 104,104,120
PA 104,104,122
PA 104,104,124
PA 104,104,126
PA 104,104,128
Fad 99,104,105
s_waitcnt vmcnt(9)
LO 88,0
HI 89,0
s_waitcnt vmcnt(4)
LO 104,4
HI 105,4
PA 88,88,104
LO 90,1
HI 91,1
LO 104,5
HI 105,5
PA 90,90,104
LO 92,2
HI 93,2
LO 104,6
HI 105,6
PA 92,92,104
LO 94,3
HI 95,3
LO 104,7
HI 105,7
PA 94,94,104
v_permlane32_swap_b32 v88, v92
v_permlane32_swap_b32 v89, v93
v_permlane32_swap_b32 v90, v94
v_permlane32_swap_b32 v91, v95
PA 64,88,92
PA 66,90,94
LO 104,40
HI 105,40
LO 106,41
HI 107,41
PA 64,64,104
PA 66,66,106
LO 88,8
HI 89,8
s_waitcnt vmcnt(3)
LO 104,12
HI 105,12
PA 88,88,104
LO 90,9
HI 91,9
LO 104,13
HI 105,13
PA 90,90,104
LO 92,10
HI 93,10
LO 104,14
HI 105,14
PA 92,92,104
LO 94,11
HI 95,11
LO 104,15
HI 105,15
PA 94,94,104
v_permlane32_swap_b32 v88, v92
v_permlane32_swap_b32 v89, v93
v_permlane32_swap_b32 v90, v94
v_permlane32_swap_b32 v91, v95
PA 68,88,92
PA 70,90,94
LO 104,42
HI 105,42
LO 106,43
HI 107,43
PA 68,68,104
PA 70,70,106
LO 88,16
HI 89,16
s_waitcnt vmcnt(2)
LO 104,20
HI 105,20
PA 88,88,104
LO 90,17
HI 91,17
LO 104,21
HI 105,21
PA 90,90,104
LO 92,18
HI 93,18
LO 104,22
HI 105,22
PA 92,92,104
LO 94,19
HI 95,19
LO 104,23
HI 105,23
PA 94,94,104
v_permlane32_swap_b32 v88, v92
v_permlane32_swap_b32 v89, v93
v_permlane32_swap_b32 v90, v94
v_permlane32_swap_b32 v91, v95
PA 72,88,92
PA 74,90,94
LO 104,44
HI 105,44
LO 106,45
HI 107,45
PA 72,72,104
PA 74,74,106
LO 88,24
HI 89,24
s_waitcnt vmcnt(1)
LO 104,28
HI 105,28
PA 88,88,104
LO 90,25
HI 91,25
LO 104,29
HI 105,29
PA 90,90,104
LO 92,26
HI 93,26
LO 104,30
HI 105,30
PA 92,92,104
LO 94,27
HI 95,27
LO 104,31
HI 105,31
PA 94,94,104
v_permlane32_swap_b32 v88, v92
v_permlane32_swap_b32 v89, v93
v_permlane32_swap_b32 v90, v94
v_permlane32_swap_b32 v91, v95
PA 76,88,92
PA 78,90,94
LO 104,46
HI 105,46
LO 106,47
HI 107,47
PA 76,76,104
PA 78,78,106
LO 88,32
HI 89,32
s_waitcnt vmcnt(0)
LO 104,36
HI 105,36
PA 88,88,104
LO 90,33
HI 91,33
LO 104,37
HI 105,37
PA 90,90,104
LO 92,34
HI 93,34
LO 104,38
HI 105,38
PA 92,92,104
LO 94,35
HI 95,35
LO 104,39
HI 105,39
PA 94,94,104
v_permlane32_swap_b32 v88, v92
v_permlane32_swap_b32 v89, v93
v_permlane32_swap_b32 v90, v94
v_permlane32_swap_b32 v91, v95
PA 80,88,92
PA 82,90,94
LO 104,48
HI 105,48
LO 106,49
HI 107,49
PA 80,80,104
PA 82,82,106
PA 104,64,66
v_pk_mul_f32 v[106:107], v[64:65], v[64:65]
v_pk_mul_f32 v[88:89], v[64:65], v[110:111]
v_pk_fma_f32 v[106:107], v[66:67], v[66:67], v[106:107]
v_pk_fma_f32 v[88:89], v[66:67], v[112:113], v[88:89]
PA 104,104,68
v_pk_fma_f32 v[106:107], v[68:69], v[68:69], v[106:107]
v_pk_fma_f32 v[88:89], v[68:69], v[114:115], v[88:89]
PA 104,104,70
v_pk_fma_f32 v[106:107], v[70:71], v[70:71], v[106:107]
v_pk_fma_f32 v[88:89], v[70:71], v[116:117], v[88:89]
PA 104,104,72
v_pk_fma_f32 v[106:107], v[72:73], v[72:73], v[106:107]
v_pk_fma_f32 v[88:89], v[72:73], v[118:119], v[88:89]
PA 104,104,74
v_pk_fma_f32 v[106:107], v[74:75], v[74:75], v[106:107]
v_pk_fma_f32 v[88:89], v[74:75], v[120:121], v[88:89]
PA 104,104,76
v_pk_fma_f32 v[106:107], v[76:77], v[76:77], v[106:107]
v_pk_fma_f32 v[88:89], v[76:77], v[122:123], v[88:89]
PA 104,104,78
v_pk_fma_f32 v[106:107], v[78:79], v[78:79], v[106:107]
v_pk_fma_f32 v[88:89], v[78:79], v[124:125], v[88:89]
PA 104,104,80
v_pk_fma_f32 v[106:107], v[80:81], v[80:81], v[106:107]
v_pk_fma_f32 v[88:89], v[80:81], v[126:127], v[88:89]
PA 104,104,82
v_pk_fma_f32 v[106:107], v[82:83], v[82:83], v[106:107]
v_pk_fma_f32 v[88:89], v[82:83], v[128:129], v[88:89]
Fad 96,104,105
Fad 97,106,107
Fad 98,88,89
v_add_f32_dpp v96, v96, v96 row_ror:8 row_mask:0xf bank_mask:0xf
v_add_f32_dpp v97, v97, v97 row_ror:8 row_mask:0xf bank_mask:0xf
v_add_f32_dpp v98, v98, v98 row_ror:8 row_mask:0xf bank_mask:0xf
v_add_f32_dpp v99, v99, v99 row_ror:8 row_mask:0xf bank_mask:0xf
MV 104,96
MV 105,97
MV 106,98
MV 107,99
v_permlane16_swap_b32 v96, v104
v_permlane16_swap_b32 v97, v105
v_permlane16_swap_b32 v98, v106
v_permlane16_swap_b32 v99, v107
Fad 96,96,104
Fad 97,97,105
Fad 98,98,106
Fad 99,99,107
MV 104,96
MV 105,97
MV 106,98
MV 107,99
v_permlane32_swap_b32 v96, v104
v_permlane32_swap_b32 v97, v105
v_permlane32_swap_b32 v98, v106
v_permlane32_swap_b32 v99, v107
Fad 96,96,104
Fad 97,97,105
Fad 98,98,106
Fad 99,99,107
ds_write_b128 v103, v[96:99]
W 0
B
Rv 0,108,0
Rv 4,108,128
Rv 8,108,256
Rv 12,108,384
W 2
PA 0,0,4
PA 2,2,6
W 0
PA 8,8,12
PA 10,10,14
PA 0,0,8
PA 2,2,10
v_mul_f32 v88, 0x3acccccd, v0
v_mul_f32 v90, 0x3acccccd, v1
v_fma_f32 v90, -v88, v88, v90
v_max_f32 v90, 0, v90
v_add_f32 v91, 0x3727c5ac, v90
v_rsq_f32 v91, v91
v_fma_f32 v92, -v88, v3, v2
Fmu 92,92,91
v_add_f32 v104, 1.0, v91
Fmu 105,88,91
v_xor_b32 v105, 0x80000000, v105
FM 64,64,104,105
FM 65,65,104,105
FM 66,66,104,105
FM 67,67,104,105
C 24,64,65
C 25,66,67
FM 68,68,104,105
FM 69,69,104,105
FM 70,70,104,105
FM 71,71,104,105
C 26,68,69
C 27,70,71
FM 72,72,104,105
FM 73,73,104,105
FM 74,74,104,105
FM 75,75,104,105
C 28,72,73
C 29,74,75
FM 76,76,104,105
FM 77,77,104,105
FM 78,78,104,105
FM 79,79,104,105
C 30,76,77
C 31,78,79
FM 80,80,104,105
FM 81,81,104,105
FM 82,82,104,105
FM 83,83,104,105
C 32,80,81
C 33,82,83
GW 224,v24
GW 225,v25
GW 226,v26
GW 227,v27
GW 228,v28
GW 229,v29
GW 230,v30
GW 231,v31
GW 232,v32
GW 233,v33
GW 234,v100
s_add_u32 s36, s14, 0x10000
s_addc_u32 s37, s15, 0
SM 38,0x8000
SM 39,0x20000
s_lshl_b32 s17, s11, 10
SM 20,0
SW_F3%=:
Lvs 128,196,36,17,0
AS 16,17,4096
Lvs 132,196,36,16,0
AS 16,17,8192
Lvs 136,196,36,16,0
AS 16,17,12288
Lvs 140,196,36,16,0
AS 16,17,16384
Lvs 144,196,36,16,0
AS 16,17,20480
Lvs 148,196,36,16,0
AS 16,17,24576
Lvs 152,196,36,16,0
AS 16,17,28672
Lvs 156,196,36,16,0
s_waitcnt vmcnt(0)
v_xor_b32 v160, s79, v131
v_xor_b32 v161, s79, v135
v_or_b32 v160, v160, v161
v_xor_b32 v161, s79, v139
v_or_b32 v160, v160, v161
v_xor_b32 v161, s79, v143
v_or_b32 v160, v160, v161
v_xor_b32 v161, s79, v147
v_or_b32 v160, v160, v161
v_xor_b32 v161, s79, v151
v_or_b32 v160, v160, v161
v_xor_b32 v161, s79, v155
v_or_b32 v160, v160, v161
v_xor_b32 v161, s79, v159
v_or_b32 v160, v160, v161
v_cmp_ne_u32 vcc, 0, v160
N 4
s_cbranch_vccz SWOK_F3%=
AS 20,20,1
s_cmp_lt_u32 s20, 0x10000
s_cbranch_scc0 SWOK_F3%=
s_sleep 1
s_branch SW_F3%=
SWOK_F3%=:
s_lshl_b32 s16, s11, 6
v_add_u32 v161, s16, v254
v_sub_u32 v161, 2047, v161
v_ashrrev_i32 v160, 31, v128
v_or_b32 v160, 0x80000000, v160
v_xor_b32 v160, v160, v128
v_cmp_o_f32 vcc, v128, v128
v_and_b32 v160, 0xfffff800, v160
N 0
v_cndmask_b32 v160, 0, v160, vcc
v_subrev_u32 v162, 0, v161
v_or_b32 v128, v160, v162
v_ashrrev_i32 v160, 31, v132
v_or_b32 v160, 0x80000000, v160
v_xor_b32 v160, v160, v132
v_cmp_o_f32 vcc, v132, v132
v_and_b32 v160, 0xfffff800, v160
N 0
v_cndmask_b32 v160, 0, v160, vcc
v_subrev_u32 v162, 256, v161
v_or_b32 v132, v160, v162
v_ashrrev_i32 v160, 31, v136
v_or_b32 v160, 0x80000000, v160
v_xor_b32 v160, v160, v136
v_cmp_o_f32 vcc, v136, v136
v_and_b32 v160, 0xfffff800, v160
N 0
v_cndmask_b32 v160, 0, v160, vcc
v_subrev_u32 v162, 512, v161
v_or_b32 v136, v160, v162
v_ashrrev_i32 v160, 31, v140
v_or_b32 v160, 0x80000000, v160
v_xor_b32 v160, v160, v140
v_cmp_o_f32 vcc, v140, v140
v_and_b32 v160, 0xfffff800, v160
N 0
v_cndmask_b32 v160, 0, v160, vcc
v_subrev_u32 v162, 768, v161
v_or_b32 v140, v160, v162
v_ashrrev_i32 v160, 31, v144
v_or_b32 v160, 0x80000000, v160
v_xor_b32 v160, v160, v144
v_cmp_o_f32 vcc, v144, v144
v_and_b32 v160, 0xfffff800, v160
N 0
v_cndmask_b32 v160, 0, v160, vcc
v_subrev_u32 v162, 1024, v161
v_or_b32 v144, v160, v162
v_ashrrev_i32 v160, 31, v148
v_or_b32 v160, 0x80000000, v160
v_xor_b32 v160, v160, v148
v_cmp_o_f32 vcc, v148, v148
v_and_b32 v160, 0xfffff800, v160
N 0
v_cndmask_b32 v160, 0, v160, vcc
v_subrev_u32 v162, 1280, v161
v_or_b32 v148, v160, v162
v_ashrrev_i32 v160, 31, v152
v_or_b32 v160, 0x80000000, v160
v_xor_b32 v160, v160, v152
v_cmp_o_f32 vcc, v152, v152
v_and_b32 v160, 0xfffff800, v160
N 0
v_cndmask_b32 v160, 0, v160, vcc
v_subrev_u32 v162, 1536, v161
v_or_b32 v152, v160, v162
v_ashrrev_i32 v160, 31, v156
v_or_b32 v160, 0x80000000, v160
v_xor_b32 v160, v160, v156
v_cmp_o_f32 vcc, v156, v156
v_and_b32 v160, 0xfffff800, v160
N 0
v_cndmask_b32 v160, 0, v160, vcc
v_subrev_u32 v162, 1792, v161
v_or_b32 v156, v160, v162
MV 164,128
v_mov_b32 v165, 0
v_mov_b32 v166, 0
v_min_u32 v160, v164, v132
v_max_u32 v164, v164, v132
v_max_u32 v165, v165, v166
v_max_u32 v165, v165, v160
v_min_u32 v160, v164, v136
v_max_u32 v164, v164, v136
v_max_u32 v165, v165, v166
v_max_u32 v165, v165, v160
v_min_u32 v160, v164, v140
v_max_u32 v164, v164, v140
v_max_u32 v165, v165, v166
v_max_u32 v165, v165, v160
v_min_u32 v160, v164, v144
v_max_u32 v164, v164, v144
v_max_u32 v165, v165, v166
v_max_u32 v165, v165, v160
v_min_u32 v160, v164, v148
v_max_u32 v164, v164, v148
v_max_u32 v165, v165, v166
v_max_u32 v165, v165, v160
v_min_u32 v160, v164, v152
v_max_u32 v164, v164, v152
v_max_u32 v165, v165, v166
v_max_u32 v165, v165, v160
v_min_u32 v160, v164, v156
v_max_u32 v164, v164, v156
v_max_u32 v165, v165, v166
v_max_u32 v165, v165, v160
v_mov_b32_dpp v167, v164 quad_perm:[1,0,3,2] row_mask:0xf bank_mask:0xf
N 0
v_mov_b32_dpp v168, v165 quad_perm:[1,0,3,2] row_mask:0xf bank_mask:0xf
v_min_u32 v160, v164, v167
v_max_u32 v164, v164, v167
v_max_u32 v165, v165, v168
v_max_u32 v165, v165, v160
v_mov_b32_dpp v167, v164 quad_perm:[2,3,0,1] row_mask:0xf bank_mask:0xf
N 0
v_mov_b32_dpp v168, v165 quad_perm:[2,3,0,1] row_mask:0xf bank_mask:0xf
v_min_u32 v160, v164, v167
v_max_u32 v164, v164, v167
v_max_u32 v165, v165, v168
v_max_u32 v165, v165, v160
v_mov_b32_dpp v167, v164 row_half_mirror row_mask:0xf bank_mask:0xf
N 0
v_mov_b32_dpp v168, v165 row_half_mirror row_mask:0xf bank_mask:0xf
v_min_u32 v160, v164, v167
v_max_u32 v164, v164, v167
v_max_u32 v165, v165, v168
v_max_u32 v165, v165, v160
)A"
R"A(v_mov_b32_dpp v167, v164 row_mirror row_mask:0xf bank_mask:0xf
N 0
v_mov_b32_dpp v168, v165 row_mirror row_mask:0xf bank_mask:0xf
v_min_u32 v160, v164, v167
v_max_u32 v164, v164, v167
v_max_u32 v165, v165, v168
v_max_u32 v165, v165, v160
MV 167,164
MV 168,165
N 0
v_permlane16_swap_b32 v164, v167
v_permlane16_swap_b32 v165, v168
v_min_u32 v160, v164, v167
v_max_u32 v164, v164, v167
v_max_u32 v165, v165, v168
v_max_u32 v165, v165, v160
MV 167,164
MV 168,165
N 0
v_permlane32_swap_b32 v164, v167
v_permlane32_swap_b32 v165, v168
v_min_u32 v160, v164, v167
v_max_u32 v164, v164, v167
v_max_u32 v165, v165, v168
v_max_u32 v165, v165, v160
v_mov_b32 v169, 151576
ds_write_b64 v169, v[164:165]
W 0
B
v_mov_b32 v169, 151552
ds_read_b128 v[172:175], v169
Rv 176,169,16
W 1
v_min_u32 v160, v172, v174
v_max_u32 v172, v172, v174
v_max_u32 v173, v173, v175
v_max_u32 v173, v173, v160
W 0
v_min_u32 v160, v172, v176
v_max_u32 v172, v172, v176
v_max_u32 v173, v173, v177
v_max_u32 v173, v173, v160
v_min_u32 v160, v172, v178
v_max_u32 v172, v172, v178
v_max_u32 v173, v173, v179
v_max_u32 v173, v173, v160
v_and_b32 v172, 2047, v172
v_and_b32 v173, 2047, v173
v_sub_u32 v172, 2047, v172
v_sub_u32 v173, 2047, v173
v_readfirstlane_b32 s68, v172
v_readfirstlane_b32 s69, v173
v_and_b32 v208, 1, v254
v_lshrrev_b32 v209, 1, v254
v_and_b32 v209, 3, v209
v_lshrrev_b32 v226, 3, v254
VA 226,15,226
v_mov_b32 v210, s68
v_mov_b32 v211, s69
v_cmp_eq_u32 vcc, 1, v208
N 1
v_cndmask_b32 v210, v210, v211, vcc
v_lshrrev_b32 v212, 4, v210
v_mul_u32_u24 v212, 20480, v212
v_lshl_add_u32 v212, v226, 10, v212
v_and_b32 v213, 15, v210
v_lshl_add_u32 v213, v209, 4, v213
v_lshl_add_u32 v212, v213, 4, v212
v_mul_u32_u24 v213, 1280, v210
v_lshl_add_u32 v213, v209, 3, v213
v_lshl_add_u32 v213, v226, 6, v213
v_lshlrev_b32 v214, 4, v210
s_mov_b32 exec_lo, -1
s_mov_b32 exec_hi, 0xff
buffer_load_dwordx4 v[216:219], v214, s[36:39], 0 offen offset:0 sc1
SM 18,0
Lvs 64,212,32,18,0
SM 18,2621440
Lvs 68,212,32,18,0
SM 18,5242880
Lvs 72,212,32,18,0
SM 18,7864320
Lvs 76,212,32,18,0
buffer_load_dwordx2 v[0:1], v213, s[44:47], 0 offen offset:0
buffer_load_dwordx2 v[2:3], v213, s[44:47], 0 offen offset:32
s_mov_b64 exec, -1
s_mul_i32 s17, s78, 40960
AS 17,17,30720
M0 112640
N 0
D 196,72,17,0
D 196,72,17,1024
D 196,72,17,2048
D 196,72,17,3072
M0 116736
AS 17,17,4096
D 196,72,17,0
D 196,72,17,1024
D 196,72,17,2048
D 196,72,17,3072
M0 120832
AS 17,17,4096
D 196,72,17,0
D 196,72,17,1024
v_lshrrev_b32 v222, 4, v254
v_and_b32 v223, 15, v254
v_lshrrev_b32 v223, 2, v223
v_lshl_add_u32 v223, v222, 2, v223
v_and_b32 v222, 3, v254
v_lshlrev_b32 v222, 3, v222
v_lshl_add_u32 v224, v223, 6, v222
VA 224,40992,224
v_mul_u32_u24 v225, 1280, v223
v_add_u32 v225, v225, v222
VA 225,81920,225
Ta 0,224,0
Ta 2,224,1024
Ta 4,224,2048
Ta 6,224,3072
Ta 8,224,4096
Ta 10,224,5120
Ta 12,224,6144
Ta 14,224,7168
Ta 16,224,8192
Ta 18,224,9216
Ta 20,224,10240
Ta 22,224,11264
Ta 24,224,12288
Ta 26,224,13312
Ta 28,224,14336
Ta 30,224,15360
Ta 32,224,16384
Ta 34,224,17408
Ta 36,224,18432
Ta 38,224,19456
Ta 40,224,20480
Ta 42,224,21504
Ta 44,224,22528
Ta 46,224,23552
Ta 48,224,24576
Ta 50,224,25600
Ta 52,224,26624
Ta 54,224,27648
Ta 56,224,28672
Ta 58,224,29696
Ta 60,224,30720
Ta 62,224,31744
Ta 64,224,32768
Ta 66,224,33792
Ta 68,224,34816
Ta 70,224,35840
Ta 72,224,36864
Ta 74,224,37888
Ta 76,224,38912
Ta 78,224,39936
v_lshlrev_b32 v221, 10, v226
v_lshl_add_u32 v220, v209, 4, v208
v_lshl_add_u32 v221, v220, 4, v221
VA 227,122880,221
v_mul_u32_u24 v221, 2560, v208
v_lshl_add_u32 v221, v209, 4, v221
v_lshl_add_u32 v221, v226, 7, v221
VA 221,143360,221
VA 220,122880,196
s_waitcnt vmcnt(0)
v_add_f32 v222, 1.0, v218
Fmu 223,217,218
v_xor_b32 v223, 0x80000000, v223
LO 40,64
HI 41,64
LO 228,68
HI 229,68
PA 40,40,228
LO 228,72
HI 229,72
PA 40,40,228
LO 228,76
HI 229,76
PA 40,40,228
LO 228,0
HI 229,0
PA 40,40,228
LO 42,65
HI 43,65
LO 228,69
HI 229,69
PA 42,42,228
LO 228,73
HI 229,73
PA 42,42,228
LO 228,77
HI 229,77
PA 42,42,228
LO 228,1
HI 229,1
PA 42,42,228
LO 44,66
HI 45,66
LO 228,70
HI 229,70
PA 44,44,228
LO 228,74
HI 229,74
PA 44,44,228
LO 228,78
HI 229,78
PA 44,44,228
LO 228,2
HI 229,2
PA 44,44,228
LO 46,67
HI 47,67
LO 228,71
HI 229,71
PA 46,46,228
LO 228,75
HI 229,75
PA 46,46,228
LO 228,79
HI 229,79
PA 46,46,228
LO 228,3
HI 229,3
PA 46,46,228
FM 48,40,218,223
FM 49,41,218,223
FM 50,42,218,223
FM 51,43,218,223
FM 52,44,218,223
FM 53,45,218,223
FM 54,46,218,223
FM 55,47,218,223
C 56,48,49
C 57,50,51
C 58,52,53
C 59,54,55
FM 40,40,222,223
FM 41,41,222,223
FM 42,42,222,223
FM 43,43,222,223
FM 44,44,222,223
FM 45,45,222,223
FM 46,46,222,223
FM 47,47,222,223
s_mov_b32 exec_lo, -1
s_mov_b32 exec_hi, 0xff
ds_write_b128 v227, v[56:59]
ds_write_b128 v221, v[40:43]
DWv 221,44,64
s_mov_b64 exec, -1
W 0
B
Rv 0,220,0
Rv 4,220,1024
Rv 8,220,2048
Rv 12,220,3072
Rv 16,220,4096
Rv 20,220,5120
Rv 24,220,6144
Rv 28,220,7168
Rv 32,220,8192
Rv 36,220,9216
Rv 40,220,10240
Rv 44,220,11264
Rv 48,220,12288
Rv 52,220,13312
Rv 56,220,14336
Rv 60,220,15360
Rv 64,220,16384
Rv 68,220,17408
Rv 72,220,18432
Rv 76,220,19456
Ta 80,225,960
Ta 82,225,21440
Ta 84,225,992
Ta 86,225,21472
Ta 88,225,1024
Ta 90,225,21504
Ta 92,225,1056
Ta 94,225,21536
Ta 96,225,1088
Ta 98,225,21568
Ta 100,225,1120
Ta 102,225,21600
Ta 104,225,1152
Ta 106,225,21632
Ta 108,225,1184
Ta 110,225,21664
Ta 112,225,1216
Ta 114,225,21696
Ta 116,225,1248
Ta 118,225,21728
W 15
Mvavz 180,0,0
W 15
Mvavz 184,4,4
W 15
Mvavc 180,8,8
W 15
Mvavc 184,12,12
W 15
Mvavc 180,16,16
W 15
Mvavc 184,20,20
W 15
Mvavc 180,24,24
W 15
Mvavc 184,28,28
W 15
Mvavc 180,32,32
W 15
Mvavc 184,36,36
W 15
Mvavc 180,40,40
W 15
Mvavc 184,44,44
W 15
Mvavc 180,48,48
W 15
Mvavc 184,52,52
W 15
Mvavc 180,56,56
W 15
Mvavc 184,60,60
W 15
Mvavc 180,64,64
W 15
Mvavc 184,68,68
W 15
Mvavc 180,72,72
W 15
Mvavc 184,76,76
VA 215,149504,196
N 6
Fad 160,180,184
Fad 161,181,185
Fad 162,182,186
Fad 163,183,187
ds_write_b128 v215, v[160:163]
W 0
B
s_add_u32 s40, s14, 0x5420000
s_addc_u32 s41, s15, 0
SM 42,0x43800
SM 43,0x20000
v_and_b32 v208, 15, v254
v_mul_u32_u24 v208, 2560, v208
v_lshrrev_b32 v209, 4, v254
v_lshl_add_u32 v208, v209, 4, v208
s_mul_i32 s16, s78, 5120
AS 16,16,1920
W 0
B
VA 215,150528,196
ds_read_b128 v[176:179], v215
W 0
Mvavz 48,80,176
Mvavz 52,84,176
Mvavz 56,88,176
Mvavz 60,92,176
Mvavz 64,96,176
Mvavz 68,100,176
Mvavz 72,104,176
Mvavz 76,108,176
Mvavz 80,112,176
Mvavz 84,116,176
s_mov_b32 exec_lo, 0x00030003
s_mov_b32 exec_hi, 0x00030003
Svs 48,208,40,16,0
Svs 52,208,40,16,64
Svs 56,208,40,16,128
Svs 60,208,40,16,192
Svs 64,208,40,16,256
Svs 68,208,40,16,320
Svs 72,208,40,16,384
Svs 76,208,40,16,448
Svs 80,208,40,16,512
Svs 84,208,40,16,576
s_mov_b64 exec, -1
s_waitcnt vmcnt(0)
B
G 24,224
G 25,225
G 26,226
G 27,227
G 28,228
G 29,229
G 30,230
G 31,231
G 32,232
G 33,233
G 100,234
v_and_b32 v160, 7, v254
v_add_u32 v160, s66, v160
v_cmp_ne_u32 vcc, s68, v160
v_cmp_ne_u32 s[16:17], s69, v160
N 4
s_and_b64 exec, vcc, s[16:17]
buffer_store_dwordx2 v[24:25], v100, s[52:55], s65 offen offset:0 sc1
buffer_store_dwordx2 v[26:27], v100, s[52:55], s65 offen offset:64 sc1
buffer_store_dwordx2 v[28:29], v100, s[52:55], s65 offen offset:128 sc1
buffer_store_dwordx2 v[30:31], v100, s[52:55], s65 offen offset:192 sc1
buffer_store_dwordx2 v[32:33], v100, s[52:55], s65 offen offset:256 sc1
s_mov_b64 exec, -1
s_cmp_lt_u32 s78, 20
s_cbranch_scc0 PE3%=
B
s_lshl_b32 s16, s78, 8
v_lshlrev_b32 v200, 2, v254
AS 16,16,215040
buffer_load_dword v0, v200, s[40:43], s16 offen sc1
AS 16,16,5120
buffer_load_dword v1, v200, s[40:43], s16 offen sc1
AS 16,16,5120
buffer_load_dword v2, v200, s[40:43], s16 offen sc1
AS 16,16,5120
buffer_load_dword v3, v200, s[40:43], s16 offen sc1
AS 16,16,5120
buffer_load_dword v4, v200, s[40:43], s16 offen sc1
AS 16,16,5120
buffer_load_dword v5, v200, s[40:43], s16 offen sc1
AS 16,16,5120
buffer_load_dword v6, v200, s[40:43], s16 offen sc1
AS 16,16,5120
buffer_load_dword v7, v200, s[40:43], s16 offen sc1
AS 16,16,5120
buffer_load_dword v8, v200, s[40:43], s16 offen sc1
AS 16,16,5120
buffer_load_dword v9, v200, s[40:43], s16 offen sc1
AS 16,16,5120
buffer_load_dword v10, v200, s[40:43], s16 offen sc1
AS 16,16,5120
buffer_load_dword v11, v200, s[40:43], s16 offen sc1
AS 16,16,5120
s_waitcnt vmcnt(10)
Fad 0,0,1
s_waitcnt vmcnt(9)
Fad 0,0,2
s_waitcnt vmcnt(8)
Fad 0,0,3
s_waitcnt vmcnt(7)
Fad 0,0,4
s_waitcnt vmcnt(6)
Fad 0,0,5
s_waitcnt vmcnt(5)
Fad 0,0,6
s_waitcnt vmcnt(4)
Fad 0,0,7
s_waitcnt vmcnt(3)
Fad 0,0,8
s_waitcnt vmcnt(2)
Fad 0,0,9
s_waitcnt vmcnt(1)
Fad 0,0,10
s_waitcnt vmcnt(0)
Fad 0,0,11
VA 201,151808,200
ds_write_b32 v201, v0 offset:768
W 0
B
PE3%=:
OE3%=:
s_branch FM3_DONE%=
XN3_SPIN%=:
SM 20,0
XN3_L%=:
s_sleep 2
global_load_dword v246, v247, s[14:15] sc1
s_waitcnt vmcnt(0)
v_cmp_ne_u32 vcc, s79, v246
N 4
s_cbranch_vccz XN3_OK%=
AS 20,20,1
s_cmp_lt_u32 s20, 65536
s_cbranch_scc1 XN3_L%=
s_branch XN3_OK%=
GOSLOW3%=:
s_cmp_eq_u32 s17, 0
s_cbranch_scc1 SBACK3%=
s_getpc_b64 s[22:23]
SRET3%=:
s_add_u32 s22, s22, SBACK3%=-SRET3%=
s_addc_u32 s23, s23, 0
s_getpc_b64 s[16:17]
SJ3%=:
s_add_u32 s16, s16, SLOW%=-SJ3%=
s_addc_u32 s17, s17, 0
s_setpc_b64 s[16:17]
SBACK3%=:
s_and_b32 s16, s10, 7
s_lshr_b32 s41, s16, 1
s_and_b32 s16, s16, 1
s_lshl_b32 s16, s16, 3
s_lshr_b32 s17, s10, 3
s_lshr_b32 s18, s17, 2
s_add_u32 s39, s16, s18
s_and_b32 s40, s17, 3
s_lshl_b32 s16, s41, 2
s_add_u32 s43, s16, s40
s_mov_b64 s[24:25], s[50:51]
SM 26,0x500000
SM 27,0x20000
s_lshl_b32 s16, s41, 10
s_lshl_b32 s17, s11, 5
s_add_u32 s16, s16, s17
s_mul_i32 s16, s16, 1280
s_mul_i32 s17, s40, 320
s_add_u32 s36, s16, s17
s_waitcnt vmcnt(0)
B
s_getpc_b64 s[16:17]
SK3%=:
s_add_u32 s16, s16, MDONE3%=-SK3%=
s_addc_u32 s17, s17, -1
s_setpc_b64 s[16:17]
FM3_DONE%=:
FM_END%=:
s_waitcnt vmcnt(0)
s_branch KEND%=
SLOW%=:
s_add_u32 s24, s14, 0x400000
s_addc_u32 s25, s15, 0
SM 26,0x1000000
SM 27,0x20000
s_add_u32 s28, s14, 0x1400000
s_addc_u32 s29, s15, 0
SM 30,0x1000000
SM 31,0x20000
s_sub_u32 s16, 15, s13
s_cmp_ge_u32 s11, 2
s_cselect_b32 s16, s13, s16
s_lshl_b32 s16, s16, 1
s_and_b32 s17, s11, 1
s_add_u32 s57, s16, s17
s_lshl_b32 s58, s12, 19
SM 56,0
GW 0,0
GW 1,0
GW 2,0
GW 3,0
GW 4,0
GW 5,0
GW 6,0
GW 7,0
GW 8,0
GW 9,0
GW 10,0
GW 11,0
GW 12,0
GW 13,0
GW 14,0
GW 15,0
GW 16,0
GW 17,0
GW 18,0
GW 19,0
GW 20,0
GW 21,0
GW 22,0
GW 23,0
GW 24,0
GW 25,0
GW 26,0
GW 27,0
GW 28,0
GW 29,0
GW 30,0
GW 31,0
GW 32,0
GW 33,0
GW 34,0
GW 35,0
GW 36,0
GW 37,0
GW 38,0
GW 39,0
GW 40,0
GW 41,0
GW 42,0
GW 43,0
GW 44,0
GW 45,0
GW 46,0
GW 47,0
GW 48,0
GW 49,0
GW 50,0
GW 51,0
GW 52,0
GW 53,0
GW 54,0
GW 55,0
GW 56,0
GW 57,0
GW 58,0
GW 59,0
GW 60,0
GW 61,0
GW 62,0
GW 63,0
GW 64,0
GW 65,0
GW 66,0
GW 67,0
GW 68,0
GW 69,0
GW 70,0
GW 71,0
GW 72,0
GW 73,0
GW 74,0
GW 75,0
GW 76,0
GW 77,0
GW 78,0
GW 79,0
GW 80,0
GW 81,0
GW 82,0
GW 83,0
GW 84,0
GW 85,0
GW 86,0
GW 87,0
GW 88,0
GW 89,0
GW 90,0
GW 91,0
GW 92,0
GW 93,0
GW 94,0
GW 95,0
GW 96,0
GW 97,0
GW 98,0
GW 99,0
GW 100,0
GW 101,0
GW 102,0
GW 103,0
GW 104,0
GW 105,0
GW 106,0
GW 107,0
GW 108,0
GW 109,0
GW 110,0
GW 111,0
GW 112,0
GW 113,0
GW 114,0
GW 115,0
GW 116,0
GW 117,0
GW 118,0
GW 119,0
GW 120,0
GW 121,0
GW 122,0
GW 123,0
GW 124,0
GW 125,0
GW 126,0
GW 127,0
v_mov_b32 v216, 0xff800000
v_mov_b32 v220, 0
v_mov_b32 v217, 0xff800000
v_mov_b32 v221, 0
v_mov_b32 v218, 0xff800000
v_mov_b32 v222, 0
v_mov_b32 v219, 0xff800000
v_mov_b32 v223, 0
SLOW_L%=:
s_mov_b64 s[44:45], -1
s_mov_b64 s[46:47], -1
s_mov_b64 s[48:49], -1
s_mov_b64 s[52:53], -1
s_mov_b64 s[54:55], -1
s_cmp_eq_u32 s56, s57
s_cbranch_scc0 SLOW_NM%=
s_mov_b64 s[44:45], 0
s_mov_b64 s[46:47], s[62:63]
s_mov_b64 s[48:49], s[64:65]
s_mov_b64 s[52:53], s[66:67]
s_mov_b64 s[54:55], s[68:69]
SLOW_NM%=:
s_mov_b32 s16, s58
Lvs 0,196,24,16,0
Lvs 128,196,28,16,0
Lvs 4,196,24,16,1024
Lvs 132,196,28,16,1024
Lvs 8,196,24,16,2048
Lvs 136,196,28,16,2048
Lvs 12,196,24,16,3072
Lvs 140,196,28,16,3072
AS 16,16,4096
Lvs 16,196,24,16,0
Lvs 144,196,28,16,0
Lvs 20,196,24,16,1024
Lvs 148,196,28,16,1024
Lvs 24,196,24,16,2048
Lvs 152,196,28,16,2048
Lvs 28,196,24,16,3072
Lvs 156,196,28,16,3072
)A"
R"A(AS 16,16,4096
Lvs 32,196,24,16,0
Lvs 160,196,28,16,0
Lvs 36,196,24,16,1024
Lvs 164,196,28,16,1024
Lvs 40,196,24,16,2048
Lvs 168,196,28,16,2048
Lvs 44,196,24,16,3072
Lvs 172,196,28,16,3072
AS 16,16,4096
Lvs 48,196,24,16,0
Lvs 176,196,28,16,0
Lvs 52,196,24,16,1024
Lvs 180,196,28,16,1024
Lvs 56,196,24,16,2048
Lvs 184,196,28,16,2048
Lvs 60,196,24,16,3072
Lvs 188,196,28,16,3072
AS 16,16,4096
s_waitcnt vmcnt(0)
Mvvaz 64,0,128
Mvvaz 72,0,144
Mvvaz 80,0,160
Mvvaz 88,0,176
Mvvac 64,4,132
Mvvac 72,4,148
Mvvac 80,4,164
Mvvac 88,4,180
Mvvac 64,8,136
Mvvac 72,8,152
Mvvac 80,8,168
Mvvac 88,8,184
Mvvac 64,12,140
Mvvac 72,12,156
Mvvac 80,12,172
Mvvac 88,12,188
Mvvaz 68,16,128
Mvvaz 76,16,144
Mvvaz 84,16,160
Mvvaz 92,16,176
Mvvac 68,20,132
Mvvac 76,20,148
Mvvac 84,20,164
Mvvac 92,20,180
Mvvac 68,24,136
Mvvac 76,24,152
Mvvac 84,24,168
Mvvac 92,24,184
Mvvac 68,28,140
Mvvac 76,28,156
Mvvac 84,28,172
Mvvac 92,28,188
Mvvaz 96,32,128
Mvvaz 104,32,144
Mvvaz 200,32,160
Mvvaz 208,32,176
Mvvac 96,36,132
Mvvac 104,36,148
Mvvac 200,36,164
Mvvac 208,36,180
Mvvac 96,40,136
Mvvac 104,40,152
Mvvac 200,40,168
Mvvac 208,40,184
Mvvac 96,44,140
Mvvac 104,44,156
Mvvac 200,44,172
Mvvac 208,44,188
Mvvaz 100,48,128
Mvvaz 108,48,144
Mvvaz 204,48,160
Mvvaz 212,48,176
Mvvac 100,52,132
Mvvac 108,52,148
Mvvac 204,52,164
Mvvac 212,52,180
Mvvac 100,56,136
Mvvac 108,56,152
Mvvac 204,56,168
Mvvac 212,56,184
Mvvac 100,60,140
Mvvac 108,60,156
Mvvac 204,60,172
Mvvac 212,60,188
N 7
K 64,199,64,46
K 65,199,65,48
K 66,199,66,52
K 67,199,67,54
K 68,199,68,44
K 69,199,69,44
K 70,199,70,44
K 71,199,71,44
K 76,199,76,46
K 77,199,77,48
K 78,199,78,52
K 79,199,79,54
K 96,199,96,44
K 97,199,97,44
K 98,199,98,44
K 99,199,99,44
K 104,199,104,44
K 105,199,105,44
K 106,199,106,44
K 107,199,107,44
K 200,199,200,46
K 201,199,201,48
K 202,199,202,52
K 203,199,203,54
K 100,199,100,44
K 101,199,101,44
K 102,199,102,44
K 103,199,103,44
K 108,199,108,44
K 109,199,109,44
K 110,199,110,44
K 111,199,111,44
K 204,199,204,44
K 205,199,205,44
K 206,199,206,44
K 207,199,207,44
K 212,199,212,46
K 213,199,213,48
K 214,199,214,52
K 215,199,215,54
v_max3_f32 v228, v64, v65, v66
Fma 228,228,67
v_max3_f32 v228, v228, v68, v69
v_max3_f32 v228, v228, v70, v71
v_max3_f32 v228, v228, v96, v97
v_max3_f32 v228, v228, v98, v99
v_max3_f32 v228, v228, v100, v101
v_max3_f32 v228, v228, v102, v103
MV 229,228
N 1
v_permlane16_swap_b32 v228, v229
Fma 228,228,229
MV 229,228
N 1
v_permlane32_swap_b32 v228, v229
Fma 228,228,229
Fma 228,228,216
Fsu 224,216,228
E 224
MV 216,228
Fmu 220,220,224
Fsu 64,64,216
E 64
N 0
Fad 220,220,64
Fsu 65,65,216
E 65
N 0
Fad 220,220,65
Fsu 66,66,216
E 66
N 0
Fad 220,220,66
Fsu 67,67,216
E 67
N 0
Fad 220,220,67
Fsu 68,68,216
E 68
N 0
Fad 220,220,68
Fsu 69,69,216
E 69
N 0
Fad 220,220,69
Fsu 70,70,216
E 70
N 0
Fad 220,220,70
Fsu 71,71,216
E 71
N 0
Fad 220,220,71
Fsu 96,96,216
E 96
N 0
Fad 220,220,96
Fsu 97,97,216
E 97
N 0
Fad 220,220,97
Fsu 98,98,216
E 98
N 0
Fad 220,220,98
Fsu 99,99,216
E 99
N 0
Fad 220,220,99
Fsu 100,100,216
E 100
N 0
Fad 220,220,100
Fsu 101,101,216
E 101
N 0
Fad 220,220,101
Fsu 102,102,216
E 102
N 0
Fad 220,220,102
Fsu 103,103,216
E 103
N 0
Fad 220,220,103
C 64,64,65
C 65,66,67
C 66,68,69
C 67,70,71
C 96,96,97
C 97,98,99
C 98,100,101
C 99,102,103
G 230,0
Fmu 230,230,224
GW 0,v230
G 230,1
Fmu 230,230,224
GW 1,v230
G 230,2
Fmu 230,230,224
GW 2,v230
G 230,3
Fmu 230,230,224
GW 3,v230
G 230,16
Fmu 230,230,224
GW 16,v230
G 230,17
Fmu 230,230,224
GW 17,v230
G 230,18
Fmu 230,230,224
GW 18,v230
G 230,19
Fmu 230,230,224
GW 19,v230
G 230,32
Fmu 230,230,224
GW 32,v230
G 230,33
Fmu 230,230,224
GW 33,v230
G 230,34
Fmu 230,230,224
GW 34,v230
G 230,35
Fmu 230,230,224
GW 35,v230
G 230,48
Fmu 230,230,224
GW 48,v230
G 230,49
Fmu 230,230,224
GW 49,v230
G 230,50
Fmu 230,230,224
GW 50,v230
G 230,51
Fmu 230,230,224
GW 51,v230
G 230,64
Fmu 230,230,224
GW 64,v230
G 230,65
Fmu 230,230,224
GW 65,v230
G 230,66
Fmu 230,230,224
GW 66,v230
G 230,67
Fmu 230,230,224
GW 67,v230
G 230,80
Fmu 230,230,224
GW 80,v230
G 230,81
Fmu 230,230,224
GW 81,v230
G 230,82
Fmu 230,230,224
GW 82,v230
G 230,83
Fmu 230,230,224
GW 83,v230
G 230,96
Fmu 230,230,224
GW 96,v230
G 230,97
Fmu 230,230,224
GW 97,v230
G 230,98
Fmu 230,230,224
GW 98,v230
G 230,99
Fmu 230,230,224
GW 99,v230
G 230,112
Fmu 230,230,224
GW 112,v230
G 230,113
Fmu 230,230,224
GW 113,v230
G 230,114
Fmu 230,230,224
GW 114,v230
G 230,115
Fmu 230,230,224
GW 115,v230
v_max3_f32 v228, v72, v73, v74
Fma 228,228,75
v_max3_f32 v228, v228, v76, v77
v_max3_f32 v228, v228, v78, v79
v_max3_f32 v228, v228, v104, v105
v_max3_f32 v228, v228, v106, v107
v_max3_f32 v228, v228, v108, v109
v_max3_f32 v228, v228, v110, v111
MV 229,228
N 1
v_permlane16_swap_b32 v228, v229
Fma 228,228,229
MV 229,228
N 1
v_permlane32_swap_b32 v228, v229
Fma 228,228,229
Fma 228,228,217
Fsu 225,217,228
E 225
MV 217,228
Fmu 221,221,225
Fsu 72,72,217
E 72
N 0
Fad 221,221,72
Fsu 73,73,217
E 73
N 0
Fad 221,221,73
Fsu 74,74,217
E 74
N 0
Fad 221,221,74
Fsu 75,75,217
E 75
N 0
Fad 221,221,75
Fsu 76,76,217
E 76
N 0
Fad 221,221,76
Fsu 77,77,217
E 77
N 0
Fad 221,221,77
Fsu 78,78,217
E 78
N 0
Fad 221,221,78
Fsu 79,79,217
E 79
N 0
Fad 221,221,79
Fsu 104,104,217
E 104
N 0
Fad 221,221,104
Fsu 105,105,217
E 105
N 0
Fad 221,221,105
Fsu 106,106,217
E 106
N 0
Fad 221,221,106
Fsu 107,107,217
E 107
N 0
Fad 221,221,107
Fsu 108,108,217
E 108
N 0
Fad 221,221,108
Fsu 109,109,217
E 109
N 0
Fad 221,221,109
Fsu 110,110,217
E 110
N 0
Fad 221,221,110
Fsu 111,111,217
E 111
N 0
Fad 221,221,111
C 72,72,73
C 73,74,75
C 74,76,77
C 75,78,79
C 104,104,105
C 105,106,107
C 106,108,109
C 107,110,111
G 230,4
Fmu 230,230,225
GW 4,v230
G 230,5
Fmu 230,230,225
GW 5,v230
G 230,6
Fmu 230,230,225
GW 6,v230
G 230,7
Fmu 230,230,225
GW 7,v230
G 230,20
Fmu 230,230,225
GW 20,v230
G 230,21
Fmu 230,230,225
GW 21,v230
G 230,22
Fmu 230,230,225
GW 22,v230
G 230,23
Fmu 230,230,225
GW 23,v230
G 230,36
Fmu 230,230,225
GW 36,v230
G 230,37
Fmu 230,230,225
GW 37,v230
G 230,38
Fmu 230,230,225
GW 38,v230
G 230,39
Fmu 230,230,225
GW 39,v230
G 230,52
Fmu 230,230,225
GW 52,v230
G 230,53
Fmu 230,230,225
GW 53,v230
G 230,54
Fmu 230,230,225
GW 54,v230
G 230,55
Fmu 230,230,225
GW 55,v230
G 230,68
Fmu 230,230,225
GW 68,v230
G 230,69
Fmu 230,230,225
GW 69,v230
G 230,70
Fmu 230,230,225
GW 70,v230
G 230,71
Fmu 230,230,225
GW 71,v230
G 230,84
Fmu 230,230,225
GW 84,v230
G 230,85
Fmu 230,230,225
GW 85,v230
G 230,86
Fmu 230,230,225
GW 86,v230
G 230,87
Fmu 230,230,225
GW 87,v230
G 230,100
Fmu 230,230,225
GW 100,v230
G 230,101
Fmu 230,230,225
GW 101,v230
G 230,102
Fmu 230,230,225
GW 102,v230
G 230,103
Fmu 230,230,225
GW 103,v230
G 230,116
Fmu 230,230,225
GW 116,v230
G 230,117
Fmu 230,230,225
GW 117,v230
G 230,118
Fmu 230,230,225
GW 118,v230
G 230,119
Fmu 230,230,225
GW 119,v230
v_max3_f32 v228, v80, v81, v82
Fma 228,228,83
v_max3_f32 v228, v228, v84, v85
v_max3_f32 v228, v228, v86, v87
v_max3_f32 v228, v228, v200, v201
v_max3_f32 v228, v228, v202, v203
v_max3_f32 v228, v228, v204, v205
v_max3_f32 v228, v228, v206, v207
MV 229,228
N 1
v_permlane16_swap_b32 v228, v229
Fma 228,228,229
MV 229,228
N 1
v_permlane32_swap_b32 v228, v229
Fma 228,228,229
Fma 228,228,218
Fsu 226,218,228
E 226
MV 218,228
Fmu 222,222,226
Fsu 80,80,218
E 80
N 0
Fad 222,222,80
Fsu 81,81,218
E 81
N 0
Fad 222,222,81
Fsu 82,82,218
E 82
N 0
Fad 222,222,82
Fsu 83,83,218
E 83
N 0
Fad 222,222,83
Fsu 84,84,218
E 84
N 0
Fad 222,222,84
Fsu 85,85,218
E 85
N 0
Fad 222,222,85
Fsu 86,86,218
E 86
N 0
Fad 222,222,86
Fsu 87,87,218
E 87
N 0
Fad 222,222,87
Fsu 200,200,218
E 200
N 0
Fad 222,222,200
Fsu 201,201,218
E 201
N 0
Fad 222,222,201
Fsu 202,202,218
E 202
N 0
Fad 222,222,202
Fsu 203,203,218
E 203
N 0
Fad 222,222,203
Fsu 204,204,218
E 204
N 0
Fad 222,222,204
Fsu 205,205,218
E 205
N 0
Fad 222,222,205
Fsu 206,206,218
E 206
N 0
Fad 222,222,206
Fsu 207,207,218
E 207
N 0
Fad 222,222,207
C 80,80,81
C 81,82,83
C 82,84,85
C 83,86,87
C 200,200,201
C 201,202,203
C 202,204,205
C 203,206,207
G 230,8
Fmu 230,230,226
GW 8,v230
G 230,9
Fmu 230,230,226
GW 9,v230
G 230,10
Fmu 230,230,226
GW 10,v230
G 230,11
Fmu 230,230,226
GW 11,v230
G 230,24
Fmu 230,230,226
GW 24,v230
G 230,25
Fmu 230,230,226
GW 25,v230
G 230,26
Fmu 230,230,226
GW 26,v230
G 230,27
Fmu 230,230,226
GW 27,v230
G 230,40
Fmu 230,230,226
GW 40,v230
G 230,41
Fmu 230,230,226
GW 41,v230
G 230,42
Fmu 230,230,226
GW 42,v230
G 230,43
Fmu 230,230,226
GW 43,v230
G 230,56
Fmu 230,230,226
GW 56,v230
G 230,57
Fmu 230,230,226
GW 57,v230
G 230,58
Fmu 230,230,226
GW 58,v230
G 230,59
Fmu 230,230,226
GW 59,v230
G 230,72
Fmu 230,230,226
GW 72,v230
G 230,73
Fmu 230,230,226
GW 73,v230
G 230,74
Fmu 230,230,226
GW 74,v230
G 230,75
Fmu 230,230,226
GW 75,v230
G 230,88
Fmu 230,230,226
GW 88,v230
G 230,89
Fmu 230,230,226
GW 89,v230
G 230,90
Fmu 230,230,226
GW 90,v230
G 230,91
Fmu 230,230,226
GW 91,v230
G 230,104
Fmu 230,230,226
GW 104,v230
G 230,105
Fmu 230,230,226
GW 105,v230
G 230,106
Fmu 230,230,226
GW 106,v230
G 230,107
Fmu 230,230,226
GW 107,v230
G 230,120
Fmu 230,230,226
GW 120,v230
G 230,121
Fmu 230,230,226
GW 121,v230
G 230,122
Fmu 230,230,226
GW 122,v230
G 230,123
Fmu 230,230,226
GW 123,v230
v_max3_f32 v228, v88, v89, v90
Fma 228,228,91
v_max3_f32 v228, v228, v92, v93
v_max3_f32 v228, v228, v94, v95
v_max3_f32 v228, v228, v208, v209
v_max3_f32 v228, v228, v210, v211
v_max3_f32 v228, v228, v212, v213
v_max3_f32 v228, v228, v214, v215
MV 229,228
N 1
v_permlane16_swap_b32 v228, v229
Fma 228,228,229
MV 229,228
N 1
v_permlane32_swap_b32 v228, v229
Fma 228,228,229
Fma 228,228,219
Fsu 227,219,228
E 227
MV 219,228
Fmu 223,223,227
Fsu 88,88,219
E 88
N 0
Fad 223,223,88
Fsu 89,89,219
E 89
N 0
Fad 223,223,89
Fsu 90,90,219
E 90
N 0
Fad 223,223,90
Fsu 91,91,219
E 91
N 0
Fad 223,223,91
Fsu 92,92,219
E 92
N 0
Fad 223,223,92
Fsu 93,93,219
E 93
N 0
Fad 223,223,93
Fsu 94,94,219
E 94
N 0
Fad 223,223,94
Fsu 95,95,219
E 95
N 0
Fad 223,223,95
Fsu 208,208,219
E 208
N 0
Fad 223,223,208
Fsu 209,209,219
E 209
N 0
Fad 223,223,209
Fsu 210,210,219
E 210
N 0
Fad 223,223,210
Fsu 211,211,219
E 211
N 0
Fad 223,223,211
Fsu 212,212,219
E 212
N 0
Fad 223,223,212
Fsu 213,213,219
E 213
N 0
Fad 223,223,213
Fsu 214,214,219
E 214
N 0
Fad 223,223,214
Fsu 215,215,219
E 215
N 0
Fad 223,223,215
C 88,88,89
C 89,90,91
C 90,92,93
C 91,94,95
C 208,208,209
C 209,210,211
C 210,212,213
C 211,214,215
G 230,12
Fmu 230,230,227
GW 12,v230
G 230,13
Fmu 230,230,227
GW 13,v230
G 230,14
Fmu 230,230,227
GW 14,v230
G 230,15
Fmu 230,230,227
GW 15,v230
G 230,28
Fmu 230,230,227
GW 28,v230
G 230,29
Fmu 230,230,227
GW 29,v230
G 230,30
Fmu 230,230,227
GW 30,v230
G 230,31
Fmu 230,230,227
GW 31,v230
G 230,44
Fmu 230,230,227
GW 44,v230
G 230,45
Fmu 230,230,227
GW 45,v230
G 230,46
Fmu 230,230,227
GW 46,v230
G 230,47
Fmu 230,230,227
GW 47,v230
G 230,60
Fmu 230,230,227
GW 60,v230
G 230,61
Fmu 230,230,227
GW 61,v230
G 230,62
Fmu 230,230,227
GW 62,v230
G 230,63
Fmu 230,230,227
GW 63,v230
G 230,76
Fmu 230,230,227
GW 76,v230
G 230,77
Fmu 230,230,227
GW 77,v230
G 230,78
Fmu 230,230,227
GW 78,v230
G 230,79
Fmu 230,230,227
GW 79,v230
G 230,92
Fmu 230,230,227
GW 92,v230
G 230,93
Fmu 230,230,227
GW 93,v230
G 230,94
Fmu 230,230,227
GW 94,v230
G 230,95
Fmu 230,230,227
GW 95,v230
G 230,108
Fmu 230,230,227
GW 108,v230
G 230,109
Fmu 230,230,227
GW 109,v230
G 230,110
Fmu 230,230,227
GW 110,v230
G 230,111
Fmu 230,230,227
GW 111,v230
G 230,124
Fmu 230,230,227
GW 124,v230
G 230,125
Fmu 230,230,227
GW 125,v230
G 230,126
Fmu 230,230,227
GW 126,v230
G 230,127
Fmu 230,230,227
GW 127,v230
N 1
Mavvc 0,128,64
Mavvc 4,128,72
Mavvc 8,128,80
Mavvc 12,128,88
Mavvc 16,132,64
Mavvc 20,132,72
Mavvc 24,132,80
Mavvc 28,132,88
Mavvc 32,136,64
Mavvc 36,136,72
Mavvc 40,136,80
Mavvc 44,136,88
Mavvc 48,140,64
Mavvc 52,140,72
Mavvc 56,140,80
Mavvc 60,140,88
Mavvc 64,144,64
Mavvc 68,144,72
Mavvc 72,144,80
Mavvc 76,144,88
Mavvc 80,148,64
Mavvc 84,148,72
Mavvc 88,148,80
Mavvc 92,148,88
Mavvc 96,152,64
Mavvc 100,152,72
Mavvc 104,152,80
Mavvc 108,152,88
Mavvc 112,156,64
Mavvc 116,156,72
Mavvc 120,156,80
Mavvc 124,156,88
Mavvc 0,160,96
Mavvc 4,160,104
Mavvc 8,160,200
Mavvc 12,160,208
)A"
R"A(Mavvc 16,164,96
Mavvc 20,164,104
Mavvc 24,164,200
Mavvc 28,164,208
Mavvc 32,168,96
Mavvc 36,168,104
Mavvc 40,168,200
Mavvc 44,168,208
Mavvc 48,172,96
Mavvc 52,172,104
Mavvc 56,172,200
Mavvc 60,172,208
Mavvc 64,176,96
Mavvc 68,176,104
Mavvc 72,176,200
Mavvc 76,176,208
Mavvc 80,180,96
Mavvc 84,180,104
Mavvc 88,180,200
Mavvc 92,180,208
Mavvc 96,184,96
Mavvc 100,184,104
Mavvc 104,184,200
Mavvc 108,184,208
Mavvc 112,188,96
Mavvc 116,188,104
Mavvc 120,188,200
Mavvc 124,188,208
AS 58,58,16384
AS 56,56,1
s_cmp_le_u32 s56, s57
s_cbranch_scc0 SLOW_E%=
s_getpc_b64 s[16:17]
SLOW_PC%=:
s_add_u32 s16, s16, SLOW_L%=-SLOW_PC%=
s_addc_u32 s17, s17, -1
s_setpc_b64 s[16:17]
SLOW_E%=:
N 7
MV 228,220
N 1
v_permlane16_swap_b32 v220, v228
Fad 220,220,228
MV 228,220
N 1
v_permlane32_swap_b32 v220, v228
Fad 220,220,228
v_rcp_f32 v220, v220
MV 228,221
N 1
v_permlane16_swap_b32 v221, v228
Fad 221,221,228
MV 228,221
N 1
v_permlane32_swap_b32 v221, v228
Fad 221,221,228
v_rcp_f32 v221, v221
MV 228,222
N 1
v_permlane16_swap_b32 v222, v228
Fad 222,222,228
MV 228,222
N 1
v_permlane32_swap_b32 v222, v228
Fad 222,222,228
v_rcp_f32 v222, v222
MV 228,223
N 1
v_permlane16_swap_b32 v223, v228
Fad 223,223,228
MV 228,223
N 1
v_permlane32_swap_b32 v223, v228
Fad 223,223,228
v_rcp_f32 v223, v223
s_sub_u32 s16, 15, s13
s_cmp_ge_u32 s11, 2
s_cselect_b32 s16, s13, s16
s_lshr_b32 s17, s16, 1
s_lshl_b32 s17, s17, 21
s_and_b32 s16, s16, 1
s_lshl_b32 s16, s16, 13
s_add_u32 s17, s17, s16
s_lshl_b32 s16, s12, 16
s_add_u32 s17, s17, s16
s_and_b32 s16, s11, 1
s_lshl_b32 s16, s16, 12
s_add_u32 s17, s17, s16
G 0,0
G 4,16
G 1,1
G 5,17
G 2,2
G 6,18
G 3,3
G 7,19
Fmu 0,0,220
Fmu 1,1,220
Fmu 2,2,220
Fmu 3,3,220
Fmu 4,4,220
Fmu 5,5,220
Fmu 6,6,220
Fmu 7,7,220
C 8,0,1
C 9,2,3
C 10,4,5
C 11,6,7
Svs 8,196,32,17,0
G 0,4
G 4,20
G 1,5
G 5,21
G 2,6
G 6,22
G 3,7
G 7,23
Fmu 0,0,221
Fmu 1,1,221
Fmu 2,2,221
Fmu 3,3,221
Fmu 4,4,221
Fmu 5,5,221
Fmu 6,6,221
Fmu 7,7,221
C 8,0,1
C 9,2,3
C 10,4,5
C 11,6,7
Svs 8,196,32,17,1024
G 0,8
G 4,24
G 1,9
G 5,25
G 2,10
G 6,26
G 3,11
G 7,27
Fmu 0,0,222
Fmu 1,1,222
Fmu 2,2,222
Fmu 3,3,222
Fmu 4,4,222
Fmu 5,5,222
Fmu 6,6,222
Fmu 7,7,222
C 8,0,1
C 9,2,3
C 10,4,5
C 11,6,7
Svs 8,196,32,17,2048
G 0,12
G 4,28
G 1,13
G 5,29
G 2,14
G 6,30
G 3,15
G 7,31
Fmu 0,0,223
Fmu 1,1,223
Fmu 2,2,223
Fmu 3,3,223
Fmu 4,4,223
Fmu 5,5,223
Fmu 6,6,223
Fmu 7,7,223
C 8,0,1
C 9,2,3
C 10,4,5
C 11,6,7
Svs 8,196,32,17,3072
AS 17,17,16384
G 0,32
G 4,48
G 1,33
G 5,49
G 2,34
G 6,50
G 3,35
G 7,51
Fmu 0,0,220
Fmu 1,1,220
Fmu 2,2,220
Fmu 3,3,220
Fmu 4,4,220
Fmu 5,5,220
Fmu 6,6,220
Fmu 7,7,220
C 8,0,1
C 9,2,3
C 10,4,5
C 11,6,7
Svs 8,196,32,17,0
G 0,36
G 4,52
G 1,37
G 5,53
G 2,38
G 6,54
G 3,39
G 7,55
Fmu 0,0,221
Fmu 1,1,221
Fmu 2,2,221
Fmu 3,3,221
Fmu 4,4,221
Fmu 5,5,221
Fmu 6,6,221
Fmu 7,7,221
C 8,0,1
C 9,2,3
C 10,4,5
C 11,6,7
Svs 8,196,32,17,1024
G 0,40
G 4,56
G 1,41
G 5,57
G 2,42
G 6,58
G 3,43
G 7,59
Fmu 0,0,222
Fmu 1,1,222
Fmu 2,2,222
Fmu 3,3,222
Fmu 4,4,222
Fmu 5,5,222
Fmu 6,6,222
Fmu 7,7,222
C 8,0,1
C 9,2,3
C 10,4,5
C 11,6,7
Svs 8,196,32,17,2048
G 0,44
G 4,60
G 1,45
G 5,61
G 2,46
G 6,62
G 3,47
G 7,63
Fmu 0,0,223
Fmu 1,1,223
Fmu 2,2,223
Fmu 3,3,223
Fmu 4,4,223
Fmu 5,5,223
Fmu 6,6,223
Fmu 7,7,223
C 8,0,1
C 9,2,3
C 10,4,5
C 11,6,7
Svs 8,196,32,17,3072
AS 17,17,16384
G 0,64
G 4,80
G 1,65
G 5,81
G 2,66
G 6,82
G 3,67
G 7,83
Fmu 0,0,220
Fmu 1,1,220
Fmu 2,2,220
Fmu 3,3,220
Fmu 4,4,220
Fmu 5,5,220
Fmu 6,6,220
Fmu 7,7,220
C 8,0,1
C 9,2,3
C 10,4,5
C 11,6,7
Svs 8,196,32,17,0
G 0,68
G 4,84
G 1,69
G 5,85
G 2,70
G 6,86
G 3,71
G 7,87
Fmu 0,0,221
Fmu 1,1,221
Fmu 2,2,221
Fmu 3,3,221
Fmu 4,4,221
Fmu 5,5,221
Fmu 6,6,221
Fmu 7,7,221
C 8,0,1
C 9,2,3
C 10,4,5
C 11,6,7
Svs 8,196,32,17,1024
G 0,72
G 4,88
G 1,73
G 5,89
G 2,74
G 6,90
G 3,75
G 7,91
Fmu 0,0,222
Fmu 1,1,222
Fmu 2,2,222
Fmu 3,3,222
Fmu 4,4,222
Fmu 5,5,222
Fmu 6,6,222
Fmu 7,7,222
C 8,0,1
C 9,2,3
C 10,4,5
C 11,6,7
Svs 8,196,32,17,2048
G 0,76
G 4,92
G 1,77
G 5,93
G 2,78
G 6,94
G 3,79
G 7,95
Fmu 0,0,223
Fmu 1,1,223
Fmu 2,2,223
Fmu 3,3,223
Fmu 4,4,223
Fmu 5,5,223
Fmu 6,6,223
Fmu 7,7,223
C 8,0,1
C 9,2,3
C 10,4,5
C 11,6,7
Svs 8,196,32,17,3072
AS 17,17,16384
G 0,96
G 4,112
G 1,97
G 5,113
G 2,98
G 6,114
G 3,99
G 7,115
Fmu 0,0,220
Fmu 1,1,220
Fmu 2,2,220
Fmu 3,3,220
Fmu 4,4,220
Fmu 5,5,220
Fmu 6,6,220
Fmu 7,7,220
C 8,0,1
C 9,2,3
C 10,4,5
C 11,6,7
Svs 8,196,32,17,0
G 0,100
G 4,116
G 1,101
G 5,117
G 2,102
G 6,118
G 3,103
G 7,119
Fmu 0,0,221
Fmu 1,1,221
Fmu 2,2,221
Fmu 3,3,221
Fmu 4,4,221
Fmu 5,5,221
Fmu 6,6,221
Fmu 7,7,221
C 8,0,1
C 9,2,3
C 10,4,5
C 11,6,7
Svs 8,196,32,17,1024
G 0,104
G 4,120
G 1,105
G 5,121
G 2,106
G 6,122
G 3,107
G 7,123
Fmu 0,0,222
Fmu 1,1,222
Fmu 2,2,222
Fmu 3,3,222
Fmu 4,4,222
Fmu 5,5,222
Fmu 6,6,222
Fmu 7,7,222
C 8,0,1
C 9,2,3
C 10,4,5
C 11,6,7
Svs 8,196,32,17,2048
G 0,108
G 4,124
G 1,109
G 5,125
G 2,110
G 6,126
G 3,111
G 7,127
Fmu 0,0,223
Fmu 1,1,223
Fmu 2,2,223
Fmu 3,3,223
Fmu 4,4,223
Fmu 5,5,223
Fmu 6,6,223
Fmu 7,7,223
C 8,0,1
C 9,2,3
C 10,4,5
C 11,6,7
Svs 8,196,32,17,3072
s_setpc_b64 s[22:23]
KEND%=:
.purgem W
.purgem SM
.purgem VA
.purgem Lv
.purgem X
.purgem AS
.purgem AM
.purgem N
.purgem D
.purgem B
.purgem Rv
.purgem T
.purgem WV
.purgem Mavvz
.purgem MV
.purgem Fad
.purgem Mavvc
.purgem Fmu
.purgem LO
.purgem HI
.purgem FM
.purgem C
.purgem Svs
.purgem M0
.purgem Ds
.purgem Mvvvz
.purgem G
.purgem Mvvvc
.purgem DWv
.purgem Ra
.purgem Mvvaz
.purgem Mvvac
.purgem Fma
.purgem Mvvan
.purgem E
.purgem K
.purgem DWa
.purgem Fsu
.purgem Ffm
.purgem PM
.purgem Las
.purgem Mavaz
.purgem Mavac
.purgem La
.purgem Lvs
.purgem PA
.purgem GW
.purgem Ta
.purgem Mvavz
.purgem Mvavc
)A"
  :: "s"(__builtin_amdgcn_kernarg_segment_ptr()), "s"((int)blockIdx.x), "v"((int)threadIdx.x)
  : "v0", "v1", "v2", "v3", "v4", "v5", "v6", "v7", "v8", "v9", "v10", "v11", "v12", "v13", "v14", "v15", "v16", "v17", "v18", "v19", "v20", "v21", "v22", "v23", "v24", "v25", "v26", "v27", "v28", "v29", "v30", "v31", "v32", "v33", "v34", "v35", "v36", "v37", "v38", "v39", "v40", "v41", "v42", "v43", "v44", "v45", "v46", "v47", "v48", "v49", "v50", "v51", "v52", "v53", "v54", "v55", "v56", "v57", "v58", "v59", "v60", "v61", "v62", "v63", "v64", "v65", "v66", "v67", "v68", "v69", "v70", "v71", "v72", "v73", "v74", "v75", "v76", "v77", "v78", "v79", "v80", "v81", "v82", "v83", "v84", "v85", "v86", "v87", "v88", "v89", "v90", "v91", "v92", "v93", "v94", "v95", "v96", "v97", "v98", "v99", "v100", "v101", "v102", "v103", "v104", "v105", "v106", "v107", "v108", "v109", "v110", "v111", "v112", "v113", "v114", "v115", "v116", "v117", "v118", "v119", "v120", "v121", "v122", "v123", "v124", "v125", "v126", "v127", "v128", "v129", "v130", "v131", "v132", "v133", "v134", "v135", "v136", "v137", "v138", "v139", "v140", "v141", "v142", "v143", "v144", "v145", "v146", "v147", "v148", "v149", "v150", "v151", "v152", "v153", "v154", "v155", "v156", "v157", "v158", "v159", "v160", "v161", "v162", "v163", "v164", "v165", "v166", "v167", "v168", "v169", "v170", "v171", "v172", "v173", "v174", "v175", "v176", "v177", "v178", "v179", "v180", "v181", "v182", "v183", "v184", "v185", "v186", "v187", "v188", "v189", "v190", "v191", "v192", "v193", "v194", "v195", "v196", "v197", "v198", "v199", "v200", "v201", "v202", "v203", "v204", "v205", "v206", "v207", "v208", "v209", "v210", "v211", "v212", "v213", "v214", "v215", "v216", "v217", "v218", "v219", "v220", "v221", "v222", "v223", "v224", "v225", "v226", "v227", "v228", "v229", "v230", "v231", "v232", "v233", "v234", "v235", "v236", "v237", "v238", "v239", "v240", "v241", "v242", "v243", "v244", "v245", "v246", "v247", "v248", "v249", "v252", "v253", "v254", "v255", "a0", "a1", "a2", "a3", "a4", "a5", "a6", "a7", "a8", "a9", "a10", "a11", "a12", "a13", "a14", "a15", "a16", "a17", "a18", "a19", "a20", "a21", "a22", "a23", "a24", "a25", "a26", "a27", "a28", "a29", "a30", "a31", "a32", "a33", "a34", "a35", "a36", "a37", "a38", "a39", "a40", "a41", "a42", "a43", "a44", "a45", "a46", "a47", "a48", "a49", "a50", "a51", "a52", "a53", "a54", "a55", "a56", "a57", "a58", "a59", "a60", "a61", "a62", "a63", "a64", "a65", "a66", "a67", "a68", "a69", "a70", "a71", "a72", "a73", "a74", "a75", "a76", "a77", "a78", "a79", "a80", "a81", "a82", "a83", "a84", "a85", "a86", "a87", "a88", "a89", "a90", "a91", "a92", "a93", "a94", "a95", "a96", "a97", "a98", "a99", "a100", "a101", "a102", "a103", "a104", "a105", "a106", "a107", "a108", "a109", "a110", "a111", "a112", "a113", "a114", "a115", "a116", "a117", "a118", "a119", "a120", "a121", "a122", "a123", "a124", "a125", "a126", "a127", "a128", "a129", "a130", "a131", "a132", "a133", "a134", "a135", "a136", "a137", "a138", "a139", "a140", "a141", "a142", "a143", "a144", "a145", "a146", "a147", "a148", "a149", "a150", "a151", "a152", "a153", "a154", "a155", "a156", "a157", "a158", "a159", "a160", "a161", "a162", "a163", "a164", "a165", "a166", "a167", "a168", "a169", "a170", "a171", "a172", "a173", "a174", "a175", "a176", "a177", "a178", "a179", "a180", "a181", "a182", "a183", "a184", "a185", "a186", "a187", "a188", "a189", "a190", "a191", "a192", "a193", "a194", "a195", "a196", "a197", "a198", "a199", "a200", "a201", "a202", "a203", "a204", "a205", "a206", "a207", "a208", "a209", "a210", "a211", "a212", "a213", "a214", "a215", "a216", "a217", "a218", "a219", "a220", "a221", "a222", "a223", "a224", "a225", "a226", "a227", "a228", "a229", "a230", "a231", "a232", "a233", "a234", "a235", "a236", "a237", "a238", "a239", "a240", "a241", "a242", "a243", "a244", "a245", "a246", "a247", "a248", "a249", "a250", "a251", "a252", "a253", "a254", "a255", "s8", "s9", "s10", "s11", "s12", "s13", "s14", "s15", "s16", "s17", "s18", "s19", "s20", "s21", "s22", "s23", "s24", "s25", "s26", "s27", "s28", "s29", "s30", "s31", "s32", "s33", "s34", "s35", "s36", "s37", "s38", "s39", "s40", "s41", "s42", "s43", "s44", "s45", "s46", "s47", "s48", "s49", "s50", "s51", "s52", "s53", "s54", "s55", "s56", "s57", "s58", "s59", "s60", "s61", "s62", "s63", "s64", "s65", "s66", "s67", "s68", "s69", "s70", "s71", "s72", "s73", "s74", "s75", "s76", "s77", "s78", "s79", "s80", "s81", "s82", "s83", "s84", "s85", "s86", "s87", "s88", "s89", "s90", "s91", "s92", "s93", "s94", "s95", "s96", "s97", "s98", "s99", "vcc", "scc", "memory");
}

extern "C" void kernel_launch(void* const* d_in, const int* in_sizes, int n_in, void* d_out, int out_size, void* d_ws, size_t ws_size, hipStream_t stream) {
  k_main<<<256, 256, 0, stream>>>(d_in[0], d_in[1], d_in[2], d_in[3], d_in[4], d_in[5], d_in[7], d_in[8], d_in[9], d_in[10], d_out, d_ws);
}
```
